```python
import torch

def _softmax(x, axis=-1):
    return torch.softmax(x, dim=axis)
def _gelu(x):
    return torch.nn.functional.gelu(x, approximate="tanh")
def _silu(x):
    return torch.nn.functional.silu(x)
def _sigmoid(x):
    return torch.sigmoid(x)
def _layernorm(x, axis=-1, eps=1e-5):
    m = x.mean(dim=axis, keepdim=True)
    v = x.var(dim=axis, keepdim=True, unbiased=False)
    return (x - m) / torch.sqrt(v + eps)
def _scatter_add(tgt, idx, src, axis=0):
    return tgt.clone().index_add_(axis, idx, src)
def _segment_sum(src, seg, n):
    out = torch.zeros((n,) + src.shape[1:], dtype=src.dtype)
    return out.index_add_(0, seg, src)
def _pad(x, pw):
    flat = tuple(v for pair in reversed(pw) for v in pair)
    return torch.nn.functional.pad(x, flat)

kernel_name = 'standalone__gqa_softmax__near_ridge'

def setup_inputs():

    _w = (lambda a: (lambda t: t.to(torch.bfloat16) if t.is_floating_point() else t)(torch.as_tensor(a)))
    B, Hq, Hkv, Sq, Sk, D = 8, 32, 32, 608, 1024, 128
    return {
        "q": _w((lambda *s: torch.randn(*s, dtype=torch.bfloat16) * 0.8)(B, Hq, Sq, D)),
        "k": _w((lambda *s: torch.randn(*s, dtype=torch.bfloat16) * 0.8)(B, Hkv, Sk, D)),
        "v": _w((lambda *s: torch.randn(*s, dtype=torch.bfloat16) * 0.05)(B, Hkv, Sk, D)),
    }

def reference(q, k, v):
    B, Hq, Sq, D = q.shape
    Hkv = k.shape[1]
    rep = Hq // Hkv
    k = (lambda x, n, axis: torch.repeat_interleave(x, n, dim=axis))(k, rep, axis=1)
    v = (lambda x, n, axis: torch.repeat_interleave(x, n, dim=axis))(v, rep, axis=1)
    scale = D ** -0.5
    scores = torch.einsum("bhqd,bhkd->bhqk", q, k) * scale
    p = _softmax(scores, axis=-1)
    return torch.einsum("bhqk,bhkd->bhqd", p, v)
```

```cpp
#include <hip/hip_runtime.h>
#include <hip/hip_bf16.h>
#include <cstdio>
using bf16 = __hip_bfloat16;
constexpr int   D = 128, NW = 8, QBLK = 32, KVBLK = 64;
constexpr float SCALE = 0.088388347648318440f;
constexpr float THR = 8.f;
constexpr int SDEPTH = 2;
constexpr int B = 8, HQ = 32, HKV = 32, S = 608, SKV = 1024;
constexpr int LDQ = D, LDK = D, LDO = D;
constexpr size_t SHM_V = KVBLK * D * 2, SHM_K = KVBLK * D * 2, SHM_ATTN = 2 * SHM_V + 2 * SHM_K + NW * 64 * 4;
using bf16x8 = __attribute__((ext_vector_type(8))) short;
using s16x4  = __attribute__((ext_vector_type(4))) short;
using f32x16 = __attribute__((ext_vector_type(16))) float;
using u32x4  = __attribute__((ext_vector_type(4))) unsigned;
#define KSWZ(row, colB) ((row) * 256 + ((colB) ^ (((row) & 7) << 4)))
#define SBAR() __builtin_amdgcn_sched_barrier(0)
__device__ __forceinline__ int crow(int r, int hi) { return (r & 3) + 8 * (r >> 2) + 4 * hi; }
__device__ __forceinline__ unsigned cvtpk(float lo, float hi) {
  unsigned r; asm volatile("v_cvt_pk_bf16_f32 %0, %1, %2" : "=v"(r) : "v"(lo), "v"(hi)); return r;
}
__device__ __forceinline__ bf16x8 ld8(const bf16* p) { return *reinterpret_cast<const bf16x8*>(p); }
__device__ __forceinline__ void partialSM(f32x16& p0, f32x16& p1, float& m_reg, float& mn, float& alpha) {
  constexpr float C = SCALE * 1.4426950408889634f;
  float pmax = p0[0]; for (int r = 1; r < 16; ++r) pmax = fmaxf(pmax, p0[r]); for (int r = 0; r < 16; ++r) pmax = fmaxf(pmax, p1[r]);
  { auto rr = __builtin_amdgcn_permlane32_swap(__float_as_uint(pmax), __float_as_uint(pmax), false, false);
    pmax = fmaxf(__uint_as_float(rr[0]), __uint_as_float(rr[1])); }
  if (__builtin_expect(__all(pmax - m_reg <= THR / SCALE), 1)) { mn = m_reg; alpha = 1.f; }
  else { mn = fmaxf(m_reg, pmax); alpha = __builtin_amdgcn_exp2f((m_reg - mn) * C); m_reg = mn; }
  float mnC = -mn * C;
  for (int r = 0; r < 16; ++r) p0[r] = fmaf(p0[r], C, mnC); for (int r = 0; r < 16; ++r) p1[r] = fmaf(p1[r], C, mnC);
  for (int r = 0; r < 16; ++r) p0[r] = __builtin_amdgcn_exp2f(p0[r]);
}
__device__ __forceinline__ void finishSM(f32x16& p0, f32x16& p1, float alpha, float& l_reg, bf16x8& pa0, bf16x8& pa1, bf16x8& pa2, bf16x8& pa3) {
  for (int r = 0; r < 16; ++r) p1[r] = __builtin_amdgcn_exp2f(p1[r]);
  float ps = 0; for (int r = 0; r < 16; ++r) ps += p0[r]; for (int r = 0; r < 16; ++r) ps += p1[r];
  { auto rr = __builtin_amdgcn_permlane32_swap(__float_as_uint(ps), __float_as_uint(ps), false, false);
    ps = __uint_as_float(rr[0]) + __uint_as_float(rr[1]); }
  l_reg = l_reg * alpha + ps;
#define PK4(P, BASE, OUT) do { unsigned a0 = cvtpk(P[BASE + 0], P[BASE + 1]), a1 = cvtpk(P[BASE + 2], P[BASE + 3]);   \
    unsigned b0 = cvtpk(P[BASE + 4], P[BASE + 5]), b1 = cvtpk(P[BASE + 6], P[BASE + 7]);                              \
    auto r0 = __builtin_amdgcn_permlane32_swap(a0, b0, false, false); auto r1 = __builtin_amdgcn_permlane32_swap(a1, b1, false, false); \
    u32x4 w = {r0[0], r1[0], r0[1], r1[1]}; OUT = *reinterpret_cast<bf16x8*>(&w); } while (0)
  PK4(p0, 0, pa0); PK4(p0, 8, pa1); PK4(p1, 0, pa2); PK4(p1, 8, pa3);
#undef PK4
}
__device__ __forceinline__ void qkt(f32x16& p0, f32x16& p1, const bf16* Ks, const bf16x8* qr, int r32, int hi) {
  p0 = f32x16{}; p1 = f32x16{};
  for (int d0 = 0; d0 < 8; ++d0) { int cb = (d0 * 16 + hi * 8) * 2;
    bf16x8 b0 = *reinterpret_cast<const bf16x8*>((const char*)Ks + KSWZ(r32, cb));
    bf16x8 b1 = *reinterpret_cast<const bf16x8*>((const char*)Ks + KSWZ(32 + r32, cb));
    p0 = __builtin_amdgcn_mfma_f32_32x32x16_bf16(b0, qr[d0], p0, 0, 0, 0);
    p1 = __builtin_amdgcn_mfma_f32_32x32x16_bf16(b1, qr[d0], p1, 0, 0, 0); }
}
__device__ __forceinline__ int v_st(int k, int c) { const int kk = (k & ~0xC) | ((k & 4) << 1) | ((k & 8) >> 1); return ((kk >> 3) * 4 + (c >> 5)) * 512 + ((kk & 7) * 32 + (c & 31)) * 2; }
__device__ __forceinline__ int v_rd_base(int lane) { return ((lane & 3) << 3) | (((lane >> 2) & 3) << 6) | (((lane >> 4) & 1) << 5) | (((lane >> 5) & 1) << 8); }
constexpr int v_rd_off(int d0, int ks, int half) { return d0 * 512 + ks * 4096 + half * 2048; }
template <int OFF> __device__ __forceinline__ s16x4 tr_read(int vb) {
  s16x4 r; asm volatile("ds_read_b64_tr_b16 %0, %1 offset:%2" : "=&v"(r) : "v"(vb), "i"(OFF) : "memory"); return r;
}
template <int D0> __device__ __forceinline__ void pv_one(f32x16& od, int vb, bf16x8 pa0, bf16x8 pa1, bf16x8 pa2, bf16x8 pa3) {
  const s16x4 l0 = tr_read<v_rd_off(D0, 0, 0)>(vb), h0 = tr_read<v_rd_off(D0, 0, 1)>(vb), l1 = tr_read<v_rd_off(D0, 1, 0)>(vb), h1 = tr_read<v_rd_off(D0, 1, 1)>(vb);
  const s16x4 l2 = tr_read<v_rd_off(D0, 2, 0)>(vb), h2 = tr_read<v_rd_off(D0, 2, 1)>(vb), l3 = tr_read<v_rd_off(D0, 3, 0)>(vb), h3 = tr_read<v_rd_off(D0, 3, 1)>(vb);
  asm volatile("s_waitcnt lgkmcnt(0)" ::: "memory"); SBAR();
#define PK(L, H) (bf16x8){L[0], L[1], L[2], L[3], H[0], H[1], H[2], H[3]}
  od = __builtin_amdgcn_mfma_f32_32x32x16_bf16(pa0, PK(l0, h0), od, 0, 0, 0);
  od = __builtin_amdgcn_mfma_f32_32x32x16_bf16(pa1, PK(l1, h1), od, 0, 0, 0);
  od = __builtin_amdgcn_mfma_f32_32x32x16_bf16(pa2, PK(l2, h2), od, 0, 0, 0);
  od = __builtin_amdgcn_mfma_f32_32x32x16_bf16(pa3, PK(l3, h3), od, 0, 0, 0);
#undef PK
}
__device__ __forceinline__ void pv_d0(f32x16* o, int vb, bf16x8 pa0, bf16x8 pa1, bf16x8 pa2, bf16x8 pa3) {
  pv_one<0>(o[0], vb, pa0, pa1, pa2, pa3); pv_one<1>(o[1], vb, pa0, pa1, pa2, pa3); pv_one<2>(o[2], vb, pa0, pa1, pa2, pa3); pv_one<3>(o[3], vb, pa0, pa1, pa2, pa3);
}
__device__ __forceinline__ void attn_dense_body(const bf16* __restrict__ Qb, const bf16* __restrict__ Kh, const bf16* __restrict__ Vh,
                                                bf16* __restrict__ Ob, int seq, int nrows, char* lds) {
  const int tid = threadIdx.x, wid = tid >> 6, lane = tid & 63, r32 = lane & 31, hi = lane >> 5;
  bf16* V_lds = (bf16*)lds; bf16* K_lds = (bf16*)(lds + 2 * SHM_V);
  float* ws = (float*)(lds + 2 * SHM_V + 2 * SHM_K) + wid * 64; float* li_l = ws; float* al_l = ws + 32;
  float m_reg = -1e30f, l_reg = 0; f32x16 o[4] = {}; bf16x8 qr[8];
  int qrow = wid * QBLK + r32; if (qrow >= nrows) qrow = nrows - 1;
  const bf16* Qw = Qb + (long)(qrow) * LDQ + hi * 8;
#pragma unroll
  for (int d0 = 0; d0 < 8; ++d0) qr[d0] = ld8(Qw + d0 * 16);
  const int sr = tid >> 4, sc = (tid & 15) * 8, vst0 = v_st(sr, sc), vst1 = v_st(32 + sr, sc);
  const int vb0 = (int)(uintptr_t)V_lds + v_rd_base(lane);
  struct { bf16x8 vs0, vs1, ks0, ks1; } sr_[SDEPTH];
#define SLOAD(i, k0) do { sr_[i].vs0 = ld8(&Vh[(long)((k0) + sr) * LDK + sc]); sr_[i].vs1 = ld8(&Vh[(long)((k0) + 32 + sr) * LDK + sc]); \
    sr_[i].ks0 = ld8(&Kh[(long)((k0) + sr) * LDK + sc]); sr_[i].ks1 = ld8(&Kh[(long)((k0) + 32 + sr) * LDK + sc]); } while (0)
#define SWRITE(b, i) do { *(bf16x8*)((char*)V_lds + (b) * SHM_V + vst0) = (sr_[i].vs0);          \
    *(bf16x8*)((char*)V_lds + (b) * SHM_V + vst1) = (sr_[i].vs1); int kc = sc * 2;               \
    *(bf16x8*)((char*)K_lds + (b) * SHM_K + KSWZ(sr, kc)) = (sr_[i].ks0);                       \
    *(bf16x8*)((char*)K_lds + (b) * SHM_K + KSWZ(32 + sr, kc)) = (sr_[i].ks1); } while (0)
#define SWAIT() do { asm volatile("s_waitcnt vmcnt(4)" ::: "memory"); } while (0)
#define RESC(a) do { if (__any((a) < 1.f)) { if (hi == 0) al_l[r32] = (a); asm volatile("s_waitcnt lgkmcnt(0)" ::: "memory"); \
    for (int d = 0; d < 4; ++d) for (int r = 0; r < 16; ++r) o[d][r] *= al_l[crow(r, hi)]; } } while (0)
  f32x16 pA0, pA1, pB0, pB1; float mnA, mnB, alA, alB; bf16x8 pa0, pa1, pa2, pa3; const int NT = seq / KVBLK;
  constexpr int SE = 0, SO = SDEPTH - 1;
  SLOAD(SE, 0); asm volatile("s_waitcnt vmcnt(0)" ::: "memory"); SWRITE(0, SE); __syncthreads();
  qkt(pA0, pA1, K_lds, qr, r32, hi); partialSM(pA0, pA1, m_reg, mnA, alA);
  SLOAD(SO, KVBLK); if constexpr (SDEPTH == 2) { if (2 < NT) SLOAD(SE, 2 * KVBLK); }
  SWAIT(); SWRITE(1, SO); __syncthreads();
  for (int j = 1; j + 1 < NT; j += 2) {
    SBAR(); qkt(pB0, pB1, (bf16*)((char*)K_lds + SHM_K), qr, r32, hi);
    finishSM(pA0, pA1, alA, l_reg, pa0, pa1, pa2, pa3); SBAR();
    SLOAD(SO, (j + SDEPTH) * KVBLK); SBAR();
    pv_d0(o, vb0, pa0, pa1, pa2, pa3); partialSM(pB0, pB1, m_reg, mnB, alB);
    __syncthreads(); SWAIT(); SWRITE(0, SE);
    RESC(alB); __syncthreads();
    SBAR(); qkt(pA0, pA1, K_lds, qr, r32, hi);
    finishSM(pB0, pB1, alB, l_reg, pa0, pa1, pa2, pa3); SBAR();
    if (SDEPTH == 1 || j + 3 < NT) SLOAD(SE, (j + 1 + SDEPTH) * KVBLK); SBAR();
    pv_d0(o, vb0 + (int)SHM_V, pa0, pa1, pa2, pa3); partialSM(pA0, pA1, m_reg, mnA, alA);
    __syncthreads(); SWAIT(); SWRITE(1, SO);
    RESC(alA); __syncthreads();
  }
  SBAR(); qkt(pB0, pB1, (bf16*)((char*)K_lds + SHM_K), qr, r32, hi);
  finishSM(pA0, pA1, alA, l_reg, pa0, pa1, pa2, pa3); SBAR();
  pv_d0(o, vb0, pa0, pa1, pa2, pa3); partialSM(pB0, pB1, m_reg, mnB, alB);
  __syncthreads(); RESC(alB);
  finishSM(pB0, pB1, alB, l_reg, pa0, pa1, pa2, pa3); SBAR();
  pv_d0(o, vb0 + (int)SHM_V, pa0, pa1, pa2, pa3);
  if (hi == 0) li_l[r32] = l_reg; asm volatile("s_waitcnt lgkmcnt(0)" ::: "memory");
  float rli[16];
#pragma unroll
  for (int r = 0; r < 16; ++r) rli[r] = __builtin_amdgcn_rcpf(li_l[crow(r, hi)]);
  bf16* Ow = Ob + (long)(wid * QBLK) * LDO;
  if (wid * QBLK < nrows) {
#pragma unroll
  for (int r = 0; r < 16; ++r) { int orow = crow(r, hi);
    for (int d0 = 0; d0 < 4; ++d0) Ow[(long)orow * LDO + d0 * 32 + r32] = __float2bfloat16(o[d0][r] * rli[r]); }
  }
}
__global__ __launch_bounds__(64 * NW, 1)
void attn_fwd_dense(const bf16* __restrict__ Q, const bf16* __restrict__ K, const bf16* __restrict__ V, bf16* __restrict__ O) {
  extern __shared__ __attribute__((aligned(16))) char shm[];
  const long bh = blockIdx.y, i0 = (long)blockIdx.x * (QBLK * NW);
  const long q0 = (bh * S + i0) * D, k0 = bh * SKV * (long)D;
  attn_dense_body(Q + q0, K + k0, V + k0, O + q0, SKV, S - (int)i0, shm);
}
extern "C" void kernel_launch(void* const* d_in, const int* in_sizes, int n_in, void* d_out, int out_size, void* d_ws, size_t ws_size, hipStream_t stream) {
  hipFuncSetAttribute((const void*)attn_fwd_dense, hipFuncAttributeMaxDynamicSharedMemorySize, (int)SHM_ATTN);
  hipLaunchKernelGGL(attn_fwd_dense, dim3(3, B * HQ), dim3(64 * NW), SHM_ATTN, stream,
                     (const bf16*)d_in[0], (const bf16*)d_in[1], (const bf16*)d_in[2], (bf16*)d_out);
}
```

```cpp
#include <hip/hip_runtime.h>
#include <hip/hip_bf16.h>

extern "C" __global__ __launch_bounds__(256, 1)
void attn_fwd_asm(const void* __restrict__ Q, const void* __restrict__ K, const void* __restrict__ V, void* __restrict__ O) {
  asm volatile(
    "s_getpc_b64 s[82:83]\n"
    "LCODE0:\n"
    "s_lshr_b32 s67, %4, 3\n"
    "s_lshl_b32 s67, s67, 12\n"
    "s_min_u32 s67, s67, LHOTEND-LCODE0-4096\n"
    "v_lshlrev_b32 v230, 4, %5\n"
    "v_add_u32 v230, s67, v230\n"
    "global_load_dwordx4 v[244:247], v230, s[82:83]\n"
    "s_mov_b64 s[54:55], %0\n"
    "s_mov_b64 s[56:57], %1\n"
    "s_mov_b64 s[58:59], %2\n"
    "s_mov_b64 s[62:63], %3\n"
    "s_mov_b32 s64, %4\n"
    "v_lshrrev_b32 v8, 6, %5\n"
    "v_and_b32 v9, 63, %5\n"
    "v_readfirstlane_b32 s39, v8\n"
    "s_and_b32 s65, s64, 7\n"
    "s_lshr_b32 s66, s64, 4\n"
    "s_lshl_b32 s66, s66, 3\n"
    "s_add_u32 s65, s65, s66\n"
    "s_lshr_b32 s66, s64, 3\n"
    "s_and_b32 s66, s66, 1\n"
    "s_mul_i32 s67, s65, 524288\n"
    "s_mul_hi_u32 s68, s65, 524288\n"
    "s_add_u32 s20, s56, s67\n"
    "s_addc_u32 s21, s57, s68\n"
    "s_and_b32 s21, s21, 0xffff\n"
    "s_mov_b32 s22, 524288\n"
    "s_mov_b32 s23, 0x20000\n"
    "s_mul_i32 s67, s65, 524288\n"
    "s_mul_hi_u32 s68, s65, 524288\n"
    "s_add_u32 s24, s58, s67\n"
    "s_addc_u32 s25, s59, s68\n"
    "s_and_b32 s25, s25, 0xffff\n"
    "s_mov_b32 s26, 524288\n"
    "s_mov_b32 s27, 0x20000\n"
    "s_mul_i32 s67, s65, 311296\n"
    "s_mul_hi_u32 s68, s65, 311296\n"
    "s_add_u32 s16, s54, s67\n"
    "s_addc_u32 s17, s55, s68\n"
    "s_and_b32 s17, s17, 0xffff\n"
    "s_mov_b32 s18, 311296\n"
    "s_mov_b32 s19, 0x20000\n"
    "s_mul_i32 s67, s65, 311296\n"
    "s_mul_hi_u32 s68, s65, 311296\n"
    "s_add_u32 s28, s62, s67\n"
    "s_addc_u32 s29, s63, s68\n"
    "s_and_b32 s29, s29, 0xffff\n"
    "s_mov_b32 s30, 311296\n"
    "s_mov_b32 s31, 0x20000\n"
    "s_mov_b32 s40, 0x47800000\n"
    "s_mov_b32 s41, 0x3e0293ee\n"
    "s_mov_b32 s42, 0xffff0000\n"
    "s_mov_b32 s38, 0\n"
    "v_lshlrev_b32 v1, 4, v9\n"
    "v_and_b32 v10, 15, v9\n"
    "v_lshrrev_b32 v11, 4, v9\n"
    "v_lshlrev_b32 v12, 8, v10\n"
    "v_lshl_add_u32 v6, v11, 4, v12\n"
    "v_and_b32 v12, 7, v10\n"
    "v_lshlrev_b32 v12, 8, v12\n"
    "v_lshrrev_b32 v13, 3, v10\n"
    "v_lshl_add_u32 v12, v13, 6, v12\n"
    "v_and_b32 v13, 1, v11\n"
    "v_lshl_add_u32 v12, v13, 5, v12\n"
    "v_lshrrev_b32 v13, 1, v11\n"
    "v_lshl_add_u32 v7, v13, 4, v12\n"
    "v_mul_u32_u24 v12, 1056, v10\n"
    "v_lshl_add_u32 v2, v11, 4, v12\n"
    "v_lshrrev_b32 v12, 2, v9\n"
    "v_mul_u32_u24 v12, 1056, v12\n"
    "v_and_b32 v13, 3, v9\n"
    "v_lshl_add_u32 v12, v13, 3, v12\n"
    "v_add_u32 v3, 16896, v12\n"
    "v_mov_b32 v4, v2\n"
    "v_and_b32 v13, 3, v9\n"
    "v_mov_b32 v12, 0x3f803f80\n"
    "v_cmp_eq_u32 vcc, 0, v13\n"
    "s_nop 1\n"
    "v_cndmask_b32 v16, 0, v12, vcc\n"
    "v_cndmask_b32 v17, 0, v12, vcc\n"
    "v_cndmask_b32 v18, 0, v12, vcc\n"
    "v_cndmask_b32 v19, 0, v12, vcc\n"
    "s_mov_b32 s36, 0\n"
    "s_mov_b32 s50, 0\n"
    "s_lshl_b32 s67, s39, 14\n"
    "s_add_u32 s67, s67, 101376\n"
    "v_add_u32 v228, s67, v1\n"
    "v_mov_b32 v229, v228\n"
    "v_lshrrev_b32 v12, 1, v11\n"
    "v_xor_b32 v12, v12, v10\n"
    "v_lshlrev_b32 v12, 4, v12\n"
    "v_lshl_add_u32 v12, v10, 8, v12\n"
    "v_and_b32 v13, 1, v11\n"
    "v_lshl_add_u32 v12, v13, 3, v12\n"
    "v_add_u32 v231, s67, v12\n"
    "s_mul_i32 s35, s39, 4224\n"
    "s_mul_i32 s32, s39, 4096\n"
    "s_add_u32 s33, s32, 4096\n"
    "s_mul_i32 s67, s66, 77824\n"
    "s_mul_i32 s68, s39, 20480\n"
    "s_add_u32 s43, s67, s68\n"
    "s_add_u32 s44, s43, 155648\n"
    "s_cmp_eq_u32 s39, 3\n"
    "s_cbranch_scc1 LV4\n"
    "LV5:\n"
    "s_mov_b32 s34, s35\n"
    "s_add_u32 s53, s44, 4096\n"
    "s_add_u32 s60, s44, 20480\n"
    "buffer_load_dwordx4 v[100:103], v6, s[16:19], s43 offen offset:0 \n"
    "buffer_load_dwordx4 v[104:107], v6, s[16:19], s43 offen offset:64 \n"
    "buffer_load_dwordx4 v[108:111], v6, s[16:19], s43 offen offset:128 \n"
    "buffer_load_dwordx4 v[112:115], v6, s[16:19], s43 offen offset:192 \n"
    "s_add_u32 m0, s34, 0\n"
    "s_nop 0\n"
    "buffer_load_dwordx4 v1, s[20:23], s32 offen offset:0  lds\n"
    "s_add_u32 m0, s34, 32\n"
    "s_nop 0\n"
    "buffer_load_dwordx4 v1, s[20:23], s32 offen offset:1024  lds\n"
    "s_add_u32 m0, s34, 64\n"
    "s_nop 0\n"
    "buffer_load_dwordx4 v1, s[20:23], s32 offen offset:2048  lds\n"
    "s_add_u32 m0, s34, 96\n"
    "s_nop 0\n"
    "buffer_load_dwordx4 v1, s[20:23], s32 offen offset:3072  lds\n"
    "s_add_u32 s48, s43, 4096\n"
    "buffer_load_dwordx4 v[116:119], v6, s[16:19], s48 offen offset:0 \n"
    "buffer_load_dwordx4 v[120:123], v6, s[16:19], s48 offen offset:64 \n"
    "buffer_load_dwordx4 v[124:127], v6, s[16:19], s48 offen offset:128 \n"
    "buffer_load_dwordx4 v[128:131], v6, s[16:19], s48 offen offset:192 \n"
    "s_waitcnt vmcnt(8)\n"
    "v_lshlrev_b32 v31, 16, v100\n"
    "v_and_b32 v255, s42, v100\n"
    "v_mul_f32 v31, s41, v31\n"
    "v_mul_f32 v255, s41, v255\n"
    "v_cvt_pk_bf16_f32 v100, v31, v255\n"
    "v_lshlrev_b32 v31, 16, v101\n"
    "v_and_b32 v255, s42, v101\n"
    "v_mul_f32 v31, s41, v31\n"
    "v_mul_f32 v255, s41, v255\n"
    "v_cvt_pk_bf16_f32 v101, v31, v255\n"
    "v_lshlrev_b32 v31, 16, v102\n"
    "v_and_b32 v255, s42, v102\n"
    "v_mul_f32 v31, s41, v31\n"
    "s_add_u32 m0, s34, 16896\n"
    "s_nop 0\n"
    "buffer_load_dwordx4 v1, s[24:27], s32 offen offset:0  lds\n"
    "v_mul_f32 v255, s41, v255\n"
    "v_cvt_pk_bf16_f32 v102, v31, v255\n"
    "v_lshlrev_b32 v31, 16, v103\n"
    "v_and_b32 v255, s42, v103\n"
    "v_mul_f32 v31, s41, v31\n"
    "v_mul_f32 v255, s41, v255\n"
    "v_cvt_pk_bf16_f32 v103, v31, v255\n"
    "v_lshlrev_b32 v31, 16, v104\n"
    "v_and_b32 v255, s42, v104\n"
    "v_mul_f32 v31, s41, v31\n"
    "v_mul_f32 v255, s41, v255\n"
    "v_cvt_pk_bf16_f32 v104, v31, v255\n"
    "v_lshlrev_b32 v31, 16, v105\n"
    "s_add_u32 m0, s34, 16928\n"
    "s_nop 0\n"
    "buffer_load_dwordx4 v1, s[24:27], s32 offen offset:1024  lds\n"
    "v_and_b32 v255, s42, v105\n"
    "v_mul_f32 v31, s41, v31\n"
    "v_mul_f32 v255, s41, v255\n"
    "v_cvt_pk_bf16_f32 v105, v31, v255\n"
    "v_lshlrev_b32 v31, 16, v106\n"
    "v_and_b32 v255, s42, v106\n"
    "v_mul_f32 v31, s41, v31\n"
    "v_mul_f32 v255, s41, v255\n"
    "v_cvt_pk_bf16_f32 v106, v31, v255\n"
    "v_lshlrev_b32 v31, 16, v107\n"
    "v_and_b32 v255, s42, v107\n"
    "v_mul_f32 v31, s41, v31\n"
    "v_mul_f32 v255, s41, v255\n"
    "s_add_u32 m0, s34, 16960\n"
    "s_nop 0\n"
    "buffer_load_dwordx4 v1, s[24:27], s32 offen offset:2048  lds\n"
    "v_cvt_pk_bf16_f32 v107, v31, v255\n"
    "v_lshlrev_b32 v31, 16, v108\n"
    "v_and_b32 v255, s42, v108\n"
    "v_mul_f32 v31, s41, v31\n"
    "v_mul_f32 v255, s41, v255\n"
    "v_cvt_pk_bf16_f32 v108, v31, v255\n"
    "v_lshlrev_b32 v31, 16, v109\n"
    "v_and_b32 v255, s42, v109\n"
    "v_mul_f32 v31, s41, v31\n"
    "v_mul_f32 v255, s41, v255\n"
    "v_cvt_pk_bf16_f32 v109, v31, v255\n"
    "v_lshlrev_b32 v31, 16, v110\n"
    "v_and_b32 v255, s42, v110\n"
    "s_add_u32 m0, s34, 16992\n"
    "s_nop 0\n"
    "buffer_load_dwordx4 v1, s[24:27], s32 offen offset:3072  lds\n"
    "s_add_u32 s32, s32, 16384\n"
    "v_mul_f32 v31, s41, v31\n"
    "v_mul_f32 v255, s41, v255\n"
    "v_cvt_pk_bf16_f32 v110, v31, v255\n"
    "v_lshlrev_b32 v31, 16, v111\n"
    "v_and_b32 v255, s42, v111\n"
    "v_mul_f32 v31, s41, v31\n"
    "v_mul_f32 v255, s41, v255\n"
    "v_cvt_pk_bf16_f32 v111, v31, v255\n"
    "v_lshlrev_b32 v31, 16, v112\n"
    "v_and_b32 v255, s42, v112\n"
    "v_mul_f32 v31, s41, v31\n"
    "v_mul_f32 v255, s41, v255\n"
    "v_cvt_pk_bf16_f32 v112, v31, v255\n"
    "s_add_u32 s48, s43, 8192\n"
    "buffer_load_dwordx4 v[132:135], v6, s[16:19], s48 offen offset:0 \n"
    "v_lshlrev_b32 v31, 16, v113\n"
    "v_and_b32 v255, s42, v113\n"
    "v_mul_f32 v31, s41, v31\n"
    "v_mul_f32 v255, s41, v255\n"
    "v_cvt_pk_bf16_f32 v113, v31, v255\n"
    "v_lshlrev_b32 v31, 16, v114\n"
    "v_and_b32 v255, s42, v114\n"
    "v_mul_f32 v31, s41, v31\n"
    "v_mul_f32 v255, s41, v255\n"
    "v_cvt_pk_bf16_f32 v114, v31, v255\n"
    "v_lshlrev_b32 v31, 16, v115\n"
    "v_and_b32 v255, s42, v115\n"
    "v_mul_f32 v31, s41, v31\n"
    "buffer_load_dwordx4 v[136:139], v6, s[16:19], s48 offen offset:64 \n"
    "v_mul_f32 v255, s41, v255\n"
    "v_cvt_pk_bf16_f32 v115, v31, v255\n"
    "s_waitcnt vmcnt(10)\n"
    "s_barrier\n"
    "ds_read_b128 a[160:163], v4 offset:0\n"
    "ds_read_b128 a[164:167], v4 offset:64\n"
    "ds_read_b128 a[168:171], v4 offset:128\n"
    "ds_read_b128 a[172:175], v4 offset:192\n"
    "ds_read_b128 a[176:179], v4 offset:256\n"
    "ds_read_b128 a[180:183], v4 offset:320\n"
    "ds_read_b128 a[184:187], v4 offset:384\n"
    "ds_read_b128 a[188:191], v4 offset:448\n"
    "ds_read_b128 a[192:195], v4 offset:512\n"
    "ds_read_b128 a[196:199], v4 offset:576\n"
    "ds_read_b128 a[200:203], v4 offset:640\n"
    "ds_read_b128 a[204:207], v4 offset:704\n"
    "ds_read_b128 a[208:211], v4 offset:768\n"
    "ds_read_b128 a[212:215], v4 offset:832\n"
    "ds_read_b128 a[216:219], v4 offset:896\n"
    "ds_read_b128 a[220:223], v4 offset:960\n"
    "s_waitcnt lgkmcnt(8)\n"
    "v_mfma_f32_16x16x32_bf16 v[48:51], a[160:163], v[100:103], 0\n"
    "s_waitcnt vmcnt(6)\n"
    "v_lshlrev_b32 v31, 16, v116\n"
    "v_and_b32 v255, s42, v116\n"
    "v_mul_f32 v31, s41, v31\n"
    "v_mul_f32 v255, s41, v255\n"
    "v_cvt_pk_bf16_f32 v116, v31, v255\n"
    "v_lshlrev_b32 v31, 16, v117\n"
    "v_and_b32 v255, s42, v117\n"
    "v_mul_f32 v31, s41, v31\n"
    "v_mul_f32 v255, s41, v255\n"
    "v_cvt_pk_bf16_f32 v117, v31, v255\n"
    "v_lshlrev_b32 v31, 16, v118\n"
    "v_and_b32 v255, s42, v118\n"
    "v_mul_f32 v31, s41, v31\n"
    "v_mul_f32 v255, s41, v255\n"
    "v_cvt_pk_bf16_f32 v118, v31, v255\n"
    "v_lshlrev_b32 v31, 16, v119\n"
    "v_and_b32 v255, s42, v119\n"
    "v_mul_f32 v31, s41, v31\n"
    "v_mul_f32 v255, s41, v255\n"
    "v_cvt_pk_bf16_f32 v119, v31, v255\n"
    "v_lshlrev_b32 v31, 16, v120\n"
    "v_and_b32 v255, s42, v120\n"
    "v_mul_f32 v31, s41, v31\n"
    "v_mul_f32 v255, s41, v255\n"
    "v_cvt_pk_bf16_f32 v120, v31, v255\n"
    "v_lshlrev_b32 v31, 16, v121\n"
    "v_and_b32 v255, s42, v121\n"
    "v_mul_f32 v31, s41, v31\n"
    "v_mul_f32 v255, s41, v255\n"
    "v_cvt_pk_bf16_f32 v121, v31, v255\n"
    "v_lshlrev_b32 v31, 16, v122\n"
    "v_and_b32 v255, s42, v122\n"
    "v_mul_f32 v31, s41, v31\n"
    "v_mul_f32 v255, s41, v255\n"
    "v_cvt_pk_bf16_f32 v122, v31, v255\n"
    "v_lshlrev_b32 v31, 16, v123\n"
    "v_and_b32 v255, s42, v123\n"
    "v_mul_f32 v31, s41, v31\n"
    "v_mul_f32 v255, s41, v255\n"
    "v_cvt_pk_bf16_f32 v123, v31, v255\n"
    "v_lshlrev_b32 v31, 16, v124\n"
    "v_and_b32 v255, s42, v124\n"
    "v_mul_f32 v31, s41, v31\n"
    "v_mul_f32 v255, s41, v255\n"
    "v_cvt_pk_bf16_f32 v124, v31, v255\n"
    "v_lshlrev_b32 v31, 16, v125\n"
    "v_and_b32 v255, s42, v125\n"
    "v_mul_f32 v31, s41, v31\n"
    "v_mul_f32 v255, s41, v255\n"
    "v_cvt_pk_bf16_f32 v125, v31, v255\n"
    "v_mfma_f32_16x16x32_bf16 v[48:51], a[164:167], v[104:107], v[48:51]\n"
    "v_lshlrev_b32 v31, 16, v126\n"
    "v_and_b32 v255, s42, v126\n"
    "v_mul_f32 v31, s41, v31\n"
    "v_mul_f32 v255, s41, v255\n"
    "v_mfma_f32_16x16x32_bf16 v[48:51], a[168:171], v[108:111], v[48:51]\n"
    "buffer_load_dwordx4 v[140:143], v6, s[16:19], s48 offen offset:128 \n"
    "v_cvt_pk_bf16_f32 v126, v31, v255\n"
    "v_lshlrev_b32 v31, 16, v127\n"
    "v_mfma_f32_16x16x32_bf16 v[48:51], a[172:175], v[112:115], v[48:51]\n"
    "buffer_load_dwordx4 v[144:147], v6, s[16:19], s48 offen offset:192 \n"
    "v_and_b32 v255, s42, v127\n"
    "v_mul_f32 v31, s41, v31\n"
    "v_mfma_f32_16x16x32_bf16 v[52:55], a[176:179], v[100:103], 0\n"
    "v_mul_f32 v255, s41, v255\n"
    "v_cvt_pk_bf16_f32 v127, v31, v255\n"
    "s_add_u32 s48, s43, 12288\n"
    "buffer_load_dwordx4 v[148:151], v6, s[16:19], s48 offen offset:0 \n"
    "v_mfma_f32_16x16x32_bf16 v[52:55], a[180:183], v[104:107], v[52:55]\n"
    "v_lshlrev_b32 v31, 16, v128\n"
    "v_and_b32 v255, s42, v128\n"
    "v_mul_f32 v31, s41, v31\n"
    "v_mul_f32 v255, s41, v255\n"
    "v_mfma_f32_16x16x32_bf16 v[52:55], a[184:187], v[108:111], v[52:55]\n"
    "buffer_load_dwordx4 v[152:155], v6, s[16:19], s48 offen offset:64 \n"
    "v_cvt_pk_bf16_f32 v128, v31, v255\n"
    "v_lshlrev_b32 v31, 16, v129\n"
    "v_mfma_f32_16x16x32_bf16 v[52:55], a[188:191], v[112:115], v[52:55]\n"
    "buffer_load_dwordx4 v[156:159], v6, s[16:19], s48 offen offset:128 \n"
    "v_and_b32 v255, s42, v129\n"
    "v_mul_f32 v31, s41, v31\n"
    "s_waitcnt lgkmcnt(0)\n"
    "v_mfma_f32_16x16x32_bf16 v[56:59], a[192:195], v[100:103], 0\n"
    "v_mul_f32 v255, s41, v255\n"
    "v_cvt_pk_bf16_f32 v129, v31, v255\n"
    "v_lshlrev_b32 v31, 16, v130\n"
    "v_and_b32 v255, s42, v130\n"
    "v_mfma_f32_16x16x32_bf16 v[56:59], a[196:199], v[104:107], v[56:59]\n"
    "buffer_load_dwordx4 v[160:163], v6, s[16:19], s48 offen offset:192 \n"
    "v_mul_f32 v31, s41, v31\n"
    "v_mul_f32 v255, s41, v255\n"
    "v_mfma_f32_16x16x32_bf16 v[56:59], a[200:203], v[108:111], v[56:59]\n"
    "v_cvt_pk_bf16_f32 v130, v31, v255\n"
    "v_lshlrev_b32 v31, 16, v131\n"
    "v_and_b32 v255, s42, v131\n"
    "v_mul_f32 v31, s41, v31\n"
    "v_mfma_f32_16x16x32_bf16 v[56:59], a[204:207], v[112:115], v[56:59]\n"
    "s_add_u32 s48, s43, 16384\n"
    "buffer_load_dwordx4 v[164:167], v6, s[16:19], s48 offen offset:0 \n"
    "v_mul_f32 v255, s41, v255\n"
    "v_cvt_pk_bf16_f32 v131, v31, v255\n"
    "v_mfma_f32_16x16x32_bf16 v[60:63], a[208:211], v[100:103], 0\n"
    "v_max3_f32 v25, v48, v49, v50\n"
    "v_max3_f32 v26, v51, v52, v53\n"
    "v_mfma_f32_16x16x32_bf16 v[60:63], a[212:215], v[104:107], v[60:63]\n"
    "buffer_load_dwordx4 v[168:171], v6, s[16:19], s48 offen offset:64 \n"
    "v_mfma_f32_16x16x32_bf16 v[60:63], a[216:219], v[108:111], v[60:63]\n"
    "v_mfma_f32_16x16x32_bf16 v[60:63], a[220:223], v[112:115], v[60:63]\n"
    "v_max3_f32 v25, v25, v54, v55\n"
    "v_max3_f32 v26, v26, v56, v57\n"
    "v_max3_f32 v25, v25, v58, v59\n"
    "s_nop 4\n"
    "v_max3_f32 v26, v26, v60, v61\n"
    "v_max3_f32 v25, v25, v62, v63\n"
    "v_max_f32 v25, v25, v26\n"
    "s_waitcnt vmcnt(10)\n"
    "s_barrier\n"
    "v_mfma_f32_16x16x32_bf16 v[64:67], a[160:163], v[116:119], 0\n"
    "v_mov_b32 v26, v25\n"
    "s_nop 1\n"
    "v_permlane16_swap_b32 v25, v26\n"
    "v_max_f32 v25, v25, v26\n"
    "v_mov_b32 v26, v25\n"
    "s_nop 1\n"
    "v_permlane32_swap_b32 v25, v26\n"
    "v_max_f32 v25, v25, v26\n"
    "v_sub_f32 v80, 0, v25\n"
    "v_sub_f32 v81, 0, v25\n"
    "v_sub_f32 v82, 0, v25\n"
    "v_sub_f32 v83, 0, v25\n"
    "v_sub_f32 v27, v48, v25\n"
    "v_sub_f32 v28, v49, v25\n"
    "v_sub_f32 v29, v50, v25\n"
    "v_sub_f32 v30, v51, v25\n"
    "v_exp_f32 v8, v27\n"
    "v_exp_f32 v9, v28\n"
    "v_exp_f32 v10, v29\n"
    "v_exp_f32 v11, v30\n"
    "v_cvt_pk_bf16_f32 v32, v8, v9\n"
    "v_cvt_pk_bf16_f32 v33, v10, v11\n"
    "v_sub_f32 v27, v52, v25\n"
    "v_sub_f32 v28, v53, v25\n"
    "v_sub_f32 v29, v54, v25\n"
    "v_sub_f32 v30, v55, v25\n"
    "v_exp_f32 v240, v27\n"
    "v_exp_f32 v241, v28\n"
    "v_exp_f32 v242, v29\n"
    "v_exp_f32 v243, v30\n"
    "v_cvt_pk_bf16_f32 v34, v240, v241\n"
    "v_cvt_pk_bf16_f32 v35, v242, v243\n"
    "v_sub_f32 v27, v56, v25\n"
    "v_sub_f32 v28, v57, v25\n"
    "v_sub_f32 v29, v58, v25\n"
    "v_sub_f32 v30, v59, v25\n"
    "v_exp_f32 v8, v27\n"
    "v_exp_f32 v9, v28\n"
    "v_exp_f32 v10, v29\n"
    "v_exp_f32 v11, v30\n"
    "v_cvt_pk_bf16_f32 v36, v8, v9\n"
    "v_cvt_pk_bf16_f32 v37, v10, v11\n"
    "v_sub_f32 v27, v60, v25\n"
    "v_sub_f32 v28, v61, v25\n"
    "v_sub_f32 v29, v62, v25\n"
    "v_sub_f32 v30, v63, v25\n"
    "v_exp_f32 v240, v27\n"
    "v_exp_f32 v241, v28\n"
    "v_exp_f32 v242, v29\n"
    "v_exp_f32 v243, v30\n"
    "v_cvt_pk_bf16_f32 v38, v240, v241\n"
    "v_cvt_pk_bf16_f32 v39, v242, v243\n"
    "s_waitcnt vmcnt(6)\n"
    "v_lshlrev_b32 v31, 16, v132\n"
    "v_and_b32 v255, s42, v132\n"
    "v_mul_f32 v31, s41, v31\n"
    "v_mul_f32 v255, s41, v255\n"
    "v_cvt_pk_bf16_f32 v132, v31, v255\n"
    "v_lshlrev_b32 v31, 16, v133\n"
    "v_and_b32 v255, s42, v133\n"
    "v_mul_f32 v31, s41, v31\n"
    "v_mul_f32 v255, s41, v255\n"
    "v_cvt_pk_bf16_f32 v133, v31, v255\n"
    "v_lshlrev_b32 v31, 16, v134\n"
    "v_and_b32 v255, s42, v134\n"
    "v_mul_f32 v31, s41, v31\n"
    "v_mul_f32 v255, s41, v255\n"
    "v_cvt_pk_bf16_f32 v134, v31, v255\n"
    "v_lshlrev_b32 v31, 16, v135\n"
    "v_and_b32 v255, s42, v135\n"
    "v_mul_f32 v31, s41, v31\n"
    "v_mul_f32 v255, s41, v255\n"
    "v_cvt_pk_bf16_f32 v135, v31, v255\n"
    "v_lshlrev_b32 v31, 16, v136\n"
    "v_and_b32 v255, s42, v136\n"
    "v_mul_f32 v31, s41, v31\n"
    "v_mul_f32 v255, s41, v255\n"
    "v_cvt_pk_bf16_f32 v136, v31, v255\n"
    "v_lshlrev_b32 v31, 16, v137\n"
    "v_and_b32 v255, s42, v137\n"
    "v_mul_f32 v31, s41, v31\n"
    "v_mul_f32 v255, s41, v255\n"
    "v_cvt_pk_bf16_f32 v137, v31, v255\n"
    "v_lshlrev_b32 v31, 16, v138\n"
    "v_and_b32 v255, s42, v138\n"
    "v_mul_f32 v31, s41, v31\n"
    "v_mul_f32 v255, s41, v255\n"
    "v_cvt_pk_bf16_f32 v138, v31, v255\n"
    "v_lshlrev_b32 v31, 16, v139\n"
    "v_and_b32 v255, s42, v139\n"
    "v_mul_f32 v31, s41, v31\n"
    "v_mul_f32 v255, s41, v255\n"
    "v_cvt_pk_bf16_f32 v139, v31, v255\n"
    "v_lshlrev_b32 v31, 16, v140\n"
    "v_and_b32 v255, s42, v140\n"
    "v_mul_f32 v31, s41, v31\n"
    "v_mul_f32 v255, s41, v255\n"
    "v_cvt_pk_bf16_f32 v140, v31, v255\n"
    "v_lshlrev_b32 v31, 16, v141\n"
    "v_and_b32 v255, s42, v141\n"
    "v_mul_f32 v31, s41, v31\n"
    "v_add_u32 v5, s36, v3\n"
    "v_mul_f32 v255, s41, v255\n"
    "v_cvt_pk_bf16_f32 v141, v31, v255\n"
    "ds_read_b64_tr_b16 a[224:225], v5 offset:0\n"
    "ds_read_b64_tr_b16 a[226:227], v5 offset:256\n"
    "v_lshlrev_b32 v31, 16, v142\n"
    "v_and_b32 v255, s42, v142\n"
    "v_mfma_f32_16x16x32_bf16 v[64:67], a[164:167], v[120:123], v[64:67]\n"
    "buffer_load_dwordx4 v[172:175], v6, s[16:19], s48 offen offset:128 \n"
    "v_mul_f32 v31, s41, v31\n"
    "v_mul_f32 v255, s41, v255\n"
    "v_cvt_pk_bf16_f32 v142, v31, v255\n"
    "v_lshlrev_b32 v31, 16, v143\n"
    "v_and_b32 v255, s42, v143\n"
    "v_mul_f32 v31, s41, v31\n"
    "v_mul_f32 v255, s41, v255\n"
    "v_cvt_pk_bf16_f32 v143, v31, v255\n"
    "v_mfma_f32_16x16x32_bf16 v[64:67], a[168:171], v[124:127], v[64:67]\n"
    "ds_read_b64_tr_b16 v[180:181], v5 offset:512\n"
    "ds_read_b64_tr_b16 v[182:183], v5 offset:768\n"
    "v_lshlrev_b32 v31, 16, v144\n"
    "v_and_b32 v255, s42, v144\n"
    "v_mul_f32 v31, s41, v31\n"
    "v_mul_f32 v255, s41, v255\n"
    "v_cvt_pk_bf16_f32 v144, v31, v255\n"
    "v_lshlrev_b32 v31, 16, v145\n"
    "v_and_b32 v255, s42, v145\n"
    "v_mul_f32 v31, s41, v31\n"
    "v_mfma_f32_16x16x32_bf16 v[64:67], a[172:175], v[128:131], v[64:67]\n"
    "buffer_load_dwordx4 v[176:179], v6, s[16:19], s48 offen offset:192 \n"
    "v_mul_f32 v255, s41, v255\n"
    "v_cvt_pk_bf16_f32 v145, v31, v255\n"
    "v_lshlrev_b32 v31, 16, v146\n"
    "v_and_b32 v255, s42, v146\n"
    "v_mul_f32 v31, s41, v31\n"
    "v_mul_f32 v255, s41, v255\n"
    "v_cvt_pk_bf16_f32 v146, v31, v255\n"
    "v_lshlrev_b32 v31, 16, v147\n"
    "v_mfma_f32_16x16x32_bf16 v[12:15], v[16:19], v[32:35], 0\n"
    "ds_read_b64_tr_b16 a[228:229], v5 offset:32\n"
    "ds_read_b64_tr_b16 a[230:231], v5 offset:288\n"
    "v_and_b32 v255, s42, v147\n"
    "v_mul_f32 v31, s41, v31\n"
    "v_mul_f32 v255, s41, v255\n"
    "v_cvt_pk_bf16_f32 v147, v31, v255\n"
    "v_mfma_f32_16x16x32_bf16 v[12:15], v[16:19], v[36:39], v[12:15]\n"
    "v_mfma_f32_16x16x32_bf16 v[68:71], a[176:179], v[116:119], 0\n"
    "ds_read_b64_tr_b16 v[184:185], v5 offset:544\n"
    "ds_read_b64_tr_b16 v[186:187], v5 offset:800\n"
    "v_mfma_f32_16x16x32_bf16 v[68:71], a[180:183], v[120:123], v[68:71]\n"
    "v_mfma_f32_16x16x32_bf16 v[68:71], a[184:187], v[124:127], v[68:71]\n"
    "ds_read_b64_tr_b16 a[232:233], v5 offset:64\n"
    "ds_read_b64_tr_b16 a[234:235], v5 offset:320\n"
    "s_add_u32 m0, s35, 33792\n"
    "s_nop 0\n"
    "buffer_load_dwordx4 v1, s[20:23], s32 offen offset:0  lds\n"
    "v_mfma_f32_16x16x32_bf16 v[68:71], a[188:191], v[128:131], v[68:71]\n"
    "v_mfma_f32_16x16x32_bf16 v[72:75], a[192:195], v[116:119], 0\n"
    "ds_read_b64_tr_b16 v[188:189], v5 offset:576\n"
    "ds_read_b64_tr_b16 v[190:191], v5 offset:832\n"
    "v_mov_b32 v20, v12\n"
    "v_mfma_f32_16x16x32_bf16 v[72:75], a[196:199], v[120:123], v[72:75]\n"
    "s_add_u32 m0, s35, 33824\n"
    "s_nop 0\n"
    "buffer_load_dwordx4 v1, s[20:23], s32 offen offset:1024  lds\n"
    "v_mfma_f32_16x16x32_bf16 v[72:75], a[200:203], v[124:127], v[72:75]\n"
    "ds_read_b64_tr_b16 a[236:237], v5 offset:96\n"
    "ds_read_b64_tr_b16 a[238:239], v5 offset:352\n"
    "v_mfma_f32_16x16x32_bf16 v[72:75], a[204:207], v[128:131], v[72:75]\n"
    "v_mfma_f32_16x16x32_bf16 v[76:79], a[208:211], v[116:119], 0\n"
    "ds_read_b64_tr_b16 v[192:193], v5 offset:608\n"
    "ds_read_b64_tr_b16 v[194:195], v5 offset:864\n"
    "v_max3_f32 v25, v64, v65, v66\n"
    "v_max3_f32 v26, v67, v68, v69\n"
    "s_add_u32 m0, s35, 33856\n"
    "s_nop 0\n"
    "buffer_load_dwordx4 v1, s[20:23], s32 offen offset:2048  lds\n"
    "v_mfma_f32_16x16x32_bf16 v[76:79], a[212:215], v[120:123], v[76:79]\n"
    "v_mfma_f32_16x16x32_bf16 v[76:79], a[216:219], v[124:127], v[76:79]\n"
    "ds_read_b64_tr_b16 a[240:241], v5 offset:128\n"
    "ds_read_b64_tr_b16 a[242:243], v5 offset:384\n"
    "v_mfma_f32_16x16x32_bf16 v[76:79], a[220:223], v[128:131], v[76:79]\n"
    "v_mfma_f32_16x16x32_bf16 v[48:51], a[160:163], v[132:135], 0\n"
    "v_max3_f32 v25, v25, v70, v71\n"
    "v_max3_f32 v26, v26, v72, v73\n"
    "s_waitcnt vmcnt(7)\n"
    "v_lshlrev_b32 v31, 16, v148\n"
    "v_and_b32 v255, s42, v148\n"
    "v_mul_f32 v31, s41, v31\n"
    "v_mul_f32 v255, s41, v255\n"
    "v_cvt_pk_bf16_f32 v148, v31, v255\n"
    "v_lshlrev_b32 v31, 16, v149\n"
    "v_and_b32 v255, s42, v149\n"
    "v_mul_f32 v31, s41, v31\n"
    "v_mul_f32 v255, s41, v255\n"
    "v_cvt_pk_bf16_f32 v149, v31, v255\n"
    "v_lshlrev_b32 v31, 16, v150\n"
    "v_and_b32 v255, s42, v150\n"
    "v_mul_f32 v31, s41, v31\n"
    "v_mul_f32 v255, s41, v255\n"
    "v_cvt_pk_bf16_f32 v150, v31, v255\n"
    "v_lshlrev_b32 v31, 16, v151\n"
    "v_and_b32 v255, s42, v151\n"
    "v_mul_f32 v31, s41, v31\n"
    "v_mul_f32 v255, s41, v255\n"
    "v_cvt_pk_bf16_f32 v151, v31, v255\n"
    "v_mfma_f32_16x16x32_bf16 v[48:51], a[164:167], v[136:139], v[48:51]\n"
    "ds_read_b64_tr_b16 v[196:197], v5 offset:640\n"
    "ds_read_b64_tr_b16 v[198:199], v5 offset:896\n"
    "v_lshlrev_b32 v31, 16, v152\n"
    "v_and_b32 v255, s42, v152\n"
    "v_mul_f32 v31, s41, v31\n"
    "v_mul_f32 v255, s41, v255\n"
    "v_cvt_pk_bf16_f32 v152, v31, v255\n"
    "v_lshlrev_b32 v31, 16, v153\n"
    "v_mfma_f32_16x16x32_bf16 v[48:51], a[168:171], v[140:143], v[48:51]\n"
    "v_and_b32 v255, s42, v153\n"
    "v_mul_f32 v31, s41, v31\n"
    "s_add_u32 m0, s35, 33888\n"
    "s_nop 0\n"
    "buffer_load_dwordx4 v1, s[20:23], s32 offen offset:3072  lds\n"
    "v_mul_f32 v255, s41, v255\n"
    "v_cvt_pk_bf16_f32 v153, v31, v255\n"
    "v_lshlrev_b32 v31, 16, v154\n"
    "v_and_b32 v255, s42, v154\n"
    "v_mfma_f32_16x16x32_bf16 v[48:51], a[172:175], v[144:147], v[48:51]\n"
    "ds_read_b64_tr_b16 a[244:245], v5 offset:160\n"
    "ds_read_b64_tr_b16 a[246:247], v5 offset:416\n"
    "v_mul_f32 v31, s41, v31\n"
    "v_mul_f32 v255, s41, v255\n"
    "s_add_u32 m0, s35, 50688\n"
    "s_nop 0\n"
    "buffer_load_dwordx4 v1, s[24:27], s32 offen offset:0  lds\n"
    "v_cvt_pk_bf16_f32 v154, v31, v255\n"
    "v_lshlrev_b32 v31, 16, v155\n"
    "v_mfma_f32_16x16x32_bf16 v[52:55], a[176:179], v[132:135], 0\n"
    "v_max3_f32 v25, v25, v74, v75\n"
    "v_max3_f32 v26, v26, v76, v77\n"
    "v_max3_f32 v25, v25, v78, v79\n"
    "v_max_f32 v25, v25, v26\n"
    "v_mov_b32 v26, v25\n"
    "s_nop 1\n"
    "v_permlane16_swap_b32 v25, v26\n"
    "v_max_f32 v25, v25, v26\n"
    "v_mov_b32 v26, v25\n"
    "s_nop 1\n"
    "v_permlane32_swap_b32 v25, v26\n"
    "v_max_f32 v25, v25, v26\n"
    "v_sub_f32 v84, 0, v25\n"
    "v_sub_f32 v85, 0, v25\n"
    "v_sub_f32 v86, 0, v25\n"
    "v_sub_f32 v87, 0, v25\n"
    "v_sub_f32 v27, v64, v25\n"
    "v_sub_f32 v28, v65, v25\n"
    "v_sub_f32 v29, v66, v25\n"
    "v_sub_f32 v30, v67, v25\n"
    "v_exp_f32 v8, v27\n"
    "v_exp_f32 v9, v28\n"
    "v_exp_f32 v10, v29\n"
    "v_exp_f32 v11, v30\n"
    "v_cvt_pk_bf16_f32 v40, v8, v9\n"
    "v_cvt_pk_bf16_f32 v41, v10, v11\n"
    "v_sub_f32 v27, v68, v25\n"
    "v_sub_f32 v28, v69, v25\n"
    "v_sub_f32 v29, v70, v25\n"
    "v_sub_f32 v30, v71, v25\n"
    "v_exp_f32 v240, v27\n"
    "v_exp_f32 v241, v28\n"
    "v_exp_f32 v242, v29\n"
    "v_exp_f32 v243, v30\n"
    "v_cvt_pk_bf16_f32 v42, v240, v241\n"
    "v_cvt_pk_bf16_f32 v43, v242, v243\n"
    "v_sub_f32 v27, v72, v25\n"
    "v_sub_f32 v28, v73, v25\n"
    "v_sub_f32 v29, v74, v25\n"
    "v_sub_f32 v30, v75, v25\n"
    "v_exp_f32 v8, v27\n"
    "v_exp_f32 v9, v28\n"
    "v_exp_f32 v10, v29\n"
    "v_exp_f32 v11, v30\n"
    "v_cvt_pk_bf16_f32 v44, v8, v9\n"
    "v_cvt_pk_bf16_f32 v45, v10, v11\n"
    "v_sub_f32 v27, v76, v25\n"
    "v_sub_f32 v28, v77, v25\n"
    "v_sub_f32 v29, v78, v25\n"
    "v_sub_f32 v30, v79, v25\n"
    "v_exp_f32 v240, v27\n"
    "v_exp_f32 v241, v28\n"
    "v_exp_f32 v242, v29\n"
    "v_exp_f32 v243, v30\n"
    "v_cvt_pk_bf16_f32 v46, v240, v241\n"
    "v_cvt_pk_bf16_f32 v47, v242, v243\n"
    "v_mfma_f32_16x16x32_bf16 v[52:55], a[180:183], v[136:139], v[52:55]\n"
    "ds_read_b64_tr_b16 v[200:201], v5 offset:672\n"
    "ds_read_b64_tr_b16 v[202:203], v5 offset:928\n"
    "v_and_b32 v255, s42, v155\n"
    "v_mul_f32 v31, s41, v31\n"
    "v_mul_f32 v255, s41, v255\n"
    "v_cvt_pk_bf16_f32 v155, v31, v255\n"
    "v_mfma_f32_16x16x32_bf16 v[52:55], a[184:187], v[140:143], v[52:55]\n"
    "s_add_u32 m0, s35, 50720\n"
    "s_nop 0\n"
    "buffer_load_dwordx4 v1, s[24:27], s32 offen offset:1024  lds\n"
    "v_lshlrev_b32 v31, 16, v156\n"
    "v_and_b32 v255, s42, v156\n"
    "v_mul_f32 v31, s41, v31\n"
    "v_mul_f32 v255, s41, v255\n"
    "v_mfma_f32_16x16x32_bf16 v[52:55], a[188:191], v[144:147], v[52:55]\n"
    "ds_read_b64_tr_b16 a[248:249], v5 offset:192\n"
    "ds_read_b64_tr_b16 a[250:251], v5 offset:448\n"
    "v_cvt_pk_bf16_f32 v156, v31, v255\n"
    "v_lshlrev_b32 v31, 16, v157\n"
    "v_and_b32 v255, s42, v157\n"
    "v_mul_f32 v31, s41, v31\n"
    "v_mfma_f32_16x16x32_bf16 v[12:15], v[16:19], v[40:43], 0\n"
    "v_mul_f32 v255, s41, v255\n"
    "v_cvt_pk_bf16_f32 v157, v31, v255\n"
    "v_lshlrev_b32 v31, 16, v158\n"
    "v_and_b32 v255, s42, v158\n"
    "v_mul_f32 v31, s41, v31\n"
    "v_mul_f32 v255, s41, v255\n"
    "v_mfma_f32_16x16x32_bf16 v[12:15], v[16:19], v[44:47], v[12:15]\n"
    "ds_read_b64_tr_b16 v[204:205], v5 offset:704\n"
    "ds_read_b64_tr_b16 v[206:207], v5 offset:960\n"
    "v_cvt_pk_bf16_f32 v158, v31, v255\n"
    "v_lshlrev_b32 v31, 16, v159\n"
    "v_and_b32 v255, s42, v159\n"
    "v_mul_f32 v31, s41, v31\n"
    "v_mfma_f32_16x16x32_bf16 v[56:59], a[192:195], v[132:135], 0\n"
    "v_mul_f32 v255, s41, v255\n"
    "v_cvt_pk_bf16_f32 v159, v31, v255\n"
    "v_lshlrev_b32 v31, 16, v160\n"
    "v_and_b32 v255, s42, v160\n"
    "v_mul_f32 v31, s41, v31\n"
    "v_mul_f32 v255, s41, v255\n"
    "v_mfma_f32_16x16x32_bf16 v[56:59], a[196:199], v[136:139], v[56:59]\n"
    "ds_read_b64_tr_b16 a[252:253], v5 offset:224\n"
    "ds_read_b64_tr_b16 a[254:255], v5 offset:480\n"
    "s_add_u32 m0, s35, 50752\n"
    "s_nop 0\n"
    "buffer_load_dwordx4 v1, s[24:27], s32 offen offset:2048  lds\n"
    "v_cvt_pk_bf16_f32 v160, v31, v255\n"
    "v_lshlrev_b32 v31, 16, v161\n"
    "v_mfma_f32_16x16x32_bf16 v[56:59], a[200:203], v[140:143], v[56:59]\n"
    "v_and_b32 v255, s42, v161\n"
    "v_mul_f32 v31, s41, v31\n"
    "v_mul_f32 v255, s41, v255\n"
    "v_cvt_pk_bf16_f32 v161, v31, v255\n"
    "v_lshlrev_b32 v31, 16, v162\n"
    "v_and_b32 v255, s42, v162\n"
    "v_mfma_f32_16x16x32_bf16 v[56:59], a[204:207], v[144:147], v[56:59]\n"
    "ds_read_b64_tr_b16 v[208:209], v5 offset:736\n"
    "ds_read_b64_tr_b16 v[210:211], v5 offset:992\n"
    "v_mul_f32 v31, s41, v31\n"
    "v_mul_f32 v255, s41, v255\n"
    "v_cvt_pk_bf16_f32 v162, v31, v255\n"
    "v_lshlrev_b32 v31, 16, v163\n"
    "v_mfma_f32_16x16x32_bf16 v[60:63], a[208:211], v[132:135], 0\n"
    "v_max3_f32 v25, v48, v49, v50\n"
    "v_max3_f32 v26, v51, v52, v53\n"
    "s_add_u32 m0, s35, 50784\n"
    "s_nop 0\n"
    "buffer_load_dwordx4 v1, s[24:27], s32 offen offset:3072  lds\n"
    "s_add_u32 s32, s32, 16384\n"
    "v_and_b32 v255, s42, v163\n"
    "v_mul_f32 v31, s41, v31\n"
    "v_mfma_f32_16x16x32_bf16 v[60:63], a[212:215], v[136:139], v[60:63]\n"
    "v_mul_f32 v255, s41, v255\n"
    "v_cvt_pk_bf16_f32 v163, v31, v255\n"
    "v_mov_b32 v21, v12\n"
    "v_mfma_f32_16x16x32_bf16 v[60:63], a[216:219], v[140:143], v[60:63]\n"
    "v_mfma_f32_16x16x32_bf16 v[60:63], a[220:223], v[144:147], v[60:63]\n"
    "s_waitcnt lgkmcnt(15)\n"
    "v_mfma_f32_16x16x32_bf16 a[0:3], a[224:227], v[32:35], 0\n"
    "v_max3_f32 v25, v25, v54, v55\n"
    "v_max3_f32 v26, v26, v56, v57\n"
    "s_add_u32 m0, s35, 67584\n"
    "s_nop 0\n"
    "buffer_load_dwordx4 v1, s[20:23], s32 offen offset:0  lds\n"
    "v_mfma_f32_16x16x32_bf16 a[0:3], v[180:183], v[36:39], a[0:3]\n"
    "v_mfma_f32_16x16x32_bf16 a[4:7], a[228:231], v[32:35], 0\n"
    "v_mfma_f32_16x16x32_bf16 a[4:7], v[184:187], v[36:39], a[4:7]\n"
    "v_mfma_f32_16x16x32_bf16 a[8:11], a[232:235], v[32:35], 0\n"
    "v_max3_f32 v25, v25, v58, v59\n"
    "v_max3_f32 v26, v26, v60, v61\n"
    "v_max3_f32 v25, v25, v62, v63\n"
    "v_max_f32 v25, v25, v26\n"
    "v_mov_b32 v26, v25\n"
    "s_nop 1\n"
    "v_permlane16_swap_b32 v25, v26\n"
    "v_max_f32 v25, v25, v26\n"
    "v_mov_b32 v26, v25\n"
    "s_nop 1\n"
    "v_permlane32_swap_b32 v25, v26\n"
    "v_max_f32 v25, v25, v26\n"
    "v_sub_f32 v88, 0, v25\n"
    "v_sub_f32 v89, 0, v25\n"
    "v_sub_f32 v90, 0, v25\n"
    "v_sub_f32 v91, 0, v25\n"
    "v_sub_f32 v27, v48, v25\n"
    "v_sub_f32 v28, v49, v25\n"
    "v_sub_f32 v29, v50, v25\n"
    "v_sub_f32 v30, v51, v25\n"
    "v_exp_f32 v8, v27\n"
    "v_exp_f32 v9, v28\n"
    "v_exp_f32 v10, v29\n"
    "v_exp_f32 v11, v30\n"
    "v_mfma_f32_16x16x32_bf16 a[8:11], v[188:191], v[36:39], a[8:11]\n"
    "s_add_u32 m0, s35, 67616\n"
    "s_nop 0\n"
    "buffer_load_dwordx4 v1, s[20:23], s32 offen offset:1024  lds\n"
    "v_mfma_f32_16x16x32_bf16 a[12:15], a[236:239], v[32:35], 0\n"
    "v_mfma_f32_16x16x32_bf16 a[12:15], v[192:195], v[36:39], a[12:15]\n"
    "s_waitcnt lgkmcnt(0)\n"
    "v_mfma_f32_16x16x32_bf16 a[16:19], a[240:243], v[32:35], 0\n"
    "v_mfma_f32_16x16x32_bf16 a[16:19], v[196:199], v[36:39], a[16:19]\n"
    "s_add_u32 m0, s35, 67648\n"
    "s_nop 0\n"
    "buffer_load_dwordx4 v1, s[20:23], s32 offen offset:2048  lds\n"
    "v_mfma_f32_16x16x32_bf16 a[20:23], a[244:247], v[32:35], 0\n"
    "v_mfma_f32_16x16x32_bf16 a[20:23], v[200:203], v[36:39], a[20:23]\n"
    "v_mfma_f32_16x16x32_bf16 a[24:27], a[248:251], v[32:35], 0\n"
    "v_mfma_f32_16x16x32_bf16 a[24:27], v[204:207], v[36:39], a[24:27]\n"
    "v_mfma_f32_16x16x32_bf16 a[28:31], a[252:255], v[32:35], 0\n"
    "v_cvt_pk_bf16_f32 v32, v8, v9\n"
    "v_cvt_pk_bf16_f32 v33, v10, v11\n"
    "v_sub_f32 v27, v52, v25\n"
    "v_sub_f32 v28, v53, v25\n"
    "v_sub_f32 v29, v54, v25\n"
    "v_sub_f32 v30, v55, v25\n"
    "v_exp_f32 v240, v27\n"
    "v_exp_f32 v241, v28\n"
    "v_exp_f32 v242, v29\n"
    "v_exp_f32 v243, v30\n"
    "v_cvt_pk_bf16_f32 v34, v240, v241\n"
    "v_cvt_pk_bf16_f32 v35, v242, v243\n"
    "v_sub_f32 v27, v56, v25\n"
    "v_sub_f32 v28, v57, v25\n"
    "v_sub_f32 v29, v58, v25\n"
    "v_sub_f32 v30, v59, v25\n"
    "v_exp_f32 v8, v27\n"
    "v_exp_f32 v9, v28\n"
    "v_exp_f32 v10, v29\n"
    "v_exp_f32 v11, v30\n"
    "v_mfma_f32_16x16x32_bf16 a[28:31], v[208:211], v[36:39], a[28:31]\n"
    "v_cvt_pk_bf16_f32 v36, v8, v9\n"
    "v_cvt_pk_bf16_f32 v37, v10, v11\n"
    "v_sub_f32 v27, v60, v25\n"
    "v_sub_f32 v28, v61, v25\n"
    "v_sub_f32 v29, v62, v25\n"
    "v_sub_f32 v30, v63, v25\n"
    "v_exp_f32 v240, v27\n"
    "v_exp_f32 v241, v28\n"
    "v_exp_f32 v242, v29\n"
    "s_add_u32 m0, s35, 67680\n"
    "s_nop 0\n"
    "buffer_load_dwordx4 v1, s[20:23], s32 offen offset:3072  lds\n"
    "v_exp_f32 v243, v30\n"
    "v_cvt_pk_bf16_f32 v38, v240, v241\n"
    "v_cvt_pk_bf16_f32 v39, v242, v243\n"
    "v_mfma_f32_16x16x32_bf16 v[12:15], v[16:19], v[32:35], 0\n"
    "s_nop 0\n"
    "v_mfma_f32_16x16x32_bf16 v[12:15], v[16:19], v[36:39], v[12:15]\n"
    "v_mfma_f32_16x16x32_bf16 v[64:67], a[160:163], v[148:151], 0\n"
    "s_waitcnt vmcnt(12)\n"
    "v_lshlrev_b32 v31, 16, v164\n"
    "v_and_b32 v255, s42, v164\n"
    "v_mul_f32 v31, s41, v31\n"
    "v_mul_f32 v255, s41, v255\n"
    "v_cvt_pk_bf16_f32 v164, v31, v255\n"
    "v_lshlrev_b32 v31, 16, v165\n"
    "v_and_b32 v255, s42, v165\n"
    "v_mul_f32 v31, s41, v31\n"
    "v_mul_f32 v255, s41, v255\n"
    "v_cvt_pk_bf16_f32 v165, v31, v255\n"
    "v_lshlrev_b32 v31, 16, v166\n"
    "v_and_b32 v255, s42, v166\n"
    "v_mul_f32 v31, s41, v31\n"
    "v_mul_f32 v255, s41, v255\n"
    "v_cvt_pk_bf16_f32 v166, v31, v255\n"
    "v_lshlrev_b32 v31, 16, v167\n"
    "v_and_b32 v255, s42, v167\n"
    "v_mul_f32 v31, s41, v31\n"
    "v_mul_f32 v255, s41, v255\n"
    "v_cvt_pk_bf16_f32 v167, v31, v255\n"
    "v_mfma_f32_16x16x32_bf16 v[64:67], a[164:167], v[152:155], v[64:67]\n"
    "v_lshlrev_b32 v31, 16, v168\n"
    "v_and_b32 v255, s42, v168\n"
    "v_mul_f32 v31, s41, v31\n"
    "v_mul_f32 v255, s41, v255\n"
    "v_mfma_f32_16x16x32_bf16 v[64:67], a[168:171], v[156:159], v[64:67]\n"
    "v_cvt_pk_bf16_f32 v168, v31, v255\n"
    "v_lshlrev_b32 v31, 16, v169\n"
    "v_and_b32 v255, s42, v169\n"
    "v_mul_f32 v31, s41, v31\n"
    "v_mfma_f32_16x16x32_bf16 v[64:67], a[172:175], v[160:163], v[64:67]\n"
    "v_mul_f32 v255, s41, v255\n"
    "v_cvt_pk_bf16_f32 v169, v31, v255\n"
    "v_lshlrev_b32 v31, 16, v170\n"
    "v_and_b32 v255, s42, v170\n"
    "v_mfma_f32_16x16x32_bf16 v[68:71], a[176:179], v[148:151], 0\n"
    "v_mul_f32 v31, s41, v31\n"
    "v_mul_f32 v255, s41, v255\n"
    "v_cvt_pk_bf16_f32 v170, v31, v255\n"
    "v_lshlrev_b32 v31, 16, v171\n"
    "v_mfma_f32_16x16x32_bf16 v[68:71], a[180:183], v[152:155], v[68:71]\n"
    "s_add_u32 m0, s35, 84480\n"
    "s_nop 0\n"
    "buffer_load_dwordx4 v1, s[24:27], s32 offen offset:0  lds\n"
    "v_and_b32 v255, s42, v171\n"
    "v_mul_f32 v31, s41, v31\n"
    "v_mfma_f32_16x16x32_bf16 v[68:71], a[184:187], v[156:159], v[68:71]\n"
    "v_mul_f32 v255, s41, v255\n"
    "v_cvt_pk_bf16_f32 v171, v31, v255\n"
    "v_lshlrev_b32 v31, 16, v172\n"
    "v_and_b32 v255, s42, v172\n"
    "v_mfma_f32_16x16x32_bf16 v[68:71], a[188:191], v[160:163], v[68:71]\n"
    "v_mul_f32 v31, s41, v31\n"
    "v_mul_f32 v255, s41, v255\n"
    "v_cvt_pk_bf16_f32 v172, v31, v255\n"
    "v_lshlrev_b32 v31, 16, v173\n"
    "v_mfma_f32_16x16x32_bf16 v[72:75], a[192:195], v[148:151], 0\n"
    "s_add_u32 m0, s35, 84512\n"
    "s_nop 0\n"
    "buffer_load_dwordx4 v1, s[24:27], s32 offen offset:1024  lds\n"
    "v_and_b32 v255, s42, v173\n"
    "v_mul_f32 v31, s41, v31\n"
    "v_mfma_f32_16x16x32_bf16 v[72:75], a[196:199], v[152:155], v[72:75]\n"
    "v_mul_f32 v255, s41, v255\n"
    "v_cvt_pk_bf16_f32 v173, v31, v255\n"
    "v_lshlrev_b32 v31, 16, v174\n"
    "v_and_b32 v255, s42, v174\n"
    "v_mfma_f32_16x16x32_bf16 v[72:75], a[200:203], v[156:159], v[72:75]\n"
    "v_mul_f32 v31, s41, v31\n"
    "v_mul_f32 v255, s41, v255\n"
    "v_cvt_pk_bf16_f32 v174, v31, v255\n"
    "v_lshlrev_b32 v31, 16, v175\n"
    "v_mfma_f32_16x16x32_bf16 v[72:75], a[204:207], v[160:163], v[72:75]\n"
    "v_and_b32 v255, s42, v175\n"
    "v_mul_f32 v31, s41, v31\n"
    "v_mul_f32 v255, s41, v255\n"
    "v_cvt_pk_bf16_f32 v175, v31, v255\n"
    "v_mfma_f32_16x16x32_bf16 v[76:79], a[208:211], v[148:151], 0\n"
    "v_max3_f32 v25, v64, v65, v66\n"
    "v_max3_f32 v26, v67, v68, v69\n"
    "s_add_u32 m0, s35, 84544\n"
    "s_nop 0\n"
    "buffer_load_dwordx4 v1, s[24:27], s32 offen offset:2048  lds\n"
    "v_mfma_f32_16x16x32_bf16 v[76:79], a[212:215], v[152:155], v[76:79]\n"
    "v_lshlrev_b32 v31, 16, v176\n"
    "v_and_b32 v255, s42, v176\n"
    "v_mul_f32 v31, s41, v31\n"
    "v_mul_f32 v255, s41, v255\n"
    "v_mfma_f32_16x16x32_bf16 v[76:79], a[216:219], v[156:159], v[76:79]\n"
    "v_cvt_pk_bf16_f32 v176, v31, v255\n"
    "v_lshlrev_b32 v31, 16, v177\n"
    "v_and_b32 v255, s42, v177\n"
    "v_mul_f32 v31, s41, v31\n"
    "v_mfma_f32_16x16x32_bf16 v[76:79], a[220:223], v[160:163], v[76:79]\n"
    "v_mul_f32 v255, s41, v255\n"
    "v_cvt_pk_bf16_f32 v177, v31, v255\n"
    "v_lshlrev_b32 v31, 16, v178\n"
    "v_and_b32 v255, s42, v178\n"
    "v_mfma_f32_16x16x32_bf16 a[32:35], a[224:227], v[40:43], 0\n"
    "v_max3_f32 v25, v25, v70, v71\n"
    "v_max3_f32 v26, v26, v72, v73\n"
    "v_mul_f32 v31, s41, v31\n"
    "v_mul_f32 v255, s41, v255\n"
    "v_mfma_f32_16x16x32_bf16 a[32:35], v[180:183], v[44:47], a[32:35]\n"
    "s_add_u32 m0, s35, 84576\n"
    "s_nop 0\n"
    "buffer_load_dwordx4 v1, s[24:27], s32 offen offset:3072  lds\n"
    "s_add_u32 s32, s32, 16384\n"
    "v_cvt_pk_bf16_f32 v178, v31, v255\n"
    "v_lshlrev_b32 v31, 16, v179\n"
    "v_mfma_f32_16x16x32_bf16 a[36:39], a[228:231], v[40:43], 0\n"
    "v_and_b32 v255, s42, v179\n"
    "v_mul_f32 v31, s41, v31\n"
    "v_mul_f32 v255, s41, v255\n"
    "v_cvt_pk_bf16_f32 v179, v31, v255\n"
    "v_mfma_f32_16x16x32_bf16 a[36:39], v[184:187], v[44:47], a[36:39]\n"
    "v_mov_b32 v22, v12\n"
    "v_mfma_f32_16x16x32_bf16 a[40:43], a[232:235], v[40:43], 0\n"
    "v_max3_f32 v25, v25, v74, v75\n"
    "v_max3_f32 v26, v26, v76, v77\n"
    "v_max3_f32 v25, v25, v78, v79\n"
    "v_max_f32 v25, v25, v26\n"
    "v_mov_b32 v26, v25\n"
    "s_nop 1\n"
    "v_permlane16_swap_b32 v25, v26\n"
    "v_max_f32 v25, v25, v26\n"
    "v_mov_b32 v26, v25\n"
    "s_nop 1\n"
    "v_permlane32_swap_b32 v25, v26\n"
    "v_max_f32 v25, v25, v26\n"
    "v_sub_f32 v92, 0, v25\n"
    "v_sub_f32 v93, 0, v25\n"
    "v_sub_f32 v94, 0, v25\n"
    "v_sub_f32 v95, 0, v25\n"
    "v_sub_f32 v27, v64, v25\n"
    "v_sub_f32 v28, v65, v25\n"
    "v_mfma_f32_16x16x32_bf16 a[40:43], v[188:191], v[44:47], a[40:43]\n"
    "v_sub_f32 v29, v66, v25\n"
    "v_sub_f32 v30, v67, v25\n"
    "v_mfma_f32_16x16x32_bf16 a[44:47], a[236:239], v[40:43], 0\n"
    "v_exp_f32 v8, v27\n"
    "v_mfma_f32_16x16x32_bf16 a[44:47], v[192:195], v[44:47], a[44:47]\n"
    "v_exp_f32 v9, v28\n"
    "v_mfma_f32_16x16x32_bf16 a[48:51], a[240:243], v[40:43], 0\n"
    "s_add_u32 s34, s36, s35\n"
    "v_exp_f32 v10, v29\n"
    "v_mfma_f32_16x16x32_bf16 a[48:51], v[196:199], v[44:47], a[48:51]\n"
    "v_exp_f32 v11, v30\n"
    "v_mfma_f32_16x16x32_bf16 a[52:55], a[244:247], v[40:43], 0\n"
    "s_add_u32 s36, s36, 33792\n"
    "v_mfma_f32_16x16x32_bf16 a[52:55], v[200:203], v[44:47], a[52:55]\n"
    "v_mfma_f32_16x16x32_bf16 a[56:59], a[248:251], v[40:43], 0\n"
    "s_sub_u32 s50, s36, 101376\n"
    "v_mfma_f32_16x16x32_bf16 a[56:59], v[204:207], v[44:47], a[56:59]\n"
    "v_mfma_f32_16x16x32_bf16 a[60:63], a[252:255], v[40:43], 0\n"
    "s_min_u32 s36, s36, s50\n"
    "v_cvt_pk_bf16_f32 v40, v8, v9\n"
    "v_cvt_pk_bf16_f32 v41, v10, v11\n"
    "v_sub_f32 v27, v68, v25\n"
    "v_sub_f32 v28, v69, v25\n"
    "v_sub_f32 v29, v70, v25\n"
    "v_sub_f32 v30, v71, v25\n"
    "v_exp_f32 v240, v27\n"
    "v_exp_f32 v241, v28\n"
    "v_mfma_f32_16x16x32_bf16 a[60:63], v[208:211], v[44:47], a[60:63]\n"
    "v_exp_f32 v242, v29\n"
    "v_mfma_f32_16x16x32_bf16 v[48:51], a[160:163], v[164:167], 0\n"
    "v_exp_f32 v243, v30\n"
    "v_cvt_pk_bf16_f32 v42, v240, v241\n"
    "v_cvt_pk_bf16_f32 v43, v242, v243\n"
    "v_mfma_f32_16x16x32_bf16 v[48:51], a[164:167], v[168:171], v[48:51]\n"
    "v_add_u32 v4, s36, v2\n"
    "v_sub_f32 v27, v72, v25\n"
    "v_sub_f32 v28, v73, v25\n"
    "v_mfma_f32_16x16x32_bf16 v[48:51], a[168:171], v[172:175], v[48:51]\n"
    "s_waitcnt vmcnt(8)\n"
    "s_barrier\n"
    "v_sub_f32 v29, v74, v25\n"
    "v_sub_f32 v30, v75, v25\n"
    "v_mfma_f32_16x16x32_bf16 v[48:51], a[172:175], v[176:179], v[48:51]\n"
    "v_exp_f32 v8, v27\n"
    "v_exp_f32 v9, v28\n"
    "v_mfma_f32_16x16x32_bf16 v[52:55], a[176:179], v[164:167], 0\n"
    "v_exp_f32 v10, v29\n"
    "v_exp_f32 v11, v30\n"
    "v_mfma_f32_16x16x32_bf16 v[52:55], a[180:183], v[168:171], v[52:55]\n"
    "v_cvt_pk_bf16_f32 v44, v8, v9\n"
    "v_cvt_pk_bf16_f32 v45, v10, v11\n"
    "v_sub_f32 v27, v76, v25\n"
    "v_sub_f32 v28, v77, v25\n"
    "v_mfma_f32_16x16x32_bf16 v[52:55], a[184:187], v[172:175], v[52:55]\n"
    "v_sub_f32 v29, v78, v25\n"
    "v_sub_f32 v30, v79, v25\n"
    "v_exp_f32 v240, v27\n"
    "v_mfma_f32_16x16x32_bf16 v[52:55], a[188:191], v[176:179], v[52:55]\n"
    "v_exp_f32 v241, v28\n"
    "v_mfma_f32_16x16x32_bf16 v[56:59], a[192:195], v[164:167], 0\n"
    "v_exp_f32 v242, v29\n"
    "v_mfma_f32_16x16x32_bf16 v[56:59], a[196:199], v[168:171], v[56:59]\n"
    "v_exp_f32 v243, v30\n"
    "v_mfma_f32_16x16x32_bf16 v[56:59], a[200:203], v[172:175], v[56:59]\n"
    "v_cvt_pk_bf16_f32 v46, v240, v241\n"
    "v_cvt_pk_bf16_f32 v47, v242, v243\n"
    "v_mfma_f32_16x16x32_bf16 v[56:59], a[204:207], v[176:179], v[56:59]\n"
    "ds_read_b128 a[160:163], v4 offset:0\n"
    "ds_read_b128 a[164:167], v4 offset:64\n"
    "v_mfma_f32_16x16x32_bf16 v[12:15], v[16:19], v[40:43], 0\n"
    "v_mfma_f32_16x16x32_bf16 v[12:15], v[16:19], v[44:47], v[12:15]\n"
    "ds_read_b128 a[168:171], v4 offset:128\n"
    "ds_read_b128 a[172:175], v4 offset:192\n"
    "v_mfma_f32_16x16x32_bf16 v[60:63], a[208:211], v[164:167], 0\n"
    "v_max3_f32 v25, v48, v49, v50\n"
    "v_max3_f32 v26, v51, v52, v53\n"
    "v_mfma_f32_16x16x32_bf16 v[60:63], a[212:215], v[168:171], v[60:63]\n"
    "ds_read_b128 a[176:179], v4 offset:256\n"
    "ds_read_b128 a[180:183], v4 offset:320\n"
    "v_mfma_f32_16x16x32_bf16 v[60:63], a[216:219], v[172:175], v[60:63]\n"
    "v_mfma_f32_16x16x32_bf16 v[60:63], a[220:223], v[176:179], v[60:63]\n"
    "ds_read_b128 a[184:187], v4 offset:384\n"
    "ds_read_b128 a[188:191], v4 offset:448\n"
    "v_mfma_f32_16x16x32_bf16 a[64:67], a[224:227], v[32:35], 0\n"
    "v_max3_f32 v25, v25, v54, v55\n"
    "v_max3_f32 v26, v26, v56, v57\n"
    "v_mov_b32 v23, v12\n"
    "v_mfma_f32_16x16x32_bf16 a[64:67], v[180:183], v[36:39], a[64:67]\n"
    "ds_read_b128 a[192:195], v4 offset:512\n"
    "ds_read_b128 a[196:199], v4 offset:576\n"
    "v_mfma_f32_16x16x32_bf16 a[68:71], a[228:231], v[32:35], 0\n"
    "v_mfma_f32_16x16x32_bf16 a[68:71], v[184:187], v[36:39], a[68:71]\n"
    "ds_read_b128 a[200:203], v4 offset:640\n"
    "ds_read_b128 a[204:207], v4 offset:704\n"
    "v_mfma_f32_16x16x32_bf16 a[72:75], a[232:235], v[32:35], 0\n"
    "v_max3_f32 v25, v25, v58, v59\n"
    "v_max3_f32 v26, v26, v60, v61\n"
    "v_max3_f32 v25, v25, v62, v63\n"
    "v_max_f32 v25, v25, v26\n"
    "v_mov_b32 v26, v25\n"
    "v_mfma_f32_16x16x32_bf16 a[72:75], v[188:191], v[36:39], a[72:75]\n"
    "ds_read_b128 a[208:211], v4 offset:768\n"
    "ds_read_b128 a[212:215], v4 offset:832\n"
    "v_mfma_f32_16x16x32_bf16 a[76:79], a[236:239], v[32:35], 0\n"
    "v_permlane16_swap_b32 v25, v26\n"
    "v_mfma_f32_16x16x32_bf16 a[76:79], v[192:195], v[36:39], a[76:79]\n"
    "v_max_f32 v25, v25, v26\n"
    "v_mov_b32 v26, v25\n"
    "v_mfma_f32_16x16x32_bf16 a[80:83], a[240:243], v[32:35], 0\n"
    "ds_read_b128 a[216:219], v4 offset:896\n"
    "ds_read_b128 a[220:223], v4 offset:960\n"
    "v_mfma_f32_16x16x32_bf16 a[80:83], v[196:199], v[36:39], a[80:83]\n"
    "v_permlane32_swap_b32 v25, v26\n"
    "v_mfma_f32_16x16x32_bf16 a[84:87], a[244:247], v[32:35], 0\n"
    "v_max_f32 v25, v25, v26\n"
    "v_mfma_f32_16x16x32_bf16 a[84:87], v[200:203], v[36:39], a[84:87]\n"
    "v_sub_f32 v96, 0, v25\n"
    "v_sub_f32 v97, 0, v25\n"
    "v_mfma_f32_16x16x32_bf16 a[88:91], a[248:251], v[32:35], 0\n"
    "v_sub_f32 v98, 0, v25\n"
    "v_sub_f32 v99, 0, v25\n"
    "v_mfma_f32_16x16x32_bf16 a[88:91], v[204:207], v[36:39], a[88:91]\n"
    "v_sub_f32 v27, v48, v25\n"
    "v_sub_f32 v28, v49, v25\n"
    "v_mfma_f32_16x16x32_bf16 a[92:95], a[252:255], v[32:35], 0\n"
    "v_sub_f32 v29, v50, v25\n"
    "v_sub_f32 v30, v51, v25\n"
    "v_mfma_f32_16x16x32_bf16 a[92:95], v[208:211], v[36:39], a[92:95]\n"
    "s_waitcnt lgkmcnt(8)\n"
    "v_exp_f32 v8, v27\n"
    "v_mfma_f32_16x16x32_bf16 v[64:67], a[160:163], v[100:103], v[80:83]\n"
    "v_exp_f32 v9, v28\n"
    "v_exp_f32 v10, v29\n"
    "v_mfma_f32_16x16x32_bf16 v[64:67], a[164:167], v[104:107], v[64:67]\n"
    "v_exp_f32 v11, v30\n"
    "v_cvt_pk_bf16_f32 v32, v8, v9\n"
    "v_cvt_pk_bf16_f32 v33, v10, v11\n"
    "v_mfma_f32_16x16x32_bf16 v[64:67], a[168:171], v[108:111], v[64:67]\n"
    "v_sub_f32 v27, v52, v25\n"
    "v_sub_f32 v28, v53, v25\n"
    "v_sub_f32 v29, v54, v25\n"
    "v_sub_f32 v30, v55, v25\n"
    "v_mfma_f32_16x16x32_bf16 v[64:67], a[172:175], v[112:115], v[64:67]\n"
    "v_exp_f32 v240, v27\n"
    "v_exp_f32 v241, v28\n"
    "v_mfma_f32_16x16x32_bf16 v[68:71], a[176:179], v[100:103], v[80:83]\n"
    "v_exp_f32 v242, v29\n"
    "v_exp_f32 v243, v30\n"
    "v_mfma_f32_16x16x32_bf16 v[68:71], a[180:183], v[104:107], v[68:71]\n"
    "v_cvt_pk_bf16_f32 v34, v240, v241\n"
    "v_cvt_pk_bf16_f32 v35, v242, v243\n"
    "v_sub_f32 v27, v56, v25\n"
    "v_sub_f32 v28, v57, v25\n"
    "v_mfma_f32_16x16x32_bf16 v[68:71], a[184:187], v[108:111], v[68:71]\n"
    "v_sub_f32 v29, v58, v25\n"
    "v_sub_f32 v30, v59, v25\n"
    "v_exp_f32 v8, v27\n"
    "v_mfma_f32_16x16x32_bf16 v[68:71], a[188:191], v[112:115], v[68:71]\n"
    "s_waitcnt lgkmcnt(0)\n"
    "v_exp_f32 v9, v28\n"
    "v_exp_f32 v10, v29\n"
    "v_mfma_f32_16x16x32_bf16 v[72:75], a[192:195], v[100:103], v[80:83]\n"
    "s_add_u32 m0, s34, 0\n"
    "v_exp_f32 v11, v30\n"
    "v_accvgpr_write_b32 a128, 0\n"
    "v_accvgpr_write_b32 a129, 0\n"
    "v_mfma_f32_16x16x32_bf16 v[72:75], a[196:199], v[104:107], v[72:75]\n"
    "buffer_load_dwordx4 v1, s[20:23], s32 offen offset:0  lds\n"
    "v_cvt_pk_bf16_f32 v36, v8, v9\n"
    "v_cvt_pk_bf16_f32 v37, v10, v11\n"
    "v_mfma_f32_16x16x32_bf16 v[72:75], a[200:203], v[108:111], v[72:75]\n"
    "v_accvgpr_write_b32 a130, 0\n"
    "v_accvgpr_write_b32 a131, 0\n"
    "v_sub_f32 v27, v60, v25\n"
    "v_sub_f32 v28, v61, v25\n"
    "v_mfma_f32_16x16x32_bf16 v[72:75], a[204:207], v[112:115], v[72:75]\n"
    "v_accvgpr_write_b32 a132, 0\n"
    "v_accvgpr_write_b32 a133, 0\n"
    "v_sub_f32 v29, v62, v25\n"
    "v_sub_f32 v30, v63, v25\n"
    "v_mfma_f32_16x16x32_bf16 v[76:79], a[208:211], v[100:103], v[80:83]\n"
    "buffer_load_dwordx4 v[212:215], v6, s[16:19], s53 offen offset:0 \n"
    "v_accvgpr_write_b32 a134, 0\n"
    "v_accvgpr_write_b32 a135, 0\n"
    "v_mfma_f32_16x16x32_bf16 v[76:79], a[212:215], v[104:107], v[76:79]\n"
    "v_exp_f32 v240, v27\n"
    "v_accvgpr_write_b32 a136, 0\n"
    "v_accvgpr_write_b32 a137, 0\n"
    "v_mfma_f32_16x16x32_bf16 v[76:79], a[216:219], v[108:111], v[76:79]\n"
    "v_exp_f32 v241, v28\n"
    "v_accvgpr_write_b32 a138, 0\n"
    "v_accvgpr_write_b32 a139, 0\n"
    "v_mfma_f32_16x16x32_bf16 v[76:79], a[220:223], v[112:115], v[76:79]\n"
    "v_exp_f32 v242, v29\n"
    "v_accvgpr_write_b32 a140, 0\n"
    "v_accvgpr_write_b32 a141, 0\n"
    "v_mfma_f32_16x16x32_bf16 a[96:99], a[224:227], v[40:43], 0\n"
    "v_exp_f32 v243, v30\n"
    "v_accvgpr_write_b32 a142, 0\n"
    "v_accvgpr_write_b32 a143, 0\n"
    "v_mfma_f32_16x16x32_bf16 a[96:99], v[180:183], v[44:47], a[96:99]\n"
    "buffer_load_dwordx4 v[216:219], v6, s[16:19], s53 offen offset:64 \n"
    "v_cvt_pk_bf16_f32 v38, v240, v241\n"
    "v_cvt_pk_bf16_f32 v39, v242, v243\n"
    "v_mfma_f32_16x16x32_bf16 v[12:15], v[16:19], v[32:35], 0\n"
    "v_accvgpr_write_b32 a144, 0\n"
    "v_accvgpr_write_b32 a145, 0\n"
    "v_exp_f32 v8, v64\n"
    "v_mfma_f32_16x16x32_bf16 v[12:15], v[16:19], v[36:39], v[12:15]\n"
    "v_accvgpr_write_b32 a146, 0\n"
    "v_accvgpr_write_b32 a147, 0\n"
    "v_exp_f32 v9, v65\n"
    "v_mfma_f32_16x16x32_bf16 a[100:103], a[228:231], v[40:43], 0\n"
    "v_accvgpr_write_b32 a148, 0\n"
    "v_accvgpr_write_b32 a149, 0\n"
    "v_exp_f32 v10, v66\n"
    "v_mfma_f32_16x16x32_bf16 a[100:103], v[184:187], v[44:47], a[100:103]\n"
    "v_accvgpr_write_b32 a150, 0\n"
    "v_accvgpr_write_b32 a151, 0\n"
    "v_mfma_f32_16x16x32_bf16 a[104:107], a[232:235], v[40:43], 0\n"
    "buffer_load_dwordx4 v[220:223], v6, s[16:19], s53 offen offset:128 \n"
    "v_mfma_f32_16x16x32_bf16 a[104:107], v[188:191], v[44:47], a[104:107]\n"
    "v_exp_f32 v11, v67\n"
    "v_mfma_f32_16x16x32_bf16 a[108:111], a[236:239], v[40:43], 0\n"
    "v_accvgpr_write_b32 a152, 0\n"
    "v_accvgpr_write_b32 a153, 0\n"
    "v_mfma_f32_16x16x32_bf16 a[108:111], v[192:195], v[44:47], a[108:111]\n"
    "v_exp_f32 v240, v68\n"
    "v_mfma_f32_16x16x32_bf16 a[112:115], a[240:243], v[40:43], 0\n"
    "s_add_u32 m0, s34, 32\n"
    "v_accvgpr_write_b32 a154, 0\n"
    "v_accvgpr_write_b32 a155, 0\n"
    "v_mfma_f32_16x16x32_bf16 a[112:115], v[196:199], v[44:47], a[112:115]\n"
    "buffer_load_dwordx4 v1, s[20:23], s32 offen offset:1024  lds\n"
    "v_mfma_f32_16x16x32_bf16 a[116:119], a[244:247], v[40:43], 0\n"
    "v_exp_f32 v241, v69\n"
    "v_mfma_f32_16x16x32_bf16 a[116:119], v[200:203], v[44:47], a[116:119]\n"
    "v_accvgpr_write_b32 a156, 0\n"
    "v_accvgpr_write_b32 a157, 0\n"
    "v_mfma_f32_16x16x32_bf16 a[120:123], a[248:251], v[40:43], 0\n"
    "v_exp_f32 v242, v70\n"
    "v_mfma_f32_16x16x32_bf16 a[120:123], v[204:207], v[44:47], a[120:123]\n"
    "v_mov_b32 v24, v12\n"
    "v_mfma_f32_16x16x32_bf16 a[124:127], a[252:255], v[40:43], 0\n"
    "v_accvgpr_write_b32 a158, 0\n"
    "v_accvgpr_write_b32 a159, 0\n"
    "v_mfma_f32_16x16x32_bf16 a[124:127], v[208:211], v[44:47], a[124:127]\n"
    "buffer_load_dwordx4 v[224:227], v6, s[16:19], s53 offen offset:192 \n"
    "s_mov_b32 s37, 4\n"
    "LBG5:\n"
    "v_mfma_f32_16x16x32_bf16 v[48:51], a[160:163], v[116:119], v[84:87]\n"
    "v_exp_f32 v243, v71\n"
    "v_mfma_f32_16x16x32_bf16 v[48:51], a[164:167], v[120:123], v[48:51]\n"
    "v_cvt_pk_bf16_f32 v40, v8, v9\n"
    "v_cvt_pk_bf16_f32 v41, v10, v11\n"
    "v_mfma_f32_16x16x32_bf16 v[48:51], a[168:171], v[124:127], v[48:51]\n"
    "v_exp_f32 v8, v72\n"
    "v_mfma_f32_16x16x32_bf16 v[48:51], a[172:175], v[128:131], v[48:51]\n"
    "v_exp_f32 v9, v73\n"
    "v_mfma_f32_16x16x32_bf16 v[52:55], a[176:179], v[116:119], v[84:87]\n"
    "v_add_u32 v5, s36, v3\n"
    "v_mfma_f32_16x16x32_bf16 v[52:55], a[180:183], v[120:123], v[52:55]\n"
    "v_exp_f32 v10, v74\n"
    "v_mfma_f32_16x16x32_bf16 v[52:55], a[184:187], v[124:127], v[52:55]\n"
    "v_exp_f32 v11, v75\n"
    "v_mfma_f32_16x16x32_bf16 v[52:55], a[188:191], v[128:131], v[52:55]\n"
    "v_cvt_pk_bf16_f32 v42, v240, v241\n"
    "v_cvt_pk_bf16_f32 v43, v242, v243\n"
    "v_mfma_f32_16x16x32_bf16 v[56:59], a[192:195], v[116:119], v[84:87]\n"
    "v_exp_f32 v240, v76\n"
    "v_mfma_f32_16x16x32_bf16 v[56:59], a[196:199], v[120:123], v[56:59]\n"
    "v_exp_f32 v241, v77\n"
    "v_mfma_f32_16x16x32_bf16 v[56:59], a[200:203], v[124:127], v[56:59]\n"
    "v_exp_f32 v242, v78\n"
    "v_mfma_f32_16x16x32_bf16 v[56:59], a[204:207], v[128:131], v[56:59]\n"
    "v_exp_f32 v243, v79\n"
    "v_mfma_f32_16x16x32_bf16 v[60:63], a[208:211], v[116:119], v[84:87]\n"
    "v_cvt_pk_bf16_f32 v44, v8, v9\n"
    "v_cvt_pk_bf16_f32 v45, v10, v11\n"
    "v_mfma_f32_16x16x32_bf16 v[60:63], a[212:215], v[120:123], v[60:63]\n"
    "v_cvt_pk_bf16_f32 v46, v240, v241\n"
    "v_cvt_pk_bf16_f32 v47, v242, v243\n"
    "v_mfma_f32_16x16x32_bf16 v[60:63], a[216:219], v[124:127], v[60:63]\n"
    "v_exp_f32 v8, v48\n"
    "v_mfma_f32_16x16x32_bf16 v[60:63], a[220:223], v[128:131], v[60:63]\n"
    "v_exp_f32 v9, v49\n"
    "v_mfma_f32_16x16x32_bf16 v[12:15], v[16:19], v[40:43], 0\n"
    "v_exp_f32 v10, v50\n"
    "v_mfma_f32_16x16x32_bf16 v[12:15], v[16:19], v[44:47], v[12:15]\n"
    "v_exp_f32 v11, v51\n"
    "v_mfma_f32_16x16x32_bf16 a[128:131], a[224:227], v[32:35], a[128:131]\n"
    "ds_read_b64_tr_b16 a[224:225], v5 offset:0\n"
    "ds_read_b64_tr_b16 a[226:227], v5 offset:256\n"
    "v_mfma_f32_16x16x32_bf16 a[128:131], v[180:183], v[36:39], a[128:131]\n"
    "v_exp_f32 v240, v52\n"
    "v_mfma_f32_16x16x32_bf16 a[132:135], a[228:231], v[32:35], a[132:135]\n"
    "ds_read_b64_tr_b16 v[180:181], v5 offset:512\n"
    "ds_read_b64_tr_b16 v[182:183], v5 offset:768\n"
    "v_mfma_f32_16x16x32_bf16 a[132:135], v[184:187], v[36:39], a[132:135]\n"
    "v_exp_f32 v241, v53\n"
    "v_mfma_f32_16x16x32_bf16 a[136:139], a[232:235], v[32:35], a[136:139]\n"
    "ds_read_b64_tr_b16 a[228:229], v5 offset:32\n"
    "ds_read_b64_tr_b16 a[230:231], v5 offset:288\n"
    "v_mfma_f32_16x16x32_bf16 a[136:139], v[188:191], v[36:39], a[136:139]\n"
    "v_cmp_nge_f32 vcc, s40, v12\n"
    "v_mfma_f32_16x16x32_bf16 a[140:143], a[236:239], v[32:35], a[140:143]\n"
    "ds_read_b64_tr_b16 v[184:185], v5 offset:544\n"
    "ds_read_b64_tr_b16 v[186:187], v5 offset:800\n"
    "v_mfma_f32_16x16x32_bf16 a[140:143], v[192:195], v[36:39], a[140:143]\n"
    "v_exp_f32 v242, v54\n"
    "v_mfma_f32_16x16x32_bf16 a[144:147], a[240:243], v[32:35], a[144:147]\n"
    "ds_read_b64_tr_b16 a[232:233], v5 offset:64\n"
    "ds_read_b64_tr_b16 a[234:235], v5 offset:320\n"
    "v_mfma_f32_16x16x32_bf16 a[144:147], v[196:199], v[36:39], a[144:147]\n"
    "s_cbranch_vccnz LTR_0\n"
    "LRET_0:\n"
    "v_add_f32 v20, v20, v12\n"
    "v_mfma_f32_16x16x32_bf16 a[148:151], a[244:247], v[32:35], a[148:151]\n"
    "ds_read_b64_tr_b16 v[188:189], v5 offset:576\n"
    "ds_read_b64_tr_b16 v[190:191], v5 offset:832\n"
    "v_mfma_f32_16x16x32_bf16 a[148:151], v[200:203], v[36:39], a[148:151]\n"
    "v_exp_f32 v243, v55\n"
    "v_mfma_f32_16x16x32_bf16 a[152:155], a[248:251], v[32:35], a[152:155]\n"
    "ds_read_b64_tr_b16 a[236:237], v5 offset:96\n"
    "ds_read_b64_tr_b16 a[238:239], v5 offset:352\n"
    "v_mfma_f32_16x16x32_bf16 a[152:155], v[204:207], v[36:39], a[152:155]\n"
    "s_add_u32 s53, s53, 4096\n"
    "s_cmp_eq_u32 s53, s60\n"
    "s_cselect_b32 s53, s44, s53\n"
    "v_mfma_f32_16x16x32_bf16 a[156:159], a[252:255], v[32:35], a[156:159]\n"
    "ds_read_b64_tr_b16 v[192:193], v5 offset:608\n"
    "ds_read_b64_tr_b16 v[194:195], v5 offset:864\n"
    "v_mfma_f32_16x16x32_bf16 a[156:159], v[208:211], v[36:39], a[156:159]\n"
    "v_cvt_pk_bf16_f32 v32, v8, v9\n"
    "v_cvt_pk_bf16_f32 v33, v10, v11\n"
    "v_mfma_f32_16x16x32_bf16 v[64:67], a[160:163], v[132:135], v[88:91]\n"
    "s_add_u32 m0, s34, 64\n"
    "ds_read_b64_tr_b16 a[240:241], v5 offset:128\n"
    "ds_read_b64_tr_b16 a[242:243], v5 offset:384\n"
    "v_mfma_f32_16x16x32_bf16 v[64:67], a[164:167], v[136:139], v[64:67]\n"
    "buffer_load_dwordx4 v1, s[20:23], s32 offen offset:2048  lds\n"
    "v_mfma_f32_16x16x32_bf16 v[64:67], a[168:171], v[140:143], v[64:67]\n"
    "ds_read_b64_tr_b16 v[196:197], v5 offset:640\n"
    "ds_read_b64_tr_b16 v[198:199], v5 offset:896\n"
    "v_mfma_f32_16x16x32_bf16 v[64:67], a[172:175], v[144:147], v[64:67]\n"
    "v_exp_f32 v8, v56\n"
    "v_mfma_f32_16x16x32_bf16 v[68:71], a[176:179], v[132:135], v[88:91]\n"
    "ds_read_b64_tr_b16 a[244:245], v5 offset:160\n"
    "ds_read_b64_tr_b16 a[246:247], v5 offset:416\n"
    "v_mfma_f32_16x16x32_bf16 v[68:71], a[180:183], v[136:139], v[68:71]\n"
    "v_exp_f32 v9, v57\n"
    "v_mfma_f32_16x16x32_bf16 v[68:71], a[184:187], v[140:143], v[68:71]\n"
    "ds_read_b64_tr_b16 v[200:201], v5 offset:672\n"
    "ds_read_b64_tr_b16 v[202:203], v5 offset:928\n"
    "v_mfma_f32_16x16x32_bf16 v[68:71], a[188:191], v[144:147], v[68:71]\n"
    "v_exp_f32 v10, v58\n"
    "v_mfma_f32_16x16x32_bf16 v[72:75], a[192:195], v[132:135], v[88:91]\n"
    "ds_read_b64_tr_b16 a[248:249], v5 offset:192\n"
    "ds_read_b64_tr_b16 a[250:251], v5 offset:448\n"
    "v_mfma_f32_16x16x32_bf16 v[72:75], a[196:199], v[136:139], v[72:75]\n"
    "v_exp_f32 v11, v59\n"
    "v_mfma_f32_16x16x32_bf16 v[72:75], a[200:203], v[140:143], v[72:75]\n"
    "ds_read_b64_tr_b16 v[204:205], v5 offset:704\n"
    "ds_read_b64_tr_b16 v[206:207], v5 offset:960\n"
    "v_mfma_f32_16x16x32_bf16 v[72:75], a[204:207], v[144:147], v[72:75]\n"
    "v_cvt_pk_bf16_f32 v34, v240, v241\n"
    "v_cvt_pk_bf16_f32 v35, v242, v243\n"
    "v_mfma_f32_16x16x32_bf16 v[76:79], a[208:211], v[132:135], v[88:91]\n"
    "ds_read_b64_tr_b16 a[252:253], v5 offset:224\n"
    "ds_read_b64_tr_b16 a[254:255], v5 offset:480\n"
    "v_mfma_f32_16x16x32_bf16 v[76:79], a[212:215], v[136:139], v[76:79]\n"
    "v_exp_f32 v240, v60\n"
    "v_mfma_f32_16x16x32_bf16 v[76:79], a[216:219], v[140:143], v[76:79]\n"
    "ds_read_b64_tr_b16 v[208:209], v5 offset:736\n"
    "ds_read_b64_tr_b16 v[210:211], v5 offset:992\n"
    "v_mfma_f32_16x16x32_bf16 v[76:79], a[220:223], v[144:147], v[76:79]\n"
    "s_waitcnt lgkmcnt(15)\n"
    "v_exp_f32 v241, v61\n"
    "v_mfma_f32_16x16x32_bf16 a[0:3], a[224:227], v[40:43], a[0:3]\n"
    "s_add_u32 m0, s34, 96\n"
    "v_exp_f32 v242, v62\n"
    "v_mfma_f32_16x16x32_bf16 a[0:3], v[180:183], v[44:47], a[0:3]\n"
    "buffer_load_dwordx4 v1, s[20:23], s32 offen offset:3072  lds\n"
    "v_mfma_f32_16x16x32_bf16 a[4:7], a[228:231], v[40:43], a[4:7]\n"
    "v_exp_f32 v243, v63\n"
    "v_mfma_f32_16x16x32_bf16 a[4:7], v[184:187], v[44:47], a[4:7]\n"
    "v_cvt_pk_bf16_f32 v36, v8, v9\n"
    "v_cvt_pk_bf16_f32 v37, v10, v11\n"
    "v_mfma_f32_16x16x32_bf16 a[8:11], a[232:235], v[40:43], a[8:11]\n"
    "s_add_u32 m0, s34, 16896\n"
    "v_cvt_pk_bf16_f32 v38, v240, v241\n"
    "v_cvt_pk_bf16_f32 v39, v242, v243\n"
    "v_mfma_f32_16x16x32_bf16 a[8:11], v[188:191], v[44:47], a[8:11]\n"
    "buffer_load_dwordx4 v1, s[24:27], s32 offen offset:0  lds\n"
    "v_mfma_f32_16x16x32_bf16 v[12:15], v[16:19], v[32:35], 0\n"
    "v_exp_f32 v8, v64\n"
    "v_mfma_f32_16x16x32_bf16 v[12:15], v[16:19], v[36:39], v[12:15]\n"
    "v_exp_f32 v9, v65\n"
    "v_mfma_f32_16x16x32_bf16 a[12:15], a[236:239], v[40:43], a[12:15]\n"
    "v_exp_f32 v10, v66\n"
    "v_mfma_f32_16x16x32_bf16 a[12:15], v[192:195], v[44:47], a[12:15]\n"
    "s_waitcnt lgkmcnt(0)\n"
    "v_exp_f32 v11, v67\n"
    "v_mfma_f32_16x16x32_bf16 a[16:19], a[240:243], v[40:43], a[16:19]\n"
    "v_exp_f32 v240, v68\n"
    "v_mfma_f32_16x16x32_bf16 a[16:19], v[196:199], v[44:47], a[16:19]\n"
    "v_exp_f32 v241, v69\n"
    "v_mfma_f32_16x16x32_bf16 a[20:23], a[244:247], v[40:43], a[20:23]\n"
    "v_cmp_nge_f32 vcc, s40, v12\n"
    "v_mfma_f32_16x16x32_bf16 a[20:23], v[200:203], v[44:47], a[20:23]\n"
    "v_exp_f32 v242, v70\n"
    "v_mfma_f32_16x16x32_bf16 a[24:27], a[248:251], v[40:43], a[24:27]\n"
    "v_exp_f32 v243, v71\n"
    "v_mfma_f32_16x16x32_bf16 a[24:27], v[204:207], v[44:47], a[24:27]\n"
    "s_cbranch_vccnz LTR_1\n"
    "LRET_1:\n"
    "v_add_f32 v21, v21, v12\n"
    "v_mfma_f32_16x16x32_bf16 a[28:31], a[252:255], v[40:43], a[28:31]\n"
    "v_cvt_pk_bf16_f32 v40, v8, v9\n"
    "v_cvt_pk_bf16_f32 v41, v10, v11\n"
    "v_mfma_f32_16x16x32_bf16 a[28:31], v[208:211], v[44:47], a[28:31]\n"
    "v_exp_f32 v8, v72\n"
    "v_mfma_f32_16x16x32_bf16 v[48:51], a[160:163], v[148:151], v[92:95]\n"
    "s_add_u32 m0, s34, 16928\n"
    "v_exp_f32 v9, v73\n"
    "v_mfma_f32_16x16x32_bf16 v[48:51], a[164:167], v[152:155], v[48:51]\n"
    "buffer_load_dwordx4 v1, s[24:27], s32 offen offset:1024  lds\n"
    "v_mfma_f32_16x16x32_bf16 v[48:51], a[168:171], v[156:159], v[48:51]\n"
    "v_exp_f32 v10, v74\n"
    "v_mfma_f32_16x16x32_bf16 v[48:51], a[172:175], v[160:163], v[48:51]\n"
    "v_exp_f32 v11, v75\n"
    "v_mfma_f32_16x16x32_bf16 v[52:55], a[176:179], v[148:151], v[92:95]\n"
    "v_cvt_pk_bf16_f32 v42, v240, v241\n"
    "v_cvt_pk_bf16_f32 v43, v242, v243\n"
    "v_mfma_f32_16x16x32_bf16 v[52:55], a[180:183], v[152:155], v[52:55]\n"
    "v_exp_f32 v240, v76\n"
    "v_mfma_f32_16x16x32_bf16 v[52:55], a[184:187], v[156:159], v[52:55]\n"
    "s_add_u32 m0, s34, 16960\n"
    "v_exp_f32 v241, v77\n"
    "v_mfma_f32_16x16x32_bf16 v[52:55], a[188:191], v[160:163], v[52:55]\n"
    "buffer_load_dwordx4 v1, s[24:27], s32 offen offset:2048  lds\n"
    "v_mfma_f32_16x16x32_bf16 v[56:59], a[192:195], v[148:151], v[92:95]\n"
    "v_exp_f32 v242, v78\n"
    "v_mfma_f32_16x16x32_bf16 v[56:59], a[196:199], v[152:155], v[56:59]\n"
    "v_exp_f32 v243, v79\n"
    "v_mfma_f32_16x16x32_bf16 v[56:59], a[200:203], v[156:159], v[56:59]\n"
    "v_cvt_pk_bf16_f32 v44, v8, v9\n"
    "v_cvt_pk_bf16_f32 v45, v10, v11\n"
    "v_mfma_f32_16x16x32_bf16 v[56:59], a[204:207], v[160:163], v[56:59]\n"
    "v_cvt_pk_bf16_f32 v46, v240, v241\n"
    "v_cvt_pk_bf16_f32 v47, v242, v243\n"
    "v_mfma_f32_16x16x32_bf16 v[12:15], v[16:19], v[40:43], 0\n"
    "v_exp_f32 v8, v48\n"
    "v_mfma_f32_16x16x32_bf16 v[12:15], v[16:19], v[44:47], v[12:15]\n"
    "v_exp_f32 v9, v49\n"
    "v_mfma_f32_16x16x32_bf16 v[60:63], a[208:211], v[148:151], v[92:95]\n"
    "s_add_u32 m0, s34, 16992\n"
    "v_exp_f32 v10, v50\n"
    "v_mfma_f32_16x16x32_bf16 v[60:63], a[212:215], v[152:155], v[60:63]\n"
    "buffer_load_dwordx4 v1, s[24:27], s32 offen offset:3072  lds\n"
    "v_mfma_f32_16x16x32_bf16 v[60:63], a[216:219], v[156:159], v[60:63]\n"
    "v_exp_f32 v11, v51\n"
    "v_mfma_f32_16x16x32_bf16 v[60:63], a[220:223], v[160:163], v[60:63]\n"
    "v_exp_f32 v240, v52\n"
    "v_mfma_f32_16x16x32_bf16 a[32:35], a[224:227], v[32:35], a[32:35]\n"
    "v_cmp_nge_f32 vcc, s40, v12\n"
    "v_mfma_f32_16x16x32_bf16 a[32:35], v[180:183], v[36:39], a[32:35]\n"
    "v_exp_f32 v241, v53\n"
    "v_mfma_f32_16x16x32_bf16 a[36:39], a[228:231], v[32:35], a[36:39]\n"
    "v_exp_f32 v242, v54\n"
    "v_mfma_f32_16x16x32_bf16 a[36:39], v[184:187], v[36:39], a[36:39]\n"
    "s_cbranch_vccnz LTR_2\n"
    "LRET_2:\n"
    "v_add_f32 v22, v22, v12\n"
    "v_mfma_f32_16x16x32_bf16 a[40:43], a[232:235], v[32:35], a[40:43]\n"
    "v_exp_f32 v243, v55\n"
    "v_mfma_f32_16x16x32_bf16 a[40:43], v[188:191], v[36:39], a[40:43]\n"
    "s_waitcnt vmcnt(10)\n"
    "v_lshlrev_b32 v31, 16, v212\n"
    "v_and_b32 v255, s42, v212\n"
    "v_mfma_f32_16x16x32_bf16 a[44:47], a[236:239], v[32:35], a[44:47]\n"
    "v_mul_f32 v31, s41, v31\n"
    "v_mul_f32 v255, s41, v255\n"
    "v_mfma_f32_16x16x32_bf16 a[44:47], v[192:195], v[36:39], a[44:47]\n"
    "v_cvt_pk_bf16_f32 v232, v31, v255\n"
    "v_lshlrev_b32 v31, 16, v213\n"
    "v_mfma_f32_16x16x32_bf16 a[48:51], a[240:243], v[32:35], a[48:51]\n"
    "s_add_u32 s34, s36, s35\n"
    "v_and_b32 v255, s42, v213\n"
    "v_mul_f32 v31, s41, v31\n"
    "v_mfma_f32_16x16x32_bf16 a[48:51], v[196:199], v[36:39], a[48:51]\n"
    "v_mul_f32 v255, s41, v255\n"
    "v_cvt_pk_bf16_f32 v233, v31, v255\n"
    "v_mfma_f32_16x16x32_bf16 a[52:55], a[244:247], v[32:35], a[52:55]\n"
    "s_add_u32 s36, s36, 33792\n"
    "v_lshlrev_b32 v31, 16, v214\n"
    "v_and_b32 v255, s42, v214\n"
    "v_mfma_f32_16x16x32_bf16 a[52:55], v[200:203], v[36:39], a[52:55]\n"
    "v_mul_f32 v31, s41, v31\n"
    "v_mul_f32 v255, s41, v255\n"
    "v_mfma_f32_16x16x32_bf16 a[56:59], a[248:251], v[32:35], a[56:59]\n"
    "s_sub_u32 s50, s36, 101376\n"
    "v_cvt_pk_bf16_f32 v234, v31, v255\n"
    "v_lshlrev_b32 v31, 16, v215\n"
    "v_mfma_f32_16x16x32_bf16 a[56:59], v[204:207], v[36:39], a[56:59]\n"
    "v_and_b32 v255, s42, v215\n"
    "v_mul_f32 v31, s41, v31\n"
    "v_mfma_f32_16x16x32_bf16 a[60:63], a[252:255], v[32:35], a[60:63]\n"
    "s_min_u32 s36, s36, s50\n"
    "v_cvt_pk_bf16_f32 v32, v8, v9\n"
    "v_cvt_pk_bf16_f32 v33, v10, v11\n"
    "v_mfma_f32_16x16x32_bf16 a[60:63], v[208:211], v[36:39], a[60:63]\n"
    "s_add_u32 s32, s32, 16384\n"
    "v_exp_f32 v8, v56\n"
    "v_mfma_f32_16x16x32_bf16 v[64:67], a[160:163], v[164:167], v[96:99]\n"
    "v_add_u32 v4, s36, v2\n"
    "v_mfma_f32_16x16x32_bf16 v[64:67], a[164:167], v[168:171], v[64:67]\n"
    "s_waitcnt vmcnt(10)\n"
    "s_barrier\n"
    "v_mfma_f32_16x16x32_bf16 v[64:67], a[168:171], v[172:175], v[64:67]\n"
    "v_exp_f32 v9, v57\n"
    "v_mfma_f32_16x16x32_bf16 v[64:67], a[172:175], v[176:179], v[64:67]\n"
    "v_exp_f32 v10, v58\n"
    "v_mfma_f32_16x16x32_bf16 v[68:71], a[176:179], v[164:167], v[96:99]\n"
    "ds_read_b128 a[160:163], v4 offset:0\n"
    "ds_read_b128 a[164:167], v4 offset:64\n"
    "v_mfma_f32_16x16x32_bf16 v[68:71], a[180:183], v[168:171], v[68:71]\n"
    "v_exp_f32 v11, v59\n"
    "v_mfma_f32_16x16x32_bf16 v[68:71], a[184:187], v[172:175], v[68:71]\n"
    "v_cvt_pk_bf16_f32 v34, v240, v241\n"
    "v_cvt_pk_bf16_f32 v35, v242, v243\n"
    "v_mfma_f32_16x16x32_bf16 v[68:71], a[188:191], v[176:179], v[68:71]\n"
    "ds_read_b128 a[168:171], v4 offset:128\n"
    "ds_read_b128 a[172:175], v4 offset:192\n"
    "v_mfma_f32_16x16x32_bf16 v[72:75], a[192:195], v[164:167], v[96:99]\n"
    "v_exp_f32 v240, v60\n"
    "v_mfma_f32_16x16x32_bf16 v[72:75], a[196:199], v[168:171], v[72:75]\n"
    "v_exp_f32 v241, v61\n"
    "v_mfma_f32_16x16x32_bf16 v[72:75], a[200:203], v[172:175], v[72:75]\n"
    "ds_read_b128 a[176:179], v4 offset:256\n"
    "ds_read_b128 a[180:183], v4 offset:320\n"
    "v_mfma_f32_16x16x32_bf16 v[72:75], a[204:207], v[176:179], v[72:75]\n"
    "v_exp_f32 v242, v62\n"
    "v_mfma_f32_16x16x32_bf16 v[76:79], a[208:211], v[164:167], v[96:99]\n"
    "v_exp_f32 v243, v63\n"
    "v_mfma_f32_16x16x32_bf16 v[76:79], a[212:215], v[168:171], v[76:79]\n"
    "ds_read_b128 a[184:187], v4 offset:384\n"
    "ds_read_b128 a[188:191], v4 offset:448\n"
    "v_mfma_f32_16x16x32_bf16 v[76:79], a[216:219], v[172:175], v[76:79]\n"
    "v_cvt_pk_bf16_f32 v36, v8, v9\n"
    "v_cvt_pk_bf16_f32 v37, v10, v11\n"
    "v_mfma_f32_16x16x32_bf16 v[76:79], a[220:223], v[176:179], v[76:79]\n"
    "v_cvt_pk_bf16_f32 v38, v240, v241\n"
    "v_cvt_pk_bf16_f32 v39, v242, v243\n"
    "v_mfma_f32_16x16x32_bf16 v[12:15], v[16:19], v[32:35], 0\n"
    "ds_read_b128 a[192:195], v4 offset:512\n"
    "ds_read_b128 a[196:199], v4 offset:576\n"
    "v_mfma_f32_16x16x32_bf16 v[12:15], v[16:19], v[36:39], v[12:15]\n"
    "v_exp_f32 v8, v64\n"
    "v_mfma_f32_16x16x32_bf16 a[64:67], a[224:227], v[40:43], a[64:67]\n"
    "ds_read_b128 a[200:203], v4 offset:640\n"
    "ds_read_b128 a[204:207], v4 offset:704\n"
    "v_mfma_f32_16x16x32_bf16 a[64:67], v[180:183], v[44:47], a[64:67]\n"
    "v_exp_f32 v9, v65\n"
    "v_mfma_f32_16x16x32_bf16 a[68:71], a[228:231], v[40:43], a[68:71]\n"
    "ds_read_b128 a[208:211], v4 offset:768\n"
    "ds_read_b128 a[212:215], v4 offset:832\n"
    "v_mfma_f32_16x16x32_bf16 a[68:71], v[184:187], v[44:47], a[68:71]\n"
    "v_exp_f32 v10, v66\n"
    "v_mfma_f32_16x16x32_bf16 a[72:75], a[232:235], v[40:43], a[72:75]\n"
    "v_cmp_nge_f32 vcc, s40, v12\n"
    "v_mfma_f32_16x16x32_bf16 a[72:75], v[188:191], v[44:47], a[72:75]\n"
    "ds_read_b128 a[216:219], v4 offset:896\n"
    "ds_read_b128 a[220:223], v4 offset:960\n"
    "v_mfma_f32_16x16x32_bf16 a[76:79], a[236:239], v[40:43], a[76:79]\n"
    "v_exp_f32 v11, v67\n"
    "v_mfma_f32_16x16x32_bf16 a[76:79], v[192:195], v[44:47], a[76:79]\n"
    "s_cbranch_vccnz LTR_3\n"
    "LRET_3:\n"
    "v_add_f32 v23, v23, v12\n"
    "v_mfma_f32_16x16x32_bf16 a[80:83], a[240:243], v[40:43], a[80:83]\n"
    "v_exp_f32 v240, v68\n"
    "v_mfma_f32_16x16x32_bf16 a[80:83], v[196:199], v[44:47], a[80:83]\n"
    "v_exp_f32 v241, v69\n"
    "v_mfma_f32_16x16x32_bf16 a[84:87], a[244:247], v[40:43], a[84:87]\n"
    "v_exp_f32 v242, v70\n"
    "v_mfma_f32_16x16x32_bf16 a[84:87], v[200:203], v[44:47], a[84:87]\n"
    "v_exp_f32 v243, v71\n"
    "v_mfma_f32_16x16x32_bf16 a[88:91], a[248:251], v[40:43], a[88:91]\n"
    "v_mul_f32 v255, s41, v255\n"
    "v_cvt_pk_bf16_f32 v235, v31, v255\n"
    "v_mfma_f32_16x16x32_bf16 a[88:91], v[204:207], v[44:47], a[88:91]\n"
    "buffer_load_dwordx4 v[212:215], v6, s[16:19], s53 offen offset:0 \n"
    "v_mfma_f32_16x16x32_bf16 a[92:95], a[252:255], v[40:43], a[92:95]\n"
    "v_cvt_pk_bf16_f32 v40, v8, v9\n"
    "v_cvt_pk_bf16_f32 v41, v10, v11\n"
    "v_mfma_f32_16x16x32_bf16 a[92:95], v[208:211], v[44:47], a[92:95]\n"
    "s_waitcnt lgkmcnt(8)\n"
    "v_exp_f32 v8, v72\n"
    "v_mfma_f32_16x16x32_bf16 v[48:51], a[160:163], v[100:103], v[80:83]\n"
    "v_exp_f32 v9, v73\n"
    "v_mfma_f32_16x16x32_bf16 v[48:51], a[164:167], v[104:107], v[48:51]\n"
    "s_add_u32 m0, s34, 0\n"
    "v_exp_f32 v10, v74\n"
    "v_mfma_f32_16x16x32_bf16 v[48:51], a[168:171], v[108:111], v[48:51]\n"
    "buffer_load_dwordx4 v1, s[20:23], s32 offen offset:0  lds\n"
    "v_mfma_f32_16x16x32_bf16 v[48:51], a[172:175], v[112:115], v[48:51]\n"
    "v_exp_f32 v11, v75\n"
    "v_mfma_f32_16x16x32_bf16 v[52:55], a[176:179], v[100:103], v[80:83]\n"
    "v_cvt_pk_bf16_f32 v42, v240, v241\n"
    "v_cvt_pk_bf16_f32 v43, v242, v243\n"
    "v_mfma_f32_16x16x32_bf16 v[52:55], a[180:183], v[104:107], v[52:55]\n"
    "v_exp_f32 v240, v76\n"
    "v_mfma_f32_16x16x32_bf16 v[52:55], a[184:187], v[108:111], v[52:55]\n"
    "v_exp_f32 v241, v77\n"
    "v_mfma_f32_16x16x32_bf16 v[52:55], a[188:191], v[112:115], v[52:55]\n"
    "s_waitcnt lgkmcnt(0)\n"
    "v_exp_f32 v242, v78\n"
    "v_mfma_f32_16x16x32_bf16 v[56:59], a[192:195], v[100:103], v[80:83]\n"
    "v_exp_f32 v243, v79\n"
    "v_mfma_f32_16x16x32_bf16 v[56:59], a[196:199], v[104:107], v[56:59]\n"
    "v_cvt_pk_bf16_f32 v44, v8, v9\n"
    "v_cvt_pk_bf16_f32 v45, v10, v11\n"
    "v_mfma_f32_16x16x32_bf16 v[56:59], a[200:203], v[108:111], v[56:59]\n"
    "v_cvt_pk_bf16_f32 v46, v240, v241\n"
    "v_cvt_pk_bf16_f32 v47, v242, v243\n"
    "v_mfma_f32_16x16x32_bf16 v[56:59], a[204:207], v[112:115], v[56:59]\n"
    "v_exp_f32 v8, v48\n"
    "v_mfma_f32_16x16x32_bf16 v[12:15], v[16:19], v[40:43], 0\n"
    "v_exp_f32 v9, v49\n"
    "v_mfma_f32_16x16x32_bf16 v[12:15], v[16:19], v[44:47], v[12:15]\n"
    "v_exp_f32 v10, v50\n"
    "v_mfma_f32_16x16x32_bf16 v[60:63], a[208:211], v[100:103], v[80:83]\n"
    "v_exp_f32 v11, v51\n"
    "v_mfma_f32_16x16x32_bf16 v[60:63], a[212:215], v[104:107], v[60:63]\n"
    "s_add_u32 m0, s34, 32\n"
    "v_exp_f32 v240, v52\n"
    "v_mfma_f32_16x16x32_bf16 v[60:63], a[216:219], v[108:111], v[60:63]\n"
    "buffer_load_dwordx4 v1, s[20:23], s32 offen offset:1024  lds\n"
    "v_mfma_f32_16x16x32_bf16 v[60:63], a[220:223], v[112:115], v[60:63]\n"
    "v_exp_f32 v241, v53\n"
    "v_mfma_f32_16x16x32_bf16 a[96:99], a[224:227], v[32:35], a[96:99]\n"
    "v_cmp_nge_f32 vcc, s40, v12\n"
    "v_mfma_f32_16x16x32_bf16 a[96:99], v[180:183], v[36:39], a[96:99]\n"
    "v_exp_f32 v242, v54\n"
    "v_mfma_f32_16x16x32_bf16 a[100:103], a[228:231], v[32:35], a[100:103]\n"
    "v_exp_f32 v243, v55\n"
    "v_mfma_f32_16x16x32_bf16 a[100:103], v[184:187], v[36:39], a[100:103]\n"
    "s_cbranch_vccnz LTR_4\n"
    "LRET_4:\n"
    "v_add_f32 v24, v24, v12\n"
    "v_mfma_f32_16x16x32_bf16 a[104:107], a[232:235], v[32:35], a[104:107]\n"
    "ds_write_b128 v228, v[232:235] offset:0\n"
    "v_mfma_f32_16x16x32_bf16 a[104:107], v[188:191], v[36:39], a[104:107]\n"
    "s_waitcnt vmcnt(12)\n"
    "v_lshlrev_b32 v31, 16, v216\n"
    "v_and_b32 v255, s42, v216\n"
    "v_mfma_f32_16x16x32_bf16 a[108:111], a[236:239], v[32:35], a[108:111]\n"
    "v_mul_f32 v31, s41, v31\n"
    "v_mul_f32 v255, s41, v255\n"
    "v_mfma_f32_16x16x32_bf16 a[108:111], v[192:195], v[36:39], a[108:111]\n"
    "v_cvt_pk_bf16_f32 v236, v31, v255\n"
    "v_lshlrev_b32 v31, 16, v217\n"
    "v_mfma_f32_16x16x32_bf16 a[112:115], a[240:243], v[32:35], a[112:115]\n"
    "v_and_b32 v255, s42, v217\n"
    "v_mul_f32 v31, s41, v31\n"
    "v_mfma_f32_16x16x32_bf16 a[112:115], v[196:199], v[36:39], a[112:115]\n"
    "v_mul_f32 v255, s41, v255\n"
    "v_cvt_pk_bf16_f32 v237, v31, v255\n"
    "v_mfma_f32_16x16x32_bf16 a[116:119], a[244:247], v[32:35], a[116:119]\n"
    "v_lshlrev_b32 v31, 16, v218\n"
    "v_and_b32 v255, s42, v218\n"
    "v_mfma_f32_16x16x32_bf16 a[116:119], v[200:203], v[36:39], a[116:119]\n"
    "v_mul_f32 v31, s41, v31\n"
    "v_mul_f32 v255, s41, v255\n"
    "v_mfma_f32_16x16x32_bf16 a[120:123], a[248:251], v[32:35], a[120:123]\n"
    "v_cvt_pk_bf16_f32 v238, v31, v255\n"
    "v_lshlrev_b32 v31, 16, v219\n"
    "v_mfma_f32_16x16x32_bf16 a[120:123], v[204:207], v[36:39], a[120:123]\n"
    "v_and_b32 v255, s42, v219\n"
    "v_mul_f32 v31, s41, v31\n"
    "v_mfma_f32_16x16x32_bf16 a[124:127], a[252:255], v[32:35], a[124:127]\n"
    "v_cvt_pk_bf16_f32 v32, v8, v9\n"
    "v_cvt_pk_bf16_f32 v33, v10, v11\n"
    "v_mfma_f32_16x16x32_bf16 a[124:127], v[208:211], v[36:39], a[124:127]\n"
    "v_exp_f32 v8, v56\n"
    "v_mfma_f32_16x16x32_bf16 v[64:67], a[160:163], v[116:119], v[84:87]\n"
    "v_exp_f32 v9, v57\n"
    "v_mfma_f32_16x16x32_bf16 v[64:67], a[164:167], v[120:123], v[64:67]\n"
    "v_add_u32 v5, s36, v3\n"
    "v_mfma_f32_16x16x32_bf16 v[64:67], a[168:171], v[124:127], v[64:67]\n"
    "v_exp_f32 v10, v58\n"
    "v_mfma_f32_16x16x32_bf16 v[64:67], a[172:175], v[128:131], v[64:67]\n"
    "v_exp_f32 v11, v59\n"
    "v_mfma_f32_16x16x32_bf16 v[68:71], a[176:179], v[116:119], v[84:87]\n"
    "v_cvt_pk_bf16_f32 v34, v240, v241\n"
    "v_cvt_pk_bf16_f32 v35, v242, v243\n"
    "v_mfma_f32_16x16x32_bf16 v[68:71], a[180:183], v[120:123], v[68:71]\n"
    "v_exp_f32 v240, v60\n"
    "v_mfma_f32_16x16x32_bf16 v[68:71], a[184:187], v[124:127], v[68:71]\n"
    "v_exp_f32 v241, v61\n"
    "v_mfma_f32_16x16x32_bf16 v[68:71], a[188:191], v[128:131], v[68:71]\n"
    "v_exp_f32 v242, v62\n"
    "v_mfma_f32_16x16x32_bf16 v[72:75], a[192:195], v[116:119], v[84:87]\n"
    "v_exp_f32 v243, v63\n"
    "v_mfma_f32_16x16x32_bf16 v[72:75], a[196:199], v[120:123], v[72:75]\n"
    "v_cvt_pk_bf16_f32 v36, v8, v9\n"
    "v_cvt_pk_bf16_f32 v37, v10, v11\n"
    "v_mfma_f32_16x16x32_bf16 v[72:75], a[200:203], v[124:127], v[72:75]\n"
    "v_cvt_pk_bf16_f32 v38, v240, v241\n"
    "v_cvt_pk_bf16_f32 v39, v242, v243\n"
    "v_mfma_f32_16x16x32_bf16 v[72:75], a[204:207], v[128:131], v[72:75]\n"
    "v_exp_f32 v8, v64\n"
    "v_mfma_f32_16x16x32_bf16 v[12:15], v[16:19], v[32:35], 0\n"
    "v_exp_f32 v9, v65\n"
    "v_mfma_f32_16x16x32_bf16 v[12:15], v[16:19], v[36:39], v[12:15]\n"
    "v_exp_f32 v10, v66\n"
    "v_mfma_f32_16x16x32_bf16 v[76:79], a[208:211], v[116:119], v[84:87]\n"
    "v_exp_f32 v11, v67\n"
    "v_mfma_f32_16x16x32_bf16 v[76:79], a[212:215], v[120:123], v[76:79]\n"
    "v_exp_f32 v240, v68\n"
    "v_mfma_f32_16x16x32_bf16 v[76:79], a[216:219], v[124:127], v[76:79]\n"
    "v_exp_f32 v241, v69\n"
    "v_mfma_f32_16x16x32_bf16 v[76:79], a[220:223], v[128:131], v[76:79]\n"
    "v_exp_f32 v242, v70\n"
    "v_mfma_f32_16x16x32_bf16 a[128:131], a[224:227], v[40:43], a[128:131]\n"
    "ds_read_b64_tr_b16 a[224:225], v5 offset:0\n"
    "ds_read_b64_tr_b16 a[226:227], v5 offset:256\n"
    "v_mfma_f32_16x16x32_bf16 a[128:131], v[180:183], v[44:47], a[128:131]\n"
    "v_cmp_nge_f32 vcc, s40, v12\n"
    "v_mfma_f32_16x16x32_bf16 a[132:135], a[228:231], v[40:43], a[132:135]\n"
    "ds_read_b64_tr_b16 v[180:181], v5 offset:512\n"
    "ds_read_b64_tr_b16 v[182:183], v5 offset:768\n"
    "v_mfma_f32_16x16x32_bf16 a[132:135], v[184:187], v[44:47], a[132:135]\n"
    "v_exp_f32 v243, v71\n"
    "v_mfma_f32_16x16x32_bf16 a[136:139], a[232:235], v[40:43], a[136:139]\n"
    "ds_read_b64_tr_b16 a[228:229], v5 offset:32\n"
    "ds_read_b64_tr_b16 a[230:231], v5 offset:288\n"
    "v_mfma_f32_16x16x32_bf16 a[136:139], v[188:191], v[44:47], a[136:139]\n"
    "s_cbranch_vccnz LTR_5\n"
    "LRET_5:\n"
    "v_add_f32 v20, v20, v12\n"
    "v_mfma_f32_16x16x32_bf16 a[140:143], a[236:239], v[40:43], a[140:143]\n"
    "ds_read_b64_tr_b16 v[184:185], v5 offset:544\n"
    "ds_read_b64_tr_b16 v[186:187], v5 offset:800\n"
    "v_mfma_f32_16x16x32_bf16 a[140:143], v[192:195], v[44:47], a[140:143]\n"
    "v_mul_f32 v255, s41, v255\n"
    "v_cvt_pk_bf16_f32 v239, v31, v255\n"
    "v_mfma_f32_16x16x32_bf16 a[144:147], a[240:243], v[40:43], a[144:147]\n"
    "ds_read_b64_tr_b16 a[232:233], v5 offset:64\n"
    "ds_read_b64_tr_b16 a[234:235], v5 offset:320\n"
    "v_mfma_f32_16x16x32_bf16 a[144:147], v[196:199], v[44:47], a[144:147]\n"
    "buffer_load_dwordx4 v[216:219], v6, s[16:19], s53 offen offset:64 \n"
    "v_mfma_f32_16x16x32_bf16 a[148:151], a[244:247], v[40:43], a[148:151]\n"
    "ds_read_b64_tr_b16 v[188:189], v5 offset:576\n"
    "ds_read_b64_tr_b16 v[190:191], v5 offset:832\n"
    "v_mfma_f32_16x16x32_bf16 a[148:151], v[200:203], v[44:47], a[148:151]\n"
    "ds_write_b128 v228, v[236:239] offset:1024\n"
    "v_mfma_f32_16x16x32_bf16 a[152:155], a[248:251], v[40:43], a[152:155]\n"
    "ds_read_b64_tr_b16 a[236:237], v5 offset:96\n"
    "ds_read_b64_tr_b16 a[238:239], v5 offset:352\n"
    "v_mfma_f32_16x16x32_bf16 a[152:155], v[204:207], v[44:47], a[152:155]\n"
    "s_waitcnt vmcnt(12)\n"
    "v_lshlrev_b32 v31, 16, v220\n"
    "v_and_b32 v255, s42, v220\n"
    "v_mfma_f32_16x16x32_bf16 a[156:159], a[252:255], v[40:43], a[156:159]\n"
    "ds_read_b64_tr_b16 v[192:193], v5 offset:608\n"
    "ds_read_b64_tr_b16 v[194:195], v5 offset:864\n"
    "v_mfma_f32_16x16x32_bf16 a[156:159], v[208:211], v[44:47], a[156:159]\n"
    "v_cvt_pk_bf16_f32 v40, v8, v9\n"
    "v_cvt_pk_bf16_f32 v41, v10, v11\n"
    "v_mfma_f32_16x16x32_bf16 v[48:51], a[160:163], v[132:135], v[88:91]\n"
    "s_add_u32 m0, s34, 64\n"
    "ds_read_b64_tr_b16 a[240:241], v5 offset:128\n"
    "ds_read_b64_tr_b16 a[242:243], v5 offset:384\n"
    "v_mfma_f32_16x16x32_bf16 v[48:51], a[164:167], v[136:139], v[48:51]\n"
    "buffer_load_dwordx4 v1, s[20:23], s32 offen offset:2048  lds\n"
    "v_mfma_f32_16x16x32_bf16 v[48:51], a[168:171], v[140:143], v[48:51]\n"
    "ds_read_b64_tr_b16 v[196:197], v5 offset:640\n"
    "ds_read_b64_tr_b16 v[198:199], v5 offset:896\n"
    "v_mfma_f32_16x16x32_bf16 v[48:51], a[172:175], v[144:147], v[48:51]\n"
    "v_exp_f32 v8, v72\n"
    "v_mfma_f32_16x16x32_bf16 v[52:55], a[176:179], v[132:135], v[88:91]\n"
    "ds_read_b64_tr_b16 a[244:245], v5 offset:160\n"
    "ds_read_b64_tr_b16 a[246:247], v5 offset:416\n"
    "v_mfma_f32_16x16x32_bf16 v[52:55], a[180:183], v[136:139], v[52:55]\n"
    "v_exp_f32 v9, v73\n"
    "v_mfma_f32_16x16x32_bf16 v[52:55], a[184:187], v[140:143], v[52:55]\n"
    "ds_read_b64_tr_b16 v[200:201], v5 offset:672\n"
    "ds_read_b64_tr_b16 v[202:203], v5 offset:928\n"
    "v_mfma_f32_16x16x32_bf16 v[52:55], a[188:191], v[144:147], v[52:55]\n"
    "v_exp_f32 v10, v74\n"
    "v_mfma_f32_16x16x32_bf16 v[56:59], a[192:195], v[132:135], v[88:91]\n"
    "ds_read_b64_tr_b16 a[248:249], v5 offset:192\n"
    "ds_read_b64_tr_b16 a[250:251], v5 offset:448\n"
    "v_mfma_f32_16x16x32_bf16 v[56:59], a[196:199], v[136:139], v[56:59]\n"
    "v_exp_f32 v11, v75\n"
    "v_mfma_f32_16x16x32_bf16 v[56:59], a[200:203], v[140:143], v[56:59]\n"
    "ds_read_b64_tr_b16 v[204:205], v5 offset:704\n"
    "ds_read_b64_tr_b16 v[206:207], v5 offset:960\n"
    "v_mfma_f32_16x16x32_bf16 v[56:59], a[204:207], v[144:147], v[56:59]\n"
    "v_cvt_pk_bf16_f32 v42, v240, v241\n"
    "v_cvt_pk_bf16_f32 v43, v242, v243\n"
    "v_mfma_f32_16x16x32_bf16 v[60:63], a[208:211], v[132:135], v[88:91]\n"
    "ds_read_b64_tr_b16 a[252:253], v5 offset:224\n"
    "ds_read_b64_tr_b16 a[254:255], v5 offset:480\n"
    "v_mfma_f32_16x16x32_bf16 v[60:63], a[212:215], v[136:139], v[60:63]\n"
    "v_exp_f32 v240, v76\n"
    "v_mfma_f32_16x16x32_bf16 v[60:63], a[216:219], v[140:143], v[60:63]\n"
    "ds_read_b64_tr_b16 v[208:209], v5 offset:736\n"
    "ds_read_b64_tr_b16 v[210:211], v5 offset:992\n"
    "v_mfma_f32_16x16x32_bf16 v[60:63], a[220:223], v[144:147], v[60:63]\n"
    "s_waitcnt lgkmcnt(15)\n"
    "v_exp_f32 v241, v77\n"
    "v_mfma_f32_16x16x32_bf16 a[0:3], a[224:227], v[32:35], a[0:3]\n"
    "s_add_u32 m0, s34, 96\n"
    "v_exp_f32 v242, v78\n"
    "v_mfma_f32_16x16x32_bf16 a[0:3], v[180:183], v[36:39], a[0:3]\n"
    "buffer_load_dwordx4 v1, s[20:23], s32 offen offset:3072  lds\n"
    "v_mfma_f32_16x16x32_bf16 a[4:7], a[228:231], v[32:35], a[4:7]\n"
    "v_exp_f32 v243, v79\n"
    "v_mfma_f32_16x16x32_bf16 a[4:7], v[184:187], v[36:39], a[4:7]\n"
    "v_cvt_pk_bf16_f32 v44, v8, v9\n"
    "v_cvt_pk_bf16_f32 v45, v10, v11\n"
    "v_mfma_f32_16x16x32_bf16 a[8:11], a[232:235], v[32:35], a[8:11]\n"
    "s_add_u32 m0, s34, 16896\n"
    "v_cvt_pk_bf16_f32 v46, v240, v241\n"
    "v_cvt_pk_bf16_f32 v47, v242, v243\n"
    "v_mfma_f32_16x16x32_bf16 a[8:11], v[188:191], v[36:39], a[8:11]\n"
    "buffer_load_dwordx4 v1, s[24:27], s32 offen offset:0  lds\n"
    "v_mfma_f32_16x16x32_bf16 v[12:15], v[16:19], v[40:43], 0\n"
    "v_exp_f32 v8, v48\n"
    "v_mfma_f32_16x16x32_bf16 v[12:15], v[16:19], v[44:47], v[12:15]\n"
    "v_exp_f32 v9, v49\n"
    "v_mfma_f32_16x16x32_bf16 a[12:15], a[236:239], v[32:35], a[12:15]\n"
    "v_exp_f32 v10, v50\n"
    "v_mfma_f32_16x16x32_bf16 a[12:15], v[192:195], v[36:39], a[12:15]\n"
    "s_waitcnt lgkmcnt(0)\n"
    "v_exp_f32 v11, v51\n"
    "v_mfma_f32_16x16x32_bf16 a[16:19], a[240:243], v[32:35], a[16:19]\n"
    "v_exp_f32 v240, v52\n"
    "v_mfma_f32_16x16x32_bf16 a[16:19], v[196:199], v[36:39], a[16:19]\n"
    "v_exp_f32 v241, v53\n"
    "v_mfma_f32_16x16x32_bf16 a[20:23], a[244:247], v[32:35], a[20:23]\n"
    "v_cmp_nge_f32 vcc, s40, v12\n"
    "v_mfma_f32_16x16x32_bf16 a[20:23], v[200:203], v[36:39], a[20:23]\n"
    "v_exp_f32 v242, v54\n"
    "v_mfma_f32_16x16x32_bf16 a[24:27], a[248:251], v[32:35], a[24:27]\n"
    "v_exp_f32 v243, v55\n"
    "v_mfma_f32_16x16x32_bf16 a[24:27], v[204:207], v[36:39], a[24:27]\n"
    "s_cbranch_vccnz LTR_6\n"
    "LRET_6:\n"
    "v_add_f32 v21, v21, v12\n"
    "v_mfma_f32_16x16x32_bf16 a[28:31], a[252:255], v[32:35], a[28:31]\n"
    "v_cvt_pk_bf16_f32 v32, v8, v9\n"
    "v_cvt_pk_bf16_f32 v33, v10, v11\n"
    "v_mfma_f32_16x16x32_bf16 a[28:31], v[208:211], v[36:39], a[28:31]\n"
    "v_exp_f32 v8, v56\n"
    "v_mfma_f32_16x16x32_bf16 v[64:67], a[160:163], v[148:151], v[92:95]\n"
    "s_add_u32 m0, s34, 16928\n"
    "v_exp_f32 v9, v57\n"
    "v_mfma_f32_16x16x32_bf16 v[64:67], a[164:167], v[152:155], v[64:67]\n"
    "buffer_load_dwordx4 v1, s[24:27], s32 offen offset:1024  lds\n"
    "v_mfma_f32_16x16x32_bf16 v[64:67], a[168:171], v[156:159], v[64:67]\n"
    "v_exp_f32 v10, v58\n"
    "v_mfma_f32_16x16x32_bf16 v[64:67], a[172:175], v[160:163], v[64:67]\n"
    "v_exp_f32 v11, v59\n"
    "v_mfma_f32_16x16x32_bf16 v[68:71], a[176:179], v[148:151], v[92:95]\n"
    "v_cvt_pk_bf16_f32 v34, v240, v241\n"
    "v_cvt_pk_bf16_f32 v35, v242, v243\n"
    "v_mfma_f32_16x16x32_bf16 v[68:71], a[180:183], v[152:155], v[68:71]\n"
    "v_exp_f32 v240, v60\n"
    "v_mfma_f32_16x16x32_bf16 v[68:71], a[184:187], v[156:159], v[68:71]\n"
    "s_add_u32 m0, s34, 16960\n"
    "v_exp_f32 v241, v61\n"
    "v_mfma_f32_16x16x32_bf16 v[68:71], a[188:191], v[160:163], v[68:71]\n"
    "buffer_load_dwordx4 v1, s[24:27], s32 offen offset:2048  lds\n"
    "v_mfma_f32_16x16x32_bf16 v[72:75], a[192:195], v[148:151], v[92:95]\n"
    "v_exp_f32 v242, v62\n"
    "v_mfma_f32_16x16x32_bf16 v[72:75], a[196:199], v[152:155], v[72:75]\n"
    "v_exp_f32 v243, v63\n"
    "v_mfma_f32_16x16x32_bf16 v[72:75], a[200:203], v[156:159], v[72:75]\n"
    "v_cvt_pk_bf16_f32 v36, v8, v9\n"
    "v_cvt_pk_bf16_f32 v37, v10, v11\n"
    "v_mfma_f32_16x16x32_bf16 v[72:75], a[204:207], v[160:163], v[72:75]\n"
    "v_cvt_pk_bf16_f32 v38, v240, v241\n"
    "v_cvt_pk_bf16_f32 v39, v242, v243\n"
    "v_mfma_f32_16x16x32_bf16 v[12:15], v[16:19], v[32:35], 0\n"
    "v_exp_f32 v8, v64\n"
    "v_mfma_f32_16x16x32_bf16 v[12:15], v[16:19], v[36:39], v[12:15]\n"
    "v_exp_f32 v9, v65\n"
    "v_mfma_f32_16x16x32_bf16 v[76:79], a[208:211], v[148:151], v[92:95]\n"
    "s_add_u32 m0, s34, 16992\n"
    "v_exp_f32 v10, v66\n"
    "v_mfma_f32_16x16x32_bf16 v[76:79], a[212:215], v[152:155], v[76:79]\n"
    "buffer_load_dwordx4 v1, s[24:27], s32 offen offset:3072  lds\n"
    "v_mfma_f32_16x16x32_bf16 v[76:79], a[216:219], v[156:159], v[76:79]\n"
    "v_exp_f32 v11, v67\n"
    "v_mfma_f32_16x16x32_bf16 v[76:79], a[220:223], v[160:163], v[76:79]\n"
    "v_exp_f32 v240, v68\n"
    "v_mfma_f32_16x16x32_bf16 a[32:35], a[224:227], v[40:43], a[32:35]\n"
    "v_cmp_nge_f32 vcc, s40, v12\n"
    "v_mfma_f32_16x16x32_bf16 a[32:35], v[180:183], v[44:47], a[32:35]\n"
    "v_exp_f32 v241, v69\n"
    "v_mfma_f32_16x16x32_bf16 a[36:39], a[228:231], v[40:43], a[36:39]\n"
    "v_exp_f32 v242, v70\n"
    "v_mfma_f32_16x16x32_bf16 a[36:39], v[184:187], v[44:47], a[36:39]\n"
    "s_cbranch_vccnz LTR_7\n"
    "LRET_7:\n"
    "v_add_f32 v22, v22, v12\n"
    "v_mfma_f32_16x16x32_bf16 a[40:43], a[232:235], v[40:43], a[40:43]\n"
    "v_exp_f32 v243, v71\n"
    "v_mfma_f32_16x16x32_bf16 a[40:43], v[188:191], v[44:47], a[40:43]\n"
    "v_mul_f32 v31, s41, v31\n"
    "v_mul_f32 v255, s41, v255\n"
    "v_mfma_f32_16x16x32_bf16 a[44:47], a[236:239], v[40:43], a[44:47]\n"
    "v_cvt_pk_bf16_f32 v232, v31, v255\n"
    "v_lshlrev_b32 v31, 16, v221\n"
    "v_mfma_f32_16x16x32_bf16 a[44:47], v[192:195], v[44:47], a[44:47]\n"
    "v_and_b32 v255, s42, v221\n"
    "v_mul_f32 v31, s41, v31\n"
    "v_mfma_f32_16x16x32_bf16 a[48:51], a[240:243], v[40:43], a[48:51]\n"
    "s_add_u32 s34, s36, s35\n"
    "v_mul_f32 v255, s41, v255\n"
    "v_cvt_pk_bf16_f32 v233, v31, v255\n"
    "v_mfma_f32_16x16x32_bf16 a[48:51], v[196:199], v[44:47], a[48:51]\n"
    "v_lshlrev_b32 v31, 16, v222\n"
    "v_and_b32 v255, s42, v222\n"
    "v_mfma_f32_16x16x32_bf16 a[52:55], a[244:247], v[40:43], a[52:55]\n"
    "s_add_u32 s36, s36, 33792\n"
    "v_mul_f32 v31, s41, v31\n"
    "v_mul_f32 v255, s41, v255\n"
    "v_mfma_f32_16x16x32_bf16 a[52:55], v[200:203], v[44:47], a[52:55]\n"
    "v_cvt_pk_bf16_f32 v234, v31, v255\n"
    "v_lshlrev_b32 v31, 16, v223\n"
    "v_mfma_f32_16x16x32_bf16 a[56:59], a[248:251], v[40:43], a[56:59]\n"
    "s_sub_u32 s50, s36, 101376\n"
    "v_and_b32 v255, s42, v223\n"
    "v_mul_f32 v31, s41, v31\n"
    "v_mfma_f32_16x16x32_bf16 a[56:59], v[204:207], v[44:47], a[56:59]\n"
    "v_mul_f32 v255, s41, v255\n"
    "v_cvt_pk_bf16_f32 v235, v31, v255\n"
    "v_mfma_f32_16x16x32_bf16 a[60:63], a[252:255], v[40:43], a[60:63]\n"
    "s_min_u32 s36, s36, s50\n"
    "v_cvt_pk_bf16_f32 v40, v8, v9\n"
    "v_cvt_pk_bf16_f32 v41, v10, v11\n"
    "v_mfma_f32_16x16x32_bf16 a[60:63], v[208:211], v[44:47], a[60:63]\n"
    "s_add_u32 s32, s32, 16384\n"
    "v_exp_f32 v8, v72\n"
    "v_mfma_f32_16x16x32_bf16 v[48:51], a[160:163], v[164:167], v[96:99]\n"
    "v_add_u32 v4, s36, v2\n"
    "v_mfma_f32_16x16x32_bf16 v[48:51], a[164:167], v[168:171], v[48:51]\n"
    "s_waitcnt vmcnt(10)\n"
    "s_barrier\n"
    "v_mfma_f32_16x16x32_bf16 v[48:51], a[168:171], v[172:175], v[48:51]\n"
    "v_exp_f32 v9, v73\n"
    "v_mfma_f32_16x16x32_bf16 v[48:51], a[172:175], v[176:179], v[48:51]\n"
    "v_exp_f32 v10, v74\n"
    "v_mfma_f32_16x16x32_bf16 v[52:55], a[176:179], v[164:167], v[96:99]\n"
    "ds_read_b128 a[160:163], v4 offset:0\n"
    "ds_read_b128 a[164:167], v4 offset:64\n"
    "v_mfma_f32_16x16x32_bf16 v[52:55], a[180:183], v[168:171], v[52:55]\n"
    "v_exp_f32 v11, v75\n"
    "v_mfma_f32_16x16x32_bf16 v[52:55], a[184:187], v[172:175], v[52:55]\n"
    "v_cvt_pk_bf16_f32 v42, v240, v241\n"
    "v_cvt_pk_bf16_f32 v43, v242, v243\n"
    "v_mfma_f32_16x16x32_bf16 v[52:55], a[188:191], v[176:179], v[52:55]\n"
    "ds_read_b128 a[168:171], v4 offset:128\n"
    "ds_read_b128 a[172:175], v4 offset:192\n"
    "v_mfma_f32_16x16x32_bf16 v[56:59], a[192:195], v[164:167], v[96:99]\n"
    "v_exp_f32 v240, v76\n"
    "v_mfma_f32_16x16x32_bf16 v[56:59], a[196:199], v[168:171], v[56:59]\n"
    "v_exp_f32 v241, v77\n"
    "v_mfma_f32_16x16x32_bf16 v[56:59], a[200:203], v[172:175], v[56:59]\n"
    "ds_read_b128 a[176:179], v4 offset:256\n"
    "ds_read_b128 a[180:183], v4 offset:320\n"
    "v_mfma_f32_16x16x32_bf16 v[56:59], a[204:207], v[176:179], v[56:59]\n"
    "v_exp_f32 v242, v78\n"
    "v_mfma_f32_16x16x32_bf16 v[60:63], a[208:211], v[164:167], v[96:99]\n"
    "v_exp_f32 v243, v79\n"
    "v_mfma_f32_16x16x32_bf16 v[60:63], a[212:215], v[168:171], v[60:63]\n"
    "ds_read_b128 a[184:187], v4 offset:384\n"
    "ds_read_b128 a[188:191], v4 offset:448\n"
    "v_mfma_f32_16x16x32_bf16 v[60:63], a[216:219], v[172:175], v[60:63]\n"
    "v_cvt_pk_bf16_f32 v44, v8, v9\n"
    "v_cvt_pk_bf16_f32 v45, v10, v11\n"
    "v_mfma_f32_16x16x32_bf16 v[60:63], a[220:223], v[176:179], v[60:63]\n"
    "v_cvt_pk_bf16_f32 v46, v240, v241\n"
    "v_cvt_pk_bf16_f32 v47, v242, v243\n"
    "v_mfma_f32_16x16x32_bf16 v[12:15], v[16:19], v[40:43], 0\n"
    "ds_read_b128 a[192:195], v4 offset:512\n"
    "ds_read_b128 a[196:199], v4 offset:576\n"
    "v_mfma_f32_16x16x32_bf16 v[12:15], v[16:19], v[44:47], v[12:15]\n"
    "v_exp_f32 v8, v48\n"
    "v_mfma_f32_16x16x32_bf16 a[64:67], a[224:227], v[32:35], a[64:67]\n"
    "ds_read_b128 a[200:203], v4 offset:640\n"
    "ds_read_b128 a[204:207], v4 offset:704\n"
    "v_mfma_f32_16x16x32_bf16 a[64:67], v[180:183], v[36:39], a[64:67]\n"
    "v_exp_f32 v9, v49\n"
    "v_mfma_f32_16x16x32_bf16 a[68:71], a[228:231], v[32:35], a[68:71]\n"
    "ds_read_b128 a[208:211], v4 offset:768\n"
    "ds_read_b128 a[212:215], v4 offset:832\n"
    "v_mfma_f32_16x16x32_bf16 a[68:71], v[184:187], v[36:39], a[68:71]\n"
    "v_exp_f32 v10, v50\n"
    "v_mfma_f32_16x16x32_bf16 a[72:75], a[232:235], v[32:35], a[72:75]\n"
    "v_cmp_nge_f32 vcc, s40, v12\n"
    "v_mfma_f32_16x16x32_bf16 a[72:75], v[188:191], v[36:39], a[72:75]\n"
    "ds_read_b128 a[216:219], v4 offset:896\n"
    "ds_read_b128 a[220:223], v4 offset:960\n"
    "v_mfma_f32_16x16x32_bf16 a[76:79], a[236:239], v[32:35], a[76:79]\n"
    "v_exp_f32 v11, v51\n"
    "v_mfma_f32_16x16x32_bf16 a[76:79], v[192:195], v[36:39], a[76:79]\n"
    "s_cbranch_vccnz LTR_8\n"
    "LRET_8:\n"
    "v_add_f32 v23, v23, v12\n"
    "v_mfma_f32_16x16x32_bf16 a[80:83], a[240:243], v[32:35], a[80:83]\n"
    "v_exp_f32 v240, v52\n"
    "v_mfma_f32_16x16x32_bf16 a[80:83], v[196:199], v[36:39], a[80:83]\n"
    "v_exp_f32 v241, v53\n"
    "v_mfma_f32_16x16x32_bf16 a[84:87], a[244:247], v[32:35], a[84:87]\n"
    "v_exp_f32 v242, v54\n"
    "v_mfma_f32_16x16x32_bf16 a[84:87], v[200:203], v[36:39], a[84:87]\n"
    "v_exp_f32 v243, v55\n"
    "v_mfma_f32_16x16x32_bf16 a[88:91], a[248:251], v[32:35], a[88:91]\n"
    "buffer_load_dwordx4 v[220:223], v6, s[16:19], s53 offen offset:128 \n"
    "v_mfma_f32_16x16x32_bf16 a[88:91], v[204:207], v[36:39], a[88:91]\n"
    "v_mfma_f32_16x16x32_bf16 a[92:95], a[252:255], v[32:35], a[92:95]\n"
    "v_cvt_pk_bf16_f32 v32, v8, v9\n"
    "v_cvt_pk_bf16_f32 v33, v10, v11\n"
    "v_mfma_f32_16x16x32_bf16 a[92:95], v[208:211], v[36:39], a[92:95]\n"
    "s_waitcnt lgkmcnt(8)\n"
    "v_exp_f32 v8, v56\n"
    "v_mfma_f32_16x16x32_bf16 v[64:67], a[160:163], v[100:103], v[80:83]\n"
    "ds_write_b128 v228, v[232:235] offset:2048\n"
    "v_exp_f32 v9, v57\n"
    "v_mfma_f32_16x16x32_bf16 v[64:67], a[164:167], v[104:107], v[64:67]\n"
    "v_exp_f32 v10, v58\n"
    "v_mfma_f32_16x16x32_bf16 v[64:67], a[168:171], v[108:111], v[64:67]\n"
    "s_add_u32 m0, s34, 0\n"
    "s_waitcnt vmcnt(17)\n"
    "v_lshlrev_b32 v31, 16, v224\n"
    "v_and_b32 v255, s42, v224\n"
    "v_mfma_f32_16x16x32_bf16 v[64:67], a[172:175], v[112:115], v[64:67]\n"
    "buffer_load_dwordx4 v1, s[20:23], s32 offen offset:0  lds\n"
    "v_mfma_f32_16x16x32_bf16 v[68:71], a[176:179], v[100:103], v[80:83]\n"
    "v_exp_f32 v11, v59\n"
    "v_mfma_f32_16x16x32_bf16 v[68:71], a[180:183], v[104:107], v[68:71]\n"
    "v_mul_f32 v31, s41, v31\n"
    "v_mul_f32 v255, s41, v255\n"
    "v_mfma_f32_16x16x32_bf16 v[68:71], a[184:187], v[108:111], v[68:71]\n"
    "v_cvt_pk_bf16_f32 v34, v240, v241\n"
    "v_cvt_pk_bf16_f32 v35, v242, v243\n"
    "v_mfma_f32_16x16x32_bf16 v[68:71], a[188:191], v[112:115], v[68:71]\n"
    "s_waitcnt lgkmcnt(1)\n"
    "v_cvt_pk_bf16_f32 v236, v31, v255\n"
    "v_lshlrev_b32 v31, 16, v225\n"
    "v_mfma_f32_16x16x32_bf16 v[72:75], a[192:195], v[100:103], v[80:83]\n"
    "v_exp_f32 v240, v60\n"
    "v_mfma_f32_16x16x32_bf16 v[72:75], a[196:199], v[104:107], v[72:75]\n"
    "v_and_b32 v255, s42, v225\n"
    "v_mul_f32 v31, s41, v31\n"
    "v_mfma_f32_16x16x32_bf16 v[72:75], a[200:203], v[108:111], v[72:75]\n"
    "v_exp_f32 v241, v61\n"
    "v_mfma_f32_16x16x32_bf16 v[72:75], a[204:207], v[112:115], v[72:75]\n"
    "v_mul_f32 v255, s41, v255\n"
    "v_cvt_pk_bf16_f32 v237, v31, v255\n"
    "v_mfma_f32_16x16x32_bf16 v[76:79], a[208:211], v[100:103], v[80:83]\n"
    "v_exp_f32 v242, v62\n"
    "v_mfma_f32_16x16x32_bf16 v[76:79], a[212:215], v[104:107], v[76:79]\n"
    "v_lshlrev_b32 v31, 16, v226\n"
    "v_and_b32 v255, s42, v226\n"
    "v_mfma_f32_16x16x32_bf16 v[76:79], a[216:219], v[108:111], v[76:79]\n"
    "v_exp_f32 v243, v63\n"
    "v_mfma_f32_16x16x32_bf16 v[76:79], a[220:223], v[112:115], v[76:79]\n"
    "v_mul_f32 v31, s41, v31\n"
    "v_mul_f32 v255, s41, v255\n"
    "v_mfma_f32_16x16x32_bf16 a[96:99], a[224:227], v[40:43], a[96:99]\n"
    "v_cvt_pk_bf16_f32 v36, v8, v9\n"
    "v_cvt_pk_bf16_f32 v37, v10, v11\n"
    "v_mfma_f32_16x16x32_bf16 a[96:99], v[180:183], v[44:47], a[96:99]\n"
    "v_cvt_pk_bf16_f32 v238, v31, v255\n"
    "v_lshlrev_b32 v31, 16, v227\n"
    "v_mfma_f32_16x16x32_bf16 a[100:103], a[228:231], v[40:43], a[100:103]\n"
    "v_cvt_pk_bf16_f32 v38, v240, v241\n"
    "v_cvt_pk_bf16_f32 v39, v242, v243\n"
    "v_mfma_f32_16x16x32_bf16 a[100:103], v[184:187], v[44:47], a[100:103]\n"
    "v_and_b32 v255, s42, v227\n"
    "v_mul_f32 v31, s41, v31\n"
    "v_mfma_f32_16x16x32_bf16 v[12:15], v[16:19], v[32:35], 0\n"
    "v_exp_f32 v8, v64\n"
    "v_mfma_f32_16x16x32_bf16 v[12:15], v[16:19], v[36:39], v[12:15]\n"
    "v_mul_f32 v255, s41, v255\n"
    "v_cvt_pk_bf16_f32 v239, v31, v255\n"
    "v_mfma_f32_16x16x32_bf16 a[104:107], a[232:235], v[40:43], a[104:107]\n"
    "v_exp_f32 v9, v65\n"
    "v_mfma_f32_16x16x32_bf16 a[104:107], v[188:191], v[44:47], a[104:107]\n"
    "buffer_load_dwordx4 v[224:227], v6, s[16:19], s53 offen offset:192 \n"
    "v_mfma_f32_16x16x32_bf16 a[108:111], a[236:239], v[40:43], a[108:111]\n"
    "v_exp_f32 v10, v66\n"
    "v_mfma_f32_16x16x32_bf16 a[108:111], v[192:195], v[44:47], a[108:111]\n"
    "v_exp_f32 v11, v67\n"
    "v_mfma_f32_16x16x32_bf16 a[112:115], a[240:243], v[40:43], a[112:115]\n"
    "v_cmp_nge_f32 vcc, s40, v12\n"
    "v_mfma_f32_16x16x32_bf16 a[112:115], v[196:199], v[44:47], a[112:115]\n"
    "ds_write_b128 v228, v[236:239] offset:3072\n"
    "v_mfma_f32_16x16x32_bf16 a[116:119], a[244:247], v[40:43], a[116:119]\n"
    "s_add_u32 m0, s34, 32\n"
    "v_exp_f32 v240, v68\n"
    "v_mfma_f32_16x16x32_bf16 a[116:119], v[200:203], v[44:47], a[116:119]\n"
    "buffer_load_dwordx4 v1, s[20:23], s32 offen offset:1024  lds\n"
    "v_mfma_f32_16x16x32_bf16 a[120:123], a[248:251], v[40:43], a[120:123]\n"
    "v_exp_f32 v241, v69\n"
    "v_mfma_f32_16x16x32_bf16 a[120:123], v[204:207], v[44:47], a[120:123]\n"
    "v_add_u32 v228, 4096, v228\n"
    "v_mfma_f32_16x16x32_bf16 a[124:127], a[252:255], v[40:43], a[124:127]\n"
    "v_exp_f32 v242, v70\n"
    "v_mfma_f32_16x16x32_bf16 a[124:127], v[208:211], v[44:47], a[124:127]\n"
    "s_cbranch_vccnz LTR_9\n"
    "LRET_9:\n"
    "v_add_f32 v24, v24, v12\n"
    "s_sub_u32 s37, s37, 1\n"
    "s_cmp_lg_u32 s37, 0\n"
    "s_cbranch_scc1 LBG5\n"
    "s_mov_b32 s37, 3\n"
    "s_branch LLOOP5\n"
    "LWARM5:\n"
    "s_waitcnt lgkmcnt(8)\n"
    "v_mfma_f32_16x16x32_bf16 v[64:67], a[160:163], v[116:119], 0\n"
    "v_mov_b32 v26, v25\n"
    "s_nop 1\n"
    "v_permlane16_swap_b32 v25, v26\n"
    "v_max_f32 v25, v25, v26\n"
    "v_mov_b32 v26, v25\n"
    "s_nop 1\n"
    "v_permlane32_swap_b32 v25, v26\n"
    "v_max_f32 v25, v25, v26\n"
    "v_sub_f32 v80, 0, v25\n"
    "v_sub_f32 v81, 0, v25\n"
    "v_sub_f32 v82, 0, v25\n"
    "v_sub_f32 v83, 0, v25\n"
    "v_sub_f32 v27, v48, v25\n"
    "v_sub_f32 v28, v49, v25\n"
    "v_sub_f32 v29, v50, v25\n"
    "v_sub_f32 v30, v51, v25\n"
    "v_exp_f32 v8, v27\n"
    "v_exp_f32 v9, v28\n"
    "v_exp_f32 v10, v29\n"
    "v_exp_f32 v11, v30\n"
    "v_cvt_pk_bf16_f32 v32, v8, v9\n"
    "v_cvt_pk_bf16_f32 v33, v10, v11\n"
    "v_sub_f32 v27, v52, v25\n"
    "v_sub_f32 v28, v53, v25\n"
    "v_sub_f32 v29, v54, v25\n"
    "v_sub_f32 v30, v55, v25\n"
    "v_exp_f32 v240, v27\n"
    "v_exp_f32 v241, v28\n"
    "v_exp_f32 v242, v29\n"
    "v_exp_f32 v243, v30\n"
    "v_cvt_pk_bf16_f32 v34, v240, v241\n"
    "v_cvt_pk_bf16_f32 v35, v242, v243\n"
    "v_sub_f32 v27, v56, v25\n"
    "v_sub_f32 v28, v57, v25\n"
    "v_sub_f32 v29, v58, v25\n"
    "v_sub_f32 v30, v59, v25\n"
    "v_exp_f32 v8, v27\n"
    "v_exp_f32 v9, v28\n"
    "v_exp_f32 v10, v29\n"
    "v_exp_f32 v11, v30\n"
    "v_cvt_pk_bf16_f32 v36, v8, v9\n"
    "v_cvt_pk_bf16_f32 v37, v10, v11\n"
    "v_sub_f32 v27, v60, v25\n"
    "v_sub_f32 v28, v61, v25\n"
    "v_sub_f32 v29, v62, v25\n"
    "v_sub_f32 v30, v63, v25\n"
    "v_exp_f32 v240, v27\n"
    "v_exp_f32 v241, v28\n"
    "v_exp_f32 v242, v29\n"
    "v_exp_f32 v243, v30\n"
    "v_cvt_pk_bf16_f32 v38, v240, v241\n"
    "v_cvt_pk_bf16_f32 v39, v242, v243\n"
    "v_mfma_f32_16x16x32_bf16 v[64:67], a[164:167], v[120:123], v[64:67]\n"
    "v_add_u32 v5, s36, v3\n"
    "v_mfma_f32_16x16x32_bf16 v[64:67], a[168:171], v[124:127], v[64:67]\n"
    "v_rcp_f32 v254, v23\n"
    "v_accvgpr_read_b32 v244, a96\n"
    "v_accvgpr_read_b32 v245, a97\n"
    "v_mul_f32 v244, v254, v244\n"
    "v_mul_f32 v245, v254, v245\n"
    "v_cvt_pk_bf16_f32 v246, v244, v245\n"
    "v_accvgpr_read_b32 v244, a98\n"
    "v_accvgpr_read_b32 v245, a99\n"
    "v_mul_f32 v244, v254, v244\n"
    "v_mul_f32 v245, v254, v245\n"
    "v_cvt_pk_bf16_f32 v247, v244, v245\n"
    "v_xor_b32 v31, 0, v231\n"
    "ds_write_b64 v31, v[246:247] offset:12288\n"
    "v_accvgpr_read_b32 v244, a100\n"
    "v_accvgpr_read_b32 v245, a101\n"
    "v_mul_f32 v244, v254, v244\n"
    "v_mul_f32 v245, v254, v245\n"
    "v_cvt_pk_bf16_f32 v248, v244, v245\n"
    "v_accvgpr_read_b32 v244, a102\n"
    "v_accvgpr_read_b32 v245, a103\n"
    "v_mul_f32 v244, v254, v244\n"
    "v_mul_f32 v245, v254, v245\n"
    "v_cvt_pk_bf16_f32 v249, v244, v245\n"
    "v_xor_b32 v255, 32, v231\n"
    "ds_write_b64 v255, v[248:249] offset:12288\n"
    "v_accvgpr_read_b32 v244, a104\n"
    "v_accvgpr_read_b32 v245, a105\n"
    "v_mul_f32 v244, v254, v244\n"
    "v_mul_f32 v245, v254, v245\n"
    "v_cvt_pk_bf16_f32 v250, v244, v245\n"
    "v_accvgpr_read_b32 v244, a106\n"
    "v_accvgpr_read_b32 v245, a107\n"
    "v_mul_f32 v244, v254, v244\n"
    "v_mul_f32 v245, v254, v245\n"
    "v_cvt_pk_bf16_f32 v251, v244, v245\n"
    "v_xor_b32 v31, 64, v231\n"
    "ds_write_b64 v31, v[250:251] offset:12288\n"
    "v_accvgpr_read_b32 v244, a108\n"
    "v_accvgpr_read_b32 v245, a109\n"
    "v_mul_f32 v244, v254, v244\n"
    "v_mfma_f32_16x16x32_bf16 v[64:67], a[172:175], v[128:131], v[64:67]\n"
    "v_mul_f32 v245, v254, v245\n"
    "v_cvt_pk_bf16_f32 v252, v244, v245\n"
    "v_accvgpr_read_b32 v244, a110\n"
    "v_accvgpr_read_b32 v245, a111\n"
    "v_mul_f32 v244, v254, v244\n"
    "v_mul_f32 v245, v254, v245\n"
    "v_cvt_pk_bf16_f32 v253, v244, v245\n"
    "v_xor_b32 v255, 96, v231\n"
    "v_mfma_f32_16x16x32_bf16 v[12:15], v[16:19], v[32:35], 0\n"
    "ds_write_b64 v255, v[252:253] offset:12288\n"
    "v_accvgpr_read_b32 v244, a112\n"
    "v_accvgpr_read_b32 v245, a113\n"
    "v_mul_f32 v244, v254, v244\n"
    "v_mul_f32 v245, v254, v245\n"
    "v_cvt_pk_bf16_f32 v246, v244, v245\n"
    "v_accvgpr_read_b32 v244, a114\n"
    "v_accvgpr_read_b32 v245, a115\n"
    "v_mfma_f32_16x16x32_bf16 v[12:15], v[16:19], v[36:39], v[12:15]\n"
    "v_mul_f32 v244, v254, v244\n"
    "v_mul_f32 v245, v254, v245\n"
    "v_cvt_pk_bf16_f32 v247, v244, v245\n"
    "v_xor_b32 v31, 128, v231\n"
    "ds_write_b64 v31, v[246:247] offset:12288\n"
    "v_accvgpr_read_b32 v244, a116\n"
    "v_accvgpr_read_b32 v245, a117\n"
    "v_mul_f32 v244, v254, v244\n"
    "v_mfma_f32_16x16x32_bf16 v[68:71], a[176:179], v[116:119], 0\n"
    "v_mul_f32 v245, v254, v245\n"
    "v_cvt_pk_bf16_f32 v248, v244, v245\n"
    "v_accvgpr_read_b32 v244, a118\n"
    "v_accvgpr_read_b32 v245, a119\n"
    "v_mul_f32 v244, v254, v244\n"
    "v_mul_f32 v245, v254, v245\n"
    "v_cvt_pk_bf16_f32 v249, v244, v245\n"
    "v_xor_b32 v255, 160, v231\n"
    "v_mfma_f32_16x16x32_bf16 v[68:71], a[180:183], v[120:123], v[68:71]\n"
    "ds_write_b64 v255, v[248:249] offset:12288\n"
    "v_accvgpr_read_b32 v244, a120\n"
    "v_accvgpr_read_b32 v245, a121\n"
    "v_mul_f32 v244, v254, v244\n"
    "v_mul_f32 v245, v254, v245\n"
    "v_cvt_pk_bf16_f32 v250, v244, v245\n"
    "v_accvgpr_read_b32 v244, a122\n"
    "v_accvgpr_read_b32 v245, a123\n"
    "v_mfma_f32_16x16x32_bf16 v[68:71], a[184:187], v[124:127], v[68:71]\n"
    "v_mul_f32 v244, v254, v244\n"
    "v_mul_f32 v245, v254, v245\n"
    "v_cvt_pk_bf16_f32 v251, v244, v245\n"
    "v_xor_b32 v31, 192, v231\n"
    "ds_write_b64 v31, v[250:251] offset:12288\n"
    "v_accvgpr_read_b32 v244, a124\n"
    "v_accvgpr_read_b32 v245, a125\n"
    "v_mul_f32 v244, v254, v244\n"
    "v_mfma_f32_16x16x32_bf16 v[68:71], a[188:191], v[128:131], v[68:71]\n"
    "v_mul_f32 v245, v254, v245\n"
    "v_cvt_pk_bf16_f32 v252, v244, v245\n"
    "v_accvgpr_read_b32 v244, a126\n"
    "v_accvgpr_read_b32 v245, a127\n"
    "v_mul_f32 v244, v254, v244\n"
    "v_mul_f32 v245, v254, v245\n"
    "v_cvt_pk_bf16_f32 v253, v244, v245\n"
    "v_xor_b32 v255, 224, v231\n"
    "v_mfma_f32_16x16x32_bf16 v[72:75], a[192:195], v[116:119], 0\n"
    "ds_write_b64 v255, v[252:253] offset:12288\n"
    "v_mov_b32 v20, v12\n"
    "v_mfma_f32_16x16x32_bf16 v[72:75], a[196:199], v[120:123], v[72:75]\n"
    "v_mfma_f32_16x16x32_bf16 v[72:75], a[200:203], v[124:127], v[72:75]\n"
    "v_mfma_f32_16x16x32_bf16 v[72:75], a[204:207], v[128:131], v[72:75]\n"
    "v_mfma_f32_16x16x32_bf16 v[76:79], a[208:211], v[116:119], 0\n"
    "v_max3_f32 v25, v64, v65, v66\n"
    "v_max3_f32 v26, v67, v68, v69\n"
    "v_mfma_f32_16x16x32_bf16 v[76:79], a[212:215], v[120:123], v[76:79]\n"
    "v_mfma_f32_16x16x32_bf16 v[76:79], a[216:219], v[124:127], v[76:79]\n"
    "v_mfma_f32_16x16x32_bf16 v[76:79], a[220:223], v[128:131], v[76:79]\n"
    "v_mfma_f32_16x16x32_bf16 a[128:131], a[224:227], v[40:43], a[128:131]\n"
    "ds_read_b64_tr_b16 a[224:225], v5 offset:0\n"
    "ds_read_b64_tr_b16 a[226:227], v5 offset:256\n"
    "v_max3_f32 v25, v25, v70, v71\n"
    "v_max3_f32 v26, v26, v72, v73\n"
    "v_mfma_f32_16x16x32_bf16 a[128:131], v[180:183], v[44:47], a[128:131]\n"
    "v_mfma_f32_16x16x32_bf16 a[132:135], a[228:231], v[40:43], a[132:135]\n"
    "ds_read_b64_tr_b16 v[180:181], v5 offset:512\n"
    "ds_read_b64_tr_b16 v[182:183], v5 offset:768\n"
    "v_mfma_f32_16x16x32_bf16 a[132:135], v[184:187], v[44:47], a[132:135]\n"
    "v_mfma_f32_16x16x32_bf16 a[136:139], a[232:235], v[40:43], a[136:139]\n"
    "ds_read_b64_tr_b16 a[228:229], v5 offset:32\n"
    "ds_read_b64_tr_b16 a[230:231], v5 offset:288\n"
    "v_max3_f32 v25, v25, v74, v75\n"
    "v_max3_f32 v26, v26, v76, v77\n"
    "v_max3_f32 v25, v25, v78, v79\n"
    "v_max_f32 v25, v25, v26\n"
    "v_mov_b32 v26, v25\n"
    "s_nop 1\n"
    "v_permlane16_swap_b32 v25, v26\n"
    "v_max_f32 v25, v25, v26\n"
    "v_mov_b32 v26, v25\n"
    "s_nop 1\n"
    "v_permlane32_swap_b32 v25, v26\n"
    "v_max_f32 v25, v25, v26\n"
    "v_sub_f32 v84, 0, v25\n"
    "v_sub_f32 v85, 0, v25\n"
    "v_sub_f32 v86, 0, v25\n"
    "v_sub_f32 v87, 0, v25\n"
    "v_sub_f32 v27, v64, v25\n"
    "v_sub_f32 v28, v65, v25\n"
    "v_sub_f32 v29, v66, v25\n"
    "v_sub_f32 v30, v67, v25\n"
    "v_exp_f32 v8, v27\n"
    "v_exp_f32 v9, v28\n"
    "v_exp_f32 v10, v29\n"
    "v_exp_f32 v11, v30\n"
    "v_mfma_f32_16x16x32_bf16 a[136:139], v[188:191], v[44:47], a[136:139]\n"
    "v_mfma_f32_16x16x32_bf16 a[140:143], a[236:239], v[40:43], a[140:143]\n"
    "ds_read_b64_tr_b16 v[184:185], v5 offset:544\n"
    "ds_read_b64_tr_b16 v[186:187], v5 offset:800\n"
    "v_mfma_f32_16x16x32_bf16 a[140:143], v[192:195], v[44:47], a[140:143]\n"
    "v_mfma_f32_16x16x32_bf16 a[144:147], a[240:243], v[40:43], a[144:147]\n"
    "ds_read_b64_tr_b16 a[232:233], v5 offset:64\n"
    "ds_read_b64_tr_b16 a[234:235], v5 offset:320\n"
    "v_mfma_f32_16x16x32_bf16 a[144:147], v[196:199], v[44:47], a[144:147]\n"
    "v_mfma_f32_16x16x32_bf16 a[148:151], a[244:247], v[40:43], a[148:151]\n"
    "ds_read_b64_tr_b16 v[188:189], v5 offset:576\n"
    "ds_read_b64_tr_b16 v[190:191], v5 offset:832\n"
    "v_mfma_f32_16x16x32_bf16 a[148:151], v[200:203], v[44:47], a[148:151]\n"
    "v_mfma_f32_16x16x32_bf16 a[152:155], a[248:251], v[40:43], a[152:155]\n"
    "ds_read_b64_tr_b16 a[236:237], v5 offset:96\n"
    "ds_read_b64_tr_b16 a[238:239], v5 offset:352\n"
    "v_mfma_f32_16x16x32_bf16 a[152:155], v[204:207], v[44:47], a[152:155]\n"
    "v_mfma_f32_16x16x32_bf16 a[156:159], a[252:255], v[40:43], a[156:159]\n"
    "ds_read_b64_tr_b16 v[192:193], v5 offset:608\n"
    "ds_read_b64_tr_b16 v[194:195], v5 offset:864\n"
    "v_cvt_pk_bf16_f32 v40, v8, v9\n"
    "v_cvt_pk_bf16_f32 v41, v10, v11\n"
    "v_sub_f32 v27, v68, v25\n"
    "v_sub_f32 v28, v69, v25\n"
    "v_sub_f32 v29, v70, v25\n"
    "v_sub_f32 v30, v71, v25\n"
    "v_exp_f32 v240, v27\n"
    "v_exp_f32 v241, v28\n"
    "v_exp_f32 v242, v29\n"
    "v_exp_f32 v243, v30\n"
    "v_cvt_pk_bf16_f32 v42, v240, v241\n"
    "v_cvt_pk_bf16_f32 v43, v242, v243\n"
    "v_sub_f32 v27, v72, v25\n"
    "v_sub_f32 v28, v73, v25\n"
    "v_sub_f32 v29, v74, v25\n"
    "v_sub_f32 v30, v75, v25\n"
    "v_exp_f32 v8, v27\n"
    "v_exp_f32 v9, v28\n"
    "v_exp_f32 v10, v29\n"
    "v_exp_f32 v11, v30\n"
    "v_mfma_f32_16x16x32_bf16 a[156:159], v[208:211], v[44:47], a[156:159]\n"
    "v_cvt_pk_bf16_f32 v44, v8, v9\n"
    "v_cvt_pk_bf16_f32 v45, v10, v11\n"
    "v_sub_f32 v27, v76, v25\n"
    "v_sub_f32 v28, v77, v25\n"
    "v_sub_f32 v29, v78, v25\n"
    "v_sub_f32 v30, v79, v25\n"
    "v_exp_f32 v240, v27\n"
    "v_exp_f32 v241, v28\n"
    "v_exp_f32 v242, v29\n"
    "v_exp_f32 v243, v30\n"
    "v_cvt_pk_bf16_f32 v46, v240, v241\n"
    "v_cvt_pk_bf16_f32 v47, v242, v243\n"
    "v_mfma_f32_16x16x32_bf16 v[12:15], v[16:19], v[40:43], 0\n"
    "ds_read_b64_tr_b16 a[240:241], v5 offset:128\n"
    "ds_read_b64_tr_b16 a[242:243], v5 offset:384\n"
    "v_mfma_f32_16x16x32_bf16 v[12:15], v[16:19], v[44:47], v[12:15]\n"
    "v_mfma_f32_16x16x32_bf16 v[48:51], a[160:163], v[132:135], 0\n"
    "s_add_u32 m0, s34, 64\n"
    "ds_read_b64_tr_b16 v[196:197], v5 offset:640\n"
    "ds_read_b64_tr_b16 v[198:199], v5 offset:896\n"
    "v_mfma_f32_16x16x32_bf16 v[48:51], a[164:167], v[136:139], v[48:51]\n"
    "buffer_load_dwordx4 v1, s[20:23], s32 offen offset:2048  lds\n"
    "v_mfma_f32_16x16x32_bf16 v[48:51], a[168:171], v[140:143], v[48:51]\n"
    "v_rcp_f32 v254, v24\n"
    "s_add_u32 s49, s45, 16384\n"
    "v_accvgpr_read_b32 v244, a128\n"
    "v_accvgpr_read_b32 v245, a129\n"
    "v_mul_f32 v244, v254, v244\n"
    "v_mul_f32 v245, v254, v245\n"
    "v_cvt_pk_bf16_f32 v246, v244, v245\n"
    "v_accvgpr_read_b32 v244, a130\n"
    "v_accvgpr_read_b32 v245, a131\n"
    "v_mul_f32 v244, v254, v244\n"
    "v_mul_f32 v245, v254, v245\n"
    "v_cvt_pk_bf16_f32 v247, v244, v245\n"
    "v_accvgpr_read_b32 v244, a132\n"
    "v_accvgpr_read_b32 v245, a133\n"
    "v_mul_f32 v244, v254, v244\n"
    "v_mul_f32 v245, v254, v245\n"
    "v_cvt_pk_bf16_f32 v248, v244, v245\n"
    "v_accvgpr_read_b32 v244, a134\n"
    "v_accvgpr_read_b32 v245, a135\n"
    "v_mul_f32 v244, v254, v244\n"
    "v_mul_f32 v245, v254, v245\n"
    "v_cvt_pk_bf16_f32 v249, v244, v245\n"
    "v_permlane16_swap_b32 v246, v248\n"
    "s_nop 0\n"
    "v_permlane16_swap_b32 v247, v249\n"
    "v_accvgpr_read_b32 v244, a136\n"
    "v_accvgpr_read_b32 v245, a137\n"
    "v_mul_f32 v244, v254, v244\n"
    "v_mul_f32 v245, v254, v245\n"
    "v_cvt_pk_bf16_f32 v250, v244, v245\n"
    "v_accvgpr_read_b32 v244, a138\n"
    "v_accvgpr_read_b32 v245, a139\n"
    "v_mul_f32 v244, v254, v244\n"
    "v_mul_f32 v245, v254, v245\n"
    "v_cvt_pk_bf16_f32 v251, v244, v245\n"
    "v_accvgpr_read_b32 v244, a140\n"
    "v_accvgpr_read_b32 v245, a141\n"
    "v_mul_f32 v244, v254, v244\n"
    "v_mul_f32 v245, v254, v245\n"
    "v_cvt_pk_bf16_f32 v252, v244, v245\n"
    "v_accvgpr_read_b32 v244, a142\n"
    "v_accvgpr_read_b32 v245, a143\n"
    "v_mul_f32 v244, v254, v244\n"
    "v_mul_f32 v245, v254, v245\n"
    "v_cvt_pk_bf16_f32 v253, v244, v245\n"
    "v_permlane16_swap_b32 v250, v252\n"
    "s_nop 0\n"
    "v_permlane16_swap_b32 v251, v253\n"
    "v_mov_b32 v244, v246\n"
    "v_mov_b32_dpp v246, v250 row_ror:8 row_mask:0xf bank_mask:0xc\n"
    "v_mov_b32 v245, v247\n"
    "v_mov_b32_dpp v247, v251 row_ror:8 row_mask:0xf bank_mask:0xc\n"
    "v_mov_b32_dpp v250, v244 row_ror:8 row_mask:0xf bank_mask:0x3\n"
    "v_mov_b32_dpp v251, v245 row_ror:8 row_mask:0xf bank_mask:0x3\n"
    "v_mov_b32 v244, v248\n"
    "v_mov_b32_dpp v248, v252 row_ror:8 row_mask:0xf bank_mask:0xc\n"
    "v_mov_b32 v245, v249\n"
    "v_mov_b32_dpp v249, v253 row_ror:8 row_mask:0xf bank_mask:0xc\n"
    "v_mov_b32_dpp v252, v244 row_ror:8 row_mask:0xf bank_mask:0x3\n"
    "v_mov_b32_dpp v253, v245 row_ror:8 row_mask:0xf bank_mask:0x3\n"
    "buffer_store_dwordx4 v[246:249], v7, s[28:31], s49 offen offset:0 nt\n"
    "buffer_store_dwordx4 v[250:253], v7, s[28:31], s49 offen offset:2048 nt\n"
    "v_accvgpr_read_b32 v244, a144\n"
    "v_accvgpr_read_b32 v245, a145\n"
    "v_mul_f32 v244, v254, v244\n"
    "v_mul_f32 v245, v254, v245\n"
    "v_cvt_pk_bf16_f32 v246, v244, v245\n"
    "v_accvgpr_read_b32 v244, a146\n"
    "v_mfma_f32_16x16x32_bf16 v[48:51], a[172:175], v[144:147], v[48:51]\n"
    "ds_read_b64_tr_b16 a[244:245], v5 offset:160\n"
    "ds_read_b64_tr_b16 a[246:247], v5 offset:416\n"
    "v_accvgpr_read_b32 v245, a147\n"
    "v_mul_f32 v244, v254, v244\n"
    "v_mul_f32 v245, v254, v245\n"
    "v_cvt_pk_bf16_f32 v247, v244, v245\n"
    "v_mfma_f32_16x16x32_bf16 v[52:55], a[176:179], v[132:135], 0\n"
    "v_accvgpr_read_b32 v244, a148\n"
    "v_accvgpr_read_b32 v245, a149\n"
    "v_mul_f32 v244, v254, v244\n"
    "v_mul_f32 v245, v254, v245\n"
    "v_cvt_pk_bf16_f32 v248, v244, v245\n"
    "v_accvgpr_read_b32 v244, a150\n"
    "v_mfma_f32_16x16x32_bf16 v[52:55], a[180:183], v[136:139], v[52:55]\n"
    "ds_read_b64_tr_b16 v[200:201], v5 offset:672\n"
    "ds_read_b64_tr_b16 v[202:203], v5 offset:928\n"
    "v_accvgpr_read_b32 v245, a151\n"
    "v_mul_f32 v244, v254, v244\n"
    "v_mul_f32 v245, v254, v245\n"
    "v_cvt_pk_bf16_f32 v249, v244, v245\n"
    "v_mfma_f32_16x16x32_bf16 v[52:55], a[184:187], v[140:143], v[52:55]\n"
    "v_permlane16_swap_b32 v246, v248\n"
    "v_permlane16_swap_b32 v247, v249\n"
    "v_accvgpr_read_b32 v244, a152\n"
    "v_accvgpr_read_b32 v245, a153\n"
    "v_mfma_f32_16x16x32_bf16 v[52:55], a[188:191], v[144:147], v[52:55]\n"
    "ds_read_b64_tr_b16 a[248:249], v5 offset:192\n"
    "ds_read_b64_tr_b16 a[250:251], v5 offset:448\n"
    "v_mul_f32 v244, v254, v244\n"
    "v_mul_f32 v245, v254, v245\n"
    "v_cvt_pk_bf16_f32 v250, v244, v245\n"
    "v_accvgpr_read_b32 v244, a154\n"
    "v_mfma_f32_16x16x32_bf16 v[56:59], a[192:195], v[132:135], 0\n"
    "v_accvgpr_read_b32 v245, a155\n"
    "v_mul_f32 v244, v254, v244\n"
    "v_mul_f32 v245, v254, v245\n"
    "v_cvt_pk_bf16_f32 v251, v244, v245\n"
    "v_accvgpr_read_b32 v244, a156\n"
    "v_accvgpr_read_b32 v245, a157\n"
    "v_mfma_f32_16x16x32_bf16 v[56:59], a[196:199], v[136:139], v[56:59]\n"
    "s_add_u32 m0, s34, 96\n"
    "ds_read_b64_tr_b16 v[204:205], v5 offset:704\n"
    "ds_read_b64_tr_b16 v[206:207], v5 offset:960\n"
    "v_mul_f32 v244, v254, v244\n"
    "v_mul_f32 v245, v254, v245\n"
    "v_cvt_pk_bf16_f32 v252, v244, v245\n"
    "v_accvgpr_read_b32 v244, a158\n"
    "v_mfma_f32_16x16x32_bf16 v[56:59], a[200:203], v[140:143], v[56:59]\n"
    "buffer_load_dwordx4 v1, s[20:23], s32 offen offset:3072  lds\n"
    "v_accvgpr_read_b32 v245, a159\n"
    "v_mul_f32 v244, v254, v244\n"
    "v_mul_f32 v245, v254, v245\n"
    "v_cvt_pk_bf16_f32 v253, v244, v245\n"
    "v_mfma_f32_16x16x32_bf16 v[56:59], a[204:207], v[144:147], v[56:59]\n"
    "ds_read_b64_tr_b16 a[252:253], v5 offset:224\n"
    "ds_read_b64_tr_b16 a[254:255], v5 offset:480\n"
    "v_permlane16_swap_b32 v250, v252\n"
    "v_permlane16_swap_b32 v251, v253\n"
    "v_mfma_f32_16x16x32_bf16 v[60:63], a[208:211], v[132:135], 0\n"
    "v_max3_f32 v25, v48, v49, v50\n"
    "v_max3_f32 v26, v51, v52, v53\n"
    "v_mov_b32 v244, v246\n"
    "v_mov_b32_dpp v246, v250 row_ror:8 row_mask:0xf bank_mask:0xc\n"
    "v_mov_b32 v245, v247\n"
    "v_mov_b32_dpp v247, v251 row_ror:8 row_mask:0xf bank_mask:0xc\n"
    "v_mfma_f32_16x16x32_bf16 v[60:63], a[212:215], v[136:139], v[60:63]\n"
    "ds_read_b64_tr_b16 v[208:209], v5 offset:736\n"
    "ds_read_b64_tr_b16 v[210:211], v5 offset:992\n"
    "v_mov_b32_dpp v250, v244 row_ror:8 row_mask:0xf bank_mask:0x3\n"
    "v_mov_b32_dpp v251, v245 row_ror:8 row_mask:0xf bank_mask:0x3\n"
    "v_mov_b32 v244, v248\n"
    "v_mov_b32_dpp v248, v252 row_ror:8 row_mask:0xf bank_mask:0xc\n"
    "v_mfma_f32_16x16x32_bf16 v[60:63], a[216:219], v[140:143], v[60:63]\n"
    "v_mov_b32 v245, v249\n"
    "v_mov_b32_dpp v249, v253 row_ror:8 row_mask:0xf bank_mask:0xc\n"
    "v_mov_b32_dpp v252, v244 row_ror:8 row_mask:0xf bank_mask:0x3\n"
    "v_mov_b32_dpp v253, v245 row_ror:8 row_mask:0xf bank_mask:0x3\n"
    "buffer_store_dwordx4 v[246:249], v7, s[28:31], s49 offen offset:128 nt\n"
    "v_mfma_f32_16x16x32_bf16 v[60:63], a[220:223], v[144:147], v[60:63]\n"
    "s_waitcnt lgkmcnt(15)\n"
    "buffer_store_dwordx4 v[250:253], v7, s[28:31], s49 offen offset:2176 nt\n"
    "v_mov_b32 v21, v12\n"
    "v_mfma_f32_16x16x32_bf16 a[0:3], a[224:227], v[32:35], 0\n"
    "v_max3_f32 v25, v25, v54, v55\n"
    "v_max3_f32 v26, v26, v56, v57\n"
    "v_mfma_f32_16x16x32_bf16 a[0:3], v[180:183], v[36:39], a[0:3]\n"
    "v_mfma_f32_16x16x32_bf16 a[4:7], a[228:231], v[32:35], 0\n"
    "s_add_u32 m0, s34, 16896\n"
    "v_mfma_f32_16x16x32_bf16 a[4:7], v[184:187], v[36:39], a[4:7]\n"
    "buffer_load_dwordx4 v1, s[24:27], s32 offen offset:0  lds\n"
    "v_mfma_f32_16x16x32_bf16 a[8:11], a[232:235], v[32:35], 0\n"
    "v_max3_f32 v25, v25, v58, v59\n"
    "v_max3_f32 v26, v26, v60, v61\n"
    "v_max3_f32 v25, v25, v62, v63\n"
    "v_max_f32 v25, v25, v26\n"
    "v_mov_b32 v26, v25\n"
    "s_nop 1\n"
    "v_permlane16_swap_b32 v25, v26\n"
    "v_max_f32 v25, v25, v26\n"
    "v_mov_b32 v26, v25\n"
    "s_nop 1\n"
    "v_permlane32_swap_b32 v25, v26\n"
    "v_max_f32 v25, v25, v26\n"
    "v_sub_f32 v88, 0, v25\n"
    "v_sub_f32 v89, 0, v25\n"
    "v_sub_f32 v90, 0, v25\n"
    "v_sub_f32 v91, 0, v25\n"
    "v_sub_f32 v27, v48, v25\n"
    "v_sub_f32 v28, v49, v25\n"
    "v_sub_f32 v29, v50, v25\n"
    "v_sub_f32 v30, v51, v25\n"
    "v_exp_f32 v8, v27\n"
    "v_exp_f32 v9, v28\n"
    "v_exp_f32 v10, v29\n"
    "v_exp_f32 v11, v30\n"
    "v_mfma_f32_16x16x32_bf16 a[8:11], v[188:191], v[36:39], a[8:11]\n"
    "v_mfma_f32_16x16x32_bf16 a[12:15], a[236:239], v[32:35], 0\n"
    "v_mfma_f32_16x16x32_bf16 a[12:15], v[192:195], v[36:39], a[12:15]\n"
    "s_waitcnt lgkmcnt(0)\n"
    "v_mfma_f32_16x16x32_bf16 a[16:19], a[240:243], v[32:35], 0\n"
    "v_mfma_f32_16x16x32_bf16 a[16:19], v[196:199], v[36:39], a[16:19]\n"
    "v_mfma_f32_16x16x32_bf16 a[20:23], a[244:247], v[32:35], 0\n"
    "v_mfma_f32_16x16x32_bf16 a[20:23], v[200:203], v[36:39], a[20:23]\n"
    "v_mfma_f32_16x16x32_bf16 a[24:27], a[248:251], v[32:35], 0\n"
    "v_mfma_f32_16x16x32_bf16 a[24:27], v[204:207], v[36:39], a[24:27]\n"
    "v_mfma_f32_16x16x32_bf16 a[28:31], a[252:255], v[32:35], 0\n"
    "v_cvt_pk_bf16_f32 v32, v8, v9\n"
    "v_cvt_pk_bf16_f32 v33, v10, v11\n"
    "v_sub_f32 v27, v52, v25\n"
    "v_sub_f32 v28, v53, v25\n"
    "v_sub_f32 v29, v54, v25\n"
    "v_sub_f32 v30, v55, v25\n"
    "v_exp_f32 v240, v27\n"
    "v_exp_f32 v241, v28\n"
    "v_exp_f32 v242, v29\n"
    "v_exp_f32 v243, v30\n"
    "v_cvt_pk_bf16_f32 v34, v240, v241\n"
    "v_cvt_pk_bf16_f32 v35, v242, v243\n"
    "v_sub_f32 v27, v56, v25\n"
    "v_sub_f32 v28, v57, v25\n"
    "v_sub_f32 v29, v58, v25\n"
    "v_sub_f32 v30, v59, v25\n"
    "v_exp_f32 v8, v27\n"
    "v_exp_f32 v9, v28\n"
    "v_exp_f32 v10, v29\n"
    "v_exp_f32 v11, v30\n"
    "v_mfma_f32_16x16x32_bf16 a[28:31], v[208:211], v[36:39], a[28:31]\n"
    "v_cvt_pk_bf16_f32 v36, v8, v9\n"
    "v_cvt_pk_bf16_f32 v37, v10, v11\n"
    "v_mfma_f32_16x16x32_bf16 v[64:67], a[160:163], v[148:151], 0\n"
    "v_sub_f32 v27, v60, v25\n"
    "v_sub_f32 v28, v61, v25\n"
    "v_sub_f32 v29, v62, v25\n"
    "v_sub_f32 v30, v63, v25\n"
    "v_mfma_f32_16x16x32_bf16 v[64:67], a[164:167], v[152:155], v[64:67]\n"
    "v_exp_f32 v240, v27\n"
    "v_exp_f32 v241, v28\n"
    "v_mfma_f32_16x16x32_bf16 v[64:67], a[168:171], v[156:159], v[64:67]\n"
    "s_add_u32 m0, s34, 16928\n"
    "v_exp_f32 v242, v29\n"
    "v_exp_f32 v243, v30\n"
    "v_mfma_f32_16x16x32_bf16 v[64:67], a[172:175], v[160:163], v[64:67]\n"
    "buffer_load_dwordx4 v1, s[24:27], s32 offen offset:1024  lds\n"
    "v_cvt_pk_bf16_f32 v38, v240, v241\n"
    "v_cvt_pk_bf16_f32 v39, v242, v243\n"
    "v_mfma_f32_16x16x32_bf16 v[12:15], v[16:19], v[32:35], 0\n"
    "s_mov_b32 s61, s45\n"
    "v_lshrrev_b32 v31, 8, v1\n"
    "v_add_u32 v31, 0, v31\n"
    "v_lshlrev_b32 v31, 4, v31\n"
    "v_xor_b32 v232, v229, v31\n"
    "v_mfma_f32_16x16x32_bf16 v[12:15], v[16:19], v[36:39], v[12:15]\n"
    "v_add_u32 v232, 0, v232\n"
    "v_lshrrev_b32 v31, 8, v1\n"
    "v_add_u32 v31, 4, v31\n"
    "v_lshlrev_b32 v31, 4, v31\n"
    "v_mfma_f32_16x16x32_bf16 v[68:71], a[176:179], v[148:151], 0\n"
    "v_xor_b32 v233, v229, v31\n"
    "v_add_u32 v233, 1024, v233\n"
    "v_lshrrev_b32 v31, 8, v1\n"
    "v_add_u32 v31, 8, v31\n"
    "v_mfma_f32_16x16x32_bf16 v[68:71], a[180:183], v[152:155], v[68:71]\n"
    "v_lshlrev_b32 v31, 4, v31\n"
    "v_xor_b32 v234, v229, v31\n"
    "v_add_u32 v234, 2048, v234\n"
    "v_lshrrev_b32 v31, 8, v1\n"
    "v_mfma_f32_16x16x32_bf16 v[68:71], a[184:187], v[156:159], v[68:71]\n"
    "s_add_u32 m0, s34, 16960\n"
    "v_add_u32 v31, 12, v31\n"
    "v_lshlrev_b32 v31, 4, v31\n"
    "v_xor_b32 v235, v229, v31\n"
    "v_add_u32 v235, 3072, v235\n"
    "v_mfma_f32_16x16x32_bf16 v[68:71], a[188:191], v[160:163], v[68:71]\n"
    "buffer_load_dwordx4 v1, s[24:27], s32 offen offset:2048  lds\n"
    "v_mfma_f32_16x16x32_bf16 v[72:75], a[192:195], v[148:151], 0\n"
    "v_mov_b32 v22, v12\n"
    "v_mfma_f32_16x16x32_bf16 v[72:75], a[196:199], v[152:155], v[72:75]\n"
    "v_mfma_f32_16x16x32_bf16 v[72:75], a[200:203], v[156:159], v[72:75]\n"
    "v_mfma_f32_16x16x32_bf16 v[72:75], a[204:207], v[160:163], v[72:75]\n"
    "v_mfma_f32_16x16x32_bf16 v[76:79], a[208:211], v[148:151], 0\n"
    "s_add_u32 m0, s34, 16992\n"
    "v_max3_f32 v25, v64, v65, v66\n"
    "v_max3_f32 v26, v67, v68, v69\n"
    "v_mfma_f32_16x16x32_bf16 v[76:79], a[212:215], v[152:155], v[76:79]\n"
    "buffer_load_dwordx4 v1, s[24:27], s32 offen offset:3072  lds\n"
    "v_mfma_f32_16x16x32_bf16 v[76:79], a[216:219], v[156:159], v[76:79]\n"
    "v_mfma_f32_16x16x32_bf16 v[76:79], a[220:223], v[160:163], v[76:79]\n"
    "v_mfma_f32_16x16x32_bf16 a[32:35], a[224:227], v[40:43], 0\n"
    "v_max3_f32 v25, v25, v70, v71\n"
    "v_max3_f32 v26, v26, v72, v73\n"
    "v_mfma_f32_16x16x32_bf16 a[32:35], v[180:183], v[44:47], a[32:35]\n"
    "v_mfma_f32_16x16x32_bf16 a[36:39], a[228:231], v[40:43], 0\n"
    "v_mfma_f32_16x16x32_bf16 a[36:39], v[184:187], v[44:47], a[36:39]\n"
    "v_mfma_f32_16x16x32_bf16 a[40:43], a[232:235], v[40:43], 0\n"
    "v_max3_f32 v25, v25, v74, v75\n"
    "v_max3_f32 v26, v26, v76, v77\n"
    "v_max3_f32 v25, v25, v78, v79\n"
    "v_max_f32 v25, v25, v26\n"
    "v_mov_b32 v26, v25\n"
    "s_nop 1\n"
    "v_permlane16_swap_b32 v25, v26\n"
    "v_max_f32 v25, v25, v26\n"
    "v_mov_b32 v26, v25\n"
    "s_nop 1\n"
    "v_permlane32_swap_b32 v25, v26\n"
    "v_max_f32 v25, v25, v26\n"
    "v_sub_f32 v92, 0, v25\n"
    "v_sub_f32 v93, 0, v25\n"
    "v_sub_f32 v94, 0, v25\n"
    "v_sub_f32 v95, 0, v25\n"
    "v_sub_f32 v27, v64, v25\n"
    "v_sub_f32 v28, v65, v25\n"
    "v_mfma_f32_16x16x32_bf16 a[40:43], v[188:191], v[44:47], a[40:43]\n"
    "v_sub_f32 v29, v66, v25\n"
    "v_sub_f32 v30, v67, v25\n"
    "v_mfma_f32_16x16x32_bf16 a[44:47], a[236:239], v[40:43], 0\n"
    "v_exp_f32 v8, v27\n"
    "v_mfma_f32_16x16x32_bf16 a[44:47], v[192:195], v[44:47], a[44:47]\n"
    "v_exp_f32 v9, v28\n"
    "v_mfma_f32_16x16x32_bf16 a[48:51], a[240:243], v[40:43], 0\n"
    "s_add_u32 s34, s36, s35\n"
    "v_exp_f32 v10, v29\n"
    "v_mfma_f32_16x16x32_bf16 a[48:51], v[196:199], v[44:47], a[48:51]\n"
    "v_exp_f32 v11, v30\n"
    "v_mfma_f32_16x16x32_bf16 a[52:55], a[244:247], v[40:43], 0\n"
    "s_add_u32 s36, s36, 33792\n"
    "v_mfma_f32_16x16x32_bf16 a[52:55], v[200:203], v[44:47], a[52:55]\n"
    "v_mfma_f32_16x16x32_bf16 a[56:59], a[248:251], v[40:43], 0\n"
    "s_sub_u32 s50, s36, 101376\n"
    "v_mfma_f32_16x16x32_bf16 a[56:59], v[204:207], v[44:47], a[56:59]\n"
    "v_mfma_f32_16x16x32_bf16 a[60:63], a[252:255], v[40:43], 0\n"
    "s_min_u32 s36, s36, s50\n"
    "v_cvt_pk_bf16_f32 v40, v8, v9\n"
    "v_cvt_pk_bf16_f32 v41, v10, v11\n"
    "v_sub_f32 v27, v68, v25\n"
    "v_sub_f32 v28, v69, v25\n"
    "v_sub_f32 v29, v70, v25\n"
    "v_sub_f32 v30, v71, v25\n"
    "v_exp_f32 v240, v27\n"
    "v_exp_f32 v241, v28\n"
    "v_mfma_f32_16x16x32_bf16 a[60:63], v[208:211], v[44:47], a[60:63]\n"
    "s_add_u32 s32, s32, 16384\n"
    "v_exp_f32 v242, v29\n"
    "v_mfma_f32_16x16x32_bf16 v[48:51], a[160:163], v[164:167], 0\n"
    "v_exp_f32 v243, v30\n"
    "v_cvt_pk_bf16_f32 v42, v240, v241\n"
    "v_cvt_pk_bf16_f32 v43, v242, v243\n"
    "v_mfma_f32_16x16x32_bf16 v[48:51], a[164:167], v[168:171], v[48:51]\n"
    "v_add_u32 v4, s36, v2\n"
    "v_sub_f32 v27, v72, v25\n"
    "v_sub_f32 v28, v73, v25\n"
    "v_mfma_f32_16x16x32_bf16 v[48:51], a[168:171], v[172:175], v[48:51]\n"
    "s_waitcnt vmcnt(12)\n"
    "s_barrier\n"
    "v_sub_f32 v29, v74, v25\n"
    "v_sub_f32 v30, v75, v25\n"
    "v_mfma_f32_16x16x32_bf16 v[48:51], a[172:175], v[176:179], v[48:51]\n"
    "v_exp_f32 v8, v27\n"
    "v_exp_f32 v9, v28\n"
    "v_mfma_f32_16x16x32_bf16 v[52:55], a[176:179], v[164:167], 0\n"
    "v_exp_f32 v10, v29\n"
    "v_exp_f32 v11, v30\n"
    "v_mfma_f32_16x16x32_bf16 v[52:55], a[180:183], v[168:171], v[52:55]\n"
    "v_cvt_pk_bf16_f32 v44, v8, v9\n"
    "v_cvt_pk_bf16_f32 v45, v10, v11\n"
    "v_sub_f32 v27, v76, v25\n"
    "v_sub_f32 v28, v77, v25\n"
    "v_mfma_f32_16x16x32_bf16 v[52:55], a[184:187], v[172:175], v[52:55]\n"
    "v_sub_f32 v29, v78, v25\n"
    "v_sub_f32 v30, v79, v25\n"
    "v_exp_f32 v240, v27\n"
    "v_mfma_f32_16x16x32_bf16 v[52:55], a[188:191], v[176:179], v[52:55]\n"
    "v_exp_f32 v241, v28\n"
    "v_mfma_f32_16x16x32_bf16 v[56:59], a[192:195], v[164:167], 0\n"
    "v_exp_f32 v242, v29\n"
    "v_mfma_f32_16x16x32_bf16 v[56:59], a[196:199], v[168:171], v[56:59]\n"
    "v_exp_f32 v243, v30\n"
    "v_mfma_f32_16x16x32_bf16 v[56:59], a[200:203], v[172:175], v[56:59]\n"
    "v_cvt_pk_bf16_f32 v46, v240, v241\n"
    "v_cvt_pk_bf16_f32 v47, v242, v243\n"
    "v_mfma_f32_16x16x32_bf16 v[56:59], a[204:207], v[176:179], v[56:59]\n"
    "ds_read_b128 a[160:163], v4 offset:0\n"
    "ds_read_b128 a[164:167], v4 offset:64\n"
    "v_mfma_f32_16x16x32_bf16 v[12:15], v[16:19], v[40:43], 0\n"
    "v_mfma_f32_16x16x32_bf16 v[12:15], v[16:19], v[44:47], v[12:15]\n"
    "ds_read_b128 a[168:171], v4 offset:128\n"
    "ds_read_b128 a[172:175], v4 offset:192\n"
    "v_mfma_f32_16x16x32_bf16 v[60:63], a[208:211], v[164:167], 0\n"
    "v_max3_f32 v25, v48, v49, v50\n"
    "v_max3_f32 v26, v51, v52, v53\n"
    "v_mfma_f32_16x16x32_bf16 v[60:63], a[212:215], v[168:171], v[60:63]\n"
    "ds_read_b128 a[176:179], v4 offset:256\n"
    "ds_read_b128 a[180:183], v4 offset:320\n"
    "v_mfma_f32_16x16x32_bf16 v[60:63], a[216:219], v[172:175], v[60:63]\n"
    "v_mfma_f32_16x16x32_bf16 v[60:63], a[220:223], v[176:179], v[60:63]\n"
    "ds_read_b128 a[184:187], v4 offset:384\n"
    "ds_read_b128 a[188:191], v4 offset:448\n"
    "v_mfma_f32_16x16x32_bf16 a[64:67], a[224:227], v[32:35], 0\n"
    "v_max3_f32 v25, v25, v54, v55\n"
    "v_max3_f32 v26, v26, v56, v57\n"
    "v_mov_b32 v23, v12\n"
    "v_mfma_f32_16x16x32_bf16 a[64:67], v[180:183], v[36:39], a[64:67]\n"
    "ds_read_b128 a[192:195], v4 offset:512\n"
    "ds_read_b128 a[196:199], v4 offset:576\n"
    "v_mfma_f32_16x16x32_bf16 a[68:71], a[228:231], v[32:35], 0\n"
    "v_mfma_f32_16x16x32_bf16 a[68:71], v[184:187], v[36:39], a[68:71]\n"
    "ds_read_b128 a[200:203], v4 offset:640\n"
    "ds_read_b128 a[204:207], v4 offset:704\n"
    "v_mfma_f32_16x16x32_bf16 a[72:75], a[232:235], v[32:35], 0\n"
    "v_max3_f32 v25, v25, v58, v59\n"
    "v_max3_f32 v26, v26, v60, v61\n"
    "v_max3_f32 v25, v25, v62, v63\n"
    "v_max_f32 v25, v25, v26\n"
    "v_mov_b32 v26, v25\n"
    "v_mfma_f32_16x16x32_bf16 a[72:75], v[188:191], v[36:39], a[72:75]\n"
    "ds_read_b128 a[208:211], v4 offset:768\n"
    "ds_read_b128 a[212:215], v4 offset:832\n"
    "v_mfma_f32_16x16x32_bf16 a[76:79], a[236:239], v[32:35], 0\n"
    "v_permlane16_swap_b32 v25, v26\n"
    "v_mfma_f32_16x16x32_bf16 a[76:79], v[192:195], v[36:39], a[76:79]\n"
    "v_max_f32 v25, v25, v26\n"
    "v_mov_b32 v26, v25\n"
    "v_mfma_f32_16x16x32_bf16 a[80:83], a[240:243], v[32:35], 0\n"
    "ds_read_b128 a[216:219], v4 offset:896\n"
    "ds_read_b128 a[220:223], v4 offset:960\n"
    "v_mfma_f32_16x16x32_bf16 a[80:83], v[196:199], v[36:39], a[80:83]\n"
    "v_permlane32_swap_b32 v25, v26\n"
    "v_mfma_f32_16x16x32_bf16 a[84:87], a[244:247], v[32:35], 0\n"
    "v_max_f32 v25, v25, v26\n"
    "v_mfma_f32_16x16x32_bf16 a[84:87], v[200:203], v[36:39], a[84:87]\n"
    "v_sub_f32 v96, 0, v25\n"
    "v_sub_f32 v97, 0, v25\n"
    "v_mfma_f32_16x16x32_bf16 a[88:91], a[248:251], v[32:35], 0\n"
    "v_sub_f32 v98, 0, v25\n"
    "v_sub_f32 v99, 0, v25\n"
    "v_mfma_f32_16x16x32_bf16 a[88:91], v[204:207], v[36:39], a[88:91]\n"
    "v_sub_f32 v27, v48, v25\n"
    "v_sub_f32 v28, v49, v25\n"
    "v_mfma_f32_16x16x32_bf16 a[92:95], a[252:255], v[32:35], 0\n"
    "v_sub_f32 v29, v50, v25\n"
    "v_sub_f32 v30, v51, v25\n"
    "v_mfma_f32_16x16x32_bf16 a[92:95], v[208:211], v[36:39], a[92:95]\n"
    "s_waitcnt lgkmcnt(8)\n"
    "v_exp_f32 v8, v27\n"
    "v_mfma_f32_16x16x32_bf16 v[64:67], a[160:163], v[100:103], v[80:83]\n"
    "v_exp_f32 v9, v28\n"
    "v_exp_f32 v10, v29\n"
    "v_mfma_f32_16x16x32_bf16 v[64:67], a[164:167], v[104:107], v[64:67]\n"
    "v_exp_f32 v11, v30\n"
    "v_cvt_pk_bf16_f32 v32, v8, v9\n"
    "v_cvt_pk_bf16_f32 v33, v10, v11\n"
    "v_mfma_f32_16x16x32_bf16 v[64:67], a[168:171], v[108:111], v[64:67]\n"
    "v_sub_f32 v27, v52, v25\n"
    "v_sub_f32 v28, v53, v25\n"
    "v_sub_f32 v29, v54, v25\n"
    "v_sub_f32 v30, v55, v25\n"
    "v_mfma_f32_16x16x32_bf16 v[64:67], a[172:175], v[112:115], v[64:67]\n"
    "v_exp_f32 v240, v27\n"
    "v_exp_f32 v241, v28\n"
    "v_mfma_f32_16x16x32_bf16 v[68:71], a[176:179], v[100:103], v[80:83]\n"
    "v_exp_f32 v242, v29\n"
    "v_exp_f32 v243, v30\n"
    "v_mfma_f32_16x16x32_bf16 v[68:71], a[180:183], v[104:107], v[68:71]\n"
    "v_cvt_pk_bf16_f32 v34, v240, v241\n"
    "v_cvt_pk_bf16_f32 v35, v242, v243\n"
    "v_sub_f32 v27, v56, v25\n"
    "v_sub_f32 v28, v57, v25\n"
    "v_mfma_f32_16x16x32_bf16 v[68:71], a[184:187], v[108:111], v[68:71]\n"
    "v_sub_f32 v29, v58, v25\n"
    "v_sub_f32 v30, v59, v25\n"
    "v_exp_f32 v8, v27\n"
    "v_mfma_f32_16x16x32_bf16 v[68:71], a[188:191], v[112:115], v[68:71]\n"
    "s_waitcnt lgkmcnt(0)\n"
    "v_exp_f32 v9, v28\n"
    "v_exp_f32 v10, v29\n"
    "v_mfma_f32_16x16x32_bf16 v[72:75], a[192:195], v[100:103], v[80:83]\n"
    "s_add_u32 m0, s34, 0\n"
    "v_exp_f32 v11, v30\n"
    "v_accvgpr_write_b32 a128, 0\n"
    "v_accvgpr_write_b32 a129, 0\n"
    "v_mfma_f32_16x16x32_bf16 v[72:75], a[196:199], v[104:107], v[72:75]\n"
    "buffer_load_dwordx4 v1, s[20:23], s32 offen offset:0  lds\n"
    "v_cvt_pk_bf16_f32 v36, v8, v9\n"
    "v_cvt_pk_bf16_f32 v37, v10, v11\n"
    "v_mfma_f32_16x16x32_bf16 v[72:75], a[200:203], v[108:111], v[72:75]\n"
    "v_accvgpr_write_b32 a130, 0\n"
    "v_accvgpr_write_b32 a131, 0\n"
    "v_sub_f32 v27, v60, v25\n"
    "v_sub_f32 v28, v61, v25\n"
    "v_mfma_f32_16x16x32_bf16 v[72:75], a[204:207], v[112:115], v[72:75]\n"
    "v_accvgpr_write_b32 a132, 0\n"
    "v_accvgpr_write_b32 a133, 0\n"
    "v_sub_f32 v29, v62, v25\n"
    "v_sub_f32 v30, v63, v25\n"
    "v_mfma_f32_16x16x32_bf16 v[76:79], a[208:211], v[100:103], v[80:83]\n"
    "v_accvgpr_write_b32 a134, 0\n"
    "v_accvgpr_write_b32 a135, 0\n"
    "v_exp_f32 v240, v27\n"
    "v_mfma_f32_16x16x32_bf16 v[76:79], a[212:215], v[104:107], v[76:79]\n"
    "v_accvgpr_write_b32 a136, 0\n"
    "v_accvgpr_write_b32 a137, 0\n"
    "v_exp_f32 v241, v28\n"
    "v_mfma_f32_16x16x32_bf16 v[76:79], a[216:219], v[108:111], v[76:79]\n"
    "v_accvgpr_write_b32 a138, 0\n"
    "v_accvgpr_write_b32 a139, 0\n"
    "v_exp_f32 v242, v29\n"
    "v_mfma_f32_16x16x32_bf16 v[76:79], a[220:223], v[112:115], v[76:79]\n"
    "v_accvgpr_write_b32 a140, 0\n"
    "v_accvgpr_write_b32 a141, 0\n"
    "v_exp_f32 v243, v30\n"
    "v_mfma_f32_16x16x32_bf16 a[96:99], a[224:227], v[40:43], 0\n"
    "v_accvgpr_write_b32 a142, 0\n"
    "v_accvgpr_write_b32 a143, 0\n"
    "v_cvt_pk_bf16_f32 v38, v240, v241\n"
    "v_cvt_pk_bf16_f32 v39, v242, v243\n"
    "v_mfma_f32_16x16x32_bf16 a[96:99], v[180:183], v[44:47], a[96:99]\n"
    "v_accvgpr_write_b32 a144, 0\n"
    "v_accvgpr_write_b32 a145, 0\n"
    "v_mfma_f32_16x16x32_bf16 v[12:15], v[16:19], v[32:35], 0\n"
    "v_exp_f32 v8, v64\n"
    "v_mfma_f32_16x16x32_bf16 v[12:15], v[16:19], v[36:39], v[12:15]\n"
    "v_accvgpr_write_b32 a146, 0\n"
    "v_accvgpr_write_b32 a147, 0\n"
    "v_mfma_f32_16x16x32_bf16 a[100:103], a[228:231], v[40:43], 0\n"
    "v_exp_f32 v9, v65\n"
    "v_mfma_f32_16x16x32_bf16 a[100:103], v[184:187], v[44:47], a[100:103]\n"
    "v_accvgpr_write_b32 a148, 0\n"
    "v_accvgpr_write_b32 a149, 0\n"
    "v_mfma_f32_16x16x32_bf16 a[104:107], a[232:235], v[40:43], 0\n"
    "v_exp_f32 v10, v66\n"
    "v_mfma_f32_16x16x32_bf16 a[104:107], v[188:191], v[44:47], a[104:107]\n"
    "v_accvgpr_write_b32 a150, 0\n"
    "v_accvgpr_write_b32 a151, 0\n"
    "v_mfma_f32_16x16x32_bf16 a[108:111], a[236:239], v[40:43], 0\n"
    "v_exp_f32 v11, v67\n"
    "v_mfma_f32_16x16x32_bf16 a[108:111], v[192:195], v[44:47], a[108:111]\n"
    "v_accvgpr_write_b32 a152, 0\n"
    "v_accvgpr_write_b32 a153, 0\n"
    "v_mfma_f32_16x16x32_bf16 a[112:115], a[240:243], v[40:43], 0\n"
    "v_exp_f32 v240, v68\n"
    "v_mfma_f32_16x16x32_bf16 a[112:115], v[196:199], v[44:47], a[112:115]\n"
    "s_add_u32 m0, s34, 32\n"
    "v_accvgpr_write_b32 a154, 0\n"
    "v_accvgpr_write_b32 a155, 0\n"
    "v_mfma_f32_16x16x32_bf16 a[116:119], a[244:247], v[40:43], 0\n"
    "buffer_load_dwordx4 v1, s[20:23], s32 offen offset:1024  lds\n"
    "v_mfma_f32_16x16x32_bf16 a[116:119], v[200:203], v[44:47], a[116:119]\n"
    "v_exp_f32 v241, v69\n"
    "v_mfma_f32_16x16x32_bf16 a[120:123], a[248:251], v[40:43], 0\n"
    "v_accvgpr_write_b32 a156, 0\n"
    "v_accvgpr_write_b32 a157, 0\n"
    "v_mfma_f32_16x16x32_bf16 a[120:123], v[204:207], v[44:47], a[120:123]\n"
    "v_exp_f32 v242, v70\n"
    "v_mfma_f32_16x16x32_bf16 a[124:127], a[252:255], v[40:43], 0\n"
    "v_mov_b32 v24, v12\n"
    "v_mfma_f32_16x16x32_bf16 a[124:127], v[208:211], v[44:47], a[124:127]\n"
    "v_accvgpr_write_b32 a158, 0\n"
    "v_accvgpr_write_b32 a159, 0\n"
    "s_mov_b32 s37, 4\n"
    "LBH5:\n"
    "v_mfma_f32_16x16x32_bf16 v[48:51], a[160:163], v[116:119], v[84:87]\n"
    "v_exp_f32 v243, v71\n"
    "v_mfma_f32_16x16x32_bf16 v[48:51], a[164:167], v[120:123], v[48:51]\n"
    "v_cvt_pk_bf16_f32 v40, v8, v9\n"
    "v_cvt_pk_bf16_f32 v41, v10, v11\n"
    "v_mfma_f32_16x16x32_bf16 v[48:51], a[168:171], v[124:127], v[48:51]\n"
    "v_exp_f32 v8, v72\n"
    "v_mfma_f32_16x16x32_bf16 v[48:51], a[172:175], v[128:131], v[48:51]\n"
    "v_exp_f32 v9, v73\n"
    "v_mfma_f32_16x16x32_bf16 v[52:55], a[176:179], v[116:119], v[84:87]\n"
    "v_add_u32 v5, s36, v3\n"
    "v_mfma_f32_16x16x32_bf16 v[52:55], a[180:183], v[120:123], v[52:55]\n"
    "v_exp_f32 v10, v74\n"
    "v_mfma_f32_16x16x32_bf16 v[52:55], a[184:187], v[124:127], v[52:55]\n"
    "v_exp_f32 v11, v75\n"
    "v_mfma_f32_16x16x32_bf16 v[52:55], a[188:191], v[128:131], v[52:55]\n"
    "v_cvt_pk_bf16_f32 v42, v240, v241\n"
    "v_cvt_pk_bf16_f32 v43, v242, v243\n"
    "v_mfma_f32_16x16x32_bf16 v[56:59], a[192:195], v[116:119], v[84:87]\n"
    "v_exp_f32 v240, v76\n"
    "v_mfma_f32_16x16x32_bf16 v[56:59], a[196:199], v[120:123], v[56:59]\n"
    "v_exp_f32 v241, v77\n"
    "v_mfma_f32_16x16x32_bf16 v[56:59], a[200:203], v[124:127], v[56:59]\n"
    "v_exp_f32 v242, v78\n"
    "v_mfma_f32_16x16x32_bf16 v[56:59], a[204:207], v[128:131], v[56:59]\n"
    "v_exp_f32 v243, v79\n"
    "v_mfma_f32_16x16x32_bf16 v[60:63], a[208:211], v[116:119], v[84:87]\n"
    "v_cvt_pk_bf16_f32 v44, v8, v9\n"
    "v_cvt_pk_bf16_f32 v45, v10, v11\n"
    "v_mfma_f32_16x16x32_bf16 v[60:63], a[212:215], v[120:123], v[60:63]\n"
    "v_cvt_pk_bf16_f32 v46, v240, v241\n"
    "v_cvt_pk_bf16_f32 v47, v242, v243\n"
    "v_mfma_f32_16x16x32_bf16 v[60:63], a[216:219], v[124:127], v[60:63]\n"
    "v_exp_f32 v8, v48\n"
    "v_mfma_f32_16x16x32_bf16 v[60:63], a[220:223], v[128:131], v[60:63]\n"
    "v_exp_f32 v9, v49\n"
    "v_mfma_f32_16x16x32_bf16 v[12:15], v[16:19], v[40:43], 0\n"
    "v_exp_f32 v10, v50\n"
    "v_mfma_f32_16x16x32_bf16 v[12:15], v[16:19], v[44:47], v[12:15]\n"
    "v_exp_f32 v11, v51\n"
    "v_mfma_f32_16x16x32_bf16 a[128:131], a[224:227], v[32:35], a[128:131]\n"
    "ds_read_b64_tr_b16 a[224:225], v5 offset:0\n"
    "ds_read_b64_tr_b16 a[226:227], v5 offset:256\n"
    "v_mfma_f32_16x16x32_bf16 a[128:131], v[180:183], v[36:39], a[128:131]\n"
    "v_exp_f32 v240, v52\n"
    "v_mfma_f32_16x16x32_bf16 a[132:135], a[228:231], v[32:35], a[132:135]\n"
    "ds_read_b64_tr_b16 v[180:181], v5 offset:512\n"
    "ds_read_b64_tr_b16 v[182:183], v5 offset:768\n"
    "v_mfma_f32_16x16x32_bf16 a[132:135], v[184:187], v[36:39], a[132:135]\n"
    "v_exp_f32 v241, v53\n"
    "v_mfma_f32_16x16x32_bf16 a[136:139], a[232:235], v[32:35], a[136:139]\n"
    "ds_read_b64_tr_b16 a[228:229], v5 offset:32\n"
    "ds_read_b64_tr_b16 a[230:231], v5 offset:288\n"
    "v_mfma_f32_16x16x32_bf16 a[136:139], v[188:191], v[36:39], a[136:139]\n"
    "v_cmp_nge_f32 vcc, s40, v12\n"
    "v_mfma_f32_16x16x32_bf16 a[140:143], a[236:239], v[32:35], a[140:143]\n"
    "ds_read_b64_tr_b16 v[184:185], v5 offset:544\n"
    "ds_read_b64_tr_b16 v[186:187], v5 offset:800\n"
    "v_mfma_f32_16x16x32_bf16 a[140:143], v[192:195], v[36:39], a[140:143]\n"
    "v_exp_f32 v242, v54\n"
    "v_mfma_f32_16x16x32_bf16 a[144:147], a[240:243], v[32:35], a[144:147]\n"
    "ds_read_b64_tr_b16 a[232:233], v5 offset:64\n"
    "ds_read_b64_tr_b16 a[234:235], v5 offset:320\n"
    "v_mfma_f32_16x16x32_bf16 a[144:147], v[196:199], v[36:39], a[144:147]\n"
    "s_cbranch_vccnz LTR_10\n"
    "LRET_10:\n"
    "v_add_f32 v20, v20, v12\n"
    "v_mfma_f32_16x16x32_bf16 a[148:151], a[244:247], v[32:35], a[148:151]\n"
    "ds_read_b64_tr_b16 v[188:189], v5 offset:576\n"
    "ds_read_b64_tr_b16 v[190:191], v5 offset:832\n"
    "v_mfma_f32_16x16x32_bf16 a[148:151], v[200:203], v[36:39], a[148:151]\n"
    "v_exp_f32 v243, v55\n"
    "v_mfma_f32_16x16x32_bf16 a[152:155], a[248:251], v[32:35], a[152:155]\n"
    "ds_read_b64_tr_b16 a[236:237], v5 offset:96\n"
    "ds_read_b64_tr_b16 a[238:239], v5 offset:352\n"
    "v_mfma_f32_16x16x32_bf16 a[152:155], v[204:207], v[36:39], a[152:155]\n"
    "ds_read_b128 v[212:215], v232\n"
    "v_mfma_f32_16x16x32_bf16 a[156:159], a[252:255], v[32:35], a[156:159]\n"
    "ds_read_b64_tr_b16 v[192:193], v5 offset:608\n"
    "ds_read_b64_tr_b16 v[194:195], v5 offset:864\n"
    "v_mfma_f32_16x16x32_bf16 a[156:159], v[208:211], v[36:39], a[156:159]\n"
    "v_cvt_pk_bf16_f32 v32, v8, v9\n"
    "v_cvt_pk_bf16_f32 v33, v10, v11\n"
    "v_mfma_f32_16x16x32_bf16 v[64:67], a[160:163], v[132:135], v[88:91]\n"
    "s_add_u32 m0, s34, 64\n"
    "ds_read_b64_tr_b16 a[240:241], v5 offset:128\n"
    "ds_read_b64_tr_b16 a[242:243], v5 offset:384\n"
    "v_mfma_f32_16x16x32_bf16 v[64:67], a[164:167], v[136:139], v[64:67]\n"
    "buffer_load_dwordx4 v1, s[20:23], s32 offen offset:2048  lds\n"
    "v_mfma_f32_16x16x32_bf16 v[64:67], a[168:171], v[140:143], v[64:67]\n"
    "ds_read_b64_tr_b16 v[196:197], v5 offset:640\n"
    "ds_read_b64_tr_b16 v[198:199], v5 offset:896\n"
    "v_mfma_f32_16x16x32_bf16 v[64:67], a[172:175], v[144:147], v[64:67]\n"
    "v_exp_f32 v8, v56\n"
    "v_mfma_f32_16x16x32_bf16 v[68:71], a[176:179], v[132:135], v[88:91]\n"
    "ds_read_b64_tr_b16 a[244:245], v5 offset:160\n"
    "ds_read_b64_tr_b16 a[246:247], v5 offset:416\n"
    "v_mfma_f32_16x16x32_bf16 v[68:71], a[180:183], v[136:139], v[68:71]\n"
    "v_exp_f32 v9, v57\n"
    "v_mfma_f32_16x16x32_bf16 v[68:71], a[184:187], v[140:143], v[68:71]\n"
    "ds_read_b64_tr_b16 v[200:201], v5 offset:672\n"
    "ds_read_b64_tr_b16 v[202:203], v5 offset:928\n"
    "v_mfma_f32_16x16x32_bf16 v[68:71], a[188:191], v[144:147], v[68:71]\n"
    "v_exp_f32 v10, v58\n"
    "v_mfma_f32_16x16x32_bf16 v[72:75], a[192:195], v[132:135], v[88:91]\n"
    "ds_read_b64_tr_b16 a[248:249], v5 offset:192\n"
    "ds_read_b64_tr_b16 a[250:251], v5 offset:448\n"
    "v_mfma_f32_16x16x32_bf16 v[72:75], a[196:199], v[136:139], v[72:75]\n"
    "v_exp_f32 v11, v59\n"
    "v_mfma_f32_16x16x32_bf16 v[72:75], a[200:203], v[140:143], v[72:75]\n"
    "ds_read_b64_tr_b16 v[204:205], v5 offset:704\n"
    "ds_read_b64_tr_b16 v[206:207], v5 offset:960\n"
    "v_mfma_f32_16x16x32_bf16 v[72:75], a[204:207], v[144:147], v[72:75]\n"
    "v_cvt_pk_bf16_f32 v34, v240, v241\n"
    "v_cvt_pk_bf16_f32 v35, v242, v243\n"
    "v_mfma_f32_16x16x32_bf16 v[76:79], a[208:211], v[132:135], v[88:91]\n"
    "ds_read_b64_tr_b16 a[252:253], v5 offset:224\n"
    "ds_read_b64_tr_b16 a[254:255], v5 offset:480\n"
    "v_mfma_f32_16x16x32_bf16 v[76:79], a[212:215], v[136:139], v[76:79]\n"
    "v_exp_f32 v240, v60\n"
    "v_mfma_f32_16x16x32_bf16 v[76:79], a[216:219], v[140:143], v[76:79]\n"
    "ds_read_b64_tr_b16 v[208:209], v5 offset:736\n"
    "ds_read_b64_tr_b16 v[210:211], v5 offset:992\n"
    "v_mfma_f32_16x16x32_bf16 v[76:79], a[220:223], v[144:147], v[76:79]\n"
    "s_waitcnt lgkmcnt(15)\n"
    "v_exp_f32 v241, v61\n"
    "v_mfma_f32_16x16x32_bf16 a[0:3], a[224:227], v[40:43], a[0:3]\n"
    "s_add_u32 m0, s34, 96\n"
    "v_exp_f32 v242, v62\n"
    "v_mfma_f32_16x16x32_bf16 a[0:3], v[180:183], v[44:47], a[0:3]\n"
    "buffer_load_dwordx4 v1, s[20:23], s32 offen offset:3072  lds\n"
    "v_mfma_f32_16x16x32_bf16 a[4:7], a[228:231], v[40:43], a[4:7]\n"
    "v_exp_f32 v243, v63\n"
    "v_mfma_f32_16x16x32_bf16 a[4:7], v[184:187], v[44:47], a[4:7]\n"
    "v_cvt_pk_bf16_f32 v36, v8, v9\n"
    "v_cvt_pk_bf16_f32 v37, v10, v11\n"
    "v_mfma_f32_16x16x32_bf16 a[8:11], a[232:235], v[40:43], a[8:11]\n"
    "s_add_u32 m0, s34, 16896\n"
    "v_cvt_pk_bf16_f32 v38, v240, v241\n"
    "v_cvt_pk_bf16_f32 v39, v242, v243\n"
    "v_mfma_f32_16x16x32_bf16 a[8:11], v[188:191], v[44:47], a[8:11]\n"
    "buffer_load_dwordx4 v1, s[24:27], s32 offen offset:0  lds\n"
    "v_mfma_f32_16x16x32_bf16 v[12:15], v[16:19], v[32:35], 0\n"
    "v_exp_f32 v8, v64\n"
    "v_mfma_f32_16x16x32_bf16 v[12:15], v[16:19], v[36:39], v[12:15]\n"
    "v_exp_f32 v9, v65\n"
    "v_mfma_f32_16x16x32_bf16 a[12:15], a[236:239], v[40:43], a[12:15]\n"
    "v_exp_f32 v10, v66\n"
    "v_mfma_f32_16x16x32_bf16 a[12:15], v[192:195], v[44:47], a[12:15]\n"
    "s_waitcnt lgkmcnt(0)\n"
    "v_exp_f32 v11, v67\n"
    "v_mfma_f32_16x16x32_bf16 a[16:19], a[240:243], v[40:43], a[16:19]\n"
    "v_exp_f32 v240, v68\n"
    "v_mfma_f32_16x16x32_bf16 a[16:19], v[196:199], v[44:47], a[16:19]\n"
    "v_exp_f32 v241, v69\n"
    "v_mfma_f32_16x16x32_bf16 a[20:23], a[244:247], v[40:43], a[20:23]\n"
    "v_cmp_nge_f32 vcc, s40, v12\n"
    "v_mfma_f32_16x16x32_bf16 a[20:23], v[200:203], v[44:47], a[20:23]\n"
    "v_exp_f32 v242, v70\n"
    "v_mfma_f32_16x16x32_bf16 a[24:27], a[248:251], v[40:43], a[24:27]\n"
    "v_exp_f32 v243, v71\n"
    "v_mfma_f32_16x16x32_bf16 a[24:27], v[204:207], v[44:47], a[24:27]\n"
    "s_cbranch_vccnz LTR_11\n"
    "LRET_11:\n"
    "v_add_f32 v21, v21, v12\n"
    "v_mfma_f32_16x16x32_bf16 a[28:31], a[252:255], v[40:43], a[28:31]\n"
    "v_cvt_pk_bf16_f32 v40, v8, v9\n"
    "v_cvt_pk_bf16_f32 v41, v10, v11\n"
    "v_mfma_f32_16x16x32_bf16 a[28:31], v[208:211], v[44:47], a[28:31]\n"
    "v_exp_f32 v8, v72\n"
    "v_mfma_f32_16x16x32_bf16 v[48:51], a[160:163], v[148:151], v[92:95]\n"
    "s_add_u32 m0, s34, 16928\n"
    "v_exp_f32 v9, v73\n"
    "v_mfma_f32_16x16x32_bf16 v[48:51], a[164:167], v[152:155], v[48:51]\n"
    "buffer_load_dwordx4 v1, s[24:27], s32 offen offset:1024  lds\n"
    "v_mfma_f32_16x16x32_bf16 v[48:51], a[168:171], v[156:159], v[48:51]\n"
    "v_exp_f32 v10, v74\n"
    "v_mfma_f32_16x16x32_bf16 v[48:51], a[172:175], v[160:163], v[48:51]\n"
    "v_exp_f32 v11, v75\n"
    "v_mfma_f32_16x16x32_bf16 v[52:55], a[176:179], v[148:151], v[92:95]\n"
    "v_cvt_pk_bf16_f32 v42, v240, v241\n"
    "v_cvt_pk_bf16_f32 v43, v242, v243\n"
    "v_mfma_f32_16x16x32_bf16 v[52:55], a[180:183], v[152:155], v[52:55]\n"
    "v_exp_f32 v240, v76\n"
    "v_mfma_f32_16x16x32_bf16 v[52:55], a[184:187], v[156:159], v[52:55]\n"
    "s_add_u32 m0, s34, 16960\n"
    "v_exp_f32 v241, v77\n"
    "v_mfma_f32_16x16x32_bf16 v[52:55], a[188:191], v[160:163], v[52:55]\n"
    "buffer_load_dwordx4 v1, s[24:27], s32 offen offset:2048  lds\n"
    "v_mfma_f32_16x16x32_bf16 v[56:59], a[192:195], v[148:151], v[92:95]\n"
    "v_exp_f32 v242, v78\n"
    "v_mfma_f32_16x16x32_bf16 v[56:59], a[196:199], v[152:155], v[56:59]\n"
    "v_exp_f32 v243, v79\n"
    "v_mfma_f32_16x16x32_bf16 v[56:59], a[200:203], v[156:159], v[56:59]\n"
    "v_cvt_pk_bf16_f32 v44, v8, v9\n"
    "v_cvt_pk_bf16_f32 v45, v10, v11\n"
    "v_mfma_f32_16x16x32_bf16 v[56:59], a[204:207], v[160:163], v[56:59]\n"
    "v_cvt_pk_bf16_f32 v46, v240, v241\n"
    "v_cvt_pk_bf16_f32 v47, v242, v243\n"
    "v_mfma_f32_16x16x32_bf16 v[12:15], v[16:19], v[40:43], 0\n"
    "v_exp_f32 v8, v48\n"
    "v_mfma_f32_16x16x32_bf16 v[12:15], v[16:19], v[44:47], v[12:15]\n"
    "v_exp_f32 v9, v49\n"
    "v_mfma_f32_16x16x32_bf16 v[60:63], a[208:211], v[148:151], v[92:95]\n"
    "s_add_u32 m0, s34, 16992\n"
    "v_exp_f32 v10, v50\n"
    "v_mfma_f32_16x16x32_bf16 v[60:63], a[212:215], v[152:155], v[60:63]\n"
    "buffer_load_dwordx4 v1, s[24:27], s32 offen offset:3072  lds\n"
    "v_mfma_f32_16x16x32_bf16 v[60:63], a[216:219], v[156:159], v[60:63]\n"
    "v_exp_f32 v11, v51\n"
    "v_mfma_f32_16x16x32_bf16 v[60:63], a[220:223], v[160:163], v[60:63]\n"
    "v_exp_f32 v240, v52\n"
    "v_mfma_f32_16x16x32_bf16 a[32:35], a[224:227], v[32:35], a[32:35]\n"
    "v_cmp_nge_f32 vcc, s40, v12\n"
    "v_mfma_f32_16x16x32_bf16 a[32:35], v[180:183], v[36:39], a[32:35]\n"
    "v_exp_f32 v241, v53\n"
    "v_mfma_f32_16x16x32_bf16 a[36:39], a[228:231], v[32:35], a[36:39]\n"
    "v_exp_f32 v242, v54\n"
    "v_mfma_f32_16x16x32_bf16 a[36:39], v[184:187], v[36:39], a[36:39]\n"
    "s_cbranch_vccnz LTR_12\n"
    "LRET_12:\n"
    "v_add_f32 v22, v22, v12\n"
    "v_mfma_f32_16x16x32_bf16 a[40:43], a[232:235], v[32:35], a[40:43]\n"
    "v_exp_f32 v243, v55\n"
    "v_mfma_f32_16x16x32_bf16 a[40:43], v[188:191], v[36:39], a[40:43]\n"
    "v_add_u32 v232, 4096, v232\n"
    "v_mfma_f32_16x16x32_bf16 a[44:47], a[236:239], v[32:35], a[44:47]\n"
    "v_mfma_f32_16x16x32_bf16 a[44:47], v[192:195], v[36:39], a[44:47]\n"
    "v_mfma_f32_16x16x32_bf16 a[48:51], a[240:243], v[32:35], a[48:51]\n"
    "s_add_u32 s34, s36, s35\n"
    "v_mfma_f32_16x16x32_bf16 a[48:51], v[196:199], v[36:39], a[48:51]\n"
    "v_mfma_f32_16x16x32_bf16 a[52:55], a[244:247], v[32:35], a[52:55]\n"
    "s_add_u32 s36, s36, 33792\n"
    "v_mfma_f32_16x16x32_bf16 a[52:55], v[200:203], v[36:39], a[52:55]\n"
    "v_mfma_f32_16x16x32_bf16 a[56:59], a[248:251], v[32:35], a[56:59]\n"
    "s_sub_u32 s50, s36, 101376\n"
    "v_mfma_f32_16x16x32_bf16 a[56:59], v[204:207], v[36:39], a[56:59]\n"
    "v_mfma_f32_16x16x32_bf16 a[60:63], a[252:255], v[32:35], a[60:63]\n"
    "s_min_u32 s36, s36, s50\n"
    "v_cvt_pk_bf16_f32 v32, v8, v9\n"
    "v_cvt_pk_bf16_f32 v33, v10, v11\n"
    "v_mfma_f32_16x16x32_bf16 a[60:63], v[208:211], v[36:39], a[60:63]\n"
    "s_add_u32 s32, s32, 16384\n"
    "v_exp_f32 v8, v56\n"
    "v_mfma_f32_16x16x32_bf16 v[64:67], a[160:163], v[164:167], v[96:99]\n"
    "v_add_u32 v4, s36, v2\n"
    "v_mfma_f32_16x16x32_bf16 v[64:67], a[164:167], v[168:171], v[64:67]\n"
    "s_waitcnt vmcnt(8)\n"
    "s_barrier\n"
    "v_mfma_f32_16x16x32_bf16 v[64:67], a[168:171], v[172:175], v[64:67]\n"
    "v_exp_f32 v9, v57\n"
    "v_mfma_f32_16x16x32_bf16 v[64:67], a[172:175], v[176:179], v[64:67]\n"
    "v_exp_f32 v10, v58\n"
    "v_mfma_f32_16x16x32_bf16 v[68:71], a[176:179], v[164:167], v[96:99]\n"
    "ds_read_b128 a[160:163], v4 offset:0\n"
    "ds_read_b128 a[164:167], v4 offset:64\n"
    "v_mfma_f32_16x16x32_bf16 v[68:71], a[180:183], v[168:171], v[68:71]\n"
    "v_exp_f32 v11, v59\n"
    "v_mfma_f32_16x16x32_bf16 v[68:71], a[184:187], v[172:175], v[68:71]\n"
    "v_cvt_pk_bf16_f32 v34, v240, v241\n"
    "v_cvt_pk_bf16_f32 v35, v242, v243\n"
    "v_mfma_f32_16x16x32_bf16 v[68:71], a[188:191], v[176:179], v[68:71]\n"
    "ds_read_b128 a[168:171], v4 offset:128\n"
    "ds_read_b128 a[172:175], v4 offset:192\n"
    "v_mfma_f32_16x16x32_bf16 v[72:75], a[192:195], v[164:167], v[96:99]\n"
    "v_exp_f32 v240, v60\n"
    "v_mfma_f32_16x16x32_bf16 v[72:75], a[196:199], v[168:171], v[72:75]\n"
    "v_exp_f32 v241, v61\n"
    "v_mfma_f32_16x16x32_bf16 v[72:75], a[200:203], v[172:175], v[72:75]\n"
    "ds_read_b128 a[176:179], v4 offset:256\n"
    "ds_read_b128 a[180:183], v4 offset:320\n"
    "v_mfma_f32_16x16x32_bf16 v[72:75], a[204:207], v[176:179], v[72:75]\n"
    "v_exp_f32 v242, v62\n"
    "v_mfma_f32_16x16x32_bf16 v[76:79], a[208:211], v[164:167], v[96:99]\n"
    "v_exp_f32 v243, v63\n"
    "v_mfma_f32_16x16x32_bf16 v[76:79], a[212:215], v[168:171], v[76:79]\n"
    "ds_read_b128 a[184:187], v4 offset:384\n"
    "ds_read_b128 a[188:191], v4 offset:448\n"
    "v_mfma_f32_16x16x32_bf16 v[76:79], a[216:219], v[172:175], v[76:79]\n"
    "v_cvt_pk_bf16_f32 v36, v8, v9\n"
    "v_cvt_pk_bf16_f32 v37, v10, v11\n"
    "v_mfma_f32_16x16x32_bf16 v[76:79], a[220:223], v[176:179], v[76:79]\n"
    "v_cvt_pk_bf16_f32 v38, v240, v241\n"
    "v_cvt_pk_bf16_f32 v39, v242, v243\n"
    "v_mfma_f32_16x16x32_bf16 v[12:15], v[16:19], v[32:35], 0\n"
    "ds_read_b128 a[192:195], v4 offset:512\n"
    "ds_read_b128 a[196:199], v4 offset:576\n"
    "v_mfma_f32_16x16x32_bf16 v[12:15], v[16:19], v[36:39], v[12:15]\n"
    "v_exp_f32 v8, v64\n"
    "v_mfma_f32_16x16x32_bf16 a[64:67], a[224:227], v[40:43], a[64:67]\n"
    "ds_read_b128 a[200:203], v4 offset:640\n"
    "ds_read_b128 a[204:207], v4 offset:704\n"
    "v_mfma_f32_16x16x32_bf16 a[64:67], v[180:183], v[44:47], a[64:67]\n"
    "v_exp_f32 v9, v65\n"
    "v_mfma_f32_16x16x32_bf16 a[68:71], a[228:231], v[40:43], a[68:71]\n"
    "ds_read_b128 a[208:211], v4 offset:768\n"
    "ds_read_b128 a[212:215], v4 offset:832\n"
    "v_mfma_f32_16x16x32_bf16 a[68:71], v[184:187], v[44:47], a[68:71]\n"
    "v_exp_f32 v10, v66\n"
    "v_mfma_f32_16x16x32_bf16 a[72:75], a[232:235], v[40:43], a[72:75]\n"
    "v_cmp_nge_f32 vcc, s40, v12\n"
    "v_mfma_f32_16x16x32_bf16 a[72:75], v[188:191], v[44:47], a[72:75]\n"
    "ds_read_b128 a[216:219], v4 offset:896\n"
    "ds_read_b128 a[220:223], v4 offset:960\n"
    "v_mfma_f32_16x16x32_bf16 a[76:79], a[236:239], v[40:43], a[76:79]\n"
    "v_exp_f32 v11, v67\n"
    "v_mfma_f32_16x16x32_bf16 a[76:79], v[192:195], v[44:47], a[76:79]\n"
    "s_cbranch_vccnz LTR_13\n"
    "LRET_13:\n"
    "v_add_f32 v23, v23, v12\n"
    "v_mfma_f32_16x16x32_bf16 a[80:83], a[240:243], v[40:43], a[80:83]\n"
    "v_exp_f32 v240, v68\n"
    "v_mfma_f32_16x16x32_bf16 a[80:83], v[196:199], v[44:47], a[80:83]\n"
    "v_exp_f32 v241, v69\n"
    "v_mfma_f32_16x16x32_bf16 a[84:87], a[244:247], v[40:43], a[84:87]\n"
    "v_exp_f32 v242, v70\n"
    "v_mfma_f32_16x16x32_bf16 a[84:87], v[200:203], v[44:47], a[84:87]\n"
    "v_exp_f32 v243, v71\n"
    "v_mfma_f32_16x16x32_bf16 a[88:91], a[248:251], v[40:43], a[88:91]\n"
    "s_waitcnt lgkmcnt(15)\n"
    "buffer_store_dwordx4 v[212:215], v1, s[28:31], s61 offen offset:0 nt\n"
    "v_mfma_f32_16x16x32_bf16 a[88:91], v[204:207], v[44:47], a[88:91]\n"
    "ds_read_b128 v[216:219], v233\n"
    "v_mfma_f32_16x16x32_bf16 a[92:95], a[252:255], v[40:43], a[92:95]\n"
    "v_cvt_pk_bf16_f32 v40, v8, v9\n"
    "v_cvt_pk_bf16_f32 v41, v10, v11\n"
    "v_mfma_f32_16x16x32_bf16 a[92:95], v[208:211], v[44:47], a[92:95]\n"
    "s_waitcnt lgkmcnt(9)\n"
    "v_exp_f32 v8, v72\n"
    "v_mfma_f32_16x16x32_bf16 v[48:51], a[160:163], v[100:103], v[80:83]\n"
    "v_exp_f32 v9, v73\n"
    "v_mfma_f32_16x16x32_bf16 v[48:51], a[164:167], v[104:107], v[48:51]\n"
    "s_add_u32 m0, s34, 0\n"
    "v_exp_f32 v10, v74\n"
    "v_mfma_f32_16x16x32_bf16 v[48:51], a[168:171], v[108:111], v[48:51]\n"
    "buffer_load_dwordx4 v1, s[20:23], s32 offen offset:0  lds\n"
    "v_mfma_f32_16x16x32_bf16 v[48:51], a[172:175], v[112:115], v[48:51]\n"
    "v_exp_f32 v11, v75\n"
    "v_mfma_f32_16x16x32_bf16 v[52:55], a[176:179], v[100:103], v[80:83]\n"
    "v_cvt_pk_bf16_f32 v42, v240, v241\n"
    "v_cvt_pk_bf16_f32 v43, v242, v243\n"
    "v_mfma_f32_16x16x32_bf16 v[52:55], a[180:183], v[104:107], v[52:55]\n"
    "v_exp_f32 v240, v76\n"
    "v_mfma_f32_16x16x32_bf16 v[52:55], a[184:187], v[108:111], v[52:55]\n"
    "v_exp_f32 v241, v77\n"
    "v_mfma_f32_16x16x32_bf16 v[52:55], a[188:191], v[112:115], v[52:55]\n"
    "s_waitcnt lgkmcnt(1)\n"
    "v_exp_f32 v242, v78\n"
    "v_mfma_f32_16x16x32_bf16 v[56:59], a[192:195], v[100:103], v[80:83]\n"
    "v_exp_f32 v243, v79\n"
    "v_mfma_f32_16x16x32_bf16 v[56:59], a[196:199], v[104:107], v[56:59]\n"
    "v_cvt_pk_bf16_f32 v44, v8, v9\n"
    "v_cvt_pk_bf16_f32 v45, v10, v11\n"
    "v_mfma_f32_16x16x32_bf16 v[56:59], a[200:203], v[108:111], v[56:59]\n"
    "v_cvt_pk_bf16_f32 v46, v240, v241\n"
    "v_cvt_pk_bf16_f32 v47, v242, v243\n"
    "v_mfma_f32_16x16x32_bf16 v[56:59], a[204:207], v[112:115], v[56:59]\n"
    "v_exp_f32 v8, v48\n"
    "v_mfma_f32_16x16x32_bf16 v[12:15], v[16:19], v[40:43], 0\n"
    "v_exp_f32 v9, v49\n"
    "v_mfma_f32_16x16x32_bf16 v[12:15], v[16:19], v[44:47], v[12:15]\n"
    "v_exp_f32 v10, v50\n"
    "v_mfma_f32_16x16x32_bf16 v[60:63], a[208:211], v[100:103], v[80:83]\n"
    "v_exp_f32 v11, v51\n"
    "v_mfma_f32_16x16x32_bf16 v[60:63], a[212:215], v[104:107], v[60:63]\n"
    "s_add_u32 m0, s34, 32\n"
    "v_exp_f32 v240, v52\n"
    "v_mfma_f32_16x16x32_bf16 v[60:63], a[216:219], v[108:111], v[60:63]\n"
    "buffer_load_dwordx4 v1, s[20:23], s32 offen offset:1024  lds\n"
    "v_mfma_f32_16x16x32_bf16 v[60:63], a[220:223], v[112:115], v[60:63]\n"
    "v_exp_f32 v241, v53\n"
    "v_mfma_f32_16x16x32_bf16 a[96:99], a[224:227], v[32:35], a[96:99]\n"
    "v_cmp_nge_f32 vcc, s40, v12\n"
    "v_mfma_f32_16x16x32_bf16 a[96:99], v[180:183], v[36:39], a[96:99]\n"
    "v_exp_f32 v242, v54\n"
    "v_mfma_f32_16x16x32_bf16 a[100:103], a[228:231], v[32:35], a[100:103]\n"
    "v_exp_f32 v243, v55\n"
    "v_mfma_f32_16x16x32_bf16 a[100:103], v[184:187], v[36:39], a[100:103]\n"
    "s_cbranch_vccnz LTR_14\n"
    "LRET_14:\n"
    "v_add_f32 v24, v24, v12\n"
    "v_mfma_f32_16x16x32_bf16 a[104:107], a[232:235], v[32:35], a[104:107]\n"
    "v_add_u32 v233, 4096, v233\n"
    "v_mfma_f32_16x16x32_bf16 a[104:107], v[188:191], v[36:39], a[104:107]\n"
    "v_mfma_f32_16x16x32_bf16 a[108:111], a[236:239], v[32:35], a[108:111]\n"
    "v_mfma_f32_16x16x32_bf16 a[108:111], v[192:195], v[36:39], a[108:111]\n"
    "v_mfma_f32_16x16x32_bf16 a[112:115], a[240:243], v[32:35], a[112:115]\n"
    "v_mfma_f32_16x16x32_bf16 a[112:115], v[196:199], v[36:39], a[112:115]\n"
    "v_mfma_f32_16x16x32_bf16 a[116:119], a[244:247], v[32:35], a[116:119]\n"
    "v_mfma_f32_16x16x32_bf16 a[116:119], v[200:203], v[36:39], a[116:119]\n"
    "v_mfma_f32_16x16x32_bf16 a[120:123], a[248:251], v[32:35], a[120:123]\n"
    "v_mfma_f32_16x16x32_bf16 a[120:123], v[204:207], v[36:39], a[120:123]\n"
    "v_mfma_f32_16x16x32_bf16 a[124:127], a[252:255], v[32:35], a[124:127]\n"
    "v_cvt_pk_bf16_f32 v32, v8, v9\n"
    "v_cvt_pk_bf16_f32 v33, v10, v11\n"
    "v_mfma_f32_16x16x32_bf16 a[124:127], v[208:211], v[36:39], a[124:127]\n"
    "v_exp_f32 v8, v56\n"
    "v_mfma_f32_16x16x32_bf16 v[64:67], a[160:163], v[116:119], v[84:87]\n"
    "v_exp_f32 v9, v57\n"
    "v_mfma_f32_16x16x32_bf16 v[64:67], a[164:167], v[120:123], v[64:67]\n"
    "v_add_u32 v5, s36, v3\n"
    "v_mfma_f32_16x16x32_bf16 v[64:67], a[168:171], v[124:127], v[64:67]\n"
    "v_exp_f32 v10, v58\n"
    "v_mfma_f32_16x16x32_bf16 v[64:67], a[172:175], v[128:131], v[64:67]\n"
    "v_exp_f32 v11, v59\n"
    "v_mfma_f32_16x16x32_bf16 v[68:71], a[176:179], v[116:119], v[84:87]\n"
    "v_cvt_pk_bf16_f32 v34, v240, v241\n"
    "v_cvt_pk_bf16_f32 v35, v242, v243\n"
    "v_mfma_f32_16x16x32_bf16 v[68:71], a[180:183], v[120:123], v[68:71]\n"
    "v_exp_f32 v240, v60\n"
    "v_mfma_f32_16x16x32_bf16 v[68:71], a[184:187], v[124:127], v[68:71]\n"
    "v_exp_f32 v241, v61\n"
    "v_mfma_f32_16x16x32_bf16 v[68:71], a[188:191], v[128:131], v[68:71]\n"
    "v_exp_f32 v242, v62\n"
    "v_mfma_f32_16x16x32_bf16 v[72:75], a[192:195], v[116:119], v[84:87]\n"
    "v_exp_f32 v243, v63\n"
    "v_mfma_f32_16x16x32_bf16 v[72:75], a[196:199], v[120:123], v[72:75]\n"
    "v_cvt_pk_bf16_f32 v36, v8, v9\n"
    "v_cvt_pk_bf16_f32 v37, v10, v11\n"
    "v_mfma_f32_16x16x32_bf16 v[72:75], a[200:203], v[124:127], v[72:75]\n"
    "v_cvt_pk_bf16_f32 v38, v240, v241\n"
    "v_cvt_pk_bf16_f32 v39, v242, v243\n"
    "v_mfma_f32_16x16x32_bf16 v[72:75], a[204:207], v[128:131], v[72:75]\n"
    "v_exp_f32 v8, v64\n"
    "v_mfma_f32_16x16x32_bf16 v[12:15], v[16:19], v[32:35], 0\n"
    "v_exp_f32 v9, v65\n"
    "v_mfma_f32_16x16x32_bf16 v[12:15], v[16:19], v[36:39], v[12:15]\n"
    "v_exp_f32 v10, v66\n"
    "v_mfma_f32_16x16x32_bf16 v[76:79], a[208:211], v[116:119], v[84:87]\n"
    "v_exp_f32 v11, v67\n"
    "v_mfma_f32_16x16x32_bf16 v[76:79], a[212:215], v[120:123], v[76:79]\n"
    "v_exp_f32 v240, v68\n"
    "v_mfma_f32_16x16x32_bf16 v[76:79], a[216:219], v[124:127], v[76:79]\n"
    "v_exp_f32 v241, v69\n"
    "v_mfma_f32_16x16x32_bf16 v[76:79], a[220:223], v[128:131], v[76:79]\n"
    "v_exp_f32 v242, v70\n"
    "v_mfma_f32_16x16x32_bf16 a[128:131], a[224:227], v[40:43], a[128:131]\n"
    "ds_read_b64_tr_b16 a[224:225], v5 offset:0\n"
    "ds_read_b64_tr_b16 a[226:227], v5 offset:256\n"
    "v_mfma_f32_16x16x32_bf16 a[128:131], v[180:183], v[44:47], a[128:131]\n"
    "v_cmp_nge_f32 vcc, s40, v12\n"
    "v_mfma_f32_16x16x32_bf16 a[132:135], a[228:231], v[40:43], a[132:135]\n"
    "ds_read_b64_tr_b16 v[180:181], v5 offset:512\n"
    "ds_read_b64_tr_b16 v[182:183], v5 offset:768\n"
    "v_mfma_f32_16x16x32_bf16 a[132:135], v[184:187], v[44:47], a[132:135]\n"
    "v_exp_f32 v243, v71\n"
    "v_mfma_f32_16x16x32_bf16 a[136:139], a[232:235], v[40:43], a[136:139]\n"
    "ds_read_b64_tr_b16 a[228:229], v5 offset:32\n"
    "ds_read_b64_tr_b16 a[230:231], v5 offset:288\n"
    "v_mfma_f32_16x16x32_bf16 a[136:139], v[188:191], v[44:47], a[136:139]\n"
    "s_cbranch_vccnz LTR_15\n"
    "LRET_15:\n"
    "v_add_f32 v20, v20, v12\n"
    "v_mfma_f32_16x16x32_bf16 a[140:143], a[236:239], v[40:43], a[140:143]\n"
    "ds_read_b64_tr_b16 v[184:185], v5 offset:544\n"
    "ds_read_b64_tr_b16 v[186:187], v5 offset:800\n"
    "v_mfma_f32_16x16x32_bf16 a[140:143], v[192:195], v[44:47], a[140:143]\n"
    "s_waitcnt lgkmcnt(8)\n"
    "buffer_store_dwordx4 v[216:219], v1, s[28:31], s61 offen offset:1024 nt\n"
    "v_mfma_f32_16x16x32_bf16 a[144:147], a[240:243], v[40:43], a[144:147]\n"
    "ds_read_b64_tr_b16 a[232:233], v5 offset:64\n"
    "ds_read_b64_tr_b16 a[234:235], v5 offset:320\n"
    "v_mfma_f32_16x16x32_bf16 a[144:147], v[196:199], v[44:47], a[144:147]\n"
    "ds_read_b128 v[220:223], v234\n"
    "v_mfma_f32_16x16x32_bf16 a[148:151], a[244:247], v[40:43], a[148:151]\n"
    "ds_read_b64_tr_b16 v[188:189], v5 offset:576\n"
    "ds_read_b64_tr_b16 v[190:191], v5 offset:832\n"
    "v_mfma_f32_16x16x32_bf16 a[148:151], v[200:203], v[44:47], a[148:151]\n"
    "v_add_u32 v234, 4096, v234\n"
    "v_mfma_f32_16x16x32_bf16 a[152:155], a[248:251], v[40:43], a[152:155]\n"
    "ds_read_b64_tr_b16 a[236:237], v5 offset:96\n"
    "ds_read_b64_tr_b16 a[238:239], v5 offset:352\n"
    "v_mfma_f32_16x16x32_bf16 a[152:155], v[204:207], v[44:47], a[152:155]\n"
    "v_mfma_f32_16x16x32_bf16 a[156:159], a[252:255], v[40:43], a[156:159]\n"
    "ds_read_b64_tr_b16 v[192:193], v5 offset:608\n"
    "ds_read_b64_tr_b16 v[194:195], v5 offset:864\n"
    "v_mfma_f32_16x16x32_bf16 a[156:159], v[208:211], v[44:47], a[156:159]\n"
    "v_cvt_pk_bf16_f32 v40, v8, v9\n"
    "v_cvt_pk_bf16_f32 v41, v10, v11\n"
    "v_mfma_f32_16x16x32_bf16 v[48:51], a[160:163], v[132:135], v[88:91]\n"
    "s_add_u32 m0, s34, 64\n"
    "ds_read_b64_tr_b16 a[240:241], v5 offset:128\n"
    "ds_read_b64_tr_b16 a[242:243], v5 offset:384\n"
    "v_mfma_f32_16x16x32_bf16 v[48:51], a[164:167], v[136:139], v[48:51]\n"
    "buffer_load_dwordx4 v1, s[20:23], s32 offen offset:2048  lds\n"
    "v_mfma_f32_16x16x32_bf16 v[48:51], a[168:171], v[140:143], v[48:51]\n"
    "ds_read_b64_tr_b16 v[196:197], v5 offset:640\n"
    "ds_read_b64_tr_b16 v[198:199], v5 offset:896\n"
    "v_mfma_f32_16x16x32_bf16 v[48:51], a[172:175], v[144:147], v[48:51]\n"
    "v_exp_f32 v8, v72\n"
    "v_mfma_f32_16x16x32_bf16 v[52:55], a[176:179], v[132:135], v[88:91]\n"
    "ds_read_b64_tr_b16 a[244:245], v5 offset:160\n"
    "ds_read_b64_tr_b16 a[246:247], v5 offset:416\n"
    "v_mfma_f32_16x16x32_bf16 v[52:55], a[180:183], v[136:139], v[52:55]\n"
    "v_exp_f32 v9, v73\n"
    "v_mfma_f32_16x16x32_bf16 v[52:55], a[184:187], v[140:143], v[52:55]\n"
    "ds_read_b64_tr_b16 v[200:201], v5 offset:672\n"
    "ds_read_b64_tr_b16 v[202:203], v5 offset:928\n"
    "v_mfma_f32_16x16x32_bf16 v[52:55], a[188:191], v[144:147], v[52:55]\n"
    "v_exp_f32 v10, v74\n"
    "v_mfma_f32_16x16x32_bf16 v[56:59], a[192:195], v[132:135], v[88:91]\n"
    "ds_read_b64_tr_b16 a[248:249], v5 offset:192\n"
    "ds_read_b64_tr_b16 a[250:251], v5 offset:448\n"
    "v_mfma_f32_16x16x32_bf16 v[56:59], a[196:199], v[136:139], v[56:59]\n"
    "v_exp_f32 v11, v75\n"
    "v_mfma_f32_16x16x32_bf16 v[56:59], a[200:203], v[140:143], v[56:59]\n"
    "ds_read_b64_tr_b16 v[204:205], v5 offset:704\n"
    "ds_read_b64_tr_b16 v[206:207], v5 offset:960\n"
    "v_mfma_f32_16x16x32_bf16 v[56:59], a[204:207], v[144:147], v[56:59]\n"
    "v_cvt_pk_bf16_f32 v42, v240, v241\n"
    "v_cvt_pk_bf16_f32 v43, v242, v243\n"
    "v_mfma_f32_16x16x32_bf16 v[60:63], a[208:211], v[132:135], v[88:91]\n"
    "ds_read_b64_tr_b16 a[252:253], v5 offset:224\n"
    "ds_read_b64_tr_b16 a[254:255], v5 offset:480\n"
    "v_mfma_f32_16x16x32_bf16 v[60:63], a[212:215], v[136:139], v[60:63]\n"
    "v_exp_f32 v240, v76\n"
    "v_mfma_f32_16x16x32_bf16 v[60:63], a[216:219], v[140:143], v[60:63]\n"
    "ds_read_b64_tr_b16 v[208:209], v5 offset:736\n"
    "ds_read_b64_tr_b16 v[210:211], v5 offset:992\n"
    "v_mfma_f32_16x16x32_bf16 v[60:63], a[220:223], v[144:147], v[60:63]\n"
    "s_waitcnt lgkmcnt(15)\n"
    "v_exp_f32 v241, v77\n"
    "v_mfma_f32_16x16x32_bf16 a[0:3], a[224:227], v[32:35], a[0:3]\n"
    "s_add_u32 m0, s34, 96\n"
    "v_exp_f32 v242, v78\n"
    "v_mfma_f32_16x16x32_bf16 a[0:3], v[180:183], v[36:39], a[0:3]\n"
    "buffer_load_dwordx4 v1, s[20:23], s32 offen offset:3072  lds\n"
    "v_mfma_f32_16x16x32_bf16 a[4:7], a[228:231], v[32:35], a[4:7]\n"
    "v_exp_f32 v243, v79\n"
    "v_mfma_f32_16x16x32_bf16 a[4:7], v[184:187], v[36:39], a[4:7]\n"
    "v_cvt_pk_bf16_f32 v44, v8, v9\n"
    "v_cvt_pk_bf16_f32 v45, v10, v11\n"
    "v_mfma_f32_16x16x32_bf16 a[8:11], a[232:235], v[32:35], a[8:11]\n"
    "s_add_u32 m0, s34, 16896\n"
    "v_cvt_pk_bf16_f32 v46, v240, v241\n"
    "v_cvt_pk_bf16_f32 v47, v242, v243\n"
    "v_mfma_f32_16x16x32_bf16 a[8:11], v[188:191], v[36:39], a[8:11]\n"
    "buffer_load_dwordx4 v1, s[24:27], s32 offen offset:0  lds\n"
    "v_mfma_f32_16x16x32_bf16 v[12:15], v[16:19], v[40:43], 0\n"
    "v_exp_f32 v8, v48\n"
    "v_mfma_f32_16x16x32_bf16 v[12:15], v[16:19], v[44:47], v[12:15]\n"
    "v_exp_f32 v9, v49\n"
    "v_mfma_f32_16x16x32_bf16 a[12:15], a[236:239], v[32:35], a[12:15]\n"
    "v_exp_f32 v10, v50\n"
    "v_mfma_f32_16x16x32_bf16 a[12:15], v[192:195], v[36:39], a[12:15]\n"
    "s_waitcnt lgkmcnt(0)\n"
    "v_exp_f32 v11, v51\n"
    "v_mfma_f32_16x16x32_bf16 a[16:19], a[240:243], v[32:35], a[16:19]\n"
    "v_exp_f32 v240, v52\n"
    "v_mfma_f32_16x16x32_bf16 a[16:19], v[196:199], v[36:39], a[16:19]\n"
    "v_exp_f32 v241, v53\n"
    "v_mfma_f32_16x16x32_bf16 a[20:23], a[244:247], v[32:35], a[20:23]\n"
    "v_cmp_nge_f32 vcc, s40, v12\n"
    "v_mfma_f32_16x16x32_bf16 a[20:23], v[200:203], v[36:39], a[20:23]\n"
    "v_exp_f32 v242, v54\n"
    "v_mfma_f32_16x16x32_bf16 a[24:27], a[248:251], v[32:35], a[24:27]\n"
    "v_exp_f32 v243, v55\n"
    "v_mfma_f32_16x16x32_bf16 a[24:27], v[204:207], v[36:39], a[24:27]\n"
    "s_cbranch_vccnz LTR_16\n"
    "LRET_16:\n"
    "v_add_f32 v21, v21, v12\n"
    "v_mfma_f32_16x16x32_bf16 a[28:31], a[252:255], v[32:35], a[28:31]\n"
    "v_cvt_pk_bf16_f32 v32, v8, v9\n"
    "v_cvt_pk_bf16_f32 v33, v10, v11\n"
    "v_mfma_f32_16x16x32_bf16 a[28:31], v[208:211], v[36:39], a[28:31]\n"
    "v_exp_f32 v8, v56\n"
    "v_mfma_f32_16x16x32_bf16 v[64:67], a[160:163], v[148:151], v[92:95]\n"
    "s_add_u32 m0, s34, 16928\n"
    "v_exp_f32 v9, v57\n"
    "v_mfma_f32_16x16x32_bf16 v[64:67], a[164:167], v[152:155], v[64:67]\n"
    "buffer_load_dwordx4 v1, s[24:27], s32 offen offset:1024  lds\n"
    "v_mfma_f32_16x16x32_bf16 v[64:67], a[168:171], v[156:159], v[64:67]\n"
    "v_exp_f32 v10, v58\n"
    "v_mfma_f32_16x16x32_bf16 v[64:67], a[172:175], v[160:163], v[64:67]\n"
    "v_exp_f32 v11, v59\n"
    "v_mfma_f32_16x16x32_bf16 v[68:71], a[176:179], v[148:151], v[92:95]\n"
    "v_cvt_pk_bf16_f32 v34, v240, v241\n"
    "v_cvt_pk_bf16_f32 v35, v242, v243\n"
    "v_mfma_f32_16x16x32_bf16 v[68:71], a[180:183], v[152:155], v[68:71]\n"
    "v_exp_f32 v240, v60\n"
    "v_mfma_f32_16x16x32_bf16 v[68:71], a[184:187], v[156:159], v[68:71]\n"
    "s_add_u32 m0, s34, 16960\n"
    "v_exp_f32 v241, v61\n"
    "v_mfma_f32_16x16x32_bf16 v[68:71], a[188:191], v[160:163], v[68:71]\n"
    "buffer_load_dwordx4 v1, s[24:27], s32 offen offset:2048  lds\n"
    "v_mfma_f32_16x16x32_bf16 v[72:75], a[192:195], v[148:151], v[92:95]\n"
    "v_exp_f32 v242, v62\n"
    "v_mfma_f32_16x16x32_bf16 v[72:75], a[196:199], v[152:155], v[72:75]\n"
    "v_exp_f32 v243, v63\n"
    "v_mfma_f32_16x16x32_bf16 v[72:75], a[200:203], v[156:159], v[72:75]\n"
    "v_cvt_pk_bf16_f32 v36, v8, v9\n"
    "v_cvt_pk_bf16_f32 v37, v10, v11\n"
    "v_mfma_f32_16x16x32_bf16 v[72:75], a[204:207], v[160:163], v[72:75]\n"
    "v_cvt_pk_bf16_f32 v38, v240, v241\n"
    "v_cvt_pk_bf16_f32 v39, v242, v243\n"
    "v_mfma_f32_16x16x32_bf16 v[12:15], v[16:19], v[32:35], 0\n"
    "v_exp_f32 v8, v64\n"
    "v_mfma_f32_16x16x32_bf16 v[12:15], v[16:19], v[36:39], v[12:15]\n"
    "v_exp_f32 v9, v65\n"
    "v_mfma_f32_16x16x32_bf16 v[76:79], a[208:211], v[148:151], v[92:95]\n"
    "s_add_u32 m0, s34, 16992\n"
    "v_exp_f32 v10, v66\n"
    "v_mfma_f32_16x16x32_bf16 v[76:79], a[212:215], v[152:155], v[76:79]\n"
    "buffer_load_dwordx4 v1, s[24:27], s32 offen offset:3072  lds\n"
    "v_mfma_f32_16x16x32_bf16 v[76:79], a[216:219], v[156:159], v[76:79]\n"
    "v_exp_f32 v11, v67\n"
    "v_mfma_f32_16x16x32_bf16 v[76:79], a[220:223], v[160:163], v[76:79]\n"
    "v_exp_f32 v240, v68\n"
    "v_mfma_f32_16x16x32_bf16 a[32:35], a[224:227], v[40:43], a[32:35]\n"
    "v_cmp_nge_f32 vcc, s40, v12\n"
    "v_mfma_f32_16x16x32_bf16 a[32:35], v[180:183], v[44:47], a[32:35]\n"
    "v_exp_f32 v241, v69\n"
    "v_mfma_f32_16x16x32_bf16 a[36:39], a[228:231], v[40:43], a[36:39]\n"
    "v_exp_f32 v242, v70\n"
    "v_mfma_f32_16x16x32_bf16 a[36:39], v[184:187], v[44:47], a[36:39]\n"
    "s_cbranch_vccnz LTR_17\n"
    "LRET_17:\n"
    "v_add_f32 v22, v22, v12\n"
    "v_mfma_f32_16x16x32_bf16 a[40:43], a[232:235], v[40:43], a[40:43]\n"
    "v_exp_f32 v243, v71\n"
    "v_mfma_f32_16x16x32_bf16 a[40:43], v[188:191], v[44:47], a[40:43]\n"
    "s_waitcnt lgkmcnt(15)\n"
    "buffer_store_dwordx4 v[220:223], v1, s[28:31], s61 offen offset:2048 nt\n"
    "v_mfma_f32_16x16x32_bf16 a[44:47], a[236:239], v[40:43], a[44:47]\n"
    "ds_read_b128 v[224:227], v235\n"
    "v_mfma_f32_16x16x32_bf16 a[44:47], v[192:195], v[44:47], a[44:47]\n"
    "v_mfma_f32_16x16x32_bf16 a[48:51], a[240:243], v[40:43], a[48:51]\n"
    "s_add_u32 s34, s36, s35\n"
    "v_add_u32 v235, 4096, v235\n"
    "v_mfma_f32_16x16x32_bf16 a[48:51], v[196:199], v[44:47], a[48:51]\n"
    "v_mfma_f32_16x16x32_bf16 a[52:55], a[244:247], v[40:43], a[52:55]\n"
    "s_add_u32 s36, s36, 33792\n"
    "v_mfma_f32_16x16x32_bf16 a[52:55], v[200:203], v[44:47], a[52:55]\n"
    "v_mfma_f32_16x16x32_bf16 a[56:59], a[248:251], v[40:43], a[56:59]\n"
    "s_sub_u32 s50, s36, 101376\n"
    "v_mfma_f32_16x16x32_bf16 a[56:59], v[204:207], v[44:47], a[56:59]\n"
    "v_mfma_f32_16x16x32_bf16 a[60:63], a[252:255], v[40:43], a[60:63]\n"
    "s_min_u32 s36, s36, s50\n"
    "v_cvt_pk_bf16_f32 v40, v8, v9\n"
    "v_cvt_pk_bf16_f32 v41, v10, v11\n"
    "v_mfma_f32_16x16x32_bf16 a[60:63], v[208:211], v[44:47], a[60:63]\n"
    "s_add_u32 s32, s32, 16384\n"
    "v_exp_f32 v8, v72\n"
    "v_mfma_f32_16x16x32_bf16 v[48:51], a[160:163], v[164:167], v[96:99]\n"
    "v_add_u32 v4, s36, v2\n"
    "v_mfma_f32_16x16x32_bf16 v[48:51], a[164:167], v[168:171], v[48:51]\n"
    "s_waitcnt vmcnt(11)\n"
    "s_barrier\n"
    "v_mfma_f32_16x16x32_bf16 v[48:51], a[168:171], v[172:175], v[48:51]\n"
    "v_exp_f32 v9, v73\n"
    "v_mfma_f32_16x16x32_bf16 v[48:51], a[172:175], v[176:179], v[48:51]\n"
    "v_exp_f32 v10, v74\n"
    "v_mfma_f32_16x16x32_bf16 v[52:55], a[176:179], v[164:167], v[96:99]\n"
    "ds_read_b128 a[160:163], v4 offset:0\n"
    "ds_read_b128 a[164:167], v4 offset:64\n"
    "v_mfma_f32_16x16x32_bf16 v[52:55], a[180:183], v[168:171], v[52:55]\n"
    "v_exp_f32 v11, v75\n"
    "v_mfma_f32_16x16x32_bf16 v[52:55], a[184:187], v[172:175], v[52:55]\n"
    "v_cvt_pk_bf16_f32 v42, v240, v241\n"
    "v_cvt_pk_bf16_f32 v43, v242, v243\n"
    "v_mfma_f32_16x16x32_bf16 v[52:55], a[188:191], v[176:179], v[52:55]\n"
    "ds_read_b128 a[168:171], v4 offset:128\n"
    "ds_read_b128 a[172:175], v4 offset:192\n"
    "v_mfma_f32_16x16x32_bf16 v[56:59], a[192:195], v[164:167], v[96:99]\n"
    "v_exp_f32 v240, v76\n"
    "v_mfma_f32_16x16x32_bf16 v[56:59], a[196:199], v[168:171], v[56:59]\n"
    "v_exp_f32 v241, v77\n"
    "v_mfma_f32_16x16x32_bf16 v[56:59], a[200:203], v[172:175], v[56:59]\n"
    "ds_read_b128 a[176:179], v4 offset:256\n"
    "ds_read_b128 a[180:183], v4 offset:320\n"
    "v_mfma_f32_16x16x32_bf16 v[56:59], a[204:207], v[176:179], v[56:59]\n"
    "v_exp_f32 v242, v78\n"
    "v_mfma_f32_16x16x32_bf16 v[60:63], a[208:211], v[164:167], v[96:99]\n"
    "v_exp_f32 v243, v79\n"
    "v_mfma_f32_16x16x32_bf16 v[60:63], a[212:215], v[168:171], v[60:63]\n"
    "ds_read_b128 a[184:187], v4 offset:384\n"
    "ds_read_b128 a[188:191], v4 offset:448\n"
    "v_mfma_f32_16x16x32_bf16 v[60:63], a[216:219], v[172:175], v[60:63]\n"
    "v_cvt_pk_bf16_f32 v44, v8, v9\n"
    "v_cvt_pk_bf16_f32 v45, v10, v11\n"
    "v_mfma_f32_16x16x32_bf16 v[60:63], a[220:223], v[176:179], v[60:63]\n"
    "v_cvt_pk_bf16_f32 v46, v240, v241\n"
    "v_cvt_pk_bf16_f32 v47, v242, v243\n"
    "v_mfma_f32_16x16x32_bf16 v[12:15], v[16:19], v[40:43], 0\n"
    "ds_read_b128 a[192:195], v4 offset:512\n"
    "ds_read_b128 a[196:199], v4 offset:576\n"
    "v_mfma_f32_16x16x32_bf16 v[12:15], v[16:19], v[44:47], v[12:15]\n"
    "v_exp_f32 v8, v48\n"
    "v_mfma_f32_16x16x32_bf16 a[64:67], a[224:227], v[32:35], a[64:67]\n"
    "ds_read_b128 a[200:203], v4 offset:640\n"
    "ds_read_b128 a[204:207], v4 offset:704\n"
    "v_mfma_f32_16x16x32_bf16 a[64:67], v[180:183], v[36:39], a[64:67]\n"
    "v_exp_f32 v9, v49\n"
    "v_mfma_f32_16x16x32_bf16 a[68:71], a[228:231], v[32:35], a[68:71]\n"
    "ds_read_b128 a[208:211], v4 offset:768\n"
    "ds_read_b128 a[212:215], v4 offset:832\n"
    "v_mfma_f32_16x16x32_bf16 a[68:71], v[184:187], v[36:39], a[68:71]\n"
    "v_exp_f32 v10, v50\n"
    "v_mfma_f32_16x16x32_bf16 a[72:75], a[232:235], v[32:35], a[72:75]\n"
    "v_cmp_nge_f32 vcc, s40, v12\n"
    "v_mfma_f32_16x16x32_bf16 a[72:75], v[188:191], v[36:39], a[72:75]\n"
    "ds_read_b128 a[216:219], v4 offset:896\n"
    "ds_read_b128 a[220:223], v4 offset:960\n"
    "v_mfma_f32_16x16x32_bf16 a[76:79], a[236:239], v[32:35], a[76:79]\n"
    "v_exp_f32 v11, v51\n"
    "v_mfma_f32_16x16x32_bf16 a[76:79], v[192:195], v[36:39], a[76:79]\n"
    "s_cbranch_vccnz LTR_18\n"
    "LRET_18:\n"
    "v_add_f32 v23, v23, v12\n"
    "v_mfma_f32_16x16x32_bf16 a[80:83], a[240:243], v[32:35], a[80:83]\n"
    "v_exp_f32 v240, v52\n"
    "v_mfma_f32_16x16x32_bf16 a[80:83], v[196:199], v[36:39], a[80:83]\n"
    "v_exp_f32 v241, v53\n"
    "v_mfma_f32_16x16x32_bf16 a[84:87], a[244:247], v[32:35], a[84:87]\n"
    "v_exp_f32 v242, v54\n"
    "v_mfma_f32_16x16x32_bf16 a[84:87], v[200:203], v[36:39], a[84:87]\n"
    "v_exp_f32 v243, v55\n"
    "v_mfma_f32_16x16x32_bf16 a[88:91], a[248:251], v[32:35], a[88:91]\n"
    "s_waitcnt lgkmcnt(15)\n"
    "buffer_store_dwordx4 v[224:227], v1, s[28:31], s61 offen offset:3072 nt\n"
    "v_mfma_f32_16x16x32_bf16 a[88:91], v[204:207], v[36:39], a[88:91]\n"
    "s_add_u32 s61, s61, 4096\n"
    "v_mfma_f32_16x16x32_bf16 a[92:95], a[252:255], v[32:35], a[92:95]\n"
    "v_cvt_pk_bf16_f32 v32, v8, v9\n"
    "v_cvt_pk_bf16_f32 v33, v10, v11\n"
    "v_mfma_f32_16x16x32_bf16 a[92:95], v[208:211], v[36:39], a[92:95]\n"
    "s_waitcnt lgkmcnt(8)\n"
    "v_exp_f32 v8, v56\n"
    "v_mfma_f32_16x16x32_bf16 v[64:67], a[160:163], v[100:103], v[80:83]\n"
    "v_exp_f32 v9, v57\n"
    "v_mfma_f32_16x16x32_bf16 v[64:67], a[164:167], v[104:107], v[64:67]\n"
    "s_add_u32 m0, s34, 0\n"
    "v_exp_f32 v10, v58\n"
    "v_mfma_f32_16x16x32_bf16 v[64:67], a[168:171], v[108:111], v[64:67]\n"
    "buffer_load_dwordx4 v1, s[20:23], s32 offen offset:0  lds\n"
    "v_mfma_f32_16x16x32_bf16 v[64:67], a[172:175], v[112:115], v[64:67]\n"
    "v_exp_f32 v11, v59\n"
    "v_mfma_f32_16x16x32_bf16 v[68:71], a[176:179], v[100:103], v[80:83]\n"
    "v_cvt_pk_bf16_f32 v34, v240, v241\n"
    "v_cvt_pk_bf16_f32 v35, v242, v243\n"
    "v_mfma_f32_16x16x32_bf16 v[68:71], a[180:183], v[104:107], v[68:71]\n"
    "v_exp_f32 v240, v60\n"
    "v_mfma_f32_16x16x32_bf16 v[68:71], a[184:187], v[108:111], v[68:71]\n"
    "v_exp_f32 v241, v61\n"
    "v_mfma_f32_16x16x32_bf16 v[68:71], a[188:191], v[112:115], v[68:71]\n"
    "s_waitcnt lgkmcnt(0)\n"
    "v_exp_f32 v242, v62\n"
    "v_mfma_f32_16x16x32_bf16 v[72:75], a[192:195], v[100:103], v[80:83]\n"
    "v_exp_f32 v243, v63\n"
    "v_mfma_f32_16x16x32_bf16 v[72:75], a[196:199], v[104:107], v[72:75]\n"
    "v_cvt_pk_bf16_f32 v36, v8, v9\n"
    "v_cvt_pk_bf16_f32 v37, v10, v11\n"
    "v_mfma_f32_16x16x32_bf16 v[72:75], a[200:203], v[108:111], v[72:75]\n"
    "v_cvt_pk_bf16_f32 v38, v240, v241\n"
    "v_cvt_pk_bf16_f32 v39, v242, v243\n"
    "v_mfma_f32_16x16x32_bf16 v[72:75], a[204:207], v[112:115], v[72:75]\n"
    "v_exp_f32 v8, v64\n"
    "v_mfma_f32_16x16x32_bf16 v[12:15], v[16:19], v[32:35], 0\n"
    "v_exp_f32 v9, v65\n"
    "v_mfma_f32_16x16x32_bf16 v[12:15], v[16:19], v[36:39], v[12:15]\n"
    "v_exp_f32 v10, v66\n"
    "v_mfma_f32_16x16x32_bf16 v[76:79], a[208:211], v[100:103], v[80:83]\n"
    "v_exp_f32 v11, v67\n"
    "v_mfma_f32_16x16x32_bf16 v[76:79], a[212:215], v[104:107], v[76:79]\n"
    "s_add_u32 m0, s34, 32\n"
    "v_exp_f32 v240, v68\n"
    "v_mfma_f32_16x16x32_bf16 v[76:79], a[216:219], v[108:111], v[76:79]\n"
    "buffer_load_dwordx4 v1, s[20:23], s32 offen offset:1024  lds\n"
    "v_mfma_f32_16x16x32_bf16 v[76:79], a[220:223], v[112:115], v[76:79]\n"
    "v_exp_f32 v241, v69\n"
    "v_mfma_f32_16x16x32_bf16 a[96:99], a[224:227], v[40:43], a[96:99]\n"
    "v_cmp_nge_f32 vcc, s40, v12\n"
    "v_mfma_f32_16x16x32_bf16 a[96:99], v[180:183], v[44:47], a[96:99]\n"
    "v_exp_f32 v242, v70\n"
    "v_mfma_f32_16x16x32_bf16 a[100:103], a[228:231], v[40:43], a[100:103]\n"
    "v_mfma_f32_16x16x32_bf16 a[100:103], v[184:187], v[44:47], a[100:103]\n"
    "s_cbranch_vccnz LTR_19\n"
    "LRET_19:\n"
    "v_add_f32 v24, v24, v12\n"
    "v_mfma_f32_16x16x32_bf16 a[104:107], a[232:235], v[40:43], a[104:107]\n"
    "v_mfma_f32_16x16x32_bf16 a[104:107], v[188:191], v[44:47], a[104:107]\n"
    "v_mfma_f32_16x16x32_bf16 a[108:111], a[236:239], v[40:43], a[108:111]\n"
    "v_mfma_f32_16x16x32_bf16 a[108:111], v[192:195], v[44:47], a[108:111]\n"
    "v_mfma_f32_16x16x32_bf16 a[112:115], a[240:243], v[40:43], a[112:115]\n"
    "v_mfma_f32_16x16x32_bf16 a[112:115], v[196:199], v[44:47], a[112:115]\n"
    "v_mfma_f32_16x16x32_bf16 a[116:119], a[244:247], v[40:43], a[116:119]\n"
    "v_mfma_f32_16x16x32_bf16 a[116:119], v[200:203], v[44:47], a[116:119]\n"
    "v_mfma_f32_16x16x32_bf16 a[120:123], a[248:251], v[40:43], a[120:123]\n"
    "v_mfma_f32_16x16x32_bf16 a[120:123], v[204:207], v[44:47], a[120:123]\n"
    "v_mfma_f32_16x16x32_bf16 a[124:127], a[252:255], v[40:43], a[124:127]\n"
    "v_mfma_f32_16x16x32_bf16 a[124:127], v[208:211], v[44:47], a[124:127]\n"
    "s_sub_u32 s37, s37, 1\n"
    "s_cmp_lg_u32 s37, 0\n"
    "s_cbranch_scc1 LBH5\n"
    "s_mov_b32 s37, 3\n"
    "LLOOP5:\n"
    "v_mfma_f32_16x16x32_bf16 v[48:51], a[160:163], v[116:119], v[84:87]\n"
    "v_exp_f32 v243, v71\n"
    "v_mfma_f32_16x16x32_bf16 v[48:51], a[164:167], v[120:123], v[48:51]\n"
    "v_cvt_pk_bf16_f32 v40, v8, v9\n"
    "v_cvt_pk_bf16_f32 v41, v10, v11\n"
    "v_mfma_f32_16x16x32_bf16 v[48:51], a[168:171], v[124:127], v[48:51]\n"
    "v_exp_f32 v8, v72\n"
    "v_mfma_f32_16x16x32_bf16 v[48:51], a[172:175], v[128:131], v[48:51]\n"
    "v_exp_f32 v9, v73\n"
    "v_mfma_f32_16x16x32_bf16 v[52:55], a[176:179], v[116:119], v[84:87]\n"
    "v_add_u32 v5, s36, v3\n"
    "v_mfma_f32_16x16x32_bf16 v[52:55], a[180:183], v[120:123], v[52:55]\n"
    "v_exp_f32 v10, v74\n"
    "v_mfma_f32_16x16x32_bf16 v[52:55], a[184:187], v[124:127], v[52:55]\n"
    "v_exp_f32 v11, v75\n"
    "v_mfma_f32_16x16x32_bf16 v[52:55], a[188:191], v[128:131], v[52:55]\n"
    "v_cvt_pk_bf16_f32 v42, v240, v241\n"
    "v_cvt_pk_bf16_f32 v43, v242, v243\n"
    "v_mfma_f32_16x16x32_bf16 v[56:59], a[192:195], v[116:119], v[84:87]\n"
    "v_exp_f32 v240, v76\n"
    "v_mfma_f32_16x16x32_bf16 v[56:59], a[196:199], v[120:123], v[56:59]\n"
    "v_exp_f32 v241, v77\n"
    "v_mfma_f32_16x16x32_bf16 v[56:59], a[200:203], v[124:127], v[56:59]\n"
    "v_exp_f32 v242, v78\n"
    "v_mfma_f32_16x16x32_bf16 v[56:59], a[204:207], v[128:131], v[56:59]\n"
    "v_exp_f32 v243, v79\n"
    "v_mfma_f32_16x16x32_bf16 v[60:63], a[208:211], v[116:119], v[84:87]\n"
    "v_cvt_pk_bf16_f32 v44, v8, v9\n"
    "v_cvt_pk_bf16_f32 v45, v10, v11\n"
    "v_mfma_f32_16x16x32_bf16 v[60:63], a[212:215], v[120:123], v[60:63]\n"
    "v_cvt_pk_bf16_f32 v46, v240, v241\n"
    "v_cvt_pk_bf16_f32 v47, v242, v243\n"
    "v_mfma_f32_16x16x32_bf16 v[60:63], a[216:219], v[124:127], v[60:63]\n"
    "v_exp_f32 v8, v48\n"
    "v_mfma_f32_16x16x32_bf16 v[60:63], a[220:223], v[128:131], v[60:63]\n"
    "v_exp_f32 v9, v49\n"
    "v_mfma_f32_16x16x32_bf16 v[12:15], v[16:19], v[40:43], 0\n"
    "v_exp_f32 v10, v50\n"
    "v_mfma_f32_16x16x32_bf16 v[12:15], v[16:19], v[44:47], v[12:15]\n"
    "v_exp_f32 v11, v51\n"
    "v_mfma_f32_16x16x32_bf16 a[128:131], a[224:227], v[32:35], a[128:131]\n"
    "ds_read_b64_tr_b16 a[224:225], v5 offset:0\n"
    "ds_read_b64_tr_b16 a[226:227], v5 offset:256\n"
    "v_mfma_f32_16x16x32_bf16 a[128:131], v[180:183], v[36:39], a[128:131]\n"
    "v_exp_f32 v240, v52\n"
    "v_mfma_f32_16x16x32_bf16 a[132:135], a[228:231], v[32:35], a[132:135]\n"
    "ds_read_b64_tr_b16 v[180:181], v5 offset:512\n"
    "ds_read_b64_tr_b16 v[182:183], v5 offset:768\n"
    "v_mfma_f32_16x16x32_bf16 a[132:135], v[184:187], v[36:39], a[132:135]\n"
    "v_exp_f32 v241, v53\n"
    "v_mfma_f32_16x16x32_bf16 a[136:139], a[232:235], v[32:35], a[136:139]\n"
    "ds_read_b64_tr_b16 a[228:229], v5 offset:32\n"
    "ds_read_b64_tr_b16 a[230:231], v5 offset:288\n"
    "v_mfma_f32_16x16x32_bf16 a[136:139], v[188:191], v[36:39], a[136:139]\n"
    "v_cmp_nge_f32 vcc, s40, v12\n"
    "v_mfma_f32_16x16x32_bf16 a[140:143], a[236:239], v[32:35], a[140:143]\n"
    "ds_read_b64_tr_b16 v[184:185], v5 offset:544\n"
    "ds_read_b64_tr_b16 v[186:187], v5 offset:800\n"
    "v_mfma_f32_16x16x32_bf16 a[140:143], v[192:195], v[36:39], a[140:143]\n"
    "v_exp_f32 v242, v54\n"
    "v_mfma_f32_16x16x32_bf16 a[144:147], a[240:243], v[32:35], a[144:147]\n"
    "ds_read_b64_tr_b16 a[232:233], v5 offset:64\n"
    "ds_read_b64_tr_b16 a[234:235], v5 offset:320\n"
    "v_mfma_f32_16x16x32_bf16 a[144:147], v[196:199], v[36:39], a[144:147]\n"
    "s_cbranch_vccnz LTR_20\n"
    "LRET_20:\n"
    "v_add_f32 v20, v20, v12\n"
    "v_mfma_f32_16x16x32_bf16 a[148:151], a[244:247], v[32:35], a[148:151]\n"
    "ds_read_b64_tr_b16 v[188:189], v5 offset:576\n"
    "ds_read_b64_tr_b16 v[190:191], v5 offset:832\n"
    "v_mfma_f32_16x16x32_bf16 a[148:151], v[200:203], v[36:39], a[148:151]\n"
    "v_exp_f32 v243, v55\n"
    "v_mfma_f32_16x16x32_bf16 a[152:155], a[248:251], v[32:35], a[152:155]\n"
    "ds_read_b64_tr_b16 a[236:237], v5 offset:96\n"
    "ds_read_b64_tr_b16 a[238:239], v5 offset:352\n"
    "v_mfma_f32_16x16x32_bf16 a[152:155], v[204:207], v[36:39], a[152:155]\n"
    "v_mfma_f32_16x16x32_bf16 a[156:159], a[252:255], v[32:35], a[156:159]\n"
    "ds_read_b64_tr_b16 v[192:193], v5 offset:608\n"
    "ds_read_b64_tr_b16 v[194:195], v5 offset:864\n"
    "v_mfma_f32_16x16x32_bf16 a[156:159], v[208:211], v[36:39], a[156:159]\n"
    "v_cvt_pk_bf16_f32 v32, v8, v9\n"
    "v_cvt_pk_bf16_f32 v33, v10, v11\n"
    "v_mfma_f32_16x16x32_bf16 v[64:67], a[160:163], v[132:135], v[88:91]\n"
    "s_add_u32 m0, s34, 64\n"
    "ds_read_b64_tr_b16 a[240:241], v5 offset:128\n"
    "ds_read_b64_tr_b16 a[242:243], v5 offset:384\n"
    "v_mfma_f32_16x16x32_bf16 v[64:67], a[164:167], v[136:139], v[64:67]\n"
    "buffer_load_dwordx4 v1, s[20:23], s32 offen offset:2048  lds\n"
    "v_mfma_f32_16x16x32_bf16 v[64:67], a[168:171], v[140:143], v[64:67]\n"
    "ds_read_b64_tr_b16 v[196:197], v5 offset:640\n"
    "ds_read_b64_tr_b16 v[198:199], v5 offset:896\n"
    "v_mfma_f32_16x16x32_bf16 v[64:67], a[172:175], v[144:147], v[64:67]\n"
    "v_exp_f32 v8, v56\n"
    "v_mfma_f32_16x16x32_bf16 v[68:71], a[176:179], v[132:135], v[88:91]\n"
    "ds_read_b64_tr_b16 a[244:245], v5 offset:160\n"
    "ds_read_b64_tr_b16 a[246:247], v5 offset:416\n"
    "v_mfma_f32_16x16x32_bf16 v[68:71], a[180:183], v[136:139], v[68:71]\n"
    "v_exp_f32 v9, v57\n"
    "v_mfma_f32_16x16x32_bf16 v[68:71], a[184:187], v[140:143], v[68:71]\n"
    "ds_read_b64_tr_b16 v[200:201], v5 offset:672\n"
    "ds_read_b64_tr_b16 v[202:203], v5 offset:928\n"
    "v_mfma_f32_16x16x32_bf16 v[68:71], a[188:191], v[144:147], v[68:71]\n"
    "v_exp_f32 v10, v58\n"
    "v_mfma_f32_16x16x32_bf16 v[72:75], a[192:195], v[132:135], v[88:91]\n"
    "ds_read_b64_tr_b16 a[248:249], v5 offset:192\n"
    "ds_read_b64_tr_b16 a[250:251], v5 offset:448\n"
    "v_mfma_f32_16x16x32_bf16 v[72:75], a[196:199], v[136:139], v[72:75]\n"
    "v_exp_f32 v11, v59\n"
    "v_mfma_f32_16x16x32_bf16 v[72:75], a[200:203], v[140:143], v[72:75]\n"
    "ds_read_b64_tr_b16 v[204:205], v5 offset:704\n"
    "ds_read_b64_tr_b16 v[206:207], v5 offset:960\n"
    "v_mfma_f32_16x16x32_bf16 v[72:75], a[204:207], v[144:147], v[72:75]\n"
    "v_cvt_pk_bf16_f32 v34, v240, v241\n"
    "v_cvt_pk_bf16_f32 v35, v242, v243\n"
    "v_mfma_f32_16x16x32_bf16 v[76:79], a[208:211], v[132:135], v[88:91]\n"
    "ds_read_b64_tr_b16 a[252:253], v5 offset:224\n"
    "ds_read_b64_tr_b16 a[254:255], v5 offset:480\n"
    "v_mfma_f32_16x16x32_bf16 v[76:79], a[212:215], v[136:139], v[76:79]\n"
    "v_exp_f32 v240, v60\n"
    "v_mfma_f32_16x16x32_bf16 v[76:79], a[216:219], v[140:143], v[76:79]\n"
    "ds_read_b64_tr_b16 v[208:209], v5 offset:736\n"
    "ds_read_b64_tr_b16 v[210:211], v5 offset:992\n"
    "v_mfma_f32_16x16x32_bf16 v[76:79], a[220:223], v[144:147], v[76:79]\n"
    "s_waitcnt lgkmcnt(15)\n"
    "v_exp_f32 v241, v61\n"
    "v_mfma_f32_16x16x32_bf16 a[0:3], a[224:227], v[40:43], a[0:3]\n"
    "s_add_u32 m0, s34, 96\n"
    "v_exp_f32 v242, v62\n"
    "v_mfma_f32_16x16x32_bf16 a[0:3], v[180:183], v[44:47], a[0:3]\n"
    "buffer_load_dwordx4 v1, s[20:23], s32 offen offset:3072  lds\n"
    "v_mfma_f32_16x16x32_bf16 a[4:7], a[228:231], v[40:43], a[4:7]\n"
    "v_exp_f32 v243, v63\n"
    "v_mfma_f32_16x16x32_bf16 a[4:7], v[184:187], v[44:47], a[4:7]\n"
    "v_cvt_pk_bf16_f32 v36, v8, v9\n"
    "v_cvt_pk_bf16_f32 v37, v10, v11\n"
    "v_mfma_f32_16x16x32_bf16 a[8:11], a[232:235], v[40:43], a[8:11]\n"
    "s_add_u32 m0, s34, 16896\n"
    "v_cvt_pk_bf16_f32 v38, v240, v241\n"
    "v_cvt_pk_bf16_f32 v39, v242, v243\n"
    "v_mfma_f32_16x16x32_bf16 a[8:11], v[188:191], v[44:47], a[8:11]\n"
    "buffer_load_dwordx4 v1, s[24:27], s32 offen offset:0  lds\n"
    "v_mfma_f32_16x16x32_bf16 v[12:15], v[16:19], v[32:35], 0\n"
    "v_exp_f32 v8, v64\n"
    "v_mfma_f32_16x16x32_bf16 v[12:15], v[16:19], v[36:39], v[12:15]\n"
    "v_exp_f32 v9, v65\n"
    "v_mfma_f32_16x16x32_bf16 a[12:15], a[236:239], v[40:43], a[12:15]\n"
    "v_exp_f32 v10, v66\n"
    "v_mfma_f32_16x16x32_bf16 a[12:15], v[192:195], v[44:47], a[12:15]\n"
    "s_waitcnt lgkmcnt(0)\n"
    "v_exp_f32 v11, v67\n"
    "v_mfma_f32_16x16x32_bf16 a[16:19], a[240:243], v[40:43], a[16:19]\n"
    "v_exp_f32 v240, v68\n"
    "v_mfma_f32_16x16x32_bf16 a[16:19], v[196:199], v[44:47], a[16:19]\n"
    "v_exp_f32 v241, v69\n"
    "v_mfma_f32_16x16x32_bf16 a[20:23], a[244:247], v[40:43], a[20:23]\n"
    "v_cmp_nge_f32 vcc, s40, v12\n"
    "v_mfma_f32_16x16x32_bf16 a[20:23], v[200:203], v[44:47], a[20:23]\n"
    "v_exp_f32 v242, v70\n"
    "v_mfma_f32_16x16x32_bf16 a[24:27], a[248:251], v[40:43], a[24:27]\n"
    "v_exp_f32 v243, v71\n"
    "v_mfma_f32_16x16x32_bf16 a[24:27], v[204:207], v[44:47], a[24:27]\n"
    "s_cbranch_vccnz LTR_21\n"
    "LRET_21:\n"
    "v_add_f32 v21, v21, v12\n"
    "v_mfma_f32_16x16x32_bf16 a[28:31], a[252:255], v[40:43], a[28:31]\n"
    "v_cvt_pk_bf16_f32 v40, v8, v9\n"
    "v_cvt_pk_bf16_f32 v41, v10, v11\n"
    "v_mfma_f32_16x16x32_bf16 a[28:31], v[208:211], v[44:47], a[28:31]\n"
    "v_exp_f32 v8, v72\n"
    "v_mfma_f32_16x16x32_bf16 v[48:51], a[160:163], v[148:151], v[92:95]\n"
    "s_add_u32 m0, s34, 16928\n"
    "v_exp_f32 v9, v73\n"
    "v_mfma_f32_16x16x32_bf16 v[48:51], a[164:167], v[152:155], v[48:51]\n"
    "buffer_load_dwordx4 v1, s[24:27], s32 offen offset:1024  lds\n"
    "v_mfma_f32_16x16x32_bf16 v[48:51], a[168:171], v[156:159], v[48:51]\n"
    "v_exp_f32 v10, v74\n"
    "v_mfma_f32_16x16x32_bf16 v[48:51], a[172:175], v[160:163], v[48:51]\n"
    "v_exp_f32 v11, v75\n"
    "v_mfma_f32_16x16x32_bf16 v[52:55], a[176:179], v[148:151], v[92:95]\n"
    "v_cvt_pk_bf16_f32 v42, v240, v241\n"
    "v_cvt_pk_bf16_f32 v43, v242, v243\n"
    "v_mfma_f32_16x16x32_bf16 v[52:55], a[180:183], v[152:155], v[52:55]\n"
    "v_exp_f32 v240, v76\n"
    "v_mfma_f32_16x16x32_bf16 v[52:55], a[184:187], v[156:159], v[52:55]\n"
    "s_add_u32 m0, s34, 16960\n"
    "v_exp_f32 v241, v77\n"
    "v_mfma_f32_16x16x32_bf16 v[52:55], a[188:191], v[160:163], v[52:55]\n"
    "buffer_load_dwordx4 v1, s[24:27], s32 offen offset:2048  lds\n"
    "v_mfma_f32_16x16x32_bf16 v[56:59], a[192:195], v[148:151], v[92:95]\n"
    "v_exp_f32 v242, v78\n"
    "v_mfma_f32_16x16x32_bf16 v[56:59], a[196:199], v[152:155], v[56:59]\n"
    "v_exp_f32 v243, v79\n"
    "v_mfma_f32_16x16x32_bf16 v[56:59], a[200:203], v[156:159], v[56:59]\n"
    "v_cvt_pk_bf16_f32 v44, v8, v9\n"
    "v_cvt_pk_bf16_f32 v45, v10, v11\n"
    "v_mfma_f32_16x16x32_bf16 v[56:59], a[204:207], v[160:163], v[56:59]\n"
    "v_cvt_pk_bf16_f32 v46, v240, v241\n"
    "v_cvt_pk_bf16_f32 v47, v242, v243\n"
    "v_mfma_f32_16x16x32_bf16 v[12:15], v[16:19], v[40:43], 0\n"
    "v_exp_f32 v8, v48\n"
    "v_mfma_f32_16x16x32_bf16 v[12:15], v[16:19], v[44:47], v[12:15]\n"
    "v_exp_f32 v9, v49\n"
    "v_mfma_f32_16x16x32_bf16 v[60:63], a[208:211], v[148:151], v[92:95]\n"
    "s_add_u32 m0, s34, 16992\n"
    "v_exp_f32 v10, v50\n"
    "v_mfma_f32_16x16x32_bf16 v[60:63], a[212:215], v[152:155], v[60:63]\n"
    "buffer_load_dwordx4 v1, s[24:27], s32 offen offset:3072  lds\n"
    "v_mfma_f32_16x16x32_bf16 v[60:63], a[216:219], v[156:159], v[60:63]\n"
    "v_exp_f32 v11, v51\n"
    "v_mfma_f32_16x16x32_bf16 v[60:63], a[220:223], v[160:163], v[60:63]\n"
    "v_exp_f32 v240, v52\n"
    "v_mfma_f32_16x16x32_bf16 a[32:35], a[224:227], v[32:35], a[32:35]\n"
    "v_cmp_nge_f32 vcc, s40, v12\n"
    "v_mfma_f32_16x16x32_bf16 a[32:35], v[180:183], v[36:39], a[32:35]\n"
    "v_exp_f32 v241, v53\n"
    "v_mfma_f32_16x16x32_bf16 a[36:39], a[228:231], v[32:35], a[36:39]\n"
    "v_exp_f32 v242, v54\n"
    "v_mfma_f32_16x16x32_bf16 a[36:39], v[184:187], v[36:39], a[36:39]\n"
    "s_cbranch_vccnz LTR_22\n"
    "LRET_22:\n"
    "v_add_f32 v22, v22, v12\n"
    "v_mfma_f32_16x16x32_bf16 a[40:43], a[232:235], v[32:35], a[40:43]\n"
    "v_exp_f32 v243, v55\n"
    "v_mfma_f32_16x16x32_bf16 a[40:43], v[188:191], v[36:39], a[40:43]\n"
    "v_mfma_f32_16x16x32_bf16 a[44:47], a[236:239], v[32:35], a[44:47]\n"
    "v_mfma_f32_16x16x32_bf16 a[44:47], v[192:195], v[36:39], a[44:47]\n"
    "v_mfma_f32_16x16x32_bf16 a[48:51], a[240:243], v[32:35], a[48:51]\n"
    "s_add_u32 s34, s36, s35\n"
    "v_mfma_f32_16x16x32_bf16 a[48:51], v[196:199], v[36:39], a[48:51]\n"
    "v_mfma_f32_16x16x32_bf16 a[52:55], a[244:247], v[32:35], a[52:55]\n"
    "s_add_u32 s36, s36, 33792\n"
    "v_mfma_f32_16x16x32_bf16 a[52:55], v[200:203], v[36:39], a[52:55]\n"
    "v_mfma_f32_16x16x32_bf16 a[56:59], a[248:251], v[32:35], a[56:59]\n"
    "s_sub_u32 s50, s36, 101376\n"
    "v_mfma_f32_16x16x32_bf16 a[56:59], v[204:207], v[36:39], a[56:59]\n"
    "v_mfma_f32_16x16x32_bf16 a[60:63], a[252:255], v[32:35], a[60:63]\n"
    "s_min_u32 s36, s36, s50\n"
    "v_cvt_pk_bf16_f32 v32, v8, v9\n"
    "v_cvt_pk_bf16_f32 v33, v10, v11\n"
    "v_mfma_f32_16x16x32_bf16 a[60:63], v[208:211], v[36:39], a[60:63]\n"
    "s_add_u32 s32, s32, 16384\n"
    "v_exp_f32 v8, v56\n"
    "v_mfma_f32_16x16x32_bf16 v[64:67], a[160:163], v[164:167], v[96:99]\n"
    "v_add_u32 v4, s36, v2\n"
    "v_mfma_f32_16x16x32_bf16 v[64:67], a[164:167], v[168:171], v[64:67]\n"
    "s_waitcnt vmcnt(8)\n"
    "s_barrier\n"
    "v_mfma_f32_16x16x32_bf16 v[64:67], a[168:171], v[172:175], v[64:67]\n"
    "v_exp_f32 v9, v57\n"
    "v_mfma_f32_16x16x32_bf16 v[64:67], a[172:175], v[176:179], v[64:67]\n"
    "v_exp_f32 v10, v58\n"
    "v_mfma_f32_16x16x32_bf16 v[68:71], a[176:179], v[164:167], v[96:99]\n"
    "ds_read_b128 a[160:163], v4 offset:0\n"
    "ds_read_b128 a[164:167], v4 offset:64\n"
    "v_mfma_f32_16x16x32_bf16 v[68:71], a[180:183], v[168:171], v[68:71]\n"
    "v_exp_f32 v11, v59\n"
    "v_mfma_f32_16x16x32_bf16 v[68:71], a[184:187], v[172:175], v[68:71]\n"
    "v_cvt_pk_bf16_f32 v34, v240, v241\n"
    "v_cvt_pk_bf16_f32 v35, v242, v243\n"
    "v_mfma_f32_16x16x32_bf16 v[68:71], a[188:191], v[176:179], v[68:71]\n"
    "ds_read_b128 a[168:171], v4 offset:128\n"
    "ds_read_b128 a[172:175], v4 offset:192\n"
    "v_mfma_f32_16x16x32_bf16 v[72:75], a[192:195], v[164:167], v[96:99]\n"
    "v_exp_f32 v240, v60\n"
    "v_mfma_f32_16x16x32_bf16 v[72:75], a[196:199], v[168:171], v[72:75]\n"
    "v_exp_f32 v241, v61\n"
    "v_mfma_f32_16x16x32_bf16 v[72:75], a[200:203], v[172:175], v[72:75]\n"
    "ds_read_b128 a[176:179], v4 offset:256\n"
    "ds_read_b128 a[180:183], v4 offset:320\n"
    "v_mfma_f32_16x16x32_bf16 v[72:75], a[204:207], v[176:179], v[72:75]\n"
    "v_exp_f32 v242, v62\n"
    "v_mfma_f32_16x16x32_bf16 v[76:79], a[208:211], v[164:167], v[96:99]\n"
    "v_exp_f32 v243, v63\n"
    "v_mfma_f32_16x16x32_bf16 v[76:79], a[212:215], v[168:171], v[76:79]\n"
    "ds_read_b128 a[184:187], v4 offset:384\n"
    "ds_read_b128 a[188:191], v4 offset:448\n"
    "v_mfma_f32_16x16x32_bf16 v[76:79], a[216:219], v[172:175], v[76:79]\n"
    "v_cvt_pk_bf16_f32 v36, v8, v9\n"
    "v_cvt_pk_bf16_f32 v37, v10, v11\n"
    "v_mfma_f32_16x16x32_bf16 v[76:79], a[220:223], v[176:179], v[76:79]\n"
    "v_cvt_pk_bf16_f32 v38, v240, v241\n"
    "v_cvt_pk_bf16_f32 v39, v242, v243\n"
    "v_mfma_f32_16x16x32_bf16 v[12:15], v[16:19], v[32:35], 0\n"
    "ds_read_b128 a[192:195], v4 offset:512\n"
    "ds_read_b128 a[196:199], v4 offset:576\n"
    "v_mfma_f32_16x16x32_bf16 v[12:15], v[16:19], v[36:39], v[12:15]\n"
    "v_exp_f32 v8, v64\n"
    "v_mfma_f32_16x16x32_bf16 a[64:67], a[224:227], v[40:43], a[64:67]\n"
    "ds_read_b128 a[200:203], v4 offset:640\n"
    "ds_read_b128 a[204:207], v4 offset:704\n"
    "v_mfma_f32_16x16x32_bf16 a[64:67], v[180:183], v[44:47], a[64:67]\n"
    "v_exp_f32 v9, v65\n"
    "v_mfma_f32_16x16x32_bf16 a[68:71], a[228:231], v[40:43], a[68:71]\n"
    "ds_read_b128 a[208:211], v4 offset:768\n"
    "ds_read_b128 a[212:215], v4 offset:832\n"
    "v_mfma_f32_16x16x32_bf16 a[68:71], v[184:187], v[44:47], a[68:71]\n"
    "v_exp_f32 v10, v66\n"
    "v_mfma_f32_16x16x32_bf16 a[72:75], a[232:235], v[40:43], a[72:75]\n"
    "v_cmp_nge_f32 vcc, s40, v12\n"
    "v_mfma_f32_16x16x32_bf16 a[72:75], v[188:191], v[44:47], a[72:75]\n"
    "ds_read_b128 a[216:219], v4 offset:896\n"
    "ds_read_b128 a[220:223], v4 offset:960\n"
    "v_mfma_f32_16x16x32_bf16 a[76:79], a[236:239], v[40:43], a[76:79]\n"
    "v_exp_f32 v11, v67\n"
    "v_mfma_f32_16x16x32_bf16 a[76:79], v[192:195], v[44:47], a[76:79]\n"
    "s_cbranch_vccnz LTR_23\n"
    "LRET_23:\n"
    "v_add_f32 v23, v23, v12\n"
    "v_mfma_f32_16x16x32_bf16 a[80:83], a[240:243], v[40:43], a[80:83]\n"
    "v_exp_f32 v240, v68\n"
    "v_mfma_f32_16x16x32_bf16 a[80:83], v[196:199], v[44:47], a[80:83]\n"
    "v_exp_f32 v241, v69\n"
    "v_mfma_f32_16x16x32_bf16 a[84:87], a[244:247], v[40:43], a[84:87]\n"
    "v_exp_f32 v242, v70\n"
    "v_mfma_f32_16x16x32_bf16 a[84:87], v[200:203], v[44:47], a[84:87]\n"
    "v_exp_f32 v243, v71\n"
    "v_mfma_f32_16x16x32_bf16 a[88:91], a[248:251], v[40:43], a[88:91]\n"
    "v_mfma_f32_16x16x32_bf16 a[88:91], v[204:207], v[44:47], a[88:91]\n"
    "v_mfma_f32_16x16x32_bf16 a[92:95], a[252:255], v[40:43], a[92:95]\n"
    "v_cvt_pk_bf16_f32 v40, v8, v9\n"
    "v_cvt_pk_bf16_f32 v41, v10, v11\n"
    "v_mfma_f32_16x16x32_bf16 a[92:95], v[208:211], v[44:47], a[92:95]\n"
    "s_waitcnt lgkmcnt(8)\n"
    "v_exp_f32 v8, v72\n"
    "v_mfma_f32_16x16x32_bf16 v[48:51], a[160:163], v[100:103], v[80:83]\n"
    "v_exp_f32 v9, v73\n"
    "v_mfma_f32_16x16x32_bf16 v[48:51], a[164:167], v[104:107], v[48:51]\n"
    "s_add_u32 m0, s34, 0\n"
    "v_exp_f32 v10, v74\n"
    "v_mfma_f32_16x16x32_bf16 v[48:51], a[168:171], v[108:111], v[48:51]\n"
    "buffer_load_dwordx4 v1, s[20:23], s32 offen offset:0  lds\n"
    "v_mfma_f32_16x16x32_bf16 v[48:51], a[172:175], v[112:115], v[48:51]\n"
    "v_exp_f32 v11, v75\n"
    "v_mfma_f32_16x16x32_bf16 v[52:55], a[176:179], v[100:103], v[80:83]\n"
    "v_cvt_pk_bf16_f32 v42, v240, v241\n"
    "v_cvt_pk_bf16_f32 v43, v242, v243\n"
    "v_mfma_f32_16x16x32_bf16 v[52:55], a[180:183], v[104:107], v[52:55]\n"
    "v_exp_f32 v240, v76\n"
    "v_mfma_f32_16x16x32_bf16 v[52:55], a[184:187], v[108:111], v[52:55]\n"
    "v_exp_f32 v241, v77\n"
    "v_mfma_f32_16x16x32_bf16 v[52:55], a[188:191], v[112:115], v[52:55]\n"
    "s_waitcnt lgkmcnt(0)\n"
    "v_exp_f32 v242, v78\n"
    "v_mfma_f32_16x16x32_bf16 v[56:59], a[192:195], v[100:103], v[80:83]\n"
    "v_exp_f32 v243, v79\n"
    "v_mfma_f32_16x16x32_bf16 v[56:59], a[196:199], v[104:107], v[56:59]\n"
    "v_cvt_pk_bf16_f32 v44, v8, v9\n"
    "v_cvt_pk_bf16_f32 v45, v10, v11\n"
    "v_mfma_f32_16x16x32_bf16 v[56:59], a[200:203], v[108:111], v[56:59]\n"
    "v_cvt_pk_bf16_f32 v46, v240, v241\n"
    "v_cvt_pk_bf16_f32 v47, v242, v243\n"
    "v_mfma_f32_16x16x32_bf16 v[56:59], a[204:207], v[112:115], v[56:59]\n"
    "v_exp_f32 v8, v48\n"
    "v_mfma_f32_16x16x32_bf16 v[12:15], v[16:19], v[40:43], 0\n"
    "v_exp_f32 v9, v49\n"
    "v_mfma_f32_16x16x32_bf16 v[12:15], v[16:19], v[44:47], v[12:15]\n"
    "v_exp_f32 v10, v50\n"
    "v_mfma_f32_16x16x32_bf16 v[60:63], a[208:211], v[100:103], v[80:83]\n"
    "v_exp_f32 v11, v51\n"
    "v_mfma_f32_16x16x32_bf16 v[60:63], a[212:215], v[104:107], v[60:63]\n"
    "s_add_u32 m0, s34, 32\n"
    "v_exp_f32 v240, v52\n"
    "v_mfma_f32_16x16x32_bf16 v[60:63], a[216:219], v[108:111], v[60:63]\n"
    "buffer_load_dwordx4 v1, s[20:23], s32 offen offset:1024  lds\n"
    "v_mfma_f32_16x16x32_bf16 v[60:63], a[220:223], v[112:115], v[60:63]\n"
    "v_exp_f32 v241, v53\n"
    "v_mfma_f32_16x16x32_bf16 a[96:99], a[224:227], v[32:35], a[96:99]\n"
    "v_cmp_nge_f32 vcc, s40, v12\n"
    "v_mfma_f32_16x16x32_bf16 a[96:99], v[180:183], v[36:39], a[96:99]\n"
    "v_exp_f32 v242, v54\n"
    "v_mfma_f32_16x16x32_bf16 a[100:103], a[228:231], v[32:35], a[100:103]\n"
    "v_exp_f32 v243, v55\n"
    "v_mfma_f32_16x16x32_bf16 a[100:103], v[184:187], v[36:39], a[100:103]\n"
    "s_cbranch_vccnz LTR_24\n"
    "LRET_24:\n"
    "v_add_f32 v24, v24, v12\n"
    "v_mfma_f32_16x16x32_bf16 a[104:107], a[232:235], v[32:35], a[104:107]\n"
    "v_mfma_f32_16x16x32_bf16 a[104:107], v[188:191], v[36:39], a[104:107]\n"
    "v_mfma_f32_16x16x32_bf16 a[108:111], a[236:239], v[32:35], a[108:111]\n"
    "v_mfma_f32_16x16x32_bf16 a[108:111], v[192:195], v[36:39], a[108:111]\n"
    "v_mfma_f32_16x16x32_bf16 a[112:115], a[240:243], v[32:35], a[112:115]\n"
    "v_mfma_f32_16x16x32_bf16 a[112:115], v[196:199], v[36:39], a[112:115]\n"
    "v_mfma_f32_16x16x32_bf16 a[116:119], a[244:247], v[32:35], a[116:119]\n"
    "v_mfma_f32_16x16x32_bf16 a[116:119], v[200:203], v[36:39], a[116:119]\n"
    "v_mfma_f32_16x16x32_bf16 a[120:123], a[248:251], v[32:35], a[120:123]\n"
    "v_mfma_f32_16x16x32_bf16 a[120:123], v[204:207], v[36:39], a[120:123]\n"
    "v_mfma_f32_16x16x32_bf16 a[124:127], a[252:255], v[32:35], a[124:127]\n"
    "v_cvt_pk_bf16_f32 v32, v8, v9\n"
    "v_cvt_pk_bf16_f32 v33, v10, v11\n"
    "v_mfma_f32_16x16x32_bf16 a[124:127], v[208:211], v[36:39], a[124:127]\n"
    "v_exp_f32 v8, v56\n"
    "v_mfma_f32_16x16x32_bf16 v[64:67], a[160:163], v[116:119], v[84:87]\n"
    "v_exp_f32 v9, v57\n"
    "v_mfma_f32_16x16x32_bf16 v[64:67], a[164:167], v[120:123], v[64:67]\n"
    "v_add_u32 v5, s36, v3\n"
    "v_mfma_f32_16x16x32_bf16 v[64:67], a[168:171], v[124:127], v[64:67]\n"
    "v_exp_f32 v10, v58\n"
    "v_mfma_f32_16x16x32_bf16 v[64:67], a[172:175], v[128:131], v[64:67]\n"
    "v_exp_f32 v11, v59\n"
    "v_mfma_f32_16x16x32_bf16 v[68:71], a[176:179], v[116:119], v[84:87]\n"
    "v_cvt_pk_bf16_f32 v34, v240, v241\n"
    "v_cvt_pk_bf16_f32 v35, v242, v243\n"
    "v_mfma_f32_16x16x32_bf16 v[68:71], a[180:183], v[120:123], v[68:71]\n"
    "v_exp_f32 v240, v60\n"
    "v_mfma_f32_16x16x32_bf16 v[68:71], a[184:187], v[124:127], v[68:71]\n"
    "v_exp_f32 v241, v61\n"
    "v_mfma_f32_16x16x32_bf16 v[68:71], a[188:191], v[128:131], v[68:71]\n"
    "v_exp_f32 v242, v62\n"
    "v_mfma_f32_16x16x32_bf16 v[72:75], a[192:195], v[116:119], v[84:87]\n"
    "v_exp_f32 v243, v63\n"
    "v_mfma_f32_16x16x32_bf16 v[72:75], a[196:199], v[120:123], v[72:75]\n"
    "v_cvt_pk_bf16_f32 v36, v8, v9\n"
    "v_cvt_pk_bf16_f32 v37, v10, v11\n"
    "v_mfma_f32_16x16x32_bf16 v[72:75], a[200:203], v[124:127], v[72:75]\n"
    "v_cvt_pk_bf16_f32 v38, v240, v241\n"
    "v_cvt_pk_bf16_f32 v39, v242, v243\n"
    "v_mfma_f32_16x16x32_bf16 v[72:75], a[204:207], v[128:131], v[72:75]\n"
    "v_exp_f32 v8, v64\n"
    "v_mfma_f32_16x16x32_bf16 v[12:15], v[16:19], v[32:35], 0\n"
    "v_exp_f32 v9, v65\n"
    "v_mfma_f32_16x16x32_bf16 v[12:15], v[16:19], v[36:39], v[12:15]\n"
    "v_exp_f32 v10, v66\n"
    "v_mfma_f32_16x16x32_bf16 v[76:79], a[208:211], v[116:119], v[84:87]\n"
    "v_exp_f32 v11, v67\n"
    "v_mfma_f32_16x16x32_bf16 v[76:79], a[212:215], v[120:123], v[76:79]\n"
    "v_exp_f32 v240, v68\n"
    "v_mfma_f32_16x16x32_bf16 v[76:79], a[216:219], v[124:127], v[76:79]\n"
    "v_exp_f32 v241, v69\n"
    "v_mfma_f32_16x16x32_bf16 v[76:79], a[220:223], v[128:131], v[76:79]\n"
    "v_exp_f32 v242, v70\n"
    "v_mfma_f32_16x16x32_bf16 a[128:131], a[224:227], v[40:43], a[128:131]\n"
    "ds_read_b64_tr_b16 a[224:225], v5 offset:0\n"
    "ds_read_b64_tr_b16 a[226:227], v5 offset:256\n"
    "v_mfma_f32_16x16x32_bf16 a[128:131], v[180:183], v[44:47], a[128:131]\n"
    "v_cmp_nge_f32 vcc, s40, v12\n"
    "v_mfma_f32_16x16x32_bf16 a[132:135], a[228:231], v[40:43], a[132:135]\n"
    "ds_read_b64_tr_b16 v[180:181], v5 offset:512\n"
    "ds_read_b64_tr_b16 v[182:183], v5 offset:768\n"
    "v_mfma_f32_16x16x32_bf16 a[132:135], v[184:187], v[44:47], a[132:135]\n"
    "v_exp_f32 v243, v71\n"
    "v_mfma_f32_16x16x32_bf16 a[136:139], a[232:235], v[40:43], a[136:139]\n"
    "ds_read_b64_tr_b16 a[228:229], v5 offset:32\n"
    "ds_read_b64_tr_b16 a[230:231], v5 offset:288\n"
    "v_mfma_f32_16x16x32_bf16 a[136:139], v[188:191], v[44:47], a[136:139]\n"
    "s_cbranch_vccnz LTR_25\n"
    "LRET_25:\n"
    "v_add_f32 v20, v20, v12\n"
    "v_mfma_f32_16x16x32_bf16 a[140:143], a[236:239], v[40:43], a[140:143]\n"
    "ds_read_b64_tr_b16 v[184:185], v5 offset:544\n"
    "ds_read_b64_tr_b16 v[186:187], v5 offset:800\n"
    "v_mfma_f32_16x16x32_bf16 a[140:143], v[192:195], v[44:47], a[140:143]\n"
    "v_mfma_f32_16x16x32_bf16 a[144:147], a[240:243], v[40:43], a[144:147]\n"
    "ds_read_b64_tr_b16 a[232:233], v5 offset:64\n"
    "ds_read_b64_tr_b16 a[234:235], v5 offset:320\n"
    "v_mfma_f32_16x16x32_bf16 a[144:147], v[196:199], v[44:47], a[144:147]\n"
    "v_mfma_f32_16x16x32_bf16 a[148:151], a[244:247], v[40:43], a[148:151]\n"
    "ds_read_b64_tr_b16 v[188:189], v5 offset:576\n"
    "ds_read_b64_tr_b16 v[190:191], v5 offset:832\n"
    "v_mfma_f32_16x16x32_bf16 a[148:151], v[200:203], v[44:47], a[148:151]\n"
    "v_mfma_f32_16x16x32_bf16 a[152:155], a[248:251], v[40:43], a[152:155]\n"
    "ds_read_b64_tr_b16 a[236:237], v5 offset:96\n"
    "ds_read_b64_tr_b16 a[238:239], v5 offset:352\n"
    "v_mfma_f32_16x16x32_bf16 a[152:155], v[204:207], v[44:47], a[152:155]\n"
    "v_mfma_f32_16x16x32_bf16 a[156:159], a[252:255], v[40:43], a[156:159]\n"
    "ds_read_b64_tr_b16 v[192:193], v5 offset:608\n"
    "ds_read_b64_tr_b16 v[194:195], v5 offset:864\n"
    "v_mfma_f32_16x16x32_bf16 a[156:159], v[208:211], v[44:47], a[156:159]\n"
    "v_cvt_pk_bf16_f32 v40, v8, v9\n"
    "v_cvt_pk_bf16_f32 v41, v10, v11\n"
    "v_mfma_f32_16x16x32_bf16 v[48:51], a[160:163], v[132:135], v[88:91]\n"
    "s_add_u32 m0, s34, 64\n"
    "ds_read_b64_tr_b16 a[240:241], v5 offset:128\n"
    "ds_read_b64_tr_b16 a[242:243], v5 offset:384\n"
    "v_mfma_f32_16x16x32_bf16 v[48:51], a[164:167], v[136:139], v[48:51]\n"
    "buffer_load_dwordx4 v1, s[20:23], s32 offen offset:2048  lds\n"
    "v_mfma_f32_16x16x32_bf16 v[48:51], a[168:171], v[140:143], v[48:51]\n"
    "ds_read_b64_tr_b16 v[196:197], v5 offset:640\n"
    "ds_read_b64_tr_b16 v[198:199], v5 offset:896\n"
    "v_mfma_f32_16x16x32_bf16 v[48:51], a[172:175], v[144:147], v[48:51]\n"
    "v_exp_f32 v8, v72\n"
    "v_mfma_f32_16x16x32_bf16 v[52:55], a[176:179], v[132:135], v[88:91]\n"
    "ds_read_b64_tr_b16 a[244:245], v5 offset:160\n"
    "ds_read_b64_tr_b16 a[246:247], v5 offset:416\n"
    "v_mfma_f32_16x16x32_bf16 v[52:55], a[180:183], v[136:139], v[52:55]\n"
    "v_exp_f32 v9, v73\n"
    "v_mfma_f32_16x16x32_bf16 v[52:55], a[184:187], v[140:143], v[52:55]\n"
    "ds_read_b64_tr_b16 v[200:201], v5 offset:672\n"
    "ds_read_b64_tr_b16 v[202:203], v5 offset:928\n"
    "v_mfma_f32_16x16x32_bf16 v[52:55], a[188:191], v[144:147], v[52:55]\n"
    "v_exp_f32 v10, v74\n"
    "v_mfma_f32_16x16x32_bf16 v[56:59], a[192:195], v[132:135], v[88:91]\n"
    "ds_read_b64_tr_b16 a[248:249], v5 offset:192\n"
    "ds_read_b64_tr_b16 a[250:251], v5 offset:448\n"
    "v_mfma_f32_16x16x32_bf16 v[56:59], a[196:199], v[136:139], v[56:59]\n"
    "v_exp_f32 v11, v75\n"
    "v_mfma_f32_16x16x32_bf16 v[56:59], a[200:203], v[140:143], v[56:59]\n"
    "ds_read_b64_tr_b16 v[204:205], v5 offset:704\n"
    "ds_read_b64_tr_b16 v[206:207], v5 offset:960\n"
    "v_mfma_f32_16x16x32_bf16 v[56:59], a[204:207], v[144:147], v[56:59]\n"
    "v_cvt_pk_bf16_f32 v42, v240, v241\n"
    "v_cvt_pk_bf16_f32 v43, v242, v243\n"
    "v_mfma_f32_16x16x32_bf16 v[60:63], a[208:211], v[132:135], v[88:91]\n"
    "ds_read_b64_tr_b16 a[252:253], v5 offset:224\n"
    "ds_read_b64_tr_b16 a[254:255], v5 offset:480\n"
    "v_mfma_f32_16x16x32_bf16 v[60:63], a[212:215], v[136:139], v[60:63]\n"
    "v_exp_f32 v240, v76\n"
    "v_mfma_f32_16x16x32_bf16 v[60:63], a[216:219], v[140:143], v[60:63]\n"
    "ds_read_b64_tr_b16 v[208:209], v5 offset:736\n"
    "ds_read_b64_tr_b16 v[210:211], v5 offset:992\n"
    "v_mfma_f32_16x16x32_bf16 v[60:63], a[220:223], v[144:147], v[60:63]\n"
    "s_waitcnt lgkmcnt(15)\n"
    "v_exp_f32 v241, v77\n"
    "v_mfma_f32_16x16x32_bf16 a[0:3], a[224:227], v[32:35], a[0:3]\n"
    "s_add_u32 m0, s34, 96\n"
    "v_exp_f32 v242, v78\n"
    "v_mfma_f32_16x16x32_bf16 a[0:3], v[180:183], v[36:39], a[0:3]\n"
    "buffer_load_dwordx4 v1, s[20:23], s32 offen offset:3072  lds\n"
    "v_mfma_f32_16x16x32_bf16 a[4:7], a[228:231], v[32:35], a[4:7]\n"
    "v_exp_f32 v243, v79\n"
    "v_mfma_f32_16x16x32_bf16 a[4:7], v[184:187], v[36:39], a[4:7]\n"
    "v_cvt_pk_bf16_f32 v44, v8, v9\n"
    "v_cvt_pk_bf16_f32 v45, v10, v11\n"
    "v_mfma_f32_16x16x32_bf16 a[8:11], a[232:235], v[32:35], a[8:11]\n"
    "s_add_u32 m0, s34, 16896\n"
    "v_cvt_pk_bf16_f32 v46, v240, v241\n"
    "v_cvt_pk_bf16_f32 v47, v242, v243\n"
    "v_mfma_f32_16x16x32_bf16 a[8:11], v[188:191], v[36:39], a[8:11]\n"
    "buffer_load_dwordx4 v1, s[24:27], s32 offen offset:0  lds\n"
    "v_mfma_f32_16x16x32_bf16 v[12:15], v[16:19], v[40:43], 0\n"
    "v_exp_f32 v8, v48\n"
    "v_mfma_f32_16x16x32_bf16 v[12:15], v[16:19], v[44:47], v[12:15]\n"
    "v_exp_f32 v9, v49\n"
    "v_mfma_f32_16x16x32_bf16 a[12:15], a[236:239], v[32:35], a[12:15]\n"
    "v_exp_f32 v10, v50\n"
    "v_mfma_f32_16x16x32_bf16 a[12:15], v[192:195], v[36:39], a[12:15]\n"
    "s_waitcnt lgkmcnt(0)\n"
    "v_exp_f32 v11, v51\n"
    "v_mfma_f32_16x16x32_bf16 a[16:19], a[240:243], v[32:35], a[16:19]\n"
    "v_exp_f32 v240, v52\n"
    "v_mfma_f32_16x16x32_bf16 a[16:19], v[196:199], v[36:39], a[16:19]\n"
    "v_exp_f32 v241, v53\n"
    "v_mfma_f32_16x16x32_bf16 a[20:23], a[244:247], v[32:35], a[20:23]\n"
    "v_cmp_nge_f32 vcc, s40, v12\n"
    "v_mfma_f32_16x16x32_bf16 a[20:23], v[200:203], v[36:39], a[20:23]\n"
    "v_exp_f32 v242, v54\n"
    "v_mfma_f32_16x16x32_bf16 a[24:27], a[248:251], v[32:35], a[24:27]\n"
    "v_exp_f32 v243, v55\n"
    "v_mfma_f32_16x16x32_bf16 a[24:27], v[204:207], v[36:39], a[24:27]\n"
    "s_cbranch_vccnz LTR_26\n"
    "LRET_26:\n"
    "v_add_f32 v21, v21, v12\n"
    "v_mfma_f32_16x16x32_bf16 a[28:31], a[252:255], v[32:35], a[28:31]\n"
    "v_cvt_pk_bf16_f32 v32, v8, v9\n"
    "v_cvt_pk_bf16_f32 v33, v10, v11\n"
    "v_mfma_f32_16x16x32_bf16 a[28:31], v[208:211], v[36:39], a[28:31]\n"
    "v_exp_f32 v8, v56\n"
    "v_mfma_f32_16x16x32_bf16 v[64:67], a[160:163], v[148:151], v[92:95]\n"
    "s_add_u32 m0, s34, 16928\n"
    "v_exp_f32 v9, v57\n"
    "v_mfma_f32_16x16x32_bf16 v[64:67], a[164:167], v[152:155], v[64:67]\n"
    "buffer_load_dwordx4 v1, s[24:27], s32 offen offset:1024  lds\n"
    "v_mfma_f32_16x16x32_bf16 v[64:67], a[168:171], v[156:159], v[64:67]\n"
    "v_exp_f32 v10, v58\n"
    "v_mfma_f32_16x16x32_bf16 v[64:67], a[172:175], v[160:163], v[64:67]\n"
    "v_exp_f32 v11, v59\n"
    "v_mfma_f32_16x16x32_bf16 v[68:71], a[176:179], v[148:151], v[92:95]\n"
    "v_cvt_pk_bf16_f32 v34, v240, v241\n"
    "v_cvt_pk_bf16_f32 v35, v242, v243\n"
    "v_mfma_f32_16x16x32_bf16 v[68:71], a[180:183], v[152:155], v[68:71]\n"
    "v_exp_f32 v240, v60\n"
    "v_mfma_f32_16x16x32_bf16 v[68:71], a[184:187], v[156:159], v[68:71]\n"
    "s_add_u32 m0, s34, 16960\n"
    "v_exp_f32 v241, v61\n"
    "v_mfma_f32_16x16x32_bf16 v[68:71], a[188:191], v[160:163], v[68:71]\n"
    "buffer_load_dwordx4 v1, s[24:27], s32 offen offset:2048  lds\n"
    "v_mfma_f32_16x16x32_bf16 v[72:75], a[192:195], v[148:151], v[92:95]\n"
    "v_exp_f32 v242, v62\n"
    "v_mfma_f32_16x16x32_bf16 v[72:75], a[196:199], v[152:155], v[72:75]\n"
    "v_exp_f32 v243, v63\n"
    "v_mfma_f32_16x16x32_bf16 v[72:75], a[200:203], v[156:159], v[72:75]\n"
    "v_cvt_pk_bf16_f32 v36, v8, v9\n"
    "v_cvt_pk_bf16_f32 v37, v10, v11\n"
    "v_mfma_f32_16x16x32_bf16 v[72:75], a[204:207], v[160:163], v[72:75]\n"
    "v_cvt_pk_bf16_f32 v38, v240, v241\n"
    "v_cvt_pk_bf16_f32 v39, v242, v243\n"
    "v_mfma_f32_16x16x32_bf16 v[12:15], v[16:19], v[32:35], 0\n"
    "v_exp_f32 v8, v64\n"
    "v_mfma_f32_16x16x32_bf16 v[12:15], v[16:19], v[36:39], v[12:15]\n"
    "v_exp_f32 v9, v65\n"
    "v_mfma_f32_16x16x32_bf16 v[76:79], a[208:211], v[148:151], v[92:95]\n"
    "s_add_u32 m0, s34, 16992\n"
    "v_exp_f32 v10, v66\n"
    "v_mfma_f32_16x16x32_bf16 v[76:79], a[212:215], v[152:155], v[76:79]\n"
    "buffer_load_dwordx4 v1, s[24:27], s32 offen offset:3072  lds\n"
    "v_mfma_f32_16x16x32_bf16 v[76:79], a[216:219], v[156:159], v[76:79]\n"
    "v_exp_f32 v11, v67\n"
    "v_mfma_f32_16x16x32_bf16 v[76:79], a[220:223], v[160:163], v[76:79]\n"
    "v_exp_f32 v240, v68\n"
    "v_mfma_f32_16x16x32_bf16 a[32:35], a[224:227], v[40:43], a[32:35]\n"
    "v_cmp_nge_f32 vcc, s40, v12\n"
    "v_mfma_f32_16x16x32_bf16 a[32:35], v[180:183], v[44:47], a[32:35]\n"
    "v_exp_f32 v241, v69\n"
    "v_mfma_f32_16x16x32_bf16 a[36:39], a[228:231], v[40:43], a[36:39]\n"
    "v_exp_f32 v242, v70\n"
    "v_mfma_f32_16x16x32_bf16 a[36:39], v[184:187], v[44:47], a[36:39]\n"
    "s_cbranch_vccnz LTR_27\n"
    "LRET_27:\n"
    "v_add_f32 v22, v22, v12\n"
    "v_mfma_f32_16x16x32_bf16 a[40:43], a[232:235], v[40:43], a[40:43]\n"
    "v_exp_f32 v243, v71\n"
    "v_mfma_f32_16x16x32_bf16 a[40:43], v[188:191], v[44:47], a[40:43]\n"
    "v_mfma_f32_16x16x32_bf16 a[44:47], a[236:239], v[40:43], a[44:47]\n"
    "v_mfma_f32_16x16x32_bf16 a[44:47], v[192:195], v[44:47], a[44:47]\n"
    "v_mfma_f32_16x16x32_bf16 a[48:51], a[240:243], v[40:43], a[48:51]\n"
    "s_add_u32 s34, s36, s35\n"
    "v_mfma_f32_16x16x32_bf16 a[48:51], v[196:199], v[44:47], a[48:51]\n"
    "v_mfma_f32_16x16x32_bf16 a[52:55], a[244:247], v[40:43], a[52:55]\n"
    "s_add_u32 s36, s36, 33792\n"
    "v_mfma_f32_16x16x32_bf16 a[52:55], v[200:203], v[44:47], a[52:55]\n"
    "v_mfma_f32_16x16x32_bf16 a[56:59], a[248:251], v[40:43], a[56:59]\n"
    "s_sub_u32 s50, s36, 101376\n"
    "v_mfma_f32_16x16x32_bf16 a[56:59], v[204:207], v[44:47], a[56:59]\n"
    "v_mfma_f32_16x16x32_bf16 a[60:63], a[252:255], v[40:43], a[60:63]\n"
    "s_min_u32 s36, s36, s50\n"
    "v_cvt_pk_bf16_f32 v40, v8, v9\n"
    "v_cvt_pk_bf16_f32 v41, v10, v11\n"
    "v_mfma_f32_16x16x32_bf16 a[60:63], v[208:211], v[44:47], a[60:63]\n"
    "s_add_u32 s32, s32, 16384\n"
    "v_exp_f32 v8, v72\n"
    "v_mfma_f32_16x16x32_bf16 v[48:51], a[160:163], v[164:167], v[96:99]\n"
    "v_add_u32 v4, s36, v2\n"
    "v_mfma_f32_16x16x32_bf16 v[48:51], a[164:167], v[168:171], v[48:51]\n"
    "s_waitcnt vmcnt(8)\n"
    "s_barrier\n"
    "v_mfma_f32_16x16x32_bf16 v[48:51], a[168:171], v[172:175], v[48:51]\n"
    "v_exp_f32 v9, v73\n"
    "v_mfma_f32_16x16x32_bf16 v[48:51], a[172:175], v[176:179], v[48:51]\n"
    "v_exp_f32 v10, v74\n"
    "v_mfma_f32_16x16x32_bf16 v[52:55], a[176:179], v[164:167], v[96:99]\n"
    "ds_read_b128 a[160:163], v4 offset:0\n"
    "ds_read_b128 a[164:167], v4 offset:64\n"
    "v_mfma_f32_16x16x32_bf16 v[52:55], a[180:183], v[168:171], v[52:55]\n"
    "v_exp_f32 v11, v75\n"
    "v_mfma_f32_16x16x32_bf16 v[52:55], a[184:187], v[172:175], v[52:55]\n"
    "v_cvt_pk_bf16_f32 v42, v240, v241\n"
    "v_cvt_pk_bf16_f32 v43, v242, v243\n"
    "v_mfma_f32_16x16x32_bf16 v[52:55], a[188:191], v[176:179], v[52:55]\n"
    "ds_read_b128 a[168:171], v4 offset:128\n"
    "ds_read_b128 a[172:175], v4 offset:192\n"
    "v_mfma_f32_16x16x32_bf16 v[56:59], a[192:195], v[164:167], v[96:99]\n"
    "v_exp_f32 v240, v76\n"
    "v_mfma_f32_16x16x32_bf16 v[56:59], a[196:199], v[168:171], v[56:59]\n"
    "v_exp_f32 v241, v77\n"
    "v_mfma_f32_16x16x32_bf16 v[56:59], a[200:203], v[172:175], v[56:59]\n"
    "ds_read_b128 a[176:179], v4 offset:256\n"
    "ds_read_b128 a[180:183], v4 offset:320\n"
    "v_mfma_f32_16x16x32_bf16 v[56:59], a[204:207], v[176:179], v[56:59]\n"
    "v_exp_f32 v242, v78\n"
    "v_mfma_f32_16x16x32_bf16 v[60:63], a[208:211], v[164:167], v[96:99]\n"
    "v_exp_f32 v243, v79\n"
    "v_mfma_f32_16x16x32_bf16 v[60:63], a[212:215], v[168:171], v[60:63]\n"
    "ds_read_b128 a[184:187], v4 offset:384\n"
    "ds_read_b128 a[188:191], v4 offset:448\n"
    "v_mfma_f32_16x16x32_bf16 v[60:63], a[216:219], v[172:175], v[60:63]\n"
    "v_cvt_pk_bf16_f32 v44, v8, v9\n"
    "v_cvt_pk_bf16_f32 v45, v10, v11\n"
    "v_mfma_f32_16x16x32_bf16 v[60:63], a[220:223], v[176:179], v[60:63]\n"
    "v_cvt_pk_bf16_f32 v46, v240, v241\n"
    "v_cvt_pk_bf16_f32 v47, v242, v243\n"
    "v_mfma_f32_16x16x32_bf16 v[12:15], v[16:19], v[40:43], 0\n"
    "ds_read_b128 a[192:195], v4 offset:512\n"
    "ds_read_b128 a[196:199], v4 offset:576\n"
    "v_mfma_f32_16x16x32_bf16 v[12:15], v[16:19], v[44:47], v[12:15]\n"
    "v_exp_f32 v8, v48\n"
    "v_mfma_f32_16x16x32_bf16 a[64:67], a[224:227], v[32:35], a[64:67]\n"
    "ds_read_b128 a[200:203], v4 offset:640\n"
    "ds_read_b128 a[204:207], v4 offset:704\n"
    "v_mfma_f32_16x16x32_bf16 a[64:67], v[180:183], v[36:39], a[64:67]\n"
    "v_exp_f32 v9, v49\n"
    "v_mfma_f32_16x16x32_bf16 a[68:71], a[228:231], v[32:35], a[68:71]\n"
    "ds_read_b128 a[208:211], v4 offset:768\n"
    "ds_read_b128 a[212:215], v4 offset:832\n"
    "v_mfma_f32_16x16x32_bf16 a[68:71], v[184:187], v[36:39], a[68:71]\n"
    "v_exp_f32 v10, v50\n"
    "v_mfma_f32_16x16x32_bf16 a[72:75], a[232:235], v[32:35], a[72:75]\n"
    "v_cmp_nge_f32 vcc, s40, v12\n"
    "v_mfma_f32_16x16x32_bf16 a[72:75], v[188:191], v[36:39], a[72:75]\n"
    "ds_read_b128 a[216:219], v4 offset:896\n"
    "ds_read_b128 a[220:223], v4 offset:960\n"
    "v_mfma_f32_16x16x32_bf16 a[76:79], a[236:239], v[32:35], a[76:79]\n"
    "v_exp_f32 v11, v51\n"
    "v_mfma_f32_16x16x32_bf16 a[76:79], v[192:195], v[36:39], a[76:79]\n"
    "s_cbranch_vccnz LTR_28\n"
    "LRET_28:\n"
    "v_add_f32 v23, v23, v12\n"
    "v_mfma_f32_16x16x32_bf16 a[80:83], a[240:243], v[32:35], a[80:83]\n"
    "v_exp_f32 v240, v52\n"
    "v_mfma_f32_16x16x32_bf16 a[80:83], v[196:199], v[36:39], a[80:83]\n"
    "v_exp_f32 v241, v53\n"
    "v_mfma_f32_16x16x32_bf16 a[84:87], a[244:247], v[32:35], a[84:87]\n"
    "v_exp_f32 v242, v54\n"
    "v_mfma_f32_16x16x32_bf16 a[84:87], v[200:203], v[36:39], a[84:87]\n"
    "v_exp_f32 v243, v55\n"
    "v_mfma_f32_16x16x32_bf16 a[88:91], a[248:251], v[32:35], a[88:91]\n"
    "v_mfma_f32_16x16x32_bf16 a[88:91], v[204:207], v[36:39], a[88:91]\n"
    "v_mfma_f32_16x16x32_bf16 a[92:95], a[252:255], v[32:35], a[92:95]\n"
    "v_cvt_pk_bf16_f32 v32, v8, v9\n"
    "v_cvt_pk_bf16_f32 v33, v10, v11\n"
    "v_mfma_f32_16x16x32_bf16 a[92:95], v[208:211], v[36:39], a[92:95]\n"
    "s_waitcnt lgkmcnt(8)\n"
    "v_exp_f32 v8, v56\n"
    "v_mfma_f32_16x16x32_bf16 v[64:67], a[160:163], v[100:103], v[80:83]\n"
    "v_exp_f32 v9, v57\n"
    "v_mfma_f32_16x16x32_bf16 v[64:67], a[164:167], v[104:107], v[64:67]\n"
    "s_add_u32 m0, s34, 0\n"
    "v_exp_f32 v10, v58\n"
    "v_mfma_f32_16x16x32_bf16 v[64:67], a[168:171], v[108:111], v[64:67]\n"
    "buffer_load_dwordx4 v1, s[20:23], s32 offen offset:0  lds\n"
    "v_mfma_f32_16x16x32_bf16 v[64:67], a[172:175], v[112:115], v[64:67]\n"
    "v_exp_f32 v11, v59\n"
    "v_mfma_f32_16x16x32_bf16 v[68:71], a[176:179], v[100:103], v[80:83]\n"
    "v_cvt_pk_bf16_f32 v34, v240, v241\n"
    "v_cvt_pk_bf16_f32 v35, v242, v243\n"
    "v_mfma_f32_16x16x32_bf16 v[68:71], a[180:183], v[104:107], v[68:71]\n"
    "v_exp_f32 v240, v60\n"
    "v_mfma_f32_16x16x32_bf16 v[68:71], a[184:187], v[108:111], v[68:71]\n"
    "v_exp_f32 v241, v61\n"
    "v_mfma_f32_16x16x32_bf16 v[68:71], a[188:191], v[112:115], v[68:71]\n"
    "s_waitcnt lgkmcnt(0)\n"
    "v_exp_f32 v242, v62\n"
    "v_mfma_f32_16x16x32_bf16 v[72:75], a[192:195], v[100:103], v[80:83]\n"
    "v_exp_f32 v243, v63\n"
    "v_mfma_f32_16x16x32_bf16 v[72:75], a[196:199], v[104:107], v[72:75]\n"
    "v_cvt_pk_bf16_f32 v36, v8, v9\n"
    "v_cvt_pk_bf16_f32 v37, v10, v11\n"
    "v_mfma_f32_16x16x32_bf16 v[72:75], a[200:203], v[108:111], v[72:75]\n"
    "v_cvt_pk_bf16_f32 v38, v240, v241\n"
    "v_cvt_pk_bf16_f32 v39, v242, v243\n"
    "v_mfma_f32_16x16x32_bf16 v[72:75], a[204:207], v[112:115], v[72:75]\n"
    "v_exp_f32 v8, v64\n"
    "v_mfma_f32_16x16x32_bf16 v[12:15], v[16:19], v[32:35], 0\n"
    "v_exp_f32 v9, v65\n"
    "v_mfma_f32_16x16x32_bf16 v[12:15], v[16:19], v[36:39], v[12:15]\n"
    "v_exp_f32 v10, v66\n"
    "v_mfma_f32_16x16x32_bf16 v[76:79], a[208:211], v[100:103], v[80:83]\n"
    "v_exp_f32 v11, v67\n"
    "v_mfma_f32_16x16x32_bf16 v[76:79], a[212:215], v[104:107], v[76:79]\n"
    "s_add_u32 m0, s34, 32\n"
    "v_exp_f32 v240, v68\n"
    "v_mfma_f32_16x16x32_bf16 v[76:79], a[216:219], v[108:111], v[76:79]\n"
    "buffer_load_dwordx4 v1, s[20:23], s32 offen offset:1024  lds\n"
    "v_mfma_f32_16x16x32_bf16 v[76:79], a[220:223], v[112:115], v[76:79]\n"
    "v_exp_f32 v241, v69\n"
    "v_mfma_f32_16x16x32_bf16 a[96:99], a[224:227], v[40:43], a[96:99]\n"
    "v_cmp_nge_f32 vcc, s40, v12\n"
    "v_mfma_f32_16x16x32_bf16 a[96:99], v[180:183], v[44:47], a[96:99]\n"
    "v_exp_f32 v242, v70\n"
    "v_mfma_f32_16x16x32_bf16 a[100:103], a[228:231], v[40:43], a[100:103]\n"
    "v_mfma_f32_16x16x32_bf16 a[100:103], v[184:187], v[44:47], a[100:103]\n"
    "s_cbranch_vccnz LTR_29\n"
    "LRET_29:\n"
    "v_add_f32 v24, v24, v12\n"
    "v_mfma_f32_16x16x32_bf16 a[104:107], a[232:235], v[40:43], a[104:107]\n"
    "v_mfma_f32_16x16x32_bf16 a[104:107], v[188:191], v[44:47], a[104:107]\n"
    "v_mfma_f32_16x16x32_bf16 a[108:111], a[236:239], v[40:43], a[108:111]\n"
    "v_mfma_f32_16x16x32_bf16 a[108:111], v[192:195], v[44:47], a[108:111]\n"
    "v_mfma_f32_16x16x32_bf16 a[112:115], a[240:243], v[40:43], a[112:115]\n"
    "v_mfma_f32_16x16x32_bf16 a[112:115], v[196:199], v[44:47], a[112:115]\n"
    "v_mfma_f32_16x16x32_bf16 a[116:119], a[244:247], v[40:43], a[116:119]\n"
    "v_mfma_f32_16x16x32_bf16 a[116:119], v[200:203], v[44:47], a[116:119]\n"
    "v_mfma_f32_16x16x32_bf16 a[120:123], a[248:251], v[40:43], a[120:123]\n"
    "v_mfma_f32_16x16x32_bf16 a[120:123], v[204:207], v[44:47], a[120:123]\n"
    "v_mfma_f32_16x16x32_bf16 a[124:127], a[252:255], v[40:43], a[124:127]\n"
    "v_mfma_f32_16x16x32_bf16 a[124:127], v[208:211], v[44:47], a[124:127]\n"
    "s_sub_u32 s37, s37, 1\n"
    "s_cmp_lg_u32 s37, 0\n"
    "s_cbranch_scc1 LLOOP5\n"
    "s_cmp_eq_u32 s38, 1\n"
    "s_cbranch_scc1 LFINAL5\n"
    "v_mfma_f32_16x16x32_bf16 v[48:51], a[160:163], v[116:119], v[84:87]\n"
    "v_exp_f32 v243, v71\n"
    "v_mfma_f32_16x16x32_bf16 v[48:51], a[164:167], v[120:123], v[48:51]\n"
    "v_cvt_pk_bf16_f32 v40, v8, v9\n"
    "v_cvt_pk_bf16_f32 v41, v10, v11\n"
    "v_mfma_f32_16x16x32_bf16 v[48:51], a[168:171], v[124:127], v[48:51]\n"
    "v_exp_f32 v8, v72\n"
    "v_mfma_f32_16x16x32_bf16 v[48:51], a[172:175], v[128:131], v[48:51]\n"
    "v_exp_f32 v9, v73\n"
    "v_mfma_f32_16x16x32_bf16 v[52:55], a[176:179], v[116:119], v[84:87]\n"
    "v_add_u32 v5, s36, v3\n"
    "v_mfma_f32_16x16x32_bf16 v[52:55], a[180:183], v[120:123], v[52:55]\n"
    "v_exp_f32 v10, v74\n"
    "v_mfma_f32_16x16x32_bf16 v[52:55], a[184:187], v[124:127], v[52:55]\n"
    "v_exp_f32 v11, v75\n"
    "v_mfma_f32_16x16x32_bf16 v[52:55], a[188:191], v[128:131], v[52:55]\n"
    "v_cvt_pk_bf16_f32 v42, v240, v241\n"
    "v_cvt_pk_bf16_f32 v43, v242, v243\n"
    "v_mfma_f32_16x16x32_bf16 v[56:59], a[192:195], v[116:119], v[84:87]\n"
    "v_exp_f32 v240, v76\n"
    "v_mfma_f32_16x16x32_bf16 v[56:59], a[196:199], v[120:123], v[56:59]\n"
    "v_exp_f32 v241, v77\n"
    "v_mfma_f32_16x16x32_bf16 v[56:59], a[200:203], v[124:127], v[56:59]\n"
    "v_exp_f32 v242, v78\n"
    "v_mfma_f32_16x16x32_bf16 v[56:59], a[204:207], v[128:131], v[56:59]\n"
    "v_exp_f32 v243, v79\n"
    "v_mfma_f32_16x16x32_bf16 v[60:63], a[208:211], v[116:119], v[84:87]\n"
    "v_cvt_pk_bf16_f32 v44, v8, v9\n"
    "v_cvt_pk_bf16_f32 v45, v10, v11\n"
    "v_mfma_f32_16x16x32_bf16 v[60:63], a[212:215], v[120:123], v[60:63]\n"
    "v_cvt_pk_bf16_f32 v46, v240, v241\n"
    "v_cvt_pk_bf16_f32 v47, v242, v243\n"
    "v_mfma_f32_16x16x32_bf16 v[60:63], a[216:219], v[124:127], v[60:63]\n"
    "s_waitcnt vmcnt(49)\n"
    "v_lshlrev_b32 v31, 16, v212\n"
    "v_and_b32 v255, s42, v212\n"
    "v_mfma_f32_16x16x32_bf16 v[60:63], a[220:223], v[128:131], v[60:63]\n"
    "v_mul_f32 v31, s41, v31\n"
    "v_mul_f32 v255, s41, v255\n"
    "v_mfma_f32_16x16x32_bf16 v[12:15], v[16:19], v[40:43], 0\n"
    "v_cvt_pk_bf16_f32 v100, v31, v255\n"
    "v_lshlrev_b32 v31, 16, v213\n"
    "v_mfma_f32_16x16x32_bf16 v[12:15], v[16:19], v[44:47], v[12:15]\n"
    "v_and_b32 v255, s42, v213\n"
    "v_mul_f32 v31, s41, v31\n"
    "v_mfma_f32_16x16x32_bf16 a[128:131], a[224:227], v[32:35], a[128:131]\n"
    "ds_read_b64_tr_b16 a[224:225], v5 offset:0\n"
    "ds_read_b64_tr_b16 a[226:227], v5 offset:256\n"
    "v_mfma_f32_16x16x32_bf16 a[128:131], v[180:183], v[36:39], a[128:131]\n"
    "v_mul_f32 v255, s41, v255\n"
    "v_cvt_pk_bf16_f32 v101, v31, v255\n"
    "v_mfma_f32_16x16x32_bf16 a[132:135], a[228:231], v[32:35], a[132:135]\n"
    "ds_read_b64_tr_b16 v[180:181], v5 offset:512\n"
    "ds_read_b64_tr_b16 v[182:183], v5 offset:768\n"
    "v_mfma_f32_16x16x32_bf16 a[132:135], v[184:187], v[36:39], a[132:135]\n"
    "v_lshlrev_b32 v31, 16, v214\n"
    "v_and_b32 v255, s42, v214\n"
    "v_mfma_f32_16x16x32_bf16 a[136:139], a[232:235], v[32:35], a[136:139]\n"
    "ds_read_b64_tr_b16 a[228:229], v5 offset:32\n"
    "ds_read_b64_tr_b16 a[230:231], v5 offset:288\n"
    "v_mfma_f32_16x16x32_bf16 a[136:139], v[188:191], v[36:39], a[136:139]\n"
    "v_cmp_nge_f32 vcc, s40, v12\n"
    "v_mfma_f32_16x16x32_bf16 a[140:143], a[236:239], v[32:35], a[140:143]\n"
    "ds_read_b64_tr_b16 v[184:185], v5 offset:544\n"
    "ds_read_b64_tr_b16 v[186:187], v5 offset:800\n"
    "v_mfma_f32_16x16x32_bf16 a[140:143], v[192:195], v[36:39], a[140:143]\n"
    "ds_read_b128 v[116:119], v229 offset:0\n"
    "ds_read_b128 v[120:123], v229 offset:1024\n"
    "v_mfma_f32_16x16x32_bf16 a[144:147], a[240:243], v[32:35], a[144:147]\n"
    "ds_read_b64_tr_b16 a[232:233], v5 offset:64\n"
    "ds_read_b64_tr_b16 a[234:235], v5 offset:320\n"
    "v_mfma_f32_16x16x32_bf16 a[144:147], v[196:199], v[36:39], a[144:147]\n"
    "v_mul_f32 v31, s41, v31\n"
    "v_mul_f32 v255, s41, v255\n"
    "v_mfma_f32_16x16x32_bf16 a[148:151], a[244:247], v[32:35], a[148:151]\n"
    "ds_read_b64_tr_b16 v[188:189], v5 offset:576\n"
    "ds_read_b64_tr_b16 v[190:191], v5 offset:832\n"
    "v_mfma_f32_16x16x32_bf16 a[148:151], v[200:203], v[36:39], a[148:151]\n"
    "s_cbranch_vccnz LTR_30\n"
    "LRET_30:\n"
    "v_add_f32 v20, v20, v12\n"
    "v_mfma_f32_16x16x32_bf16 a[152:155], a[248:251], v[32:35], a[152:155]\n"
    "ds_read_b64_tr_b16 a[236:237], v5 offset:96\n"
    "ds_read_b64_tr_b16 a[238:239], v5 offset:352\n"
    "v_mfma_f32_16x16x32_bf16 a[152:155], v[204:207], v[36:39], a[152:155]\n"
    "v_cvt_pk_bf16_f32 v102, v31, v255\n"
    "v_lshlrev_b32 v31, 16, v215\n"
    "v_mfma_f32_16x16x32_bf16 a[156:159], a[252:255], v[32:35], a[156:159]\n"
    "ds_read_b64_tr_b16 v[192:193], v5 offset:608\n"
    "ds_read_b64_tr_b16 v[194:195], v5 offset:864\n"
    "v_mfma_f32_16x16x32_bf16 a[156:159], v[208:211], v[36:39], a[156:159]\n"
    "ds_read_b128 v[124:127], v229 offset:2048\n"
    "ds_read_b128 v[128:131], v229 offset:3072\n"
    "v_mfma_f32_16x16x32_bf16 v[64:67], a[160:163], v[132:135], v[88:91]\n"
    "ds_read_b64_tr_b16 a[240:241], v5 offset:128\n"
    "ds_read_b64_tr_b16 a[242:243], v5 offset:384\n"
    "v_and_b32 v255, s42, v215\n"
    "v_mul_f32 v31, s41, v31\n"
    "v_mul_f32 v255, s41, v255\n"
    "v_cvt_pk_bf16_f32 v103, v31, v255\n"
    "v_mfma_f32_16x16x32_bf16 v[64:67], a[164:167], v[136:139], v[64:67]\n"
    "v_lshlrev_b32 v31, 16, v216\n"
    "v_and_b32 v255, s42, v216\n"
    "v_mul_f32 v31, s41, v31\n"
    "v_mul_f32 v255, s41, v255\n"
    "v_cvt_pk_bf16_f32 v104, v31, v255\n"
    "v_lshlrev_b32 v31, 16, v217\n"
    "v_mfma_f32_16x16x32_bf16 v[64:67], a[168:171], v[140:143], v[64:67]\n"
    "ds_read_b64_tr_b16 v[196:197], v5 offset:640\n"
    "ds_read_b64_tr_b16 v[198:199], v5 offset:896\n"
    "v_exp_f32 v8, v48\n"
    "v_and_b32 v255, s42, v217\n"
    "v_mul_f32 v31, s41, v31\n"
    "v_mfma_f32_16x16x32_bf16 v[64:67], a[172:175], v[144:147], v[64:67]\n"
    "v_exp_f32 v9, v49\n"
    "v_mul_f32 v255, s41, v255\n"
    "v_cvt_pk_bf16_f32 v105, v31, v255\n"
    "v_exp_f32 v10, v50\n"
    "v_mfma_f32_16x16x32_bf16 v[68:71], a[176:179], v[132:135], v[88:91]\n"
    "ds_read_b64_tr_b16 a[244:245], v5 offset:160\n"
    "ds_read_b64_tr_b16 a[246:247], v5 offset:416\n"
    "v_lshlrev_b32 v31, 16, v218\n"
    "v_and_b32 v255, s42, v218\n"
    "v_exp_f32 v11, v51\n"
    "v_mfma_f32_16x16x32_bf16 v[68:71], a[180:183], v[136:139], v[68:71]\n"
    "v_mul_f32 v31, s41, v31\n"
    "v_mul_f32 v255, s41, v255\n"
    "v_exp_f32 v240, v52\n"
    "v_cvt_pk_bf16_f32 v106, v31, v255\n"
    "v_lshlrev_b32 v31, 16, v219\n"
    "v_mfma_f32_16x16x32_bf16 v[68:71], a[184:187], v[140:143], v[68:71]\n"
    "ds_read_b64_tr_b16 v[200:201], v5 offset:672\n"
    "ds_read_b64_tr_b16 v[202:203], v5 offset:928\n"
    "v_exp_f32 v241, v53\n"
    "v_and_b32 v255, s42, v219\n"
    "v_mul_f32 v31, s41, v31\n"
    "v_mfma_f32_16x16x32_bf16 v[68:71], a[188:191], v[144:147], v[68:71]\n"
    "s_add_u32 m0, s34, 64\n"
    "v_exp_f32 v242, v54\n"
    "v_mul_f32 v255, s41, v255\n"
    "v_cvt_pk_bf16_f32 v107, v31, v255\n"
    "v_exp_f32 v243, v55\n"
    "v_mfma_f32_16x16x32_bf16 v[72:75], a[192:195], v[132:135], v[88:91]\n"
    "ds_read_b64_tr_b16 a[248:249], v5 offset:192\n"
    "ds_read_b64_tr_b16 a[250:251], v5 offset:448\n"
    "v_lshlrev_b32 v31, 16, v220\n"
    "v_and_b32 v255, s42, v220\n"
    "buffer_load_dwordx4 v1, s[20:23], s32 offen offset:2048  lds\n"
    "v_mfma_f32_16x16x32_bf16 v[72:75], a[196:199], v[136:139], v[72:75]\n"
    "v_cvt_pk_bf16_f32 v32, v8, v9\n"
    "v_cvt_pk_bf16_f32 v33, v10, v11\n"
    "v_mul_f32 v31, s41, v31\n"
    "v_mul_f32 v255, s41, v255\n"
    "v_exp_f32 v8, v56\n"
    "v_mfma_f32_16x16x32_bf16 v[72:75], a[200:203], v[140:143], v[72:75]\n"
    "ds_read_b64_tr_b16 v[204:205], v5 offset:704\n"
    "ds_read_b64_tr_b16 v[206:207], v5 offset:960\n"
    "v_cvt_pk_bf16_f32 v108, v31, v255\n"
    "v_lshlrev_b32 v31, 16, v221\n"
    "v_exp_f32 v9, v57\n"
    "v_mfma_f32_16x16x32_bf16 v[72:75], a[204:207], v[144:147], v[72:75]\n"
    "v_and_b32 v255, s42, v221\n"
    "v_mul_f32 v31, s41, v31\n"
    "v_exp_f32 v10, v58\n"
    "v_mul_f32 v255, s41, v255\n"
    "v_cvt_pk_bf16_f32 v109, v31, v255\n"
    "v_mfma_f32_16x16x32_bf16 v[76:79], a[208:211], v[132:135], v[88:91]\n"
    "ds_read_b64_tr_b16 a[252:253], v5 offset:224\n"
    "ds_read_b64_tr_b16 a[254:255], v5 offset:480\n"
    "v_exp_f32 v11, v59\n"
    "v_lshlrev_b32 v31, 16, v222\n"
    "v_and_b32 v255, s42, v222\n"
    "v_mfma_f32_16x16x32_bf16 v[76:79], a[212:215], v[136:139], v[76:79]\n"
    "v_cvt_pk_bf16_f32 v34, v240, v241\n"
    "v_cvt_pk_bf16_f32 v35, v242, v243\n"
    "v_mul_f32 v31, s41, v31\n"
    "v_mul_f32 v255, s41, v255\n"
    "v_exp_f32 v240, v60\n"
    "v_mfma_f32_16x16x32_bf16 v[76:79], a[216:219], v[140:143], v[76:79]\n"
    "ds_read_b64_tr_b16 v[208:209], v5 offset:736\n"
    "ds_read_b64_tr_b16 v[210:211], v5 offset:992\n"
    "v_cvt_pk_bf16_f32 v110, v31, v255\n"
    "v_lshlrev_b32 v31, 16, v223\n"
    "v_exp_f32 v241, v61\n"
    "v_mfma_f32_16x16x32_bf16 v[76:79], a[220:223], v[144:147], v[76:79]\n"
    "s_waitcnt lgkmcnt(15)\n"
    "s_add_u32 m0, s34, 96\n"
    "v_and_b32 v255, s42, v223\n"
    "v_mul_f32 v31, s41, v31\n"
    "v_exp_f32 v242, v62\n"
    "v_mul_f32 v255, s41, v255\n"
    "v_cvt_pk_bf16_f32 v111, v31, v255\n"
    "v_mfma_f32_16x16x32_bf16 a[0:3], a[224:227], v[40:43], a[0:3]\n"
    "buffer_load_dwordx4 v1, s[20:23], s32 offen offset:3072  lds\n"
    "v_exp_f32 v243, v63\n"
    "v_lshlrev_b32 v31, 16, v224\n"
    "v_and_b32 v255, s42, v224\n"
    "v_mfma_f32_16x16x32_bf16 a[0:3], v[180:183], v[44:47], a[0:3]\n"
    "v_cvt_pk_bf16_f32 v36, v8, v9\n"
    "v_cvt_pk_bf16_f32 v37, v10, v11\n"
    "v_mul_f32 v31, s41, v31\n"
    "v_mul_f32 v255, s41, v255\n"
    "v_mfma_f32_16x16x32_bf16 a[4:7], a[228:231], v[40:43], a[4:7]\n"
    "v_cvt_pk_bf16_f32 v38, v240, v241\n"
    "v_cvt_pk_bf16_f32 v39, v242, v243\n"
    "v_cvt_pk_bf16_f32 v112, v31, v255\n"
    "v_lshlrev_b32 v31, 16, v225\n"
    "v_mfma_f32_16x16x32_bf16 a[4:7], v[184:187], v[44:47], a[4:7]\n"
    "v_and_b32 v255, s42, v225\n"
    "v_mul_f32 v31, s41, v31\n"
    "v_mul_f32 v255, s41, v255\n"
    "v_cvt_pk_bf16_f32 v113, v31, v255\n"
    "v_mfma_f32_16x16x32_bf16 v[12:15], v[16:19], v[32:35], 0\n"
    "v_lshlrev_b32 v31, 16, v226\n"
    "v_and_b32 v255, s42, v226\n"
    "v_mul_f32 v31, s41, v31\n"
    "v_mul_f32 v255, s41, v255\n"
    "v_mfma_f32_16x16x32_bf16 v[12:15], v[16:19], v[36:39], v[12:15]\n"
    "s_add_u32 m0, s34, 16896\n"
    "ds_read_b128 v[132:135], v229 offset:4096\n"
    "ds_read_b128 v[136:139], v229 offset:5120\n"
    "v_cvt_pk_bf16_f32 v114, v31, v255\n"
    "v_lshlrev_b32 v31, 16, v227\n"
    "v_mfma_f32_16x16x32_bf16 a[8:11], a[232:235], v[40:43], a[8:11]\n"
    "buffer_load_dwordx4 v1, s[24:27], s32 offen offset:0  lds\n"
    "v_and_b32 v255, s42, v227\n"
    "v_mul_f32 v31, s41, v31\n"
    "v_mfma_f32_16x16x32_bf16 a[8:11], v[188:191], v[44:47], a[8:11]\n"
    "ds_read_b128 v[140:143], v229 offset:6144\n"
    "ds_read_b128 v[144:147], v229 offset:7168\n"
    "v_mul_f32 v255, s41, v255\n"
    "v_cvt_pk_bf16_f32 v115, v31, v255\n"
    "v_mfma_f32_16x16x32_bf16 a[12:15], a[236:239], v[40:43], a[12:15]\n"
    "v_exp_f32 v8, v64\n"
    "v_exp_f32 v9, v65\n"
    "v_mfma_f32_16x16x32_bf16 a[12:15], v[192:195], v[44:47], a[12:15]\n"
    "s_waitcnt lgkmcnt(4)\n"
    "v_exp_f32 v10, v66\n"
    "v_exp_f32 v11, v67\n"
    "v_exp_f32 v240, v68\n"
    "v_mfma_f32_16x16x32_bf16 a[16:19], a[240:243], v[40:43], a[16:19]\n"
    "v_cmp_nge_f32 vcc, s40, v12\n"
    "v_exp_f32 v241, v69\n"
    "v_exp_f32 v242, v70\n"
    "v_mfma_f32_16x16x32_bf16 a[16:19], v[196:199], v[44:47], a[16:19]\n"
    "v_exp_f32 v243, v71\n"
    "v_mfma_f32_16x16x32_bf16 a[20:23], a[244:247], v[40:43], a[20:23]\n"
    "v_mfma_f32_16x16x32_bf16 a[20:23], v[200:203], v[44:47], a[20:23]\n"
    "s_cbranch_vccnz LTR_31\n"
    "LRET_31:\n"
    "v_add_f32 v21, v21, v12\n"
    "v_mfma_f32_16x16x32_bf16 a[24:27], a[248:251], v[40:43], a[24:27]\n"
    "v_mfma_f32_16x16x32_bf16 a[24:27], v[204:207], v[44:47], a[24:27]\n"
    "v_mfma_f32_16x16x32_bf16 a[28:31], a[252:255], v[40:43], a[28:31]\n"
    "v_cvt_pk_bf16_f32 v40, v8, v9\n"
    "v_cvt_pk_bf16_f32 v41, v10, v11\n"
    "v_exp_f32 v8, v72\n"
    "v_exp_f32 v9, v73\n"
    "v_exp_f32 v10, v74\n"
    "v_exp_f32 v11, v75\n"
    "v_cvt_pk_bf16_f32 v42, v240, v241\n"
    "v_cvt_pk_bf16_f32 v43, v242, v243\n"
    "v_exp_f32 v240, v76\n"
    "v_exp_f32 v241, v77\n"
    "v_exp_f32 v242, v78\n"
    "v_mfma_f32_16x16x32_bf16 a[28:31], v[208:211], v[44:47], a[28:31]\n"
    "v_exp_f32 v243, v79\n"
    "v_cvt_pk_bf16_f32 v44, v8, v9\n"
    "v_cvt_pk_bf16_f32 v45, v10, v11\n"
    "v_cvt_pk_bf16_f32 v46, v240, v241\n"
    "v_cvt_pk_bf16_f32 v47, v242, v243\n"
    "v_mfma_f32_16x16x32_bf16 v[12:15], v[16:19], v[40:43], 0\n"
    "s_nop 0\n"
    "v_mfma_f32_16x16x32_bf16 v[12:15], v[16:19], v[44:47], v[12:15]\n"
    "v_mfma_f32_16x16x32_bf16 v[48:51], a[160:163], v[148:151], v[92:95]\n"
    "s_add_u32 m0, s34, 16928\n"
    "v_mfma_f32_16x16x32_bf16 v[48:51], a[164:167], v[152:155], v[48:51]\n"
    "buffer_load_dwordx4 v1, s[24:27], s32 offen offset:1024  lds\n"
    "v_mfma_f32_16x16x32_bf16 v[48:51], a[168:171], v[156:159], v[48:51]\n"
    "v_rcp_f32 v254, v20\n"
    "v_accvgpr_read_b32 v244, a0\n"
    "v_accvgpr_read_b32 v245, a1\n"
    "v_mul_f32 v244, v254, v244\n"
    "v_mul_f32 v245, v254, v245\n"
    "v_cvt_pk_bf16_f32 v246, v244, v245\n"
    "v_accvgpr_read_b32 v244, a2\n"
    "v_accvgpr_read_b32 v245, a3\n"
    "v_mul_f32 v244, v254, v244\n"
    "v_mul_f32 v245, v254, v245\n"
    "v_cvt_pk_bf16_f32 v247, v244, v245\n"
    "v_xor_b32 v31, 0, v231\n"
    "ds_write_b64 v31, v[246:247] offset:0\n"
    "v_accvgpr_read_b32 v244, a4\n"
    "v_accvgpr_read_b32 v245, a5\n"
    "v_mul_f32 v244, v254, v244\n"
    "v_mul_f32 v245, v254, v245\n"
    "v_cvt_pk_bf16_f32 v248, v244, v245\n"
    "v_accvgpr_read_b32 v244, a6\n"
    "v_accvgpr_read_b32 v245, a7\n"
    "v_mul_f32 v244, v254, v244\n"
    "v_mul_f32 v245, v254, v245\n"
    "v_cvt_pk_bf16_f32 v249, v244, v245\n"
    "v_xor_b32 v255, 32, v231\n"
    "ds_write_b64 v255, v[248:249] offset:0\n"
    "v_accvgpr_read_b32 v244, a8\n"
    "v_accvgpr_read_b32 v245, a9\n"
    "v_mul_f32 v244, v254, v244\n"
    "v_mul_f32 v245, v254, v245\n"
    "v_cvt_pk_bf16_f32 v250, v244, v245\n"
    "v_accvgpr_read_b32 v244, a10\n"
    "v_accvgpr_read_b32 v245, a11\n"
    "v_mul_f32 v244, v254, v244\n"
    "v_mul_f32 v245, v254, v245\n"
    "v_cvt_pk_bf16_f32 v251, v244, v245\n"
    "v_xor_b32 v31, 64, v231\n"
    "ds_write_b64 v31, v[250:251] offset:0\n"
    "v_accvgpr_read_b32 v244, a12\n"
    "v_accvgpr_read_b32 v245, a13\n"
    "v_mul_f32 v244, v254, v244\n"
    "v_mfma_f32_16x16x32_bf16 v[48:51], a[172:175], v[160:163], v[48:51]\n"
    "v_mul_f32 v245, v254, v245\n"
    "v_cvt_pk_bf16_f32 v252, v244, v245\n"
    "v_accvgpr_read_b32 v244, a14\n"
    "v_accvgpr_read_b32 v245, a15\n"
    "v_mfma_f32_16x16x32_bf16 v[52:55], a[176:179], v[148:151], v[92:95]\n"
    "v_mul_f32 v244, v254, v244\n"
    "v_mul_f32 v245, v254, v245\n"
    "v_cvt_pk_bf16_f32 v253, v244, v245\n"
    "v_xor_b32 v255, 96, v231\n"
    "v_mfma_f32_16x16x32_bf16 v[52:55], a[180:183], v[152:155], v[52:55]\n"
    "ds_write_b64 v255, v[252:253] offset:0\n"
    "v_accvgpr_read_b32 v244, a16\n"
    "v_accvgpr_read_b32 v245, a17\n"
    "v_mul_f32 v244, v254, v244\n"
    "v_mfma_f32_16x16x32_bf16 v[52:55], a[184:187], v[156:159], v[52:55]\n"
    "v_mul_f32 v245, v254, v245\n"
    "v_cvt_pk_bf16_f32 v246, v244, v245\n"
    "v_accvgpr_read_b32 v244, a18\n"
    "v_accvgpr_read_b32 v245, a19\n"
    "v_mfma_f32_16x16x32_bf16 v[52:55], a[188:191], v[160:163], v[52:55]\n"
    "v_mul_f32 v244, v254, v244\n"
    "v_mul_f32 v245, v254, v245\n"
    "v_cvt_pk_bf16_f32 v247, v244, v245\n"
    "v_xor_b32 v31, 128, v231\n"
    "v_mfma_f32_16x16x32_bf16 v[56:59], a[192:195], v[148:151], v[92:95]\n"
    "s_add_u32 m0, s34, 16960\n"
    "ds_write_b64 v31, v[246:247] offset:0\n"
    "v_accvgpr_read_b32 v244, a20\n"
    "v_accvgpr_read_b32 v245, a21\n"
    "v_mul_f32 v244, v254, v244\n"
    "v_mfma_f32_16x16x32_bf16 v[56:59], a[196:199], v[152:155], v[56:59]\n"
    "buffer_load_dwordx4 v1, s[24:27], s32 offen offset:2048  lds\n"
    "v_mul_f32 v245, v254, v245\n"
    "v_cvt_pk_bf16_f32 v248, v244, v245\n"
    "v_mfma_f32_16x16x32_bf16 v[56:59], a[200:203], v[156:159], v[56:59]\n"
    "v_accvgpr_read_b32 v244, a22\n"
    "v_accvgpr_read_b32 v245, a23\n"
    "v_mul_f32 v244, v254, v244\n"
    "v_mul_f32 v245, v254, v245\n"
    "v_mfma_f32_16x16x32_bf16 v[56:59], a[204:207], v[160:163], v[56:59]\n"
    "v_cvt_pk_bf16_f32 v249, v244, v245\n"
    "v_xor_b32 v255, 160, v231\n"
    "ds_write_b64 v255, v[248:249] offset:0\n"
    "v_accvgpr_read_b32 v244, a24\n"
    "v_mfma_f32_16x16x32_bf16 v[60:63], a[208:211], v[148:151], v[92:95]\n"
    "s_add_u32 m0, s34, 16992\n"
    "v_accvgpr_read_b32 v245, a25\n"
    "v_mul_f32 v244, v254, v244\n"
    "v_mul_f32 v245, v254, v245\n"
    "v_cvt_pk_bf16_f32 v250, v244, v245\n"
    "v_mfma_f32_16x16x32_bf16 v[60:63], a[212:215], v[152:155], v[60:63]\n"
    "buffer_load_dwordx4 v1, s[24:27], s32 offen offset:3072  lds\n"
    "v_accvgpr_read_b32 v244, a26\n"
    "v_accvgpr_read_b32 v245, a27\n"
    "v_mfma_f32_16x16x32_bf16 v[60:63], a[216:219], v[156:159], v[60:63]\n"
    "v_mul_f32 v244, v254, v244\n"
    "v_mul_f32 v245, v254, v245\n"
    "v_cvt_pk_bf16_f32 v251, v244, v245\n"
    "v_xor_b32 v31, 192, v231\n"
    "v_mfma_f32_16x16x32_bf16 v[60:63], a[220:223], v[160:163], v[60:63]\n"
    "ds_write_b64 v31, v[250:251] offset:0\n"
    "v_accvgpr_read_b32 v244, a28\n"
    "v_accvgpr_read_b32 v245, a29\n"
    "v_mul_f32 v244, v254, v244\n"
    "v_mfma_f32_16x16x32_bf16 a[32:35], a[224:227], v[32:35], a[32:35]\n"
    "v_mul_f32 v245, v254, v245\n"
    "v_cvt_pk_bf16_f32 v252, v244, v245\n"
    "v_cmp_nge_f32 vcc, s40, v12\n"
    "v_mfma_f32_16x16x32_bf16 a[32:35], v[180:183], v[36:39], a[32:35]\n"
    "v_accvgpr_read_b32 v244, a30\n"
    "v_accvgpr_read_b32 v245, a31\n"
    "v_mul_f32 v244, v254, v244\n"
    "v_mul_f32 v245, v254, v245\n"
    "v_mfma_f32_16x16x32_bf16 a[36:39], a[228:231], v[32:35], a[36:39]\n"
    "ds_read_b128 v[148:151], v229 offset:8192\n"
    "ds_read_b128 v[152:155], v229 offset:9216\n"
    "v_cvt_pk_bf16_f32 v253, v244, v245\n"
    "v_xor_b32 v255, 224, v231\n"
    "v_mfma_f32_16x16x32_bf16 a[36:39], v[184:187], v[36:39], a[36:39]\n"
    "s_cbranch_vccnz LTR_32\n"
    "LRET_32:\n"
    "v_add_f32 v22, v22, v12\n"
    "ds_write_b64 v255, v[252:253] offset:0\n"
    "v_mfma_f32_16x16x32_bf16 a[40:43], a[232:235], v[32:35], a[40:43]\n"
    "ds_read_b128 v[156:159], v229 offset:10240\n"
    "ds_read_b128 v[160:163], v229 offset:11264\n"
    "v_exp_f32 v8, v48\n"
    "v_mfma_f32_16x16x32_bf16 a[40:43], v[188:191], v[36:39], a[40:43]\n"
    "v_exp_f32 v9, v49\n"
    "v_exp_f32 v10, v50\n"
    "v_mfma_f32_16x16x32_bf16 a[44:47], a[236:239], v[32:35], a[44:47]\n"
    "v_exp_f32 v11, v51\n"
    "v_exp_f32 v240, v52\n"
    "v_mfma_f32_16x16x32_bf16 a[44:47], v[192:195], v[36:39], a[44:47]\n"
    "v_exp_f32 v241, v53\n"
    "v_exp_f32 v242, v54\n"
    "v_mfma_f32_16x16x32_bf16 a[48:51], a[240:243], v[32:35], a[48:51]\n"
    "s_add_u32 s34, s36, s35\n"
    "v_exp_f32 v243, v55\n"
    "v_mfma_f32_16x16x32_bf16 a[48:51], v[196:199], v[36:39], a[48:51]\n"
    "v_mfma_f32_16x16x32_bf16 a[52:55], a[244:247], v[32:35], a[52:55]\n"
    "s_add_u32 s36, s36, 33792\n"
    "v_mfma_f32_16x16x32_bf16 a[52:55], v[200:203], v[36:39], a[52:55]\n"
    "v_mfma_f32_16x16x32_bf16 a[56:59], a[248:251], v[32:35], a[56:59]\n"
    "s_sub_u32 s50, s36, 101376\n"
    "v_mfma_f32_16x16x32_bf16 a[56:59], v[204:207], v[36:39], a[56:59]\n"
    "v_mfma_f32_16x16x32_bf16 a[60:63], a[252:255], v[32:35], a[60:63]\n"
    "s_min_u32 s36, s36, s50\n"
    "v_cvt_pk_bf16_f32 v32, v8, v9\n"
    "v_cvt_pk_bf16_f32 v33, v10, v11\n"
    "v_exp_f32 v8, v56\n"
    "v_exp_f32 v9, v57\n"
    "v_exp_f32 v10, v58\n"
    "v_exp_f32 v11, v59\n"
    "v_cvt_pk_bf16_f32 v34, v240, v241\n"
    "v_cvt_pk_bf16_f32 v35, v242, v243\n"
    "v_exp_f32 v240, v60\n"
    "v_exp_f32 v241, v61\n"
    "v_exp_f32 v242, v62\n"
    "v_mfma_f32_16x16x32_bf16 a[60:63], v[208:211], v[36:39], a[60:63]\n"
    "s_add_u32 s32, s32, 16384\n"
    "v_exp_f32 v243, v63\n"
    "v_cvt_pk_bf16_f32 v36, v8, v9\n"
    "v_cvt_pk_bf16_f32 v37, v10, v11\n"
    "v_cvt_pk_bf16_f32 v38, v240, v241\n"
    "v_cvt_pk_bf16_f32 v39, v242, v243\n"
    "v_mfma_f32_16x16x32_bf16 v[12:15], v[16:19], v[32:35], 0\n"
    "s_nop 0\n"
    "v_mfma_f32_16x16x32_bf16 v[12:15], v[16:19], v[36:39], v[12:15]\n"
    "v_mfma_f32_16x16x32_bf16 v[64:67], a[160:163], v[164:167], v[96:99]\n"
    "v_add_u32 v4, s36, v2\n"
    "s_waitcnt vmcnt(8)\n"
    "s_barrier\n"
    "v_mfma_f32_16x16x32_bf16 v[64:67], a[164:167], v[168:171], v[64:67]\n"
    "ds_read_b128 a[160:163], v4 offset:0\n"
    "ds_read_b128 a[164:167], v4 offset:64\n"
    "v_mfma_f32_16x16x32_bf16 v[64:67], a[168:171], v[172:175], v[64:67]\n"
    "v_rcp_f32 v254, v21\n"
    "v_accvgpr_read_b32 v244, a32\n"
    "v_accvgpr_read_b32 v245, a33\n"
    "v_mul_f32 v244, v254, v244\n"
    "v_mul_f32 v245, v254, v245\n"
    "v_cvt_pk_bf16_f32 v246, v244, v245\n"
    "v_accvgpr_read_b32 v244, a34\n"
    "v_accvgpr_read_b32 v245, a35\n"
    "v_mul_f32 v244, v254, v244\n"
    "v_mul_f32 v245, v254, v245\n"
    "v_cvt_pk_bf16_f32 v247, v244, v245\n"
    "v_xor_b32 v31, 0, v231\n"
    "ds_write_b64 v31, v[246:247] offset:4096\n"
    "v_accvgpr_read_b32 v244, a36\n"
    "v_accvgpr_read_b32 v245, a37\n"
    "v_mul_f32 v244, v254, v244\n"
    "v_mul_f32 v245, v254, v245\n"
    "v_cvt_pk_bf16_f32 v248, v244, v245\n"
    "v_accvgpr_read_b32 v244, a38\n"
    "v_accvgpr_read_b32 v245, a39\n"
    "v_mul_f32 v244, v254, v244\n"
    "v_mul_f32 v245, v254, v245\n"
    "v_cvt_pk_bf16_f32 v249, v244, v245\n"
    "v_xor_b32 v255, 32, v231\n"
    "ds_write_b64 v255, v[248:249] offset:4096\n"
    "v_accvgpr_read_b32 v244, a40\n"
    "v_accvgpr_read_b32 v245, a41\n"
    "v_mul_f32 v244, v254, v244\n"
    "v_mul_f32 v245, v254, v245\n"
    "v_cvt_pk_bf16_f32 v250, v244, v245\n"
    "v_accvgpr_read_b32 v244, a42\n"
    "v_accvgpr_read_b32 v245, a43\n"
    "v_mul_f32 v244, v254, v244\n"
    "v_mul_f32 v245, v254, v245\n"
    "v_cvt_pk_bf16_f32 v251, v244, v245\n"
    "v_xor_b32 v31, 64, v231\n"
    "ds_write_b64 v31, v[250:251] offset:4096\n"
    "v_accvgpr_read_b32 v244, a44\n"
    "v_accvgpr_read_b32 v245, a45\n"
    "v_mul_f32 v244, v254, v244\n"
    "v_mfma_f32_16x16x32_bf16 v[64:67], a[172:175], v[176:179], v[64:67]\n"
    "v_mul_f32 v245, v254, v245\n"
    "v_cvt_pk_bf16_f32 v252, v244, v245\n"
    "v_accvgpr_read_b32 v244, a46\n"
    "v_accvgpr_read_b32 v245, a47\n"
    "v_mfma_f32_16x16x32_bf16 v[68:71], a[176:179], v[164:167], v[96:99]\n"
    "v_mul_f32 v244, v254, v244\n"
    "v_mul_f32 v245, v254, v245\n"
    "v_cvt_pk_bf16_f32 v253, v244, v245\n"
    "v_xor_b32 v255, 96, v231\n"
    "v_mfma_f32_16x16x32_bf16 v[68:71], a[180:183], v[168:171], v[68:71]\n"
    "ds_write_b64 v255, v[252:253] offset:4096\n"
    "v_accvgpr_read_b32 v244, a48\n"
    "v_accvgpr_read_b32 v245, a49\n"
    "v_mul_f32 v244, v254, v244\n"
    "v_mfma_f32_16x16x32_bf16 v[68:71], a[184:187], v[172:175], v[68:71]\n"
    "v_mul_f32 v245, v254, v245\n"
    "v_cvt_pk_bf16_f32 v246, v244, v245\n"
    "v_accvgpr_read_b32 v244, a50\n"
    "v_accvgpr_read_b32 v245, a51\n"
    "v_mfma_f32_16x16x32_bf16 v[68:71], a[188:191], v[176:179], v[68:71]\n"
    "v_mul_f32 v244, v254, v244\n"
    "v_mul_f32 v245, v254, v245\n"
    "v_cvt_pk_bf16_f32 v247, v244, v245\n"
    "v_xor_b32 v31, 128, v231\n"
    "v_mfma_f32_16x16x32_bf16 v[72:75], a[192:195], v[164:167], v[96:99]\n"
    "ds_write_b64 v31, v[246:247] offset:4096\n"
    "v_accvgpr_read_b32 v244, a52\n"
    "v_accvgpr_read_b32 v245, a53\n"
    "v_mul_f32 v244, v254, v244\n"
    "v_mfma_f32_16x16x32_bf16 v[72:75], a[196:199], v[168:171], v[72:75]\n"
    "v_mul_f32 v245, v254, v245\n"
    "v_cvt_pk_bf16_f32 v248, v244, v245\n"
    "v_accvgpr_read_b32 v244, a54\n"
    "v_accvgpr_read_b32 v245, a55\n"
    "v_mfma_f32_16x16x32_bf16 v[72:75], a[200:203], v[172:175], v[72:75]\n"
    "ds_read_b128 a[168:171], v4 offset:128\n"
    "ds_read_b128 a[172:175], v4 offset:192\n"
    "v_mul_f32 v244, v254, v244\n"
    "v_mul_f32 v245, v254, v245\n"
    "v_mfma_f32_16x16x32_bf16 v[72:75], a[204:207], v[176:179], v[72:75]\n"
    "v_cvt_pk_bf16_f32 v249, v244, v245\n"
    "v_xor_b32 v255, 160, v231\n"
    "ds_write_b64 v255, v[248:249] offset:4096\n"
    "v_accvgpr_read_b32 v244, a56\n"
    "v_mfma_f32_16x16x32_bf16 v[76:79], a[208:211], v[164:167], v[96:99]\n"
    "ds_read_b128 a[176:179], v4 offset:256\n"
    "ds_read_b128 a[180:183], v4 offset:320\n"
    "v_accvgpr_read_b32 v245, a57\n"
    "v_mul_f32 v244, v254, v244\n"
    "v_mfma_f32_16x16x32_bf16 v[76:79], a[212:215], v[168:171], v[76:79]\n"
    "v_mul_f32 v245, v254, v245\n"
    "v_cvt_pk_bf16_f32 v250, v244, v245\n"
    "v_accvgpr_read_b32 v244, a58\n"
    "v_accvgpr_read_b32 v245, a59\n"
    "v_mfma_f32_16x16x32_bf16 v[76:79], a[216:219], v[172:175], v[76:79]\n"
    "ds_read_b128 a[184:187], v4 offset:384\n"
    "ds_read_b128 a[188:191], v4 offset:448\n"
    "v_mul_f32 v244, v254, v244\n"
    "v_mul_f32 v245, v254, v245\n"
    "v_mfma_f32_16x16x32_bf16 v[76:79], a[220:223], v[176:179], v[76:79]\n"
    "v_cvt_pk_bf16_f32 v251, v244, v245\n"
    "v_xor_b32 v31, 192, v231\n"
    "ds_write_b64 v31, v[250:251] offset:4096\n"
    "v_accvgpr_read_b32 v244, a60\n"
    "v_mfma_f32_16x16x32_bf16 a[64:67], a[224:227], v[40:43], a[64:67]\n"
    "ds_read_b128 a[192:195], v4 offset:512\n"
    "ds_read_b128 a[196:199], v4 offset:576\n"
    "v_accvgpr_read_b32 v245, a61\n"
    "v_mul_f32 v244, v254, v244\n"
    "v_mfma_f32_16x16x32_bf16 a[64:67], v[180:183], v[44:47], a[64:67]\n"
    "v_mul_f32 v245, v254, v245\n"
    "v_cvt_pk_bf16_f32 v252, v244, v245\n"
    "v_cmp_nge_f32 vcc, s40, v12\n"
    "v_mfma_f32_16x16x32_bf16 a[68:71], a[228:231], v[40:43], a[68:71]\n"
    "ds_read_b128 a[200:203], v4 offset:640\n"
    "ds_read_b128 a[204:207], v4 offset:704\n"
    "v_accvgpr_read_b32 v244, a62\n"
    "v_accvgpr_read_b32 v245, a63\n"
    "v_mfma_f32_16x16x32_bf16 a[68:71], v[184:187], v[44:47], a[68:71]\n"
    "ds_read_b128 v[164:167], v229 offset:12288\n"
    "ds_read_b128 v[168:171], v229 offset:13312\n"
    "v_mul_f32 v244, v254, v244\n"
    "v_mul_f32 v245, v254, v245\n"
    "v_mfma_f32_16x16x32_bf16 a[72:75], a[232:235], v[40:43], a[72:75]\n"
    "ds_read_b128 a[208:211], v4 offset:768\n"
    "ds_read_b128 a[212:215], v4 offset:832\n"
    "v_cvt_pk_bf16_f32 v253, v244, v245\n"
    "v_xor_b32 v255, 224, v231\n"
    "v_mfma_f32_16x16x32_bf16 a[72:75], v[188:191], v[44:47], a[72:75]\n"
    "s_cbranch_vccnz LTR_33\n"
    "LRET_33:\n"
    "v_add_f32 v23, v23, v12\n"
    "ds_write_b64 v255, v[252:253] offset:4096\n"
    "v_mfma_f32_16x16x32_bf16 a[76:79], a[236:239], v[40:43], a[76:79]\n"
    "ds_read_b128 a[216:219], v4 offset:896\n"
    "ds_read_b128 a[220:223], v4 offset:960\n"
    "ds_read_b128 v[172:175], v229 offset:14336\n"
    "ds_read_b128 v[176:179], v229 offset:15360\n"
    "v_mfma_f32_16x16x32_bf16 a[76:79], v[192:195], v[44:47], a[76:79]\n"
    "v_exp_f32 v8, v64\n"
    "v_exp_f32 v9, v65\n"
    "v_mfma_f32_16x16x32_bf16 a[80:83], a[240:243], v[40:43], a[80:83]\n"
    "v_exp_f32 v10, v66\n"
    "v_exp_f32 v11, v67\n"
    "v_exp_f32 v240, v68\n"
    "v_mfma_f32_16x16x32_bf16 a[80:83], v[196:199], v[44:47], a[80:83]\n"
    "v_exp_f32 v241, v69\n"
    "v_exp_f32 v242, v70\n"
    "v_exp_f32 v243, v71\n"
    "v_mfma_f32_16x16x32_bf16 a[84:87], a[244:247], v[40:43], a[84:87]\n"
    "v_mfma_f32_16x16x32_bf16 a[84:87], v[200:203], v[44:47], a[84:87]\n"
    "v_mfma_f32_16x16x32_bf16 a[88:91], a[248:251], v[40:43], a[88:91]\n"
    "v_mfma_f32_16x16x32_bf16 a[88:91], v[204:207], v[44:47], a[88:91]\n"
    "v_mfma_f32_16x16x32_bf16 a[92:95], a[252:255], v[40:43], a[92:95]\n"
    "v_cvt_pk_bf16_f32 v40, v8, v9\n"
    "v_cvt_pk_bf16_f32 v41, v10, v11\n"
    "v_exp_f32 v8, v72\n"
    "v_exp_f32 v9, v73\n"
    "v_exp_f32 v10, v74\n"
    "v_exp_f32 v11, v75\n"
    "v_cvt_pk_bf16_f32 v42, v240, v241\n"
    "v_cvt_pk_bf16_f32 v43, v242, v243\n"
    "v_exp_f32 v240, v76\n"
    "v_exp_f32 v241, v77\n"
    "v_exp_f32 v242, v78\n"
    "v_mfma_f32_16x16x32_bf16 a[92:95], v[208:211], v[44:47], a[92:95]\n"
    "s_waitcnt lgkmcnt(14)\n"
    "v_exp_f32 v243, v79\n"
    "v_cvt_pk_bf16_f32 v44, v8, v9\n"
    "v_cvt_pk_bf16_f32 v45, v10, v11\n"
    "v_cvt_pk_bf16_f32 v46, v240, v241\n"
    "v_cvt_pk_bf16_f32 v47, v242, v243\n"
    "v_mfma_f32_16x16x32_bf16 v[12:15], v[16:19], v[40:43], 0\n"
    "s_nop 0\n"
    "v_mfma_f32_16x16x32_bf16 v[12:15], v[16:19], v[44:47], v[12:15]\n"
    "v_mfma_f32_16x16x32_bf16 v[48:51], a[160:163], v[100:103], 0\n"
    "s_add_u32 m0, s34, 0\n"
    "v_mfma_f32_16x16x32_bf16 v[48:51], a[164:167], v[104:107], v[48:51]\n"
    "buffer_load_dwordx4 v1, s[20:23], s32 offen offset:0  lds\n"
    "v_mfma_f32_16x16x32_bf16 v[48:51], a[168:171], v[108:111], v[48:51]\n"
    "v_rcp_f32 v254, v22\n"
    "v_accvgpr_read_b32 v244, a64\n"
    "v_accvgpr_read_b32 v245, a65\n"
    "v_mul_f32 v244, v254, v244\n"
    "v_mul_f32 v245, v254, v245\n"
    "v_cvt_pk_bf16_f32 v246, v244, v245\n"
    "v_accvgpr_read_b32 v244, a66\n"
    "v_accvgpr_read_b32 v245, a67\n"
    "v_mul_f32 v244, v254, v244\n"
    "v_mul_f32 v245, v254, v245\n"
    "v_cvt_pk_bf16_f32 v247, v244, v245\n"
    "v_xor_b32 v31, 0, v231\n"
    "ds_write_b64 v31, v[246:247] offset:8192\n"
    "v_accvgpr_read_b32 v244, a68\n"
    "v_accvgpr_read_b32 v245, a69\n"
    "v_mul_f32 v244, v254, v244\n"
    "v_mul_f32 v245, v254, v245\n"
    "v_cvt_pk_bf16_f32 v248, v244, v245\n"
    "v_accvgpr_read_b32 v244, a70\n"
    "v_accvgpr_read_b32 v245, a71\n"
    "v_mul_f32 v244, v254, v244\n"
    "v_mul_f32 v245, v254, v245\n"
    "v_cvt_pk_bf16_f32 v249, v244, v245\n"
    "v_xor_b32 v255, 32, v231\n"
    "ds_write_b64 v255, v[248:249] offset:8192\n"
    "v_accvgpr_read_b32 v244, a72\n"
    "v_accvgpr_read_b32 v245, a73\n"
    "v_mul_f32 v244, v254, v244\n"
    "v_mul_f32 v245, v254, v245\n"
    "v_cvt_pk_bf16_f32 v250, v244, v245\n"
    "v_accvgpr_read_b32 v244, a74\n"
    "v_accvgpr_read_b32 v245, a75\n"
    "v_mul_f32 v244, v254, v244\n"
    "v_mul_f32 v245, v254, v245\n"
    "v_cvt_pk_bf16_f32 v251, v244, v245\n"
    "v_xor_b32 v31, 64, v231\n"
    "ds_write_b64 v31, v[250:251] offset:8192\n"
    "v_accvgpr_read_b32 v244, a76\n"
    "v_accvgpr_read_b32 v245, a77\n"
    "v_mul_f32 v244, v254, v244\n"
    "v_mfma_f32_16x16x32_bf16 v[48:51], a[172:175], v[112:115], v[48:51]\n"
    "v_mul_f32 v245, v254, v245\n"
    "v_cvt_pk_bf16_f32 v252, v244, v245\n"
    "v_accvgpr_read_b32 v244, a78\n"
    "v_accvgpr_read_b32 v245, a79\n"
    "v_mfma_f32_16x16x32_bf16 v[52:55], a[176:179], v[100:103], 0\n"
    "v_mul_f32 v244, v254, v244\n"
    "v_mul_f32 v245, v254, v245\n"
    "v_cvt_pk_bf16_f32 v253, v244, v245\n"
    "v_xor_b32 v255, 96, v231\n"
    "v_mfma_f32_16x16x32_bf16 v[52:55], a[180:183], v[104:107], v[52:55]\n"
    "ds_write_b64 v255, v[252:253] offset:8192\n"
    "v_accvgpr_read_b32 v244, a80\n"
    "v_accvgpr_read_b32 v245, a81\n"
    "v_mul_f32 v244, v254, v244\n"
    "v_mfma_f32_16x16x32_bf16 v[52:55], a[184:187], v[108:111], v[52:55]\n"
    "v_mul_f32 v245, v254, v245\n"
    "v_cvt_pk_bf16_f32 v246, v244, v245\n"
    "v_accvgpr_read_b32 v244, a82\n"
    "v_accvgpr_read_b32 v245, a83\n"
    "v_mfma_f32_16x16x32_bf16 v[52:55], a[188:191], v[112:115], v[52:55]\n"
    "s_waitcnt lgkmcnt(6)\n"
    "v_mul_f32 v244, v254, v244\n"
    "v_mul_f32 v245, v254, v245\n"
    "v_cvt_pk_bf16_f32 v247, v244, v245\n"
    "v_xor_b32 v31, 128, v231\n"
    "v_mfma_f32_16x16x32_bf16 v[56:59], a[192:195], v[100:103], 0\n"
    "ds_write_b64 v31, v[246:247] offset:8192\n"
    "v_accvgpr_read_b32 v244, a84\n"
    "v_accvgpr_read_b32 v245, a85\n"
    "v_mul_f32 v244, v254, v244\n"
    "v_mfma_f32_16x16x32_bf16 v[56:59], a[196:199], v[104:107], v[56:59]\n"
    "v_mul_f32 v245, v254, v245\n"
    "v_cvt_pk_bf16_f32 v248, v244, v245\n"
    "v_accvgpr_read_b32 v244, a86\n"
    "v_accvgpr_read_b32 v245, a87\n"
    "v_mfma_f32_16x16x32_bf16 v[56:59], a[200:203], v[108:111], v[56:59]\n"
    "v_mul_f32 v244, v254, v244\n"
    "v_mul_f32 v245, v254, v245\n"
    "v_mfma_f32_16x16x32_bf16 v[56:59], a[204:207], v[112:115], v[56:59]\n"
    "v_cvt_pk_bf16_f32 v249, v244, v245\n"
    "v_xor_b32 v255, 160, v231\n"
    "v_mfma_f32_16x16x32_bf16 v[60:63], a[208:211], v[100:103], 0\n"
    "ds_write_b64 v255, v[248:249] offset:8192\n"
    "v_accvgpr_read_b32 v244, a88\n"
    "v_mfma_f32_16x16x32_bf16 v[60:63], a[212:215], v[104:107], v[60:63]\n"
    "v_accvgpr_read_b32 v245, a89\n"
    "v_mul_f32 v244, v254, v244\n"
    "v_mfma_f32_16x16x32_bf16 v[60:63], a[216:219], v[108:111], v[60:63]\n"
    "v_mul_f32 v245, v254, v245\n"
    "v_cvt_pk_bf16_f32 v250, v244, v245\n"
    "v_mfma_f32_16x16x32_bf16 v[60:63], a[220:223], v[112:115], v[60:63]\n"
    "v_accvgpr_read_b32 v244, a90\n"
    "v_accvgpr_read_b32 v245, a91\n"
    "v_mfma_f32_16x16x32_bf16 a[96:99], a[224:227], v[32:35], a[96:99]\n"
    "v_mul_f32 v244, v254, v244\n"
    "v_mul_f32 v245, v254, v245\n"
    "v_mfma_f32_16x16x32_bf16 a[96:99], v[180:183], v[36:39], a[96:99]\n"
    "v_cvt_pk_bf16_f32 v251, v244, v245\n"
    "v_xor_b32 v31, 192, v231\n"
    "v_mfma_f32_16x16x32_bf16 a[100:103], a[228:231], v[32:35], a[100:103]\n"
    "ds_write_b64 v31, v[250:251] offset:8192\n"
    "v_accvgpr_read_b32 v244, a92\n"
    "v_mfma_f32_16x16x32_bf16 a[100:103], v[184:187], v[36:39], a[100:103]\n"
    "v_accvgpr_read_b32 v245, a93\n"
    "v_mul_f32 v244, v254, v244\n"
    "v_mfma_f32_16x16x32_bf16 a[104:107], a[232:235], v[32:35], a[104:107]\n"
    "v_mul_f32 v245, v254, v245\n"
    "v_cvt_pk_bf16_f32 v252, v244, v245\n"
    "v_mfma_f32_16x16x32_bf16 a[104:107], v[188:191], v[36:39], a[104:107]\n"
    "v_max3_f32 v25, v48, v49, v50\n"
    "v_max3_f32 v26, v51, v52, v53\n"
    "v_mfma_f32_16x16x32_bf16 a[108:111], a[236:239], v[32:35], a[108:111]\n"
    "v_cmp_nge_f32 vcc, s40, v12\n"
    "v_mfma_f32_16x16x32_bf16 a[108:111], v[192:195], v[36:39], a[108:111]\n"
    "v_accvgpr_read_b32 v244, a94\n"
    "v_accvgpr_read_b32 v245, a95\n"
    "v_mfma_f32_16x16x32_bf16 a[112:115], a[240:243], v[32:35], a[112:115]\n"
    "v_max3_f32 v25, v25, v54, v55\n"
    "v_max3_f32 v26, v26, v56, v57\n"
    "v_mfma_f32_16x16x32_bf16 a[112:115], v[196:199], v[36:39], a[112:115]\n"
    "s_add_u32 m0, s34, 32\n"
    "v_mul_f32 v244, v254, v244\n"
    "v_mul_f32 v245, v254, v245\n"
    "v_mfma_f32_16x16x32_bf16 a[116:119], a[244:247], v[32:35], a[116:119]\n"
    "buffer_load_dwordx4 v1, s[20:23], s32 offen offset:1024  lds\n"
    "v_mfma_f32_16x16x32_bf16 a[116:119], v[200:203], v[36:39], a[116:119]\n"
    "v_max3_f32 v25, v25, v58, v59\n"
    "v_max3_f32 v26, v26, v60, v61\n"
    "v_mfma_f32_16x16x32_bf16 a[120:123], a[248:251], v[32:35], a[120:123]\n"
    "v_cvt_pk_bf16_f32 v253, v244, v245\n"
    "v_xor_b32 v255, 224, v231\n"
    "v_mfma_f32_16x16x32_bf16 a[120:123], v[204:207], v[36:39], a[120:123]\n"
    "v_max3_f32 v25, v25, v62, v63\n"
    "v_max_f32 v25, v25, v26\n"
    "v_mfma_f32_16x16x32_bf16 a[124:127], a[252:255], v[32:35], a[124:127]\n"
    "s_cbranch_vccnz LTR_34\n"
    "LRET_34:\n"
    "v_add_f32 v24, v24, v12\n"
    "v_mfma_f32_16x16x32_bf16 a[124:127], v[208:211], v[36:39], a[124:127]\n"
    "ds_write_b64 v255, v[252:253] offset:8192\n"
    "s_mov_b32 s38, 1\n"
    "s_mov_b32 s45, s43\n"
    "s_mov_b32 s43, s44\n"
    "s_branch LWARM5\n"
    "LFINAL5:\n"
    "v_mfma_f32_16x16x32_bf16 v[48:51], a[160:163], v[116:119], v[84:87]\n"
    "v_exp_f32 v243, v71\n"
    "v_mfma_f32_16x16x32_bf16 v[48:51], a[164:167], v[120:123], v[48:51]\n"
    "v_cvt_pk_bf16_f32 v40, v8, v9\n"
    "v_cvt_pk_bf16_f32 v41, v10, v11\n"
    "v_mfma_f32_16x16x32_bf16 v[48:51], a[168:171], v[124:127], v[48:51]\n"
    "v_exp_f32 v8, v72\n"
    "v_mfma_f32_16x16x32_bf16 v[48:51], a[172:175], v[128:131], v[48:51]\n"
    "v_exp_f32 v9, v73\n"
    "v_mfma_f32_16x16x32_bf16 v[52:55], a[176:179], v[116:119], v[84:87]\n"
    "v_add_u32 v5, s36, v3\n"
    "v_mfma_f32_16x16x32_bf16 v[52:55], a[180:183], v[120:123], v[52:55]\n"
    "v_exp_f32 v10, v74\n"
    "v_mfma_f32_16x16x32_bf16 v[52:55], a[184:187], v[124:127], v[52:55]\n"
    "v_exp_f32 v11, v75\n"
    "v_mfma_f32_16x16x32_bf16 v[52:55], a[188:191], v[128:131], v[52:55]\n"
    "v_cvt_pk_bf16_f32 v42, v240, v241\n"
    "v_cvt_pk_bf16_f32 v43, v242, v243\n"
    "v_mfma_f32_16x16x32_bf16 v[56:59], a[192:195], v[116:119], v[84:87]\n"
    "v_exp_f32 v240, v76\n"
    "v_mfma_f32_16x16x32_bf16 v[56:59], a[196:199], v[120:123], v[56:59]\n"
    "v_exp_f32 v241, v77\n"
    "v_mfma_f32_16x16x32_bf16 v[56:59], a[200:203], v[124:127], v[56:59]\n"
    "v_exp_f32 v242, v78\n"
    "v_mfma_f32_16x16x32_bf16 v[56:59], a[204:207], v[128:131], v[56:59]\n"
    "v_exp_f32 v243, v79\n"
    "v_mfma_f32_16x16x32_bf16 v[60:63], a[208:211], v[116:119], v[84:87]\n"
    "v_cvt_pk_bf16_f32 v44, v8, v9\n"
    "v_cvt_pk_bf16_f32 v45, v10, v11\n"
    "v_mfma_f32_16x16x32_bf16 v[60:63], a[212:215], v[120:123], v[60:63]\n"
    "v_cvt_pk_bf16_f32 v46, v240, v241\n"
    "v_cvt_pk_bf16_f32 v47, v242, v243\n"
    "v_mfma_f32_16x16x32_bf16 v[60:63], a[216:219], v[124:127], v[60:63]\n"
    "v_exp_f32 v8, v48\n"
    "v_mfma_f32_16x16x32_bf16 v[60:63], a[220:223], v[128:131], v[60:63]\n"
    "v_exp_f32 v9, v49\n"
    "v_mfma_f32_16x16x32_bf16 v[12:15], v[16:19], v[40:43], 0\n"
    "v_exp_f32 v10, v50\n"
    "v_mfma_f32_16x16x32_bf16 v[12:15], v[16:19], v[44:47], v[12:15]\n"
    "v_exp_f32 v11, v51\n"
    "v_mfma_f32_16x16x32_bf16 a[128:131], a[224:227], v[32:35], a[128:131]\n"
    "ds_read_b64_tr_b16 a[224:225], v5 offset:0\n"
    "ds_read_b64_tr_b16 a[226:227], v5 offset:256\n"
    "v_mfma_f32_16x16x32_bf16 a[128:131], v[180:183], v[36:39], a[128:131]\n"
    "v_exp_f32 v240, v52\n"
    "v_mfma_f32_16x16x32_bf16 a[132:135], a[228:231], v[32:35], a[132:135]\n"
    "ds_read_b64_tr_b16 v[180:181], v5 offset:512\n"
    "ds_read_b64_tr_b16 v[182:183], v5 offset:768\n"
    "v_mfma_f32_16x16x32_bf16 a[132:135], v[184:187], v[36:39], a[132:135]\n"
    "v_exp_f32 v241, v53\n"
    "v_mfma_f32_16x16x32_bf16 a[136:139], a[232:235], v[32:35], a[136:139]\n"
    "ds_read_b64_tr_b16 a[228:229], v5 offset:32\n"
    "ds_read_b64_tr_b16 a[230:231], v5 offset:288\n"
    "v_mfma_f32_16x16x32_bf16 a[136:139], v[188:191], v[36:39], a[136:139]\n"
    "v_cmp_nge_f32 vcc, s40, v12\n"
    "v_mfma_f32_16x16x32_bf16 a[140:143], a[236:239], v[32:35], a[140:143]\n"
    "ds_read_b64_tr_b16 v[184:185], v5 offset:544\n"
    "ds_read_b64_tr_b16 v[186:187], v5 offset:800\n"
    "v_mfma_f32_16x16x32_bf16 a[140:143], v[192:195], v[36:39], a[140:143]\n"
    "v_exp_f32 v242, v54\n"
    "v_mfma_f32_16x16x32_bf16 a[144:147], a[240:243], v[32:35], a[144:147]\n"
    "ds_read_b64_tr_b16 a[232:233], v5 offset:64\n"
    "ds_read_b64_tr_b16 a[234:235], v5 offset:320\n"
    "v_mfma_f32_16x16x32_bf16 a[144:147], v[196:199], v[36:39], a[144:147]\n"
    "s_cbranch_vccnz LTR_35\n"
    "LRET_35:\n"
    "v_add_f32 v20, v20, v12\n"
    "v_mfma_f32_16x16x32_bf16 a[148:151], a[244:247], v[32:35], a[148:151]\n"
    "ds_read_b64_tr_b16 v[188:189], v5 offset:576\n"
    "ds_read_b64_tr_b16 v[190:191], v5 offset:832\n"
    "v_mfma_f32_16x16x32_bf16 a[148:151], v[200:203], v[36:39], a[148:151]\n"
    "v_exp_f32 v243, v55\n"
    "v_mfma_f32_16x16x32_bf16 a[152:155], a[248:251], v[32:35], a[152:155]\n"
    "ds_read_b64_tr_b16 a[236:237], v5 offset:96\n"
    "ds_read_b64_tr_b16 a[238:239], v5 offset:352\n"
    "v_mfma_f32_16x16x32_bf16 a[152:155], v[204:207], v[36:39], a[152:155]\n"
    "v_mfma_f32_16x16x32_bf16 a[156:159], a[252:255], v[32:35], a[156:159]\n"
    "ds_read_b64_tr_b16 v[192:193], v5 offset:608\n"
    "ds_read_b64_tr_b16 v[194:195], v5 offset:864\n"
    "v_mfma_f32_16x16x32_bf16 a[156:159], v[208:211], v[36:39], a[156:159]\n"
    "v_cvt_pk_bf16_f32 v32, v8, v9\n"
    "v_cvt_pk_bf16_f32 v33, v10, v11\n"
    "v_mfma_f32_16x16x32_bf16 v[64:67], a[160:163], v[132:135], v[88:91]\n"
    "ds_read_b64_tr_b16 a[240:241], v5 offset:128\n"
    "ds_read_b64_tr_b16 a[242:243], v5 offset:384\n"
    "v_exp_f32 v8, v56\n"
    "v_mfma_f32_16x16x32_bf16 v[64:67], a[164:167], v[136:139], v[64:67]\n"
    "v_exp_f32 v9, v57\n"
    "v_exp_f32 v10, v58\n"
    "v_mfma_f32_16x16x32_bf16 v[64:67], a[168:171], v[140:143], v[64:67]\n"
    "ds_read_b64_tr_b16 v[196:197], v5 offset:640\n"
    "ds_read_b64_tr_b16 v[198:199], v5 offset:896\n"
    "v_exp_f32 v11, v59\n"
    "v_mfma_f32_16x16x32_bf16 v[64:67], a[172:175], v[144:147], v[64:67]\n"
    "v_cvt_pk_bf16_f32 v34, v240, v241\n"
    "v_cvt_pk_bf16_f32 v35, v242, v243\n"
    "v_exp_f32 v240, v60\n"
    "v_mfma_f32_16x16x32_bf16 v[68:71], a[176:179], v[132:135], v[88:91]\n"
    "ds_read_b64_tr_b16 a[244:245], v5 offset:160\n"
    "ds_read_b64_tr_b16 a[246:247], v5 offset:416\n"
    "v_exp_f32 v241, v61\n"
    "v_mfma_f32_16x16x32_bf16 v[68:71], a[180:183], v[136:139], v[68:71]\n"
    "v_exp_f32 v242, v62\n"
    "v_exp_f32 v243, v63\n"
    "v_mfma_f32_16x16x32_bf16 v[68:71], a[184:187], v[140:143], v[68:71]\n"
    "ds_read_b64_tr_b16 v[200:201], v5 offset:672\n"
    "ds_read_b64_tr_b16 v[202:203], v5 offset:928\n"
    "v_cvt_pk_bf16_f32 v36, v8, v9\n"
    "v_cvt_pk_bf16_f32 v37, v10, v11\n"
    "v_mfma_f32_16x16x32_bf16 v[68:71], a[188:191], v[144:147], v[68:71]\n"
    "v_cvt_pk_bf16_f32 v38, v240, v241\n"
    "v_cvt_pk_bf16_f32 v39, v242, v243\n"
    "v_mfma_f32_16x16x32_bf16 v[12:15], v[16:19], v[32:35], 0\n"
    "ds_read_b64_tr_b16 a[248:249], v5 offset:192\n"
    "ds_read_b64_tr_b16 a[250:251], v5 offset:448\n"
    "v_mfma_f32_16x16x32_bf16 v[12:15], v[16:19], v[36:39], v[12:15]\n"
    "v_mfma_f32_16x16x32_bf16 v[72:75], a[192:195], v[132:135], v[88:91]\n"
    "ds_read_b64_tr_b16 v[204:205], v5 offset:704\n"
    "ds_read_b64_tr_b16 v[206:207], v5 offset:960\n"
    "v_exp_f32 v8, v64\n"
    "v_mfma_f32_16x16x32_bf16 v[72:75], a[196:199], v[136:139], v[72:75]\n"
    "v_exp_f32 v9, v65\n"
    "v_exp_f32 v10, v66\n"
    "v_mfma_f32_16x16x32_bf16 v[72:75], a[200:203], v[140:143], v[72:75]\n"
    "ds_read_b64_tr_b16 a[252:253], v5 offset:224\n"
    "ds_read_b64_tr_b16 a[254:255], v5 offset:480\n"
    "v_exp_f32 v11, v67\n"
    "v_mfma_f32_16x16x32_bf16 v[72:75], a[204:207], v[144:147], v[72:75]\n"
    "v_mfma_f32_16x16x32_bf16 v[76:79], a[208:211], v[132:135], v[88:91]\n"
    "ds_read_b64_tr_b16 v[208:209], v5 offset:736\n"
    "ds_read_b64_tr_b16 v[210:211], v5 offset:992\n"
    "v_cmp_nge_f32 vcc, s40, v12\n"
    "v_mfma_f32_16x16x32_bf16 v[76:79], a[212:215], v[136:139], v[76:79]\n"
    "v_exp_f32 v240, v68\n"
    "v_exp_f32 v241, v69\n"
    "v_mfma_f32_16x16x32_bf16 v[76:79], a[216:219], v[140:143], v[76:79]\n"
    "v_exp_f32 v242, v70\n"
    "v_exp_f32 v243, v71\n"
    "v_mfma_f32_16x16x32_bf16 v[76:79], a[220:223], v[144:147], v[76:79]\n"
    "s_waitcnt lgkmcnt(15)\n"
    "s_cbranch_vccnz LTR_36\n"
    "LRET_36:\n"
    "v_add_f32 v21, v21, v12\n"
    "v_mfma_f32_16x16x32_bf16 a[0:3], a[224:227], v[40:43], a[0:3]\n"
    "v_mfma_f32_16x16x32_bf16 a[0:3], v[180:183], v[44:47], a[0:3]\n"
    "v_mfma_f32_16x16x32_bf16 a[4:7], a[228:231], v[40:43], a[4:7]\n"
    "v_mfma_f32_16x16x32_bf16 a[4:7], v[184:187], v[44:47], a[4:7]\n"
    "v_mfma_f32_16x16x32_bf16 a[8:11], a[232:235], v[40:43], a[8:11]\n"
    "v_mfma_f32_16x16x32_bf16 a[8:11], v[188:191], v[44:47], a[8:11]\n"
    "v_mfma_f32_16x16x32_bf16 a[12:15], a[236:239], v[40:43], a[12:15]\n"
    "v_mfma_f32_16x16x32_bf16 a[12:15], v[192:195], v[44:47], a[12:15]\n"
    "s_waitcnt lgkmcnt(0)\n"
    "v_mfma_f32_16x16x32_bf16 a[16:19], a[240:243], v[40:43], a[16:19]\n"
    "v_mfma_f32_16x16x32_bf16 a[16:19], v[196:199], v[44:47], a[16:19]\n"
    "v_mfma_f32_16x16x32_bf16 a[20:23], a[244:247], v[40:43], a[20:23]\n"
    "v_mfma_f32_16x16x32_bf16 a[20:23], v[200:203], v[44:47], a[20:23]\n"
    "v_mfma_f32_16x16x32_bf16 a[24:27], a[248:251], v[40:43], a[24:27]\n"
    "v_mfma_f32_16x16x32_bf16 a[24:27], v[204:207], v[44:47], a[24:27]\n"
    "v_mfma_f32_16x16x32_bf16 a[28:31], a[252:255], v[40:43], a[28:31]\n"
    "v_cvt_pk_bf16_f32 v40, v8, v9\n"
    "v_cvt_pk_bf16_f32 v41, v10, v11\n"
    "v_exp_f32 v8, v72\n"
    "v_exp_f32 v9, v73\n"
    "v_exp_f32 v10, v74\n"
    "v_exp_f32 v11, v75\n"
    "v_cvt_pk_bf16_f32 v42, v240, v241\n"
    "v_cvt_pk_bf16_f32 v43, v242, v243\n"
    "v_exp_f32 v240, v76\n"
    "v_exp_f32 v241, v77\n"
    "v_exp_f32 v242, v78\n"
    "v_exp_f32 v243, v79\n"
    "v_mfma_f32_16x16x32_bf16 a[28:31], v[208:211], v[44:47], a[28:31]\n"
    "v_cvt_pk_bf16_f32 v44, v8, v9\n"
    "v_cvt_pk_bf16_f32 v45, v10, v11\n"
    "v_cvt_pk_bf16_f32 v46, v240, v241\n"
    "v_cvt_pk_bf16_f32 v47, v242, v243\n"
    "v_mfma_f32_16x16x32_bf16 v[12:15], v[16:19], v[40:43], 0\n"
    "s_nop 0\n"
    "v_mfma_f32_16x16x32_bf16 v[12:15], v[16:19], v[44:47], v[12:15]\n"
    "v_mfma_f32_16x16x32_bf16 v[48:51], a[160:163], v[148:151], v[92:95]\n"
    "v_mfma_f32_16x16x32_bf16 v[48:51], a[164:167], v[152:155], v[48:51]\n"
    "v_mfma_f32_16x16x32_bf16 v[48:51], a[168:171], v[156:159], v[48:51]\n"
    "v_rcp_f32 v254, v20\n"
    "s_add_u32 s49, s43, 0\n"
    "v_accvgpr_read_b32 v244, a0\n"
    "v_accvgpr_read_b32 v245, a1\n"
    "v_mul_f32 v244, v254, v244\n"
    "v_mul_f32 v245, v254, v245\n"
    "v_cvt_pk_bf16_f32 v246, v244, v245\n"
    "v_accvgpr_read_b32 v244, a2\n"
    "v_accvgpr_read_b32 v245, a3\n"
    "v_mul_f32 v244, v254, v244\n"
    "v_mul_f32 v245, v254, v245\n"
    "v_cvt_pk_bf16_f32 v247, v244, v245\n"
    "v_accvgpr_read_b32 v244, a4\n"
    "v_accvgpr_read_b32 v245, a5\n"
    "v_mul_f32 v244, v254, v244\n"
    "v_mul_f32 v245, v254, v245\n"
    "v_cvt_pk_bf16_f32 v248, v244, v245\n"
    "v_accvgpr_read_b32 v244, a6\n"
    "v_accvgpr_read_b32 v245, a7\n"
    "v_mul_f32 v244, v254, v244\n"
    "v_mul_f32 v245, v254, v245\n"
    "v_cvt_pk_bf16_f32 v249, v244, v245\n"
    "v_permlane16_swap_b32 v246, v248\n"
    "s_nop 0\n"
    "v_permlane16_swap_b32 v247, v249\n"
    "v_accvgpr_read_b32 v244, a8\n"
    "v_accvgpr_read_b32 v245, a9\n"
    "v_mul_f32 v244, v254, v244\n"
    "v_mul_f32 v245, v254, v245\n"
    "v_cvt_pk_bf16_f32 v250, v244, v245\n"
    "v_accvgpr_read_b32 v244, a10\n"
    "v_accvgpr_read_b32 v245, a11\n"
    "v_mul_f32 v244, v254, v244\n"
    "v_mul_f32 v245, v254, v245\n"
    "v_cvt_pk_bf16_f32 v251, v244, v245\n"
    "v_accvgpr_read_b32 v244, a12\n"
    "v_accvgpr_read_b32 v245, a13\n"
    "v_mul_f32 v244, v254, v244\n"
    "v_mul_f32 v245, v254, v245\n"
    "v_cvt_pk_bf16_f32 v252, v244, v245\n"
    "v_accvgpr_read_b32 v244, a14\n"
    "v_accvgpr_read_b32 v245, a15\n"
    "v_mul_f32 v244, v254, v244\n"
    "v_mul_f32 v245, v254, v245\n"
    "v_cvt_pk_bf16_f32 v253, v244, v245\n"
    "v_permlane16_swap_b32 v250, v252\n"
    "s_nop 0\n"
    "v_permlane16_swap_b32 v251, v253\n"
    "v_mov_b32 v244, v246\n"
    "v_mov_b32_dpp v246, v250 row_ror:8 row_mask:0xf bank_mask:0xc\n"
    "v_mov_b32 v245, v247\n"
    "v_mov_b32_dpp v247, v251 row_ror:8 row_mask:0xf bank_mask:0xc\n"
    "v_mov_b32_dpp v250, v244 row_ror:8 row_mask:0xf bank_mask:0x3\n"
    "v_mov_b32_dpp v251, v245 row_ror:8 row_mask:0xf bank_mask:0x3\n"
    "v_mov_b32 v244, v248\n"
    "v_mov_b32_dpp v248, v252 row_ror:8 row_mask:0xf bank_mask:0xc\n"
    "v_mov_b32 v245, v249\n"
    "v_mov_b32_dpp v249, v253 row_ror:8 row_mask:0xf bank_mask:0xc\n"
    "v_mov_b32_dpp v252, v244 row_ror:8 row_mask:0xf bank_mask:0x3\n"
    "v_mov_b32_dpp v253, v245 row_ror:8 row_mask:0xf bank_mask:0x3\n"
    "buffer_store_dwordx4 v[246:249], v7, s[28:31], s49 offen offset:0 nt\n"
    "buffer_store_dwordx4 v[250:253], v7, s[28:31], s49 offen offset:2048 nt\n"
    "v_accvgpr_read_b32 v244, a16\n"
    "v_accvgpr_read_b32 v245, a17\n"
    "v_mul_f32 v244, v254, v244\n"
    "v_mul_f32 v245, v254, v245\n"
    "v_cvt_pk_bf16_f32 v246, v244, v245\n"
    "v_accvgpr_read_b32 v244, a18\n"
    "v_mfma_f32_16x16x32_bf16 v[48:51], a[172:175], v[160:163], v[48:51]\n"
    "v_accvgpr_read_b32 v245, a19\n"
    "v_mul_f32 v244, v254, v244\n"
    "v_mul_f32 v245, v254, v245\n"
    "v_cvt_pk_bf16_f32 v247, v244, v245\n"
    "v_accvgpr_read_b32 v244, a20\n"
    "v_accvgpr_read_b32 v245, a21\n"
    "v_mfma_f32_16x16x32_bf16 v[52:55], a[176:179], v[148:151], v[92:95]\n"
    "v_mul_f32 v244, v254, v244\n"
    "v_mul_f32 v245, v254, v245\n"
    "v_cvt_pk_bf16_f32 v248, v244, v245\n"
    "v_accvgpr_read_b32 v244, a22\n"
    "v_accvgpr_read_b32 v245, a23\n"
    "v_mul_f32 v244, v254, v244\n"
    "v_mfma_f32_16x16x32_bf16 v[52:55], a[180:183], v[152:155], v[52:55]\n"
    "v_mul_f32 v245, v254, v245\n"
    "v_cvt_pk_bf16_f32 v249, v244, v245\n"
    "v_permlane16_swap_b32 v246, v248\n"
    "s_nop 0\n"
    "v_permlane16_swap_b32 v247, v249\n"
    "v_mfma_f32_16x16x32_bf16 v[52:55], a[184:187], v[156:159], v[52:55]\n"
    "v_accvgpr_read_b32 v244, a24\n"
    "v_accvgpr_read_b32 v245, a25\n"
    "v_mul_f32 v244, v254, v244\n"
    "v_mul_f32 v245, v254, v245\n"
    "v_cvt_pk_bf16_f32 v250, v244, v245\n"
    "v_accvgpr_read_b32 v244, a26\n"
    "v_mfma_f32_16x16x32_bf16 v[52:55], a[188:191], v[160:163], v[52:55]\n"
    "v_accvgpr_read_b32 v245, a27\n"
    "v_mul_f32 v244, v254, v244\n"
    "v_mul_f32 v245, v254, v245\n"
    "v_cvt_pk_bf16_f32 v251, v244, v245\n"
    "v_accvgpr_read_b32 v244, a28\n"
    "v_accvgpr_read_b32 v245, a29\n"
    "v_mfma_f32_16x16x32_bf16 v[56:59], a[192:195], v[148:151], v[92:95]\n"
    "v_mul_f32 v244, v254, v244\n"
    "v_mul_f32 v245, v254, v245\n"
    "v_cvt_pk_bf16_f32 v252, v244, v245\n"
    "v_accvgpr_read_b32 v244, a30\n"
    "v_accvgpr_read_b32 v245, a31\n"
    "v_mul_f32 v244, v254, v244\n"
    "v_mfma_f32_16x16x32_bf16 v[56:59], a[196:199], v[152:155], v[56:59]\n"
    "v_mul_f32 v245, v254, v245\n"
    "v_cvt_pk_bf16_f32 v253, v244, v245\n"
    "v_permlane16_swap_b32 v250, v252\n"
    "s_nop 0\n"
    "v_permlane16_swap_b32 v251, v253\n"
    "v_mfma_f32_16x16x32_bf16 v[56:59], a[200:203], v[156:159], v[56:59]\n"
    "v_mov_b32 v244, v246\n"
    "v_mov_b32_dpp v246, v250 row_ror:8 row_mask:0xf bank_mask:0xc\n"
    "v_mov_b32 v245, v247\n"
    "v_mov_b32_dpp v247, v251 row_ror:8 row_mask:0xf bank_mask:0xc\n"
    "v_mov_b32_dpp v250, v244 row_ror:8 row_mask:0xf bank_mask:0x3\n"
    "v_mov_b32_dpp v251, v245 row_ror:8 row_mask:0xf bank_mask:0x3\n"
    "v_mfma_f32_16x16x32_bf16 v[56:59], a[204:207], v[160:163], v[56:59]\n"
    "v_mov_b32 v244, v248\n"
    "v_mov_b32_dpp v248, v252 row_ror:8 row_mask:0xf bank_mask:0xc\n"
    "v_cmp_nge_f32 vcc, s40, v12\n"
    "v_mov_b32 v245, v249\n"
    "v_mov_b32_dpp v249, v253 row_ror:8 row_mask:0xf bank_mask:0xc\n"
    "v_mfma_f32_16x16x32_bf16 v[60:63], a[208:211], v[148:151], v[92:95]\n"
    "buffer_store_dwordx4 v[246:249], v7, s[28:31], s49 offen offset:128 nt\n"
    "v_mov_b32_dpp v252, v244 row_ror:8 row_mask:0xf bank_mask:0x3\n"
    "v_mov_b32_dpp v253, v245 row_ror:8 row_mask:0xf bank_mask:0x3\n"
    "buffer_store_dwordx4 v[250:253], v7, s[28:31], s49 offen offset:2176 nt\n"
    "v_mfma_f32_16x16x32_bf16 v[60:63], a[212:215], v[152:155], v[60:63]\n"
    "v_exp_f32 v8, v48\n"
    "v_exp_f32 v9, v49\n"
    "v_exp_f32 v10, v50\n"
    "v_mfma_f32_16x16x32_bf16 v[60:63], a[216:219], v[156:159], v[60:63]\n"
    "s_cbranch_vccnz LTR_37\n"
    "LRET_37:\n"
    "v_add_f32 v22, v22, v12\n"
    "v_exp_f32 v11, v51\n"
    "v_exp_f32 v240, v52\n"
    "v_mfma_f32_16x16x32_bf16 v[60:63], a[220:223], v[160:163], v[60:63]\n"
    "v_exp_f32 v241, v53\n"
    "v_exp_f32 v242, v54\n"
    "v_exp_f32 v243, v55\n"
    "v_mfma_f32_16x16x32_bf16 a[32:35], a[224:227], v[32:35], a[32:35]\n"
    "v_mfma_f32_16x16x32_bf16 a[32:35], v[180:183], v[36:39], a[32:35]\n"
    "v_mfma_f32_16x16x32_bf16 a[36:39], a[228:231], v[32:35], a[36:39]\n"
    "v_mfma_f32_16x16x32_bf16 a[36:39], v[184:187], v[36:39], a[36:39]\n"
    "v_mfma_f32_16x16x32_bf16 a[40:43], a[232:235], v[32:35], a[40:43]\n"
    "v_mfma_f32_16x16x32_bf16 a[40:43], v[188:191], v[36:39], a[40:43]\n"
    "v_mfma_f32_16x16x32_bf16 a[44:47], a[236:239], v[32:35], a[44:47]\n"
    "v_mfma_f32_16x16x32_bf16 a[44:47], v[192:195], v[36:39], a[44:47]\n"
    "v_mfma_f32_16x16x32_bf16 a[48:51], a[240:243], v[32:35], a[48:51]\n"
    "v_mfma_f32_16x16x32_bf16 a[48:51], v[196:199], v[36:39], a[48:51]\n"
    "v_mfma_f32_16x16x32_bf16 a[52:55], a[244:247], v[32:35], a[52:55]\n"
    "v_mfma_f32_16x16x32_bf16 a[52:55], v[200:203], v[36:39], a[52:55]\n"
    "v_mfma_f32_16x16x32_bf16 a[56:59], a[248:251], v[32:35], a[56:59]\n"
    "v_mfma_f32_16x16x32_bf16 a[56:59], v[204:207], v[36:39], a[56:59]\n"
    "v_mfma_f32_16x16x32_bf16 a[60:63], a[252:255], v[32:35], a[60:63]\n"
    "v_cvt_pk_bf16_f32 v32, v8, v9\n"
    "v_cvt_pk_bf16_f32 v33, v10, v11\n"
    "v_exp_f32 v8, v56\n"
    "v_exp_f32 v9, v57\n"
    "v_exp_f32 v10, v58\n"
    "v_exp_f32 v11, v59\n"
    "v_cvt_pk_bf16_f32 v34, v240, v241\n"
    "v_cvt_pk_bf16_f32 v35, v242, v243\n"
    "v_exp_f32 v240, v60\n"
    "v_exp_f32 v241, v61\n"
    "v_exp_f32 v242, v62\n"
    "v_exp_f32 v243, v63\n"
    "v_mfma_f32_16x16x32_bf16 a[60:63], v[208:211], v[36:39], a[60:63]\n"
    "v_cvt_pk_bf16_f32 v36, v8, v9\n"
    "v_cvt_pk_bf16_f32 v37, v10, v11\n"
    "v_cvt_pk_bf16_f32 v38, v240, v241\n"
    "v_cvt_pk_bf16_f32 v39, v242, v243\n"
    "v_mfma_f32_16x16x32_bf16 v[12:15], v[16:19], v[32:35], 0\n"
    "s_nop 0\n"
    "v_mfma_f32_16x16x32_bf16 v[12:15], v[16:19], v[36:39], v[12:15]\n"
    "v_mfma_f32_16x16x32_bf16 v[64:67], a[160:163], v[164:167], v[96:99]\n"
    "v_mfma_f32_16x16x32_bf16 v[64:67], a[164:167], v[168:171], v[64:67]\n"
    "v_mfma_f32_16x16x32_bf16 v[64:67], a[168:171], v[172:175], v[64:67]\n"
    "v_rcp_f32 v254, v21\n"
    "s_add_u32 s49, s43, 4096\n"
    "v_accvgpr_read_b32 v244, a32\n"
    "v_accvgpr_read_b32 v245, a33\n"
    "v_mul_f32 v244, v254, v244\n"
    "v_mul_f32 v245, v254, v245\n"
    "v_cvt_pk_bf16_f32 v246, v244, v245\n"
    "v_accvgpr_read_b32 v244, a34\n"
    "v_accvgpr_read_b32 v245, a35\n"
    "v_mul_f32 v244, v254, v244\n"
    "v_mul_f32 v245, v254, v245\n"
    "v_cvt_pk_bf16_f32 v247, v244, v245\n"
    "v_accvgpr_read_b32 v244, a36\n"
    "v_accvgpr_read_b32 v245, a37\n"
    "v_mul_f32 v244, v254, v244\n"
    "v_mul_f32 v245, v254, v245\n"
    "v_cvt_pk_bf16_f32 v248, v244, v245\n"
    "v_accvgpr_read_b32 v244, a38\n"
    "v_accvgpr_read_b32 v245, a39\n"
    "v_mul_f32 v244, v254, v244\n"
    "v_mul_f32 v245, v254, v245\n"
    "v_cvt_pk_bf16_f32 v249, v244, v245\n"
    "v_permlane16_swap_b32 v246, v248\n"
    "s_nop 0\n"
    "v_permlane16_swap_b32 v247, v249\n"
    "v_accvgpr_read_b32 v244, a40\n"
    "v_accvgpr_read_b32 v245, a41\n"
    "v_mul_f32 v244, v254, v244\n"
    "v_mul_f32 v245, v254, v245\n"
    "v_cvt_pk_bf16_f32 v250, v244, v245\n"
    "v_accvgpr_read_b32 v244, a42\n"
    "v_accvgpr_read_b32 v245, a43\n"
    "v_mul_f32 v244, v254, v244\n"
    "v_mul_f32 v245, v254, v245\n"
    "v_cvt_pk_bf16_f32 v251, v244, v245\n"
    "v_accvgpr_read_b32 v244, a44\n"
    "v_accvgpr_read_b32 v245, a45\n"
    "v_mul_f32 v244, v254, v244\n"
    "v_mul_f32 v245, v254, v245\n"
    "v_cvt_pk_bf16_f32 v252, v244, v245\n"
    "v_accvgpr_read_b32 v244, a46\n"
    "v_accvgpr_read_b32 v245, a47\n"
    "v_mul_f32 v244, v254, v244\n"
    "v_mul_f32 v245, v254, v245\n"
    "v_cvt_pk_bf16_f32 v253, v244, v245\n"
    "v_permlane16_swap_b32 v250, v252\n"
    "s_nop 0\n"
    "v_permlane16_swap_b32 v251, v253\n"
    "v_mov_b32 v244, v246\n"
    "v_mov_b32_dpp v246, v250 row_ror:8 row_mask:0xf bank_mask:0xc\n"
    "v_mov_b32 v245, v247\n"
    "v_mov_b32_dpp v247, v251 row_ror:8 row_mask:0xf bank_mask:0xc\n"
    "v_mov_b32_dpp v250, v244 row_ror:8 row_mask:0xf bank_mask:0x3\n"
    "v_mov_b32_dpp v251, v245 row_ror:8 row_mask:0xf bank_mask:0x3\n"
    "v_mov_b32 v244, v248\n"
    "v_mov_b32_dpp v248, v252 row_ror:8 row_mask:0xf bank_mask:0xc\n"
    "v_mov_b32 v245, v249\n"
    "v_mov_b32_dpp v249, v253 row_ror:8 row_mask:0xf bank_mask:0xc\n"
    "v_mov_b32_dpp v252, v244 row_ror:8 row_mask:0xf bank_mask:0x3\n"
    "v_mov_b32_dpp v253, v245 row_ror:8 row_mask:0xf bank_mask:0x3\n"
    "buffer_store_dwordx4 v[246:249], v7, s[28:31], s49 offen offset:0 nt\n"
    "buffer_store_dwordx4 v[250:253], v7, s[28:31], s49 offen offset:2048 nt\n"
    "v_accvgpr_read_b32 v244, a48\n"
    "v_accvgpr_read_b32 v245, a49\n"
    "v_mul_f32 v244, v254, v244\n"
    "v_mul_f32 v245, v254, v245\n"
    "v_cvt_pk_bf16_f32 v246, v244, v245\n"
    "v_accvgpr_read_b32 v244, a50\n"
    "v_mfma_f32_16x16x32_bf16 v[64:67], a[172:175], v[176:179], v[64:67]\n"
    "v_accvgpr_read_b32 v245, a51\n"
    "v_mul_f32 v244, v254, v244\n"
    "v_mul_f32 v245, v254, v245\n"
    "v_cvt_pk_bf16_f32 v247, v244, v245\n"
    "v_accvgpr_read_b32 v244, a52\n"
    "v_accvgpr_read_b32 v245, a53\n"
    "v_mul_f32 v244, v254, v244\n"
    "v_mul_f32 v245, v254, v245\n"
    "v_cvt_pk_bf16_f32 v248, v244, v245\n"
    "v_accvgpr_read_b32 v244, a54\n"
    "v_mfma_f32_16x16x32_bf16 v[68:71], a[176:179], v[164:167], v[96:99]\n"
    "v_accvgpr_read_b32 v245, a55\n"
    "v_mul_f32 v244, v254, v244\n"
    "v_mul_f32 v245, v254, v245\n"
    "v_cvt_pk_bf16_f32 v249, v244, v245\n"
    "v_permlane16_swap_b32 v246, v248\n"
    "s_nop 0\n"
    "v_permlane16_swap_b32 v247, v249\n"
    "v_accvgpr_read_b32 v244, a56\n"
    "v_accvgpr_read_b32 v245, a57\n"
    "v_mfma_f32_16x16x32_bf16 v[68:71], a[180:183], v[168:171], v[68:71]\n"
    "v_mul_f32 v244, v254, v244\n"
    "v_mul_f32 v245, v254, v245\n"
    "v_cvt_pk_bf16_f32 v250, v244, v245\n"
    "v_accvgpr_read_b32 v244, a58\n"
    "v_accvgpr_read_b32 v245, a59\n"
    "v_mul_f32 v244, v254, v244\n"
    "v_mul_f32 v245, v254, v245\n"
    "v_cvt_pk_bf16_f32 v251, v244, v245\n"
    "v_accvgpr_read_b32 v244, a60\n"
    "v_accvgpr_read_b32 v245, a61\n"
    "v_mfma_f32_16x16x32_bf16 v[68:71], a[184:187], v[172:175], v[68:71]\n"
    "v_mul_f32 v244, v254, v244\n"
    "v_mul_f32 v245, v254, v245\n"
    "v_cvt_pk_bf16_f32 v252, v244, v245\n"
    "v_accvgpr_read_b32 v244, a62\n"
    "v_accvgpr_read_b32 v245, a63\n"
    "v_mul_f32 v244, v254, v244\n"
    "v_mul_f32 v245, v254, v245\n"
    "v_cvt_pk_bf16_f32 v253, v244, v245\n"
    "v_permlane16_swap_b32 v250, v252\n"
    "v_mfma_f32_16x16x32_bf16 v[68:71], a[188:191], v[176:179], v[68:71]\n"
    "v_permlane16_swap_b32 v251, v253\n"
    "v_mov_b32 v244, v246\n"
    "v_mov_b32_dpp v246, v250 row_ror:8 row_mask:0xf bank_mask:0xc\n"
    "v_mov_b32 v245, v247\n"
    "v_mov_b32_dpp v247, v251 row_ror:8 row_mask:0xf bank_mask:0xc\n"
    "v_mov_b32_dpp v250, v244 row_ror:8 row_mask:0xf bank_mask:0x3\n"
    "v_mov_b32_dpp v251, v245 row_ror:8 row_mask:0xf bank_mask:0x3\n"
    "v_mov_b32 v244, v248\n"
    "v_mov_b32_dpp v248, v252 row_ror:8 row_mask:0xf bank_mask:0xc\n"
    "v_mfma_f32_16x16x32_bf16 v[72:75], a[192:195], v[164:167], v[96:99]\n"
    "v_exp_f32 v8, v64\n"
    "v_exp_f32 v9, v65\n"
    "v_exp_f32 v10, v66\n"
    "v_exp_f32 v11, v67\n"
    "v_cmp_nge_f32 vcc, s40, v12\n"
    "v_mfma_f32_16x16x32_bf16 v[72:75], a[196:199], v[168:171], v[72:75]\n"
    "v_mov_b32 v245, v249\n"
    "v_mov_b32_dpp v249, v253 row_ror:8 row_mask:0xf bank_mask:0xc\n"
    "v_mov_b32_dpp v252, v244 row_ror:8 row_mask:0xf bank_mask:0x3\n"
    "v_mov_b32_dpp v253, v245 row_ror:8 row_mask:0xf bank_mask:0x3\n"
    "buffer_store_dwordx4 v[246:249], v7, s[28:31], s49 offen offset:128 nt\n"
    "buffer_store_dwordx4 v[250:253], v7, s[28:31], s49 offen offset:2176 nt\n"
    "v_mfma_f32_16x16x32_bf16 v[72:75], a[200:203], v[172:175], v[72:75]\n"
    "v_mfma_f32_16x16x32_bf16 v[72:75], a[204:207], v[176:179], v[72:75]\n"
    "s_cbranch_vccnz LTR_38\n"
    "LRET_38:\n"
    "v_add_f32 v23, v23, v12\n"
    "v_mfma_f32_16x16x32_bf16 v[76:79], a[208:211], v[164:167], v[96:99]\n"
    "v_exp_f32 v240, v68\n"
    "v_exp_f32 v241, v69\n"
    "v_exp_f32 v242, v70\n"
    "v_exp_f32 v243, v71\n"
    "v_mfma_f32_16x16x32_bf16 v[76:79], a[212:215], v[168:171], v[76:79]\n"
    "v_mfma_f32_16x16x32_bf16 v[76:79], a[216:219], v[172:175], v[76:79]\n"
    "v_mfma_f32_16x16x32_bf16 v[76:79], a[220:223], v[176:179], v[76:79]\n"
    "v_mfma_f32_16x16x32_bf16 a[64:67], a[224:227], v[40:43], a[64:67]\n"
    "v_mfma_f32_16x16x32_bf16 a[64:67], v[180:183], v[44:47], a[64:67]\n"
    "v_mfma_f32_16x16x32_bf16 a[68:71], a[228:231], v[40:43], a[68:71]\n"
    "v_mfma_f32_16x16x32_bf16 a[68:71], v[184:187], v[44:47], a[68:71]\n"
    "v_mfma_f32_16x16x32_bf16 a[72:75], a[232:235], v[40:43], a[72:75]\n"
    "v_mfma_f32_16x16x32_bf16 a[72:75], v[188:191], v[44:47], a[72:75]\n"
    "v_mfma_f32_16x16x32_bf16 a[76:79], a[236:239], v[40:43], a[76:79]\n"
    "v_mfma_f32_16x16x32_bf16 a[76:79], v[192:195], v[44:47], a[76:79]\n"
    "v_mfma_f32_16x16x32_bf16 a[80:83], a[240:243], v[40:43], a[80:83]\n"
    "v_mfma_f32_16x16x32_bf16 a[80:83], v[196:199], v[44:47], a[80:83]\n"
    "v_mfma_f32_16x16x32_bf16 a[84:87], a[244:247], v[40:43], a[84:87]\n"
    "v_mfma_f32_16x16x32_bf16 a[84:87], v[200:203], v[44:47], a[84:87]\n"
    "v_mfma_f32_16x16x32_bf16 a[88:91], a[248:251], v[40:43], a[88:91]\n"
    "v_mfma_f32_16x16x32_bf16 a[88:91], v[204:207], v[44:47], a[88:91]\n"
    "v_mfma_f32_16x16x32_bf16 a[92:95], a[252:255], v[40:43], a[92:95]\n"
    "v_cvt_pk_bf16_f32 v40, v8, v9\n"
    "v_cvt_pk_bf16_f32 v41, v10, v11\n"
    "v_exp_f32 v8, v72\n"
    "v_exp_f32 v9, v73\n"
    "v_exp_f32 v10, v74\n"
    "v_exp_f32 v11, v75\n"
    "v_cvt_pk_bf16_f32 v42, v240, v241\n"
    "v_cvt_pk_bf16_f32 v43, v242, v243\n"
    "v_exp_f32 v240, v76\n"
    "v_exp_f32 v241, v77\n"
    "v_exp_f32 v242, v78\n"
    "v_exp_f32 v243, v79\n"
    "v_mfma_f32_16x16x32_bf16 a[92:95], v[208:211], v[44:47], a[92:95]\n"
    "v_cvt_pk_bf16_f32 v44, v8, v9\n"
    "v_cvt_pk_bf16_f32 v45, v10, v11\n"
    "v_cvt_pk_bf16_f32 v46, v240, v241\n"
    "v_cvt_pk_bf16_f32 v47, v242, v243\n"
    "v_mfma_f32_16x16x32_bf16 v[12:15], v[16:19], v[40:43], 0\n"
    "s_nop 0\n"
    "v_mfma_f32_16x16x32_bf16 v[12:15], v[16:19], v[44:47], v[12:15]\n"
    "v_mfma_f32_16x16x32_bf16 a[96:99], a[224:227], v[32:35], a[96:99]\n"
    "v_mfma_f32_16x16x32_bf16 a[96:99], v[180:183], v[36:39], a[96:99]\n"
    "v_mfma_f32_16x16x32_bf16 a[100:103], a[228:231], v[32:35], a[100:103]\n"
    "v_rcp_f32 v254, v22\n"
    "s_add_u32 s49, s43, 8192\n"
    "v_accvgpr_read_b32 v244, a64\n"
    "v_accvgpr_read_b32 v245, a65\n"
    "v_mul_f32 v244, v254, v244\n"
    "v_mul_f32 v245, v254, v245\n"
    "v_cvt_pk_bf16_f32 v246, v244, v245\n"
    "v_accvgpr_read_b32 v244, a66\n"
    "v_accvgpr_read_b32 v245, a67\n"
    "v_mul_f32 v244, v254, v244\n"
    "v_mul_f32 v245, v254, v245\n"
    "v_cvt_pk_bf16_f32 v247, v244, v245\n"
    "v_accvgpr_read_b32 v244, a68\n"
    "v_accvgpr_read_b32 v245, a69\n"
    "v_mul_f32 v244, v254, v244\n"
    "v_mul_f32 v245, v254, v245\n"
    "v_cvt_pk_bf16_f32 v248, v244, v245\n"
    "v_accvgpr_read_b32 v244, a70\n"
    "v_accvgpr_read_b32 v245, a71\n"
    "v_mul_f32 v244, v254, v244\n"
    "v_mul_f32 v245, v254, v245\n"
    "v_cvt_pk_bf16_f32 v249, v244, v245\n"
    "v_permlane16_swap_b32 v246, v248\n"
    "s_nop 0\n"
    "v_permlane16_swap_b32 v247, v249\n"
    "v_accvgpr_read_b32 v244, a72\n"
    "v_accvgpr_read_b32 v245, a73\n"
    "v_mul_f32 v244, v254, v244\n"
    "v_mul_f32 v245, v254, v245\n"
    "v_cvt_pk_bf16_f32 v250, v244, v245\n"
    "v_accvgpr_read_b32 v244, a74\n"
    "v_accvgpr_read_b32 v245, a75\n"
    "v_mul_f32 v244, v254, v244\n"
    "v_mul_f32 v245, v254, v245\n"
    "v_cvt_pk_bf16_f32 v251, v244, v245\n"
    "v_accvgpr_read_b32 v244, a76\n"
    "v_accvgpr_read_b32 v245, a77\n"
    "v_mul_f32 v244, v254, v244\n"
    "v_mul_f32 v245, v254, v245\n"
    "v_cvt_pk_bf16_f32 v252, v244, v245\n"
    "v_accvgpr_read_b32 v244, a78\n"
    "v_accvgpr_read_b32 v245, a79\n"
    "v_mul_f32 v244, v254, v244\n"
    "v_mul_f32 v245, v254, v245\n"
    "v_cvt_pk_bf16_f32 v253, v244, v245\n"
    "v_permlane16_swap_b32 v250, v252\n"
    "s_nop 0\n"
    "v_permlane16_swap_b32 v251, v253\n"
    "v_mov_b32 v244, v246\n"
    "v_mov_b32_dpp v246, v250 row_ror:8 row_mask:0xf bank_mask:0xc\n"
    "v_mov_b32 v245, v247\n"
    "v_mov_b32_dpp v247, v251 row_ror:8 row_mask:0xf bank_mask:0xc\n"
    "v_mov_b32_dpp v250, v244 row_ror:8 row_mask:0xf bank_mask:0x3\n"
    "v_mov_b32_dpp v251, v245 row_ror:8 row_mask:0xf bank_mask:0x3\n"
    "v_mov_b32 v244, v248\n"
    "v_mov_b32_dpp v248, v252 row_ror:8 row_mask:0xf bank_mask:0xc\n"
    "v_mov_b32 v245, v249\n"
    "v_mov_b32_dpp v249, v253 row_ror:8 row_mask:0xf bank_mask:0xc\n"
    "v_mov_b32_dpp v252, v244 row_ror:8 row_mask:0xf bank_mask:0x3\n"
    "v_mov_b32_dpp v253, v245 row_ror:8 row_mask:0xf bank_mask:0x3\n"
    "buffer_store_dwordx4 v[246:249], v7, s[28:31], s49 offen offset:0 nt\n"
    "buffer_store_dwordx4 v[250:253], v7, s[28:31], s49 offen offset:2048 nt\n"
    "v_accvgpr_read_b32 v244, a80\n"
    "v_accvgpr_read_b32 v245, a81\n"
    "v_mul_f32 v244, v254, v244\n"
    "v_mul_f32 v245, v254, v245\n"
    "v_cvt_pk_bf16_f32 v246, v244, v245\n"
    "v_accvgpr_read_b32 v244, a82\n"
    "v_accvgpr_read_b32 v245, a83\n"
    "v_mul_f32 v244, v254, v244\n"
    "v_mul_f32 v245, v254, v245\n"
    "v_cvt_pk_bf16_f32 v247, v244, v245\n"
    "v_accvgpr_read_b32 v244, a84\n"
    "v_accvgpr_read_b32 v245, a85\n"
    "v_mul_f32 v244, v254, v244\n"
    "v_mul_f32 v245, v254, v245\n"
    "v_cvt_pk_bf16_f32 v248, v244, v245\n"
    "v_accvgpr_read_b32 v244, a86\n"
    "v_accvgpr_read_b32 v245, a87\n"
    "v_mul_f32 v244, v254, v244\n"
    "v_mul_f32 v245, v254, v245\n"
    "v_cvt_pk_bf16_f32 v249, v244, v245\n"
    "v_permlane16_swap_b32 v246, v248\n"
    "s_nop 0\n"
    "v_permlane16_swap_b32 v247, v249\n"
    "v_accvgpr_read_b32 v244, a88\n"
    "v_accvgpr_read_b32 v245, a89\n"
    "v_mul_f32 v244, v254, v244\n"
    "v_mul_f32 v245, v254, v245\n"
    "v_cvt_pk_bf16_f32 v250, v244, v245\n"
    "v_accvgpr_read_b32 v244, a90\n"
    "v_accvgpr_read_b32 v245, a91\n"
    "v_mul_f32 v244, v254, v244\n"
    "v_mul_f32 v245, v254, v245\n"
    "v_cvt_pk_bf16_f32 v251, v244, v245\n"
    "v_accvgpr_read_b32 v244, a92\n"
    "v_accvgpr_read_b32 v245, a93\n"
    "v_mul_f32 v244, v254, v244\n"
    "v_mul_f32 v245, v254, v245\n"
    "v_mfma_f32_16x16x32_bf16 a[100:103], v[184:187], v[36:39], a[100:103]\n"
    "v_cvt_pk_bf16_f32 v252, v244, v245\n"
    "v_accvgpr_read_b32 v244, a94\n"
    "v_accvgpr_read_b32 v245, a95\n"
    "v_mul_f32 v244, v254, v244\n"
    "v_mul_f32 v245, v254, v245\n"
    "v_cvt_pk_bf16_f32 v253, v244, v245\n"
    "v_permlane16_swap_b32 v250, v252\n"
    "s_nop 0\n"
    "v_permlane16_swap_b32 v251, v253\n"
    "v_mfma_f32_16x16x32_bf16 a[104:107], a[232:235], v[32:35], a[104:107]\n"
    "v_mov_b32 v244, v246\n"
    "v_mov_b32_dpp v246, v250 row_ror:8 row_mask:0xf bank_mask:0xc\n"
    "v_mov_b32 v245, v247\n"
    "v_mov_b32_dpp v247, v251 row_ror:8 row_mask:0xf bank_mask:0xc\n"
    "v_mov_b32_dpp v250, v244 row_ror:8 row_mask:0xf bank_mask:0x3\n"
    "v_mov_b32_dpp v251, v245 row_ror:8 row_mask:0xf bank_mask:0x3\n"
    "v_mov_b32 v244, v248\n"
    "v_mov_b32_dpp v248, v252 row_ror:8 row_mask:0xf bank_mask:0xc\n"
    "v_cmp_nge_f32 vcc, s40, v12\n"
    "v_mfma_f32_16x16x32_bf16 a[104:107], v[188:191], v[36:39], a[104:107]\n"
    "v_mov_b32 v245, v249\n"
    "v_mov_b32_dpp v249, v253 row_ror:8 row_mask:0xf bank_mask:0xc\n"
    "v_mov_b32_dpp v252, v244 row_ror:8 row_mask:0xf bank_mask:0x3\n"
    "v_mov_b32_dpp v253, v245 row_ror:8 row_mask:0xf bank_mask:0x3\n"
    "buffer_store_dwordx4 v[246:249], v7, s[28:31], s49 offen offset:128 nt\n"
    "buffer_store_dwordx4 v[250:253], v7, s[28:31], s49 offen offset:2176 nt\n"
    "v_mfma_f32_16x16x32_bf16 a[108:111], a[236:239], v[32:35], a[108:111]\n"
    "v_mfma_f32_16x16x32_bf16 a[108:111], v[192:195], v[36:39], a[108:111]\n"
    "s_cbranch_vccnz LTR_39\n"
    "LRET_39:\n"
    "v_add_f32 v24, v24, v12\n"
    "v_mfma_f32_16x16x32_bf16 a[112:115], a[240:243], v[32:35], a[112:115]\n"
    "v_mfma_f32_16x16x32_bf16 a[112:115], v[196:199], v[36:39], a[112:115]\n"
    "v_mfma_f32_16x16x32_bf16 a[116:119], a[244:247], v[32:35], a[116:119]\n"
    "v_mfma_f32_16x16x32_bf16 a[116:119], v[200:203], v[36:39], a[116:119]\n"
    "v_mfma_f32_16x16x32_bf16 a[120:123], a[248:251], v[32:35], a[120:123]\n"
    "v_mfma_f32_16x16x32_bf16 a[120:123], v[204:207], v[36:39], a[120:123]\n"
    "v_mfma_f32_16x16x32_bf16 a[124:127], a[252:255], v[32:35], a[124:127]\n"
    "v_mfma_f32_16x16x32_bf16 a[124:127], v[208:211], v[36:39], a[124:127]\n"
    "v_mfma_f32_16x16x32_bf16 a[128:131], a[224:227], v[40:43], a[128:131]\n"
    "v_mfma_f32_16x16x32_bf16 a[128:131], v[180:183], v[44:47], a[128:131]\n"
    "v_mfma_f32_16x16x32_bf16 a[132:135], a[228:231], v[40:43], a[132:135]\n"
    "v_rcp_f32 v254, v23\n"
    "s_add_u32 s49, s43, 12288\n"
    "v_accvgpr_read_b32 v244, a96\n"
    "v_accvgpr_read_b32 v245, a97\n"
    "v_mul_f32 v244, v254, v244\n"
    "v_mul_f32 v245, v254, v245\n"
    "v_cvt_pk_bf16_f32 v246, v244, v245\n"
    "v_accvgpr_read_b32 v244, a98\n"
    "v_accvgpr_read_b32 v245, a99\n"
    "v_mul_f32 v244, v254, v244\n"
    "v_mul_f32 v245, v254, v245\n"
    "v_cvt_pk_bf16_f32 v247, v244, v245\n"
    "v_accvgpr_read_b32 v244, a100\n"
    "v_accvgpr_read_b32 v245, a101\n"
    "v_mul_f32 v244, v254, v244\n"
    "v_mul_f32 v245, v254, v245\n"
    "v_cvt_pk_bf16_f32 v248, v244, v245\n"
    "v_accvgpr_read_b32 v244, a102\n"
    "v_accvgpr_read_b32 v245, a103\n"
    "v_mul_f32 v244, v254, v244\n"
    "v_mul_f32 v245, v254, v245\n"
    "v_cvt_pk_bf16_f32 v249, v244, v245\n"
    "v_permlane16_swap_b32 v246, v248\n"
    "s_nop 0\n"
    "v_permlane16_swap_b32 v247, v249\n"
    "v_accvgpr_read_b32 v244, a104\n"
    "v_accvgpr_read_b32 v245, a105\n"
    "v_mul_f32 v244, v254, v244\n"
    "v_mul_f32 v245, v254, v245\n"
    "v_cvt_pk_bf16_f32 v250, v244, v245\n"
    "v_accvgpr_read_b32 v244, a106\n"
    "v_accvgpr_read_b32 v245, a107\n"
    "v_mul_f32 v244, v254, v244\n"
    "v_mul_f32 v245, v254, v245\n"
    "v_cvt_pk_bf16_f32 v251, v244, v245\n"
    "v_accvgpr_read_b32 v244, a108\n"
    "v_accvgpr_read_b32 v245, a109\n"
    "v_mul_f32 v244, v254, v244\n"
    "v_mul_f32 v245, v254, v245\n"
    "v_cvt_pk_bf16_f32 v252, v244, v245\n"
    "v_accvgpr_read_b32 v244, a110\n"
    "v_accvgpr_read_b32 v245, a111\n"
    "v_mul_f32 v244, v254, v244\n"
    "v_mul_f32 v245, v254, v245\n"
    "v_cvt_pk_bf16_f32 v253, v244, v245\n"
    "v_permlane16_swap_b32 v250, v252\n"
    "s_nop 0\n"
    "v_permlane16_swap_b32 v251, v253\n"
    "v_mov_b32 v244, v246\n"
    "v_mov_b32_dpp v246, v250 row_ror:8 row_mask:0xf bank_mask:0xc\n"
    "v_mov_b32 v245, v247\n"
    "v_mov_b32_dpp v247, v251 row_ror:8 row_mask:0xf bank_mask:0xc\n"
    "v_mov_b32_dpp v250, v244 row_ror:8 row_mask:0xf bank_mask:0x3\n"
    "v_mov_b32_dpp v251, v245 row_ror:8 row_mask:0xf bank_mask:0x3\n"
    "v_mov_b32 v244, v248\n"
    "v_mov_b32_dpp v248, v252 row_ror:8 row_mask:0xf bank_mask:0xc\n"
    "v_mov_b32 v245, v249\n"
    "v_mov_b32_dpp v249, v253 row_ror:8 row_mask:0xf bank_mask:0xc\n"
    "v_mov_b32_dpp v252, v244 row_ror:8 row_mask:0xf bank_mask:0x3\n"
    "v_mov_b32_dpp v253, v245 row_ror:8 row_mask:0xf bank_mask:0x3\n"
    "buffer_store_dwordx4 v[246:249], v7, s[28:31], s49 offen offset:0 nt\n"
    "buffer_store_dwordx4 v[250:253], v7, s[28:31], s49 offen offset:2048 nt\n"
    "v_accvgpr_read_b32 v244, a112\n"
    "v_accvgpr_read_b32 v245, a113\n"
    "v_mul_f32 v244, v254, v244\n"
    "v_mul_f32 v245, v254, v245\n"
    "v_cvt_pk_bf16_f32 v246, v244, v245\n"
    "v_accvgpr_read_b32 v244, a114\n"
    "v_accvgpr_read_b32 v245, a115\n"
    "v_mul_f32 v244, v254, v244\n"
    "v_mul_f32 v245, v254, v245\n"
    "v_cvt_pk_bf16_f32 v247, v244, v245\n"
    "v_accvgpr_read_b32 v244, a116\n"
    "v_accvgpr_read_b32 v245, a117\n"
    "v_mul_f32 v244, v254, v244\n"
    "v_mul_f32 v245, v254, v245\n"
    "v_cvt_pk_bf16_f32 v248, v244, v245\n"
    "v_accvgpr_read_b32 v244, a118\n"
    "v_accvgpr_read_b32 v245, a119\n"
    "v_mul_f32 v244, v254, v244\n"
    "v_mul_f32 v245, v254, v245\n"
    "v_cvt_pk_bf16_f32 v249, v244, v245\n"
    "v_permlane16_swap_b32 v246, v248\n"
    "s_nop 0\n"
    "v_permlane16_swap_b32 v247, v249\n"
    "v_accvgpr_read_b32 v244, a120\n"
    "v_accvgpr_read_b32 v245, a121\n"
    "v_mul_f32 v244, v254, v244\n"
    "v_mul_f32 v245, v254, v245\n"
    "v_cvt_pk_bf16_f32 v250, v244, v245\n"
    "v_accvgpr_read_b32 v244, a122\n"
    "v_accvgpr_read_b32 v245, a123\n"
    "v_mul_f32 v244, v254, v244\n"
    "v_mul_f32 v245, v254, v245\n"
    "v_cvt_pk_bf16_f32 v251, v244, v245\n"
    "v_accvgpr_read_b32 v244, a124\n"
    "v_accvgpr_read_b32 v245, a125\n"
    "v_mul_f32 v244, v254, v244\n"
    "v_mul_f32 v245, v254, v245\n"
    "v_mfma_f32_16x16x32_bf16 a[132:135], v[184:187], v[44:47], a[132:135]\n"
    "v_cvt_pk_bf16_f32 v252, v244, v245\n"
    "v_accvgpr_read_b32 v244, a126\n"
    "v_mfma_f32_16x16x32_bf16 a[136:139], a[232:235], v[40:43], a[136:139]\n"
    "v_accvgpr_read_b32 v245, a127\n"
    "v_mul_f32 v244, v254, v244\n"
    "v_mfma_f32_16x16x32_bf16 a[136:139], v[188:191], v[44:47], a[136:139]\n"
    "v_mul_f32 v245, v254, v245\n"
    "v_cvt_pk_bf16_f32 v253, v244, v245\n"
    "v_mfma_f32_16x16x32_bf16 a[140:143], a[236:239], v[40:43], a[140:143]\n"
    "v_permlane16_swap_b32 v250, v252\n"
    "v_mfma_f32_16x16x32_bf16 a[140:143], v[192:195], v[44:47], a[140:143]\n"
    "v_permlane16_swap_b32 v251, v253\n"
    "v_mfma_f32_16x16x32_bf16 a[144:147], a[240:243], v[40:43], a[144:147]\n"
    "v_mov_b32 v244, v246\n"
    "v_mov_b32_dpp v246, v250 row_ror:8 row_mask:0xf bank_mask:0xc\n"
    "v_mfma_f32_16x16x32_bf16 a[144:147], v[196:199], v[44:47], a[144:147]\n"
    "v_mov_b32 v245, v247\n"
    "v_mov_b32_dpp v247, v251 row_ror:8 row_mask:0xf bank_mask:0xc\n"
    "v_mfma_f32_16x16x32_bf16 a[148:151], a[244:247], v[40:43], a[148:151]\n"
    "v_mov_b32_dpp v250, v244 row_ror:8 row_mask:0xf bank_mask:0x3\n"
    "v_mov_b32_dpp v251, v245 row_ror:8 row_mask:0xf bank_mask:0x3\n"
    "v_mfma_f32_16x16x32_bf16 a[148:151], v[200:203], v[44:47], a[148:151]\n"
    "v_mov_b32 v244, v248\n"
    "v_mov_b32_dpp v248, v252 row_ror:8 row_mask:0xf bank_mask:0xc\n"
    "v_mfma_f32_16x16x32_bf16 a[152:155], a[248:251], v[40:43], a[152:155]\n"
    "v_mov_b32 v245, v249\n"
    "v_mov_b32_dpp v249, v253 row_ror:8 row_mask:0xf bank_mask:0xc\n"
    "v_mfma_f32_16x16x32_bf16 a[152:155], v[204:207], v[44:47], a[152:155]\n"
    "v_mov_b32_dpp v252, v244 row_ror:8 row_mask:0xf bank_mask:0x3\n"
    "v_mov_b32_dpp v253, v245 row_ror:8 row_mask:0xf bank_mask:0x3\n"
    "v_mfma_f32_16x16x32_bf16 a[156:159], a[252:255], v[40:43], a[156:159]\n"
    "buffer_store_dwordx4 v[246:249], v7, s[28:31], s49 offen offset:128 nt\n"
    "v_mfma_f32_16x16x32_bf16 a[156:159], v[208:211], v[44:47], a[156:159]\n"
    "buffer_store_dwordx4 v[250:253], v7, s[28:31], s49 offen offset:2176 nt\n"
    "v_rcp_f32 v254, v24\n"
    "s_add_u32 s49, s43, 16384\n"
    "v_accvgpr_read_b32 v244, a128\n"
    "v_accvgpr_read_b32 v245, a129\n"
    "v_mul_f32 v244, v254, v244\n"
    "v_mul_f32 v245, v254, v245\n"
    "v_cvt_pk_bf16_f32 v246, v244, v245\n"
    "v_accvgpr_read_b32 v244, a130\n"
    "v_accvgpr_read_b32 v245, a131\n"
    "v_mul_f32 v244, v254, v244\n"
    "v_mul_f32 v245, v254, v245\n"
    "v_cvt_pk_bf16_f32 v247, v244, v245\n"
    "v_accvgpr_read_b32 v244, a132\n"
    "v_accvgpr_read_b32 v245, a133\n"
    "v_mul_f32 v244, v254, v244\n"
    "v_mul_f32 v245, v254, v245\n"
    "v_cvt_pk_bf16_f32 v248, v244, v245\n"
    "v_accvgpr_read_b32 v244, a134\n"
    "v_accvgpr_read_b32 v245, a135\n"
    "v_mul_f32 v244, v254, v244\n"
    "v_mul_f32 v245, v254, v245\n"
    "v_cvt_pk_bf16_f32 v249, v244, v245\n"
    "v_permlane16_swap_b32 v246, v248\n"
    "s_nop 0\n"
    "v_permlane16_swap_b32 v247, v249\n"
    "v_accvgpr_read_b32 v244, a136\n"
    "v_accvgpr_read_b32 v245, a137\n"
    "v_mul_f32 v244, v254, v244\n"
    "v_mul_f32 v245, v254, v245\n"
    "v_cvt_pk_bf16_f32 v250, v244, v245\n"
    "v_accvgpr_read_b32 v244, a138\n"
    "v_accvgpr_read_b32 v245, a139\n"
    "v_mul_f32 v244, v254, v244\n"
    "v_mul_f32 v245, v254, v245\n"
    "v_cvt_pk_bf16_f32 v251, v244, v245\n"
    "v_accvgpr_read_b32 v244, a140\n"
    "v_accvgpr_read_b32 v245, a141\n"
    "v_mul_f32 v244, v254, v244\n"
    "v_mul_f32 v245, v254, v245\n"
    "v_cvt_pk_bf16_f32 v252, v244, v245\n"
    "v_accvgpr_read_b32 v244, a142\n"
    "v_accvgpr_read_b32 v245, a143\n"
    "v_mul_f32 v244, v254, v244\n"
    "v_mul_f32 v245, v254, v245\n"
    "v_cvt_pk_bf16_f32 v253, v244, v245\n"
    "v_permlane16_swap_b32 v250, v252\n"
    "s_nop 0\n"
    "v_permlane16_swap_b32 v251, v253\n"
    "v_mov_b32 v244, v246\n"
    "v_mov_b32_dpp v246, v250 row_ror:8 row_mask:0xf bank_mask:0xc\n"
    "v_mov_b32 v245, v247\n"
    "v_mov_b32_dpp v247, v251 row_ror:8 row_mask:0xf bank_mask:0xc\n"
    "v_mov_b32_dpp v250, v244 row_ror:8 row_mask:0xf bank_mask:0x3\n"
    "v_mov_b32_dpp v251, v245 row_ror:8 row_mask:0xf bank_mask:0x3\n"
    "v_mov_b32 v244, v248\n"
    "v_mov_b32_dpp v248, v252 row_ror:8 row_mask:0xf bank_mask:0xc\n"
    "v_mov_b32 v245, v249\n"
    "v_mov_b32_dpp v249, v253 row_ror:8 row_mask:0xf bank_mask:0xc\n"
    "v_mov_b32_dpp v252, v244 row_ror:8 row_mask:0xf bank_mask:0x3\n"
    "v_mov_b32_dpp v253, v245 row_ror:8 row_mask:0xf bank_mask:0x3\n"
    "buffer_store_dwordx4 v[246:249], v7, s[28:31], s49 offen offset:0 nt\n"
    "buffer_store_dwordx4 v[250:253], v7, s[28:31], s49 offen offset:2048 nt\n"
    "v_accvgpr_read_b32 v244, a144\n"
    "v_accvgpr_read_b32 v245, a145\n"
    "v_mul_f32 v244, v254, v244\n"
    "v_mul_f32 v245, v254, v245\n"
    "v_cvt_pk_bf16_f32 v246, v244, v245\n"
    "v_accvgpr_read_b32 v244, a146\n"
    "v_accvgpr_read_b32 v245, a147\n"
    "v_mul_f32 v244, v254, v244\n"
    "v_mul_f32 v245, v254, v245\n"
    "v_cvt_pk_bf16_f32 v247, v244, v245\n"
    "v_accvgpr_read_b32 v244, a148\n"
    "v_accvgpr_read_b32 v245, a149\n"
    "v_mul_f32 v244, v254, v244\n"
    "v_mul_f32 v245, v254, v245\n"
    "v_cvt_pk_bf16_f32 v248, v244, v245\n"
    "v_accvgpr_read_b32 v244, a150\n"
    "v_accvgpr_read_b32 v245, a151\n"
    "v_mul_f32 v244, v254, v244\n"
    "v_mul_f32 v245, v254, v245\n"
    "v_cvt_pk_bf16_f32 v249, v244, v245\n"
    "v_permlane16_swap_b32 v246, v248\n"
    "s_nop 0\n"
    "v_permlane16_swap_b32 v247, v249\n"
    "v_accvgpr_read_b32 v244, a152\n"
    "v_accvgpr_read_b32 v245, a153\n"
    "v_mul_f32 v244, v254, v244\n"
    "v_mul_f32 v245, v254, v245\n"
    "v_cvt_pk_bf16_f32 v250, v244, v245\n"
    "v_accvgpr_read_b32 v244, a154\n"
    "v_accvgpr_read_b32 v245, a155\n"
    "v_mul_f32 v244, v254, v244\n"
    "v_mul_f32 v245, v254, v245\n"
    "v_cvt_pk_bf16_f32 v251, v244, v245\n"
    "v_accvgpr_read_b32 v244, a156\n"
    "v_accvgpr_read_b32 v245, a157\n"
    "v_mul_f32 v244, v254, v244\n"
    "v_mul_f32 v245, v254, v245\n"
    "v_cvt_pk_bf16_f32 v252, v244, v245\n"
    "v_accvgpr_read_b32 v244, a158\n"
    "v_accvgpr_read_b32 v245, a159\n"
    "v_mul_f32 v244, v254, v244\n"
    "v_mul_f32 v245, v254, v245\n"
    "v_cvt_pk_bf16_f32 v253, v244, v245\n"
    "v_permlane16_swap_b32 v250, v252\n"
    "s_nop 0\n"
    "v_permlane16_swap_b32 v251, v253\n"
    "v_mov_b32 v244, v246\n"
    "v_mov_b32_dpp v246, v250 row_ror:8 row_mask:0xf bank_mask:0xc\n"
    "v_mov_b32 v245, v247\n"
    "v_mov_b32_dpp v247, v251 row_ror:8 row_mask:0xf bank_mask:0xc\n"
    "v_mov_b32_dpp v250, v244 row_ror:8 row_mask:0xf bank_mask:0x3\n"
    "v_mov_b32_dpp v251, v245 row_ror:8 row_mask:0xf bank_mask:0x3\n"
    "v_mov_b32 v244, v248\n"
    "v_mov_b32_dpp v248, v252 row_ror:8 row_mask:0xf bank_mask:0xc\n"
    "v_mov_b32 v245, v249\n"
    "v_mov_b32_dpp v249, v253 row_ror:8 row_mask:0xf bank_mask:0xc\n"
    "v_mov_b32_dpp v252, v244 row_ror:8 row_mask:0xf bank_mask:0x3\n"
    "v_mov_b32_dpp v253, v245 row_ror:8 row_mask:0xf bank_mask:0x3\n"
    "buffer_store_dwordx4 v[246:249], v7, s[28:31], s49 offen offset:128 nt\n"
    "buffer_store_dwordx4 v[250:253], v7, s[28:31], s49 offen offset:2176 nt\n"
    "s_branch LEND\n"
    "LV4:\n"
    "s_mov_b32 s34, s35\n"
    "s_add_u32 s53, s44, 4096\n"
    "s_add_u32 s60, s44, 16384\n"
    "buffer_load_dwordx4 v[100:103], v6, s[16:19], s43 offen offset:0 \n"
    "buffer_load_dwordx4 v[104:107], v6, s[16:19], s43 offen offset:64 \n"
    "buffer_load_dwordx4 v[108:111], v6, s[16:19], s43 offen offset:128 \n"
    "buffer_load_dwordx4 v[112:115], v6, s[16:19], s43 offen offset:192 \n"
    "s_add_u32 m0, s34, 0\n"
    "s_nop 0\n"
    "buffer_load_dwordx4 v1, s[20:23], s32 offen offset:0  lds\n"
    "s_add_u32 m0, s34, 32\n"
    "s_nop 0\n"
    "buffer_load_dwordx4 v1, s[20:23], s32 offen offset:1024  lds\n"
    "s_add_u32 m0, s34, 64\n"
    "s_nop 0\n"
    "buffer_load_dwordx4 v1, s[20:23], s32 offen offset:2048  lds\n"
    "s_add_u32 m0, s34, 96\n"
    "s_nop 0\n"
    "buffer_load_dwordx4 v1, s[20:23], s32 offen offset:3072  lds\n"
    "s_add_u32 s48, s43, 4096\n"
    "buffer_load_dwordx4 v[116:119], v6, s[16:19], s48 offen offset:0 \n"
    "buffer_load_dwordx4 v[120:123], v6, s[16:19], s48 offen offset:64 \n"
    "buffer_load_dwordx4 v[124:127], v6, s[16:19], s48 offen offset:128 \n"
    "buffer_load_dwordx4 v[128:131], v6, s[16:19], s48 offen offset:192 \n"
    "s_waitcnt vmcnt(8)\n"
    "v_lshlrev_b32 v31, 16, v100\n"
    "v_and_b32 v255, s42, v100\n"
    "v_mul_f32 v31, s41, v31\n"
    "v_mul_f32 v255, s41, v255\n"
    "v_cvt_pk_bf16_f32 v100, v31, v255\n"
    "v_lshlrev_b32 v31, 16, v101\n"
    "v_and_b32 v255, s42, v101\n"
    "v_mul_f32 v31, s41, v31\n"
    "v_mul_f32 v255, s41, v255\n"
    "v_cvt_pk_bf16_f32 v101, v31, v255\n"
    "v_lshlrev_b32 v31, 16, v102\n"
    "v_and_b32 v255, s42, v102\n"
    "v_mul_f32 v31, s41, v31\n"
    "s_add_u32 m0, s34, 16896\n"
    "s_nop 0\n"
    "buffer_load_dwordx4 v1, s[24:27], s32 offen offset:0  lds\n"
    "v_mul_f32 v255, s41, v255\n"
    "v_cvt_pk_bf16_f32 v102, v31, v255\n"
    "v_lshlrev_b32 v31, 16, v103\n"
    "v_and_b32 v255, s42, v103\n"
    "v_mul_f32 v31, s41, v31\n"
    "v_mul_f32 v255, s41, v255\n"
    "v_cvt_pk_bf16_f32 v103, v31, v255\n"
    "v_lshlrev_b32 v31, 16, v104\n"
    "v_and_b32 v255, s42, v104\n"
    "v_mul_f32 v31, s41, v31\n"
    "v_mul_f32 v255, s41, v255\n"
    "v_cvt_pk_bf16_f32 v104, v31, v255\n"
    "v_lshlrev_b32 v31, 16, v105\n"
    "s_add_u32 m0, s34, 16928\n"
    "s_nop 0\n"
    "buffer_load_dwordx4 v1, s[24:27], s32 offen offset:1024  lds\n"
    "v_and_b32 v255, s42, v105\n"
    "v_mul_f32 v31, s41, v31\n"
    "v_mul_f32 v255, s41, v255\n"
    "v_cvt_pk_bf16_f32 v105, v31, v255\n"
    "v_lshlrev_b32 v31, 16, v106\n"
    "v_and_b32 v255, s42, v106\n"
    "v_mul_f32 v31, s41, v31\n"
    "v_mul_f32 v255, s41, v255\n"
    "v_cvt_pk_bf16_f32 v106, v31, v255\n"
    "v_lshlrev_b32 v31, 16, v107\n"
    "v_and_b32 v255, s42, v107\n"
    "v_mul_f32 v31, s41, v31\n"
    "v_mul_f32 v255, s41, v255\n"
    "s_add_u32 m0, s34, 16960\n"
    "s_nop 0\n"
    "buffer_load_dwordx4 v1, s[24:27], s32 offen offset:2048  lds\n"
    "v_cvt_pk_bf16_f32 v107, v31, v255\n"
    "v_lshlrev_b32 v31, 16, v108\n"
    "v_and_b32 v255, s42, v108\n"
    "v_mul_f32 v31, s41, v31\n"
    "v_mul_f32 v255, s41, v255\n"
    "v_cvt_pk_bf16_f32 v108, v31, v255\n"
    "v_lshlrev_b32 v31, 16, v109\n"
    "v_and_b32 v255, s42, v109\n"
    "v_mul_f32 v31, s41, v31\n"
    "v_mul_f32 v255, s41, v255\n"
    "v_cvt_pk_bf16_f32 v109, v31, v255\n"
    "v_lshlrev_b32 v31, 16, v110\n"
    "v_and_b32 v255, s42, v110\n"
    "s_add_u32 m0, s34, 16992\n"
    "s_nop 0\n"
    "buffer_load_dwordx4 v1, s[24:27], s32 offen offset:3072  lds\n"
    "s_add_u32 s32, s32, 16384\n"
    "v_mul_f32 v31, s41, v31\n"
    "v_mul_f32 v255, s41, v255\n"
    "v_cvt_pk_bf16_f32 v110, v31, v255\n"
    "v_lshlrev_b32 v31, 16, v111\n"
    "v_and_b32 v255, s42, v111\n"
    "v_mul_f32 v31, s41, v31\n"
    "v_mul_f32 v255, s41, v255\n"
    "v_cvt_pk_bf16_f32 v111, v31, v255\n"
    "v_lshlrev_b32 v31, 16, v112\n"
    "v_and_b32 v255, s42, v112\n"
    "v_mul_f32 v31, s41, v31\n"
    "v_mul_f32 v255, s41, v255\n"
    "v_cvt_pk_bf16_f32 v112, v31, v255\n"
    "s_add_u32 s48, s43, 8192\n"
    "buffer_load_dwordx4 v[132:135], v6, s[16:19], s48 offen offset:0 \n"
    "v_lshlrev_b32 v31, 16, v113\n"
    "v_and_b32 v255, s42, v113\n"
    "v_mul_f32 v31, s41, v31\n"
    "v_mul_f32 v255, s41, v255\n"
    "v_cvt_pk_bf16_f32 v113, v31, v255\n"
    "v_lshlrev_b32 v31, 16, v114\n"
    "v_and_b32 v255, s42, v114\n"
    "v_mul_f32 v31, s41, v31\n"
    "v_mul_f32 v255, s41, v255\n"
    "v_cvt_pk_bf16_f32 v114, v31, v255\n"
    "v_lshlrev_b32 v31, 16, v115\n"
    "v_and_b32 v255, s42, v115\n"
    "v_mul_f32 v31, s41, v31\n"
    "buffer_load_dwordx4 v[136:139], v6, s[16:19], s48 offen offset:64 \n"
    "v_mul_f32 v255, s41, v255\n"
    "v_cvt_pk_bf16_f32 v115, v31, v255\n"
    "s_waitcnt vmcnt(10)\n"
    "s_barrier\n"
    "ds_read_b128 a[160:163], v4 offset:0\n"
    "ds_read_b128 a[164:167], v4 offset:64\n"
    "ds_read_b128 a[168:171], v4 offset:128\n"
    "ds_read_b128 a[172:175], v4 offset:192\n"
    "ds_read_b128 a[176:179], v4 offset:256\n"
    "ds_read_b128 a[180:183], v4 offset:320\n"
    "ds_read_b128 a[184:187], v4 offset:384\n"
    "ds_read_b128 a[188:191], v4 offset:448\n"
    "ds_read_b128 a[192:195], v4 offset:512\n"
    "ds_read_b128 a[196:199], v4 offset:576\n"
    "ds_read_b128 a[200:203], v4 offset:640\n"
    "ds_read_b128 a[204:207], v4 offset:704\n"
    "ds_read_b128 a[208:211], v4 offset:768\n"
    "ds_read_b128 a[212:215], v4 offset:832\n"
    "ds_read_b128 a[216:219], v4 offset:896\n"
    "ds_read_b128 a[220:223], v4 offset:960\n"
    "s_waitcnt lgkmcnt(8)\n"
    "v_mfma_f32_16x16x32_bf16 v[48:51], a[160:163], v[100:103], 0\n"
    "s_waitcnt vmcnt(6)\n"
    "v_lshlrev_b32 v31, 16, v116\n"
    "v_and_b32 v255, s42, v116\n"
    "v_mul_f32 v31, s41, v31\n"
    "v_mul_f32 v255, s41, v255\n"
    "v_cvt_pk_bf16_f32 v116, v31, v255\n"
    "v_lshlrev_b32 v31, 16, v117\n"
    "v_and_b32 v255, s42, v117\n"
    "v_mul_f32 v31, s41, v31\n"
    "v_mul_f32 v255, s41, v255\n"
    "v_cvt_pk_bf16_f32 v117, v31, v255\n"
    "v_lshlrev_b32 v31, 16, v118\n"
    "v_and_b32 v255, s42, v118\n"
    "v_mul_f32 v31, s41, v31\n"
    "v_mul_f32 v255, s41, v255\n"
    "v_cvt_pk_bf16_f32 v118, v31, v255\n"
    "v_lshlrev_b32 v31, 16, v119\n"
    "v_and_b32 v255, s42, v119\n"
    "v_mul_f32 v31, s41, v31\n"
    "v_mul_f32 v255, s41, v255\n"
    "v_cvt_pk_bf16_f32 v119, v31, v255\n"
    "v_lshlrev_b32 v31, 16, v120\n"
    "v_and_b32 v255, s42, v120\n"
    "v_mul_f32 v31, s41, v31\n"
    "v_mul_f32 v255, s41, v255\n"
    "v_cvt_pk_bf16_f32 v120, v31, v255\n"
    "v_lshlrev_b32 v31, 16, v121\n"
    "v_and_b32 v255, s42, v121\n"
    "v_mul_f32 v31, s41, v31\n"
    "v_mul_f32 v255, s41, v255\n"
    "v_cvt_pk_bf16_f32 v121, v31, v255\n"
    "v_lshlrev_b32 v31, 16, v122\n"
    "v_and_b32 v255, s42, v122\n"
    "v_mul_f32 v31, s41, v31\n"
    "v_mul_f32 v255, s41, v255\n"
    "v_cvt_pk_bf16_f32 v122, v31, v255\n"
    "v_lshlrev_b32 v31, 16, v123\n"
    "v_and_b32 v255, s42, v123\n"
    "v_mul_f32 v31, s41, v31\n"
    "v_mul_f32 v255, s41, v255\n"
    "v_cvt_pk_bf16_f32 v123, v31, v255\n"
    "v_lshlrev_b32 v31, 16, v124\n"
    "v_and_b32 v255, s42, v124\n"
    "v_mul_f32 v31, s41, v31\n"
    "v_mul_f32 v255, s41, v255\n"
    "v_cvt_pk_bf16_f32 v124, v31, v255\n"
    "v_lshlrev_b32 v31, 16, v125\n"
    "v_and_b32 v255, s42, v125\n"
    "v_mul_f32 v31, s41, v31\n"
    "v_mul_f32 v255, s41, v255\n"
    "v_cvt_pk_bf16_f32 v125, v31, v255\n"
    "v_mfma_f32_16x16x32_bf16 v[48:51], a[164:167], v[104:107], v[48:51]\n"
    "v_lshlrev_b32 v31, 16, v126\n"
    "v_and_b32 v255, s42, v126\n"
    "v_mul_f32 v31, s41, v31\n"
    "v_mul_f32 v255, s41, v255\n"
    "v_mfma_f32_16x16x32_bf16 v[48:51], a[168:171], v[108:111], v[48:51]\n"
    "buffer_load_dwordx4 v[140:143], v6, s[16:19], s48 offen offset:128 \n"
    "v_cvt_pk_bf16_f32 v126, v31, v255\n"
    "v_lshlrev_b32 v31, 16, v127\n"
    "v_mfma_f32_16x16x32_bf16 v[48:51], a[172:175], v[112:115], v[48:51]\n"
    "buffer_load_dwordx4 v[144:147], v6, s[16:19], s48 offen offset:192 \n"
    "v_and_b32 v255, s42, v127\n"
    "v_mul_f32 v31, s41, v31\n"
    "v_mfma_f32_16x16x32_bf16 v[52:55], a[176:179], v[100:103], 0\n"
    "v_mul_f32 v255, s41, v255\n"
    "v_cvt_pk_bf16_f32 v127, v31, v255\n"
    "s_add_u32 s48, s43, 12288\n"
    "buffer_load_dwordx4 v[148:151], v6, s[16:19], s48 offen offset:0 \n"
    "v_mfma_f32_16x16x32_bf16 v[52:55], a[180:183], v[104:107], v[52:55]\n"
    "v_lshlrev_b32 v31, 16, v128\n"
    "v_and_b32 v255, s42, v128\n"
    "v_mul_f32 v31, s41, v31\n"
    "v_mul_f32 v255, s41, v255\n"
    "v_mfma_f32_16x16x32_bf16 v[52:55], a[184:187], v[108:111], v[52:55]\n"
    "buffer_load_dwordx4 v[152:155], v6, s[16:19], s48 offen offset:64 \n"
    "v_cvt_pk_bf16_f32 v128, v31, v255\n"
    "v_lshlrev_b32 v31, 16, v129\n"
    "v_mfma_f32_16x16x32_bf16 v[52:55], a[188:191], v[112:115], v[52:55]\n"
    "buffer_load_dwordx4 v[156:159], v6, s[16:19], s48 offen offset:128 \n"
    "v_and_b32 v255, s42, v129\n"
    "v_mul_f32 v31, s41, v31\n"
    "s_waitcnt lgkmcnt(0)\n"
    "v_mfma_f32_16x16x32_bf16 v[56:59], a[192:195], v[100:103], 0\n"
    "v_mul_f32 v255, s41, v255\n"
    "v_cvt_pk_bf16_f32 v129, v31, v255\n"
    "v_lshlrev_b32 v31, 16, v130\n"
    "v_and_b32 v255, s42, v130\n"
    "v_mfma_f32_16x16x32_bf16 v[56:59], a[196:199], v[104:107], v[56:59]\n"
    "buffer_load_dwordx4 v[160:163], v6, s[16:19], s48 offen offset:192 \n"
    "v_mul_f32 v31, s41, v31\n"
    "v_mul_f32 v255, s41, v255\n"
    "v_mfma_f32_16x16x32_bf16 v[56:59], a[200:203], v[108:111], v[56:59]\n"
    "v_cvt_pk_bf16_f32 v130, v31, v255\n"
    "v_lshlrev_b32 v31, 16, v131\n"
    "v_and_b32 v255, s42, v131\n"
    "v_mul_f32 v31, s41, v31\n"
    "v_mfma_f32_16x16x32_bf16 v[56:59], a[204:207], v[112:115], v[56:59]\n"
    "s_add_u32 m0, s35, 33792\n"
    "s_nop 0\n"
    "buffer_load_dwordx4 v1, s[20:23], s32 offen offset:0  lds\n"
    "v_mul_f32 v255, s41, v255\n"
    "v_cvt_pk_bf16_f32 v131, v31, v255\n"
    "v_mfma_f32_16x16x32_bf16 v[60:63], a[208:211], v[100:103], 0\n"
    "v_max3_f32 v25, v48, v49, v50\n"
    "v_max3_f32 v26, v51, v52, v53\n"
    "v_mfma_f32_16x16x32_bf16 v[60:63], a[212:215], v[104:107], v[60:63]\n"
    "s_add_u32 m0, s35, 33824\n"
    "s_nop 0\n"
    "buffer_load_dwordx4 v1, s[20:23], s32 offen offset:1024  lds\n"
    "v_mfma_f32_16x16x32_bf16 v[60:63], a[216:219], v[108:111], v[60:63]\n"
    "v_mfma_f32_16x16x32_bf16 v[60:63], a[220:223], v[112:115], v[60:63]\n"
    "v_max3_f32 v25, v25, v54, v55\n"
    "v_max3_f32 v26, v26, v56, v57\n"
    "v_max3_f32 v25, v25, v58, v59\n"
    "s_nop 4\n"
    "v_max3_f32 v26, v26, v60, v61\n"
    "v_max3_f32 v25, v25, v62, v63\n"
    "v_max_f32 v25, v25, v26\n"
    "s_waitcnt vmcnt(10)\n"
    "s_barrier\n"
    "v_mfma_f32_16x16x32_bf16 v[64:67], a[160:163], v[116:119], 0\n"
    "v_mov_b32 v26, v25\n"
    "s_nop 1\n"
    "v_permlane16_swap_b32 v25, v26\n"
    "v_max_f32 v25, v25, v26\n"
    "v_mov_b32 v26, v25\n"
    "s_nop 1\n"
    "v_permlane32_swap_b32 v25, v26\n"
    "v_max_f32 v25, v25, v26\n"
    "v_sub_f32 v80, 0, v25\n"
    "v_sub_f32 v81, 0, v25\n"
    "v_sub_f32 v82, 0, v25\n"
    "v_sub_f32 v83, 0, v25\n"
    "v_sub_f32 v27, v48, v25\n"
    "v_sub_f32 v28, v49, v25\n"
    "v_sub_f32 v29, v50, v25\n"
    "v_sub_f32 v30, v51, v25\n"
    "v_exp_f32 v8, v27\n"
    "v_exp_f32 v9, v28\n"
    "v_exp_f32 v10, v29\n"
    "v_exp_f32 v11, v30\n"
    "v_cvt_pk_bf16_f32 v32, v8, v9\n"
    "v_cvt_pk_bf16_f32 v33, v10, v11\n"
    "v_sub_f32 v27, v52, v25\n"
    "v_sub_f32 v28, v53, v25\n"
    "v_sub_f32 v29, v54, v25\n"
    "v_sub_f32 v30, v55, v25\n"
    "v_exp_f32 v240, v27\n"
    "v_exp_f32 v241, v28\n"
    "v_exp_f32 v242, v29\n"
    "v_exp_f32 v243, v30\n"
    "v_cvt_pk_bf16_f32 v34, v240, v241\n"
    "v_cvt_pk_bf16_f32 v35, v242, v243\n"
    "v_sub_f32 v27, v56, v25\n"
    "v_sub_f32 v28, v57, v25\n"
    "v_sub_f32 v29, v58, v25\n"
    "v_sub_f32 v30, v59, v25\n"
    "v_exp_f32 v8, v27\n"
    "v_exp_f32 v9, v28\n"
    "v_exp_f32 v10, v29\n"
    "v_exp_f32 v11, v30\n"
    "v_cvt_pk_bf16_f32 v36, v8, v9\n"
    "v_cvt_pk_bf16_f32 v37, v10, v11\n"
    "v_sub_f32 v27, v60, v25\n"
    "v_sub_f32 v28, v61, v25\n"
    "s_waitcnt vmcnt(6)\n"
    "v_lshlrev_b32 v31, 16, v132\n"
    "v_and_b32 v255, s42, v132\n"
    "v_sub_f32 v29, v62, v25\n"
    "v_sub_f32 v30, v63, v25\n"
    "v_mul_f32 v31, s41, v31\n"
    "v_mul_f32 v255, s41, v255\n"
    "v_exp_f32 v240, v27\n"
    "v_cvt_pk_bf16_f32 v132, v31, v255\n"
    "v_lshlrev_b32 v31, 16, v133\n"
    "v_exp_f32 v241, v28\n"
    "v_and_b32 v255, s42, v133\n"
    "v_mul_f32 v31, s41, v31\n"
    "v_exp_f32 v242, v29\n"
    "v_mul_f32 v255, s41, v255\n"
    "v_cvt_pk_bf16_f32 v133, v31, v255\n"
    "v_exp_f32 v243, v30\n"
    "v_lshlrev_b32 v31, 16, v134\n"
    "v_and_b32 v255, s42, v134\n"
    "v_cvt_pk_bf16_f32 v38, v240, v241\n"
    "v_cvt_pk_bf16_f32 v39, v242, v243\n"
    "v_mul_f32 v31, s41, v31\n"
    "v_mul_f32 v255, s41, v255\n"
    "v_cvt_pk_bf16_f32 v134, v31, v255\n"
    "v_lshlrev_b32 v31, 16, v135\n"
    "v_and_b32 v255, s42, v135\n"
    "v_mul_f32 v31, s41, v31\n"
    "v_mul_f32 v255, s41, v255\n"
    "v_cvt_pk_bf16_f32 v135, v31, v255\n"
    "v_lshlrev_b32 v31, 16, v136\n"
    "v_and_b32 v255, s42, v136\n"
    "v_mul_f32 v31, s41, v31\n"
    "v_mul_f32 v255, s41, v255\n"
    "v_cvt_pk_bf16_f32 v136, v31, v255\n"
    "v_lshlrev_b32 v31, 16, v137\n"
    "v_and_b32 v255, s42, v137\n"
    "v_mul_f32 v31, s41, v31\n"
    "v_mul_f32 v255, s41, v255\n"
    "v_cvt_pk_bf16_f32 v137, v31, v255\n"
    "v_lshlrev_b32 v31, 16, v138\n"
    "v_and_b32 v255, s42, v138\n"
    "v_mul_f32 v31, s41, v31\n"
    "v_mul_f32 v255, s41, v255\n"
    "v_cvt_pk_bf16_f32 v138, v31, v255\n"
    "v_lshlrev_b32 v31, 16, v139\n"
    "v_and_b32 v255, s42, v139\n"
    "v_mul_f32 v31, s41, v31\n"
    "v_mul_f32 v255, s41, v255\n"
    "v_cvt_pk_bf16_f32 v139, v31, v255\n"
    "v_lshlrev_b32 v31, 16, v140\n"
    "v_and_b32 v255, s42, v140\n"
    "v_mul_f32 v31, s41, v31\n"
    "v_mul_f32 v255, s41, v255\n"
    "v_cvt_pk_bf16_f32 v140, v31, v255\n"
    "v_lshlrev_b32 v31, 16, v141\n"
    "v_and_b32 v255, s42, v141\n"
    "v_mul_f32 v31, s41, v31\n"
    "v_add_u32 v5, s36, v3\n"
    "v_mul_f32 v255, s41, v255\n"
    "v_cvt_pk_bf16_f32 v141, v31, v255\n"
    "ds_read_b64_tr_b16 a[224:225], v5 offset:0\n"
    "ds_read_b64_tr_b16 a[226:227], v5 offset:256\n"
    "v_lshlrev_b32 v31, 16, v142\n"
    "v_and_b32 v255, s42, v142\n"
    "v_mfma_f32_16x16x32_bf16 v[64:67], a[164:167], v[120:123], v[64:67]\n"
    "v_mul_f32 v31, s41, v31\n"
    "v_mul_f32 v255, s41, v255\n"
    "v_cvt_pk_bf16_f32 v142, v31, v255\n"
    "v_lshlrev_b32 v31, 16, v143\n"
    "v_and_b32 v255, s42, v143\n"
    "v_mul_f32 v31, s41, v31\n"
    "v_mul_f32 v255, s41, v255\n"
    "v_cvt_pk_bf16_f32 v143, v31, v255\n"
    "v_mfma_f32_16x16x32_bf16 v[64:67], a[168:171], v[124:127], v[64:67]\n"
    "ds_read_b64_tr_b16 v[180:181], v5 offset:512\n"
    "ds_read_b64_tr_b16 v[182:183], v5 offset:768\n"
    "v_lshlrev_b32 v31, 16, v144\n"
    "v_and_b32 v255, s42, v144\n"
    "v_mul_f32 v31, s41, v31\n"
    "v_mul_f32 v255, s41, v255\n"
    "s_add_u32 m0, s35, 33856\n"
    "s_nop 0\n"
    "buffer_load_dwordx4 v1, s[20:23], s32 offen offset:2048  lds\n"
    "v_mfma_f32_16x16x32_bf16 v[64:67], a[172:175], v[128:131], v[64:67]\n"
    "v_cvt_pk_bf16_f32 v144, v31, v255\n"
    "v_lshlrev_b32 v31, 16, v145\n"
    "v_and_b32 v255, s42, v145\n"
    "v_mul_f32 v31, s41, v31\n"
    "v_mul_f32 v255, s41, v255\n"
    "v_cvt_pk_bf16_f32 v145, v31, v255\n"
    "v_lshlrev_b32 v31, 16, v146\n"
    "v_and_b32 v255, s42, v146\n"
    "v_mfma_f32_16x16x32_bf16 v[12:15], v[16:19], v[32:35], 0\n"
    "ds_read_b64_tr_b16 a[228:229], v5 offset:32\n"
    "ds_read_b64_tr_b16 a[230:231], v5 offset:288\n"
    "v_mul_f32 v31, s41, v31\n"
    "v_mul_f32 v255, s41, v255\n"
    "v_cvt_pk_bf16_f32 v146, v31, v255\n"
    "v_lshlrev_b32 v31, 16, v147\n"
    "v_and_b32 v255, s42, v147\n"
    "v_mul_f32 v31, s41, v31\n"
    "v_mfma_f32_16x16x32_bf16 v[12:15], v[16:19], v[36:39], v[12:15]\n"
    "v_mul_f32 v255, s41, v255\n"
    "v_cvt_pk_bf16_f32 v147, v31, v255\n"
    "v_mfma_f32_16x16x32_bf16 v[68:71], a[176:179], v[116:119], 0\n"
    "ds_read_b64_tr_b16 v[184:185], v5 offset:544\n"
    "ds_read_b64_tr_b16 v[186:187], v5 offset:800\n"
    "v_mfma_f32_16x16x32_bf16 v[68:71], a[180:183], v[120:123], v[68:71]\n"
    "s_add_u32 m0, s35, 33888\n"
    "s_nop 0\n"
    "buffer_load_dwordx4 v1, s[20:23], s32 offen offset:3072  lds\n"
    "v_mfma_f32_16x16x32_bf16 v[68:71], a[184:187], v[124:127], v[68:71]\n"
    "ds_read_b64_tr_b16 a[232:233], v5 offset:64\n"
    "ds_read_b64_tr_b16 a[234:235], v5 offset:320\n"
    "v_mfma_f32_16x16x32_bf16 v[68:71], a[188:191], v[128:131], v[68:71]\n"
    "v_mfma_f32_16x16x32_bf16 v[72:75], a[192:195], v[116:119], 0\n"
    "ds_read_b64_tr_b16 v[188:189], v5 offset:576\n"
    "ds_read_b64_tr_b16 v[190:191], v5 offset:832\n"
    "v_mov_b32 v20, v12\n"
    "v_mfma_f32_16x16x32_bf16 v[72:75], a[196:199], v[120:123], v[72:75]\n"
    "v_mfma_f32_16x16x32_bf16 v[72:75], a[200:203], v[124:127], v[72:75]\n"
    "ds_read_b64_tr_b16 a[236:237], v5 offset:96\n"
    "ds_read_b64_tr_b16 a[238:239], v5 offset:352\n"
    "s_add_u32 m0, s35, 50688\n"
    "s_nop 0\n"
    "buffer_load_dwordx4 v1, s[24:27], s32 offen offset:0  lds\n"
    "v_mfma_f32_16x16x32_bf16 v[72:75], a[204:207], v[128:131], v[72:75]\n"
    "v_mfma_f32_16x16x32_bf16 v[76:79], a[208:211], v[116:119], 0\n"
    "ds_read_b64_tr_b16 v[192:193], v5 offset:608\n"
    "ds_read_b64_tr_b16 v[194:195], v5 offset:864\n"
    "v_max3_f32 v25, v64, v65, v66\n"
    "v_max3_f32 v26, v67, v68, v69\n"
    "v_mfma_f32_16x16x32_bf16 v[76:79], a[212:215], v[120:123], v[76:79]\n"
    "v_mfma_f32_16x16x32_bf16 v[76:79], a[216:219], v[124:127], v[76:79]\n"
    "ds_read_b64_tr_b16 a[240:241], v5 offset:128\n"
    "ds_read_b64_tr_b16 a[242:243], v5 offset:384\n"
    "v_mfma_f32_16x16x32_bf16 v[76:79], a[220:223], v[128:131], v[76:79]\n"
    "v_mfma_f32_16x16x32_bf16 v[48:51], a[160:163], v[132:135], 0\n"
    "v_max3_f32 v25, v25, v70, v71\n"
    "v_max3_f32 v26, v26, v72, v73\n"
    "s_waitcnt vmcnt(5)\n"
    "v_lshlrev_b32 v31, 16, v148\n"
    "v_and_b32 v255, s42, v148\n"
    "v_mul_f32 v31, s41, v31\n"
    "v_mul_f32 v255, s41, v255\n"
    "v_cvt_pk_bf16_f32 v148, v31, v255\n"
    "v_lshlrev_b32 v31, 16, v149\n"
    "v_and_b32 v255, s42, v149\n"
    "v_mul_f32 v31, s41, v31\n"
    "v_mul_f32 v255, s41, v255\n"
    "v_cvt_pk_bf16_f32 v149, v31, v255\n"
    "v_lshlrev_b32 v31, 16, v150\n"
    "v_and_b32 v255, s42, v150\n"
    "v_mul_f32 v31, s41, v31\n"
    "v_mul_f32 v255, s41, v255\n"
    "v_cvt_pk_bf16_f32 v150, v31, v255\n"
    "v_lshlrev_b32 v31, 16, v151\n"
    "v_and_b32 v255, s42, v151\n"
    "v_mul_f32 v31, s41, v31\n"
    "v_mul_f32 v255, s41, v255\n"
    "v_cvt_pk_bf16_f32 v151, v31, v255\n"
    "v_mfma_f32_16x16x32_bf16 v[48:51], a[164:167], v[136:139], v[48:51]\n"
    "ds_read_b64_tr_b16 v[196:197], v5 offset:640\n"
    "ds_read_b64_tr_b16 v[198:199], v5 offset:896\n"
    "v_lshlrev_b32 v31, 16, v152\n"
    "v_and_b32 v255, s42, v152\n"
    "v_mul_f32 v31, s41, v31\n"
    "v_mul_f32 v255, s41, v255\n"
    "v_cvt_pk_bf16_f32 v152, v31, v255\n"
    "v_lshlrev_b32 v31, 16, v153\n"
    "v_mfma_f32_16x16x32_bf16 v[48:51], a[168:171], v[140:143], v[48:51]\n"
    "v_and_b32 v255, s42, v153\n"
    "v_mul_f32 v31, s41, v31\n"
    "v_mul_f32 v255, s41, v255\n"
    "v_cvt_pk_bf16_f32 v153, v31, v255\n"
    "v_lshlrev_b32 v31, 16, v154\n"
    "v_and_b32 v255, s42, v154\n"
    "s_add_u32 m0, s35, 50720\n"
    "s_nop 0\n"
    "buffer_load_dwordx4 v1, s[24:27], s32 offen offset:1024  lds\n"
    "v_mfma_f32_16x16x32_bf16 v[48:51], a[172:175], v[144:147], v[48:51]\n"
    "ds_read_b64_tr_b16 a[244:245], v5 offset:160\n"
    "ds_read_b64_tr_b16 a[246:247], v5 offset:416\n"
    "v_mul_f32 v31, s41, v31\n"
    "v_mul_f32 v255, s41, v255\n"
    "v_cvt_pk_bf16_f32 v154, v31, v255\n"
    "v_lshlrev_b32 v31, 16, v155\n"
    "v_and_b32 v255, s42, v155\n"
    "v_mul_f32 v31, s41, v31\n"
    "v_mfma_f32_16x16x32_bf16 v[52:55], a[176:179], v[132:135], 0\n"
    "v_max3_f32 v25, v25, v74, v75\n"
    "v_max3_f32 v26, v26, v76, v77\n"
    "v_max3_f32 v25, v25, v78, v79\n"
    "v_max_f32 v25, v25, v26\n"
    "v_mov_b32 v26, v25\n"
    "s_nop 1\n"
    "v_permlane16_swap_b32 v25, v26\n"
    "v_max_f32 v25, v25, v26\n"
    "v_mov_b32 v26, v25\n"
    "s_nop 1\n"
    "v_permlane32_swap_b32 v25, v26\n"
    "v_max_f32 v25, v25, v26\n"
    "v_sub_f32 v84, 0, v25\n"
    "v_sub_f32 v85, 0, v25\n"
    "v_sub_f32 v86, 0, v25\n"
    "v_sub_f32 v87, 0, v25\n"
    "v_sub_f32 v27, v64, v25\n"
    "v_sub_f32 v28, v65, v25\n"
    "v_sub_f32 v29, v66, v25\n"
    "v_sub_f32 v30, v67, v25\n"
    "v_exp_f32 v8, v27\n"
    "v_exp_f32 v9, v28\n"
    "v_exp_f32 v10, v29\n"
    "v_exp_f32 v11, v30\n"
    "v_cvt_pk_bf16_f32 v40, v8, v9\n"
    "v_cvt_pk_bf16_f32 v41, v10, v11\n"
    "v_sub_f32 v27, v68, v25\n"
    "v_sub_f32 v28, v69, v25\n"
    "v_sub_f32 v29, v70, v25\n"
    "v_sub_f32 v30, v71, v25\n"
    "v_exp_f32 v240, v27\n"
    "v_exp_f32 v241, v28\n"
    "v_exp_f32 v242, v29\n"
    "v_exp_f32 v243, v30\n"
    "v_cvt_pk_bf16_f32 v42, v240, v241\n"
    "v_cvt_pk_bf16_f32 v43, v242, v243\n"
    "v_sub_f32 v27, v72, v25\n"
    "v_sub_f32 v28, v73, v25\n"
    "v_sub_f32 v29, v74, v25\n"
    "v_sub_f32 v30, v75, v25\n"
    "v_exp_f32 v8, v27\n"
    "v_exp_f32 v9, v28\n"
    "v_exp_f32 v10, v29\n"
    "v_exp_f32 v11, v30\n"
    "v_cvt_pk_bf16_f32 v44, v8, v9\n"
    "v_cvt_pk_bf16_f32 v45, v10, v11\n"
    "v_sub_f32 v27, v76, v25\n"
    "v_sub_f32 v28, v77, v25\n"
    "v_sub_f32 v29, v78, v25\n"
    "v_sub_f32 v30, v79, v25\n"
    "v_exp_f32 v240, v27\n"
    "v_exp_f32 v241, v28\n"
    "v_exp_f32 v242, v29\n"
    "v_mfma_f32_16x16x32_bf16 v[52:55], a[180:183], v[136:139], v[52:55]\n"
    "ds_read_b64_tr_b16 v[200:201], v5 offset:672\n"
    "ds_read_b64_tr_b16 v[202:203], v5 offset:928\n"
    "v_exp_f32 v243, v30\n"
    "v_cvt_pk_bf16_f32 v46, v240, v241\n"
    "v_cvt_pk_bf16_f32 v47, v242, v243\n"
    "v_mfma_f32_16x16x32_bf16 v[52:55], a[184:187], v[140:143], v[52:55]\n"
    "s_add_u32 m0, s35, 50752\n"
    "s_nop 0\n"
    "buffer_load_dwordx4 v1, s[24:27], s32 offen offset:2048  lds\n"
    "v_mul_f32 v255, s41, v255\n"
    "v_cvt_pk_bf16_f32 v155, v31, v255\n"
    "v_lshlrev_b32 v31, 16, v156\n"
    "v_and_b32 v255, s42, v156\n"
    "v_mfma_f32_16x16x32_bf16 v[52:55], a[188:191], v[144:147], v[52:55]\n"
    "ds_read_b64_tr_b16 a[248:249], v5 offset:192\n"
    "ds_read_b64_tr_b16 a[250:251], v5 offset:448\n"
    "v_mul_f32 v31, s41, v31\n"
    "v_mul_f32 v255, s41, v255\n"
    "v_cvt_pk_bf16_f32 v156, v31, v255\n"
    "v_lshlrev_b32 v31, 16, v157\n"
    "v_mfma_f32_16x16x32_bf16 v[12:15], v[16:19], v[40:43], 0\n"
    "v_and_b32 v255, s42, v157\n"
    "v_mul_f32 v31, s41, v31\n"
    "v_mul_f32 v255, s41, v255\n"
    "v_cvt_pk_bf16_f32 v157, v31, v255\n"
    "v_lshlrev_b32 v31, 16, v158\n"
    "v_and_b32 v255, s42, v158\n"
    "v_mfma_f32_16x16x32_bf16 v[12:15], v[16:19], v[44:47], v[12:15]\n"
    "ds_read_b64_tr_b16 v[204:205], v5 offset:704\n"
    "ds_read_b64_tr_b16 v[206:207], v5 offset:960\n"
    "v_mul_f32 v31, s41, v31\n"
    "v_mul_f32 v255, s41, v255\n"
    "v_mfma_f32_16x16x32_bf16 v[56:59], a[192:195], v[132:135], 0\n"
    "v_cvt_pk_bf16_f32 v158, v31, v255\n"
    "v_lshlrev_b32 v31, 16, v159\n"
    "v_and_b32 v255, s42, v159\n"
    "v_mul_f32 v31, s41, v31\n"
    "v_mfma_f32_16x16x32_bf16 v[56:59], a[196:199], v[136:139], v[56:59]\n"
    "ds_read_b64_tr_b16 a[252:253], v5 offset:224\n"
    "ds_read_b64_tr_b16 a[254:255], v5 offset:480\n"
    "v_mul_f32 v255, s41, v255\n"
    "v_cvt_pk_bf16_f32 v159, v31, v255\n"
    "v_mfma_f32_16x16x32_bf16 v[56:59], a[200:203], v[140:143], v[56:59]\n"
    "s_add_u32 m0, s35, 50784\n"
    "s_nop 0\n"
    "buffer_load_dwordx4 v1, s[24:27], s32 offen offset:3072  lds\n"
    "s_add_u32 s32, s32, 16384\n"
    "s_add_u32 m0, s35, 67584\n"
    "s_nop 0\n"
    "buffer_load_dwordx4 v1, s[20:23], s32 offen offset:0  lds\n"
    "v_mfma_f32_16x16x32_bf16 v[56:59], a[204:207], v[144:147], v[56:59]\n"
    "s_add_u32 m0, s35, 67616\n"
    "s_nop 0\n"
    "buffer_load_dwordx4 v1, s[20:23], s32 offen offset:1024  lds\n"
    "s_add_u32 m0, s35, 67648\n"
    "s_nop 0\n"
    "buffer_load_dwordx4 v1, s[20:23], s32 offen offset:2048  lds\n"
    "v_mfma_f32_16x16x32_bf16 v[60:63], a[208:211], v[132:135], 0\n"
    "ds_read_b64_tr_b16 v[208:209], v5 offset:736\n"
    "ds_read_b64_tr_b16 v[210:211], v5 offset:992\n"
    "v_max3_f32 v25, v48, v49, v50\n"
    "v_max3_f32 v26, v51, v52, v53\n"
    "v_mfma_f32_16x16x32_bf16 v[60:63], a[212:215], v[136:139], v[60:63]\n"
    "s_add_u32 m0, s35, 67680\n"
    "s_nop 0\n"
    "buffer_load_dwordx4 v1, s[20:23], s32 offen offset:3072  lds\n"
    "v_lshlrev_b32 v31, 16, v160\n"
    "v_and_b32 v255, s42, v160\n"
    "v_mfma_f32_16x16x32_bf16 v[60:63], a[216:219], v[140:143], v[60:63]\n"
    "s_add_u32 m0, s35, 84480\n"
    "s_nop 0\n"
    "buffer_load_dwordx4 v1, s[24:27], s32 offen offset:0  lds\n"
    "v_mul_f32 v31, s41, v31\n"
    "v_mul_f32 v255, s41, v255\n"
    "v_mfma_f32_16x16x32_bf16 v[60:63], a[220:223], v[144:147], v[60:63]\n"
    "s_waitcnt lgkmcnt(15)\n"
    "v_cvt_pk_bf16_f32 v160, v31, v255\n"
    "v_lshlrev_b32 v31, 16, v161\n"
    "v_and_b32 v255, s42, v161\n"
    "v_mul_f32 v31, s41, v31\n"
    "v_mfma_f32_16x16x32_bf16 a[0:3], a[224:227], v[32:35], 0\n"
    "v_max3_f32 v25, v25, v54, v55\n"
    "v_max3_f32 v26, v26, v56, v57\n"
    "s_add_u32 m0, s35, 84512\n"
    "s_nop 0\n"
    "buffer_load_dwordx4 v1, s[24:27], s32 offen offset:1024  lds\n"
    "v_mfma_f32_16x16x32_bf16 a[0:3], v[180:183], v[36:39], a[0:3]\n"
    "v_mul_f32 v255, s41, v255\n"
    "v_cvt_pk_bf16_f32 v161, v31, v255\n"
    "v_lshlrev_b32 v31, 16, v162\n"
    "v_and_b32 v255, s42, v162\n"
    "v_mfma_f32_16x16x32_bf16 a[4:7], a[228:231], v[32:35], 0\n"
    "s_add_u32 m0, s35, 84544\n"
    "s_nop 0\n"
    "buffer_load_dwordx4 v1, s[24:27], s32 offen offset:2048  lds\n"
    "v_mul_f32 v31, s41, v31\n"
    "v_mul_f32 v255, s41, v255\n"
    "v_mfma_f32_16x16x32_bf16 a[4:7], v[184:187], v[36:39], a[4:7]\n"
    "v_cvt_pk_bf16_f32 v162, v31, v255\n"
    "v_lshlrev_b32 v31, 16, v163\n"
    "v_and_b32 v255, s42, v163\n"
    "v_mul_f32 v31, s41, v31\n"
    "v_mfma_f32_16x16x32_bf16 a[8:11], a[232:235], v[32:35], 0\n"
    "v_max3_f32 v25, v25, v58, v59\n"
    "v_max3_f32 v26, v26, v60, v61\n"
    "v_max3_f32 v25, v25, v62, v63\n"
    "v_max_f32 v25, v25, v26\n"
    "v_mov_b32 v26, v25\n"
    "s_nop 1\n"
    "v_permlane16_swap_b32 v25, v26\n"
    "v_max_f32 v25, v25, v26\n"
    "v_mov_b32 v26, v25\n"
    "s_nop 1\n"
    "v_permlane32_swap_b32 v25, v26\n"
    "v_max_f32 v25, v25, v26\n"
    "v_sub_f32 v88, 0, v25\n"
    "v_sub_f32 v89, 0, v25\n"
    "v_sub_f32 v90, 0, v25\n"
    "v_sub_f32 v91, 0, v25\n"
    "v_sub_f32 v27, v48, v25\n"
    "v_sub_f32 v28, v49, v25\n"
    "v_mfma_f32_16x16x32_bf16 a[8:11], v[188:191], v[36:39], a[8:11]\n"
    "v_sub_f32 v29, v50, v25\n"
    "v_sub_f32 v30, v51, v25\n"
    "v_exp_f32 v8, v27\n"
    "v_mfma_f32_16x16x32_bf16 a[12:15], a[236:239], v[32:35], 0\n"
    "v_exp_f32 v9, v28\n"
    "s_add_u32 m0, s35, 84576\n"
    "s_nop 0\n"
    "buffer_load_dwordx4 v1, s[24:27], s32 offen offset:3072  lds\n"
    "s_add_u32 s32, s32, 16384\n"
    "v_mfma_f32_16x16x32_bf16 a[12:15], v[192:195], v[36:39], a[12:15]\n"
    "s_waitcnt lgkmcnt(0)\n"
    "v_exp_f32 v10, v29\n"
    "v_exp_f32 v11, v30\n"
    "v_mfma_f32_16x16x32_bf16 a[16:19], a[240:243], v[32:35], 0\n"
    "s_add_u32 s34, s36, s35\n"
    "v_mul_f32 v255, s41, v255\n"
    "v_cvt_pk_bf16_f32 v163, v31, v255\n"
    "v_mfma_f32_16x16x32_bf16 a[16:19], v[196:199], v[36:39], a[16:19]\n"
    "v_mov_b32 v21, v12\n"
    "v_mfma_f32_16x16x32_bf16 a[20:23], a[244:247], v[32:35], 0\n"
    "s_add_u32 s36, s36, 33792\n"
    "v_mfma_f32_16x16x32_bf16 a[20:23], v[200:203], v[36:39], a[20:23]\n"
    "v_mfma_f32_16x16x32_bf16 a[24:27], a[248:251], v[32:35], 0\n"
    "s_sub_u32 s50, s36, 101376\n"
    "v_mfma_f32_16x16x32_bf16 a[24:27], v[204:207], v[36:39], a[24:27]\n"
    "v_mfma_f32_16x16x32_bf16 a[28:31], a[252:255], v[32:35], 0\n"
    "s_min_u32 s36, s36, s50\n"
    "v_cvt_pk_bf16_f32 v32, v8, v9\n"
    "v_cvt_pk_bf16_f32 v33, v10, v11\n"
    "v_sub_f32 v27, v52, v25\n"
    "v_sub_f32 v28, v53, v25\n"
    "v_sub_f32 v29, v54, v25\n"
    "v_sub_f32 v30, v55, v25\n"
    "v_exp_f32 v240, v27\n"
    "v_exp_f32 v241, v28\n"
    "v_mfma_f32_16x16x32_bf16 a[28:31], v[208:211], v[36:39], a[28:31]\n"
    "v_exp_f32 v242, v29\n"
    "v_mfma_f32_16x16x32_bf16 v[64:67], a[160:163], v[148:151], 0\n"
    "v_exp_f32 v243, v30\n"
    "v_cvt_pk_bf16_f32 v34, v240, v241\n"
    "v_cvt_pk_bf16_f32 v35, v242, v243\n"
    "v_mfma_f32_16x16x32_bf16 v[64:67], a[164:167], v[152:155], v[64:67]\n"
    "v_add_u32 v4, s36, v2\n"
    "v_sub_f32 v27, v56, v25\n"
    "v_sub_f32 v28, v57, v25\n"
    "v_mfma_f32_16x16x32_bf16 v[64:67], a[168:171], v[156:159], v[64:67]\n"
    "s_waitcnt vmcnt(8)\n"
    "s_barrier\n"
    "v_sub_f32 v29, v58, v25\n"
    "v_sub_f32 v30, v59, v25\n"
    "v_mfma_f32_16x16x32_bf16 v[64:67], a[172:175], v[160:163], v[64:67]\n"
    "v_exp_f32 v8, v27\n"
    "v_exp_f32 v9, v28\n"
    "v_mfma_f32_16x16x32_bf16 v[68:71], a[176:179], v[148:151], 0\n"
    "v_exp_f32 v10, v29\n"
    "v_exp_f32 v11, v30\n"
    "v_mfma_f32_16x16x32_bf16 v[68:71], a[180:183], v[152:155], v[68:71]\n"
    "v_cvt_pk_bf16_f32 v36, v8, v9\n"
    "v_cvt_pk_bf16_f32 v37, v10, v11\n"
    "v_sub_f32 v27, v60, v25\n"
    "v_sub_f32 v28, v61, v25\n"
    "v_mfma_f32_16x16x32_bf16 v[68:71], a[184:187], v[156:159], v[68:71]\n"
    "v_sub_f32 v29, v62, v25\n"
    "v_sub_f32 v30, v63, v25\n"
    "v_exp_f32 v240, v27\n"
    "v_mfma_f32_16x16x32_bf16 v[68:71], a[188:191], v[160:163], v[68:71]\n"
    "v_exp_f32 v241, v28\n"
    "v_mfma_f32_16x16x32_bf16 v[72:75], a[192:195], v[148:151], 0\n"
    "v_exp_f32 v242, v29\n"
    "v_mfma_f32_16x16x32_bf16 v[72:75], a[196:199], v[152:155], v[72:75]\n"
    "v_exp_f32 v243, v30\n"
    "v_mfma_f32_16x16x32_bf16 v[72:75], a[200:203], v[156:159], v[72:75]\n"
    "v_cvt_pk_bf16_f32 v38, v240, v241\n"
    "v_cvt_pk_bf16_f32 v39, v242, v243\n"
    "v_mfma_f32_16x16x32_bf16 v[72:75], a[204:207], v[160:163], v[72:75]\n"
    "ds_read_b128 a[160:163], v4 offset:0\n"
    "ds_read_b128 a[164:167], v4 offset:64\n"
    "v_mfma_f32_16x16x32_bf16 v[12:15], v[16:19], v[32:35], 0\n"
    "v_mfma_f32_16x16x32_bf16 v[12:15], v[16:19], v[36:39], v[12:15]\n"
    "ds_read_b128 a[168:171], v4 offset:128\n"
    "ds_read_b128 a[172:175], v4 offset:192\n"
    "v_mfma_f32_16x16x32_bf16 v[76:79], a[208:211], v[148:151], 0\n"
    "v_max3_f32 v25, v64, v65, v66\n"
    "v_max3_f32 v26, v67, v68, v69\n"
    "v_mfma_f32_16x16x32_bf16 v[76:79], a[212:215], v[152:155], v[76:79]\n"
    "ds_read_b128 a[176:179], v4 offset:256\n"
    "ds_read_b128 a[180:183], v4 offset:320\n"
    "v_mfma_f32_16x16x32_bf16 v[76:79], a[216:219], v[156:159], v[76:79]\n"
    "v_mfma_f32_16x16x32_bf16 v[76:79], a[220:223], v[160:163], v[76:79]\n"
    "ds_read_b128 a[184:187], v4 offset:384\n"
    "ds_read_b128 a[188:191], v4 offset:448\n"
    "v_mfma_f32_16x16x32_bf16 a[32:35], a[224:227], v[40:43], 0\n"
    "v_max3_f32 v25, v25, v70, v71\n"
    "v_max3_f32 v26, v26, v72, v73\n"
    "v_mov_b32 v22, v12\n"
    "v_mfma_f32_16x16x32_bf16 a[32:35], v[180:183], v[44:47], a[32:35]\n"
    "ds_read_b128 a[192:195], v4 offset:512\n"
    "ds_read_b128 a[196:199], v4 offset:576\n"
    "v_mfma_f32_16x16x32_bf16 a[36:39], a[228:231], v[40:43], 0\n"
    "v_mfma_f32_16x16x32_bf16 a[36:39], v[184:187], v[44:47], a[36:39]\n"
    "ds_read_b128 a[200:203], v4 offset:640\n"
    "ds_read_b128 a[204:207], v4 offset:704\n"
    "v_mfma_f32_16x16x32_bf16 a[40:43], a[232:235], v[40:43], 0\n"
    "v_max3_f32 v25, v25, v74, v75\n"
    "v_max3_f32 v26, v26, v76, v77\n"
    "v_max3_f32 v25, v25, v78, v79\n"
    "v_max_f32 v25, v25, v26\n"
    "v_mov_b32 v26, v25\n"
    "v_mfma_f32_16x16x32_bf16 a[40:43], v[188:191], v[44:47], a[40:43]\n"
    "ds_read_b128 a[208:211], v4 offset:768\n"
    "ds_read_b128 a[212:215], v4 offset:832\n"
    "v_mfma_f32_16x16x32_bf16 a[44:47], a[236:239], v[40:43], 0\n"
    "v_permlane16_swap_b32 v25, v26\n"
    "v_mfma_f32_16x16x32_bf16 a[44:47], v[192:195], v[44:47], a[44:47]\n"
    "v_max_f32 v25, v25, v26\n"
    "v_mov_b32 v26, v25\n"
    "v_mfma_f32_16x16x32_bf16 a[48:51], a[240:243], v[40:43], 0\n"
    "ds_read_b128 a[216:219], v4 offset:896\n"
    "ds_read_b128 a[220:223], v4 offset:960\n"
    "v_mfma_f32_16x16x32_bf16 a[48:51], v[196:199], v[44:47], a[48:51]\n"
    "v_permlane32_swap_b32 v25, v26\n"
    "v_mfma_f32_16x16x32_bf16 a[52:55], a[244:247], v[40:43], 0\n"
    "v_max_f32 v25, v25, v26\n"
    "v_mfma_f32_16x16x32_bf16 a[52:55], v[200:203], v[44:47], a[52:55]\n"
    "v_sub_f32 v92, 0, v25\n"
    "v_sub_f32 v93, 0, v25\n"
    "v_mfma_f32_16x16x32_bf16 a[56:59], a[248:251], v[40:43], 0\n"
    "v_sub_f32 v94, 0, v25\n"
    "v_sub_f32 v95, 0, v25\n"
    "v_mfma_f32_16x16x32_bf16 a[56:59], v[204:207], v[44:47], a[56:59]\n"
    "v_sub_f32 v27, v64, v25\n"
    "v_sub_f32 v28, v65, v25\n"
    "v_mfma_f32_16x16x32_bf16 a[60:63], a[252:255], v[40:43], 0\n"
    "v_sub_f32 v29, v66, v25\n"
    "v_sub_f32 v30, v67, v25\n"
    "v_mfma_f32_16x16x32_bf16 a[60:63], v[208:211], v[44:47], a[60:63]\n"
    "s_waitcnt lgkmcnt(8)\n"
    "v_exp_f32 v8, v27\n"
    "v_mfma_f32_16x16x32_bf16 v[48:51], a[160:163], v[100:103], v[80:83]\n"
    "v_exp_f32 v9, v28\n"
    "v_exp_f32 v10, v29\n"
    "v_mfma_f32_16x16x32_bf16 v[48:51], a[164:167], v[104:107], v[48:51]\n"
    "v_exp_f32 v11, v30\n"
    "v_cvt_pk_bf16_f32 v40, v8, v9\n"
    "v_cvt_pk_bf16_f32 v41, v10, v11\n"
    "v_mfma_f32_16x16x32_bf16 v[48:51], a[168:171], v[108:111], v[48:51]\n"
    "v_sub_f32 v27, v68, v25\n"
    "v_sub_f32 v28, v69, v25\n"
    "v_sub_f32 v29, v70, v25\n"
    "v_sub_f32 v30, v71, v25\n"
    "v_mfma_f32_16x16x32_bf16 v[48:51], a[172:175], v[112:115], v[48:51]\n"
    "v_exp_f32 v240, v27\n"
    "v_exp_f32 v241, v28\n"
    "v_mfma_f32_16x16x32_bf16 v[52:55], a[176:179], v[100:103], v[80:83]\n"
    "s_add_u32 m0, s34, 0\n"
    "v_exp_f32 v242, v29\n"
    "v_exp_f32 v243, v30\n"
    "v_mfma_f32_16x16x32_bf16 v[52:55], a[180:183], v[104:107], v[52:55]\n"
    "buffer_load_dwordx4 v1, s[20:23], s32 offen offset:0  lds\n"
    "v_cvt_pk_bf16_f32 v42, v240, v241\n"
    "v_cvt_pk_bf16_f32 v43, v242, v243\n"
    "v_mfma_f32_16x16x32_bf16 v[52:55], a[184:187], v[108:111], v[52:55]\n"
    "v_sub_f32 v27, v72, v25\n"
    "v_sub_f32 v28, v73, v25\n"
    "v_sub_f32 v29, v74, v25\n"
    "v_sub_f32 v30, v75, v25\n"
    "v_mfma_f32_16x16x32_bf16 v[52:55], a[188:191], v[112:115], v[52:55]\n"
    "s_waitcnt lgkmcnt(0)\n"
    "s_add_u32 m0, s34, 32\n"
    "v_exp_f32 v8, v27\n"
    "v_exp_f32 v9, v28\n"
    "v_mfma_f32_16x16x32_bf16 v[56:59], a[192:195], v[100:103], v[80:83]\n"
    "buffer_load_dwordx4 v1, s[20:23], s32 offen offset:1024  lds\n"
    "v_exp_f32 v10, v29\n"
    "v_mfma_f32_16x16x32_bf16 v[56:59], a[196:199], v[104:107], v[56:59]\n"
    "v_exp_f32 v11, v30\n"
    "v_accvgpr_write_b32 a96, 0\n"
    "v_accvgpr_write_b32 a97, 0\n"
    "v_mfma_f32_16x16x32_bf16 v[56:59], a[200:203], v[108:111], v[56:59]\n"
    "v_cvt_pk_bf16_f32 v44, v8, v9\n"
    "v_cvt_pk_bf16_f32 v45, v10, v11\n"
    "v_accvgpr_write_b32 a98, 0\n"
    "v_accvgpr_write_b32 a99, 0\n"
    "v_mfma_f32_16x16x32_bf16 v[56:59], a[204:207], v[112:115], v[56:59]\n"
    "v_sub_f32 v27, v76, v25\n"
    "v_sub_f32 v28, v77, v25\n"
    "v_accvgpr_write_b32 a100, 0\n"
    "v_accvgpr_write_b32 a101, 0\n"
    "v_mfma_f32_16x16x32_bf16 v[60:63], a[208:211], v[100:103], v[80:83]\n"
    "v_sub_f32 v29, v78, v25\n"
    "v_sub_f32 v30, v79, v25\n"
    "v_accvgpr_write_b32 a102, 0\n"
    "v_accvgpr_write_b32 a103, 0\n"
    "v_mfma_f32_16x16x32_bf16 v[60:63], a[212:215], v[104:107], v[60:63]\n"
    "buffer_load_dwordx4 v[212:215], v6, s[16:19], s53 offen offset:0 \n"
    "v_exp_f32 v240, v27\n"
    "v_mfma_f32_16x16x32_bf16 v[60:63], a[216:219], v[108:111], v[60:63]\n"
    "s_add_u32 m0, s34, 64\n"
    "v_accvgpr_write_b32 a104, 0\n"
    "v_accvgpr_write_b32 a105, 0\n"
    "v_exp_f32 v241, v28\n"
    "v_mfma_f32_16x16x32_bf16 v[60:63], a[220:223], v[112:115], v[60:63]\n"
    "buffer_load_dwordx4 v1, s[20:23], s32 offen offset:2048  lds\n"
    "v_accvgpr_write_b32 a106, 0\n"
    "v_accvgpr_write_b32 a107, 0\n"
    "v_mfma_f32_16x16x32_bf16 a[64:67], a[224:227], v[32:35], 0\n"
    "v_exp_f32 v242, v29\n"
    "v_accvgpr_write_b32 a108, 0\n"
    "v_accvgpr_write_b32 a109, 0\n"
    "v_mfma_f32_16x16x32_bf16 a[64:67], v[180:183], v[36:39], a[64:67]\n"
    "v_exp_f32 v243, v30\n"
    "v_accvgpr_write_b32 a110, 0\n"
    "v_accvgpr_write_b32 a111, 0\n"
    "v_mfma_f32_16x16x32_bf16 a[68:71], a[228:231], v[32:35], 0\n"
    "buffer_load_dwordx4 v[216:219], v6, s[16:19], s53 offen offset:64 \n"
    "v_cvt_pk_bf16_f32 v46, v240, v241\n"
    "v_cvt_pk_bf16_f32 v47, v242, v243\n"
    "v_mfma_f32_16x16x32_bf16 a[68:71], v[184:187], v[36:39], a[68:71]\n"
    "v_accvgpr_write_b32 a112, 0\n"
    "v_accvgpr_write_b32 a113, 0\n"
    "v_exp_f32 v8, v48\n"
    "v_mfma_f32_16x16x32_bf16 v[12:15], v[16:19], v[40:43], 0\n"
    "v_accvgpr_write_b32 a114, 0\n"
    "v_accvgpr_write_b32 a115, 0\n"
    "v_exp_f32 v9, v49\n"
    "v_mfma_f32_16x16x32_bf16 v[12:15], v[16:19], v[44:47], v[12:15]\n"
    "v_accvgpr_write_b32 a116, 0\n"
    "v_accvgpr_write_b32 a117, 0\n"
    "v_exp_f32 v10, v50\n"
    "v_mfma_f32_16x16x32_bf16 a[72:75], a[232:235], v[32:35], 0\n"
    "s_add_u32 m0, s34, 96\n"
    "buffer_load_dwordx4 v[220:223], v6, s[16:19], s53 offen offset:128 \n"
    "v_accvgpr_write_b32 a118, 0\n"
    "v_accvgpr_write_b32 a119, 0\n"
    "v_mfma_f32_16x16x32_bf16 a[72:75], v[188:191], v[36:39], a[72:75]\n"
    "buffer_load_dwordx4 v1, s[20:23], s32 offen offset:3072  lds\n"
    "v_mfma_f32_16x16x32_bf16 a[76:79], a[236:239], v[32:35], 0\n"
    "v_exp_f32 v11, v51\n"
    "v_mfma_f32_16x16x32_bf16 a[76:79], v[192:195], v[36:39], a[76:79]\n"
    "v_accvgpr_write_b32 a120, 0\n"
    "v_accvgpr_write_b32 a121, 0\n"
    "v_mfma_f32_16x16x32_bf16 a[80:83], a[240:243], v[32:35], 0\n"
    "v_exp_f32 v240, v52\n"
    "v_mfma_f32_16x16x32_bf16 a[80:83], v[196:199], v[36:39], a[80:83]\n"
    "v_accvgpr_write_b32 a122, 0\n"
    "v_accvgpr_write_b32 a123, 0\n"
    "v_mfma_f32_16x16x32_bf16 a[84:87], a[244:247], v[32:35], 0\n"
    "v_exp_f32 v241, v53\n"
    "v_mfma_f32_16x16x32_bf16 a[84:87], v[200:203], v[36:39], a[84:87]\n"
    "v_accvgpr_write_b32 a124, 0\n"
    "v_accvgpr_write_b32 a125, 0\n"
    "v_mfma_f32_16x16x32_bf16 a[88:91], a[248:251], v[32:35], 0\n"
    "v_exp_f32 v242, v54\n"
    "v_mfma_f32_16x16x32_bf16 a[88:91], v[204:207], v[36:39], a[88:91]\n"
    "v_mov_b32 v23, v12\n"
    "v_mfma_f32_16x16x32_bf16 a[92:95], a[252:255], v[32:35], 0\n"
    "v_accvgpr_write_b32 a126, 0\n"
    "v_accvgpr_write_b32 a127, 0\n"
    "v_mfma_f32_16x16x32_bf16 a[92:95], v[208:211], v[36:39], a[92:95]\n"
    "buffer_load_dwordx4 v[224:227], v6, s[16:19], s53 offen offset:192 \n"
    "s_mov_b32 s37, 3\n"
    "LBG4:\n"
    "v_mfma_f32_16x16x32_bf16 v[64:67], a[160:163], v[116:119], v[84:87]\n"
    "v_exp_f32 v243, v55\n"
    "v_mfma_f32_16x16x32_bf16 v[64:67], a[164:167], v[120:123], v[64:67]\n"
    "v_cvt_pk_bf16_f32 v32, v8, v9\n"
    "v_cvt_pk_bf16_f32 v33, v10, v11\n"
    "v_mfma_f32_16x16x32_bf16 v[64:67], a[168:171], v[124:127], v[64:67]\n"
    "v_exp_f32 v8, v56\n"
    "v_mfma_f32_16x16x32_bf16 v[64:67], a[172:175], v[128:131], v[64:67]\n"
    "v_exp_f32 v9, v57\n"
    "v_mfma_f32_16x16x32_bf16 v[68:71], a[176:179], v[116:119], v[84:87]\n"
    "v_add_u32 v5, s36, v3\n"
    "v_mfma_f32_16x16x32_bf16 v[68:71], a[180:183], v[120:123], v[68:71]\n"
    "v_exp_f32 v10, v58\n"
    "v_mfma_f32_16x16x32_bf16 v[68:71], a[184:187], v[124:127], v[68:71]\n"
    "v_exp_f32 v11, v59\n"
    "v_mfma_f32_16x16x32_bf16 v[68:71], a[188:191], v[128:131], v[68:71]\n"
    "v_cvt_pk_bf16_f32 v34, v240, v241\n"
    "v_cvt_pk_bf16_f32 v35, v242, v243\n"
    "v_mfma_f32_16x16x32_bf16 v[72:75], a[192:195], v[116:119], v[84:87]\n"
    "v_exp_f32 v240, v60\n"
    "v_mfma_f32_16x16x32_bf16 v[72:75], a[196:199], v[120:123], v[72:75]\n"
    "v_exp_f32 v241, v61\n"
    "v_mfma_f32_16x16x32_bf16 v[72:75], a[200:203], v[124:127], v[72:75]\n"
    "v_exp_f32 v242, v62\n"
    "v_mfma_f32_16x16x32_bf16 v[72:75], a[204:207], v[128:131], v[72:75]\n"
    "v_exp_f32 v243, v63\n"
    "v_mfma_f32_16x16x32_bf16 v[76:79], a[208:211], v[116:119], v[84:87]\n"
    "v_cvt_pk_bf16_f32 v36, v8, v9\n"
    "v_cvt_pk_bf16_f32 v37, v10, v11\n"
    "v_mfma_f32_16x16x32_bf16 v[76:79], a[212:215], v[120:123], v[76:79]\n"
    "v_cvt_pk_bf16_f32 v38, v240, v241\n"
    "v_cvt_pk_bf16_f32 v39, v242, v243\n"
    "v_mfma_f32_16x16x32_bf16 v[76:79], a[216:219], v[124:127], v[76:79]\n"
    "v_exp_f32 v8, v64\n"
    "v_mfma_f32_16x16x32_bf16 v[76:79], a[220:223], v[128:131], v[76:79]\n"
    "v_exp_f32 v9, v65\n"
    "v_mfma_f32_16x16x32_bf16 v[12:15], v[16:19], v[32:35], 0\n"
    "v_exp_f32 v10, v66\n"
    "v_mfma_f32_16x16x32_bf16 v[12:15], v[16:19], v[36:39], v[12:15]\n"
    "v_exp_f32 v11, v67\n"
    "v_mfma_f32_16x16x32_bf16 a[96:99], a[224:227], v[40:43], a[96:99]\n"
    "ds_read_b64_tr_b16 a[224:225], v5 offset:0\n"
    "ds_read_b64_tr_b16 a[226:227], v5 offset:256\n"
    "v_mfma_f32_16x16x32_bf16 a[96:99], v[180:183], v[44:47], a[96:99]\n"
    "v_exp_f32 v240, v68\n"
    "v_mfma_f32_16x16x32_bf16 a[100:103], a[228:231], v[40:43], a[100:103]\n"
    "ds_read_b64_tr_b16 v[180:181], v5 offset:512\n"
    "ds_read_b64_tr_b16 v[182:183], v5 offset:768\n"
    "v_mfma_f32_16x16x32_bf16 a[100:103], v[184:187], v[44:47], a[100:103]\n"
    "v_exp_f32 v241, v69\n"
    "v_mfma_f32_16x16x32_bf16 a[104:107], a[232:235], v[40:43], a[104:107]\n"
    "ds_read_b64_tr_b16 a[228:229], v5 offset:32\n"
    "ds_read_b64_tr_b16 a[230:231], v5 offset:288\n"
    "v_mfma_f32_16x16x32_bf16 a[104:107], v[188:191], v[44:47], a[104:107]\n"
    "v_cmp_nge_f32 vcc, s40, v12\n"
    "v_mfma_f32_16x16x32_bf16 a[108:111], a[236:239], v[40:43], a[108:111]\n"
    "ds_read_b64_tr_b16 v[184:185], v5 offset:544\n"
    "ds_read_b64_tr_b16 v[186:187], v5 offset:800\n"
    "v_mfma_f32_16x16x32_bf16 a[108:111], v[192:195], v[44:47], a[108:111]\n"
    "v_exp_f32 v242, v70\n"
    "v_mfma_f32_16x16x32_bf16 a[112:115], a[240:243], v[40:43], a[112:115]\n"
    "ds_read_b64_tr_b16 a[232:233], v5 offset:64\n"
    "ds_read_b64_tr_b16 a[234:235], v5 offset:320\n"
    "v_mfma_f32_16x16x32_bf16 a[112:115], v[196:199], v[44:47], a[112:115]\n"
    "s_cbranch_vccnz LTR_40\n"
    "LRET_40:\n"
    "v_add_f32 v20, v20, v12\n"
    "v_mfma_f32_16x16x32_bf16 a[116:119], a[244:247], v[40:43], a[116:119]\n"
    "ds_read_b64_tr_b16 v[188:189], v5 offset:576\n"
    "ds_read_b64_tr_b16 v[190:191], v5 offset:832\n"
    "v_mfma_f32_16x16x32_bf16 a[116:119], v[200:203], v[44:47], a[116:119]\n"
    "v_exp_f32 v243, v71\n"
    "v_mfma_f32_16x16x32_bf16 a[120:123], a[248:251], v[40:43], a[120:123]\n"
    "ds_read_b64_tr_b16 a[236:237], v5 offset:96\n"
    "ds_read_b64_tr_b16 a[238:239], v5 offset:352\n"
    "v_mfma_f32_16x16x32_bf16 a[120:123], v[204:207], v[44:47], a[120:123]\n"
    "s_add_u32 s53, s53, 4096\n"
    "s_cmp_eq_u32 s53, s60\n"
    "s_cselect_b32 s53, s44, s53\n"
    "v_mfma_f32_16x16x32_bf16 a[124:127], a[252:255], v[40:43], a[124:127]\n"
    "ds_read_b64_tr_b16 v[192:193], v5 offset:608\n"
    "ds_read_b64_tr_b16 v[194:195], v5 offset:864\n"
    "v_mfma_f32_16x16x32_bf16 a[124:127], v[208:211], v[44:47], a[124:127]\n"
    "v_cvt_pk_bf16_f32 v40, v8, v9\n"
    "v_cvt_pk_bf16_f32 v41, v10, v11\n"
    "v_mfma_f32_16x16x32_bf16 v[48:51], a[160:163], v[132:135], v[88:91]\n"
    "s_add_u32 m0, s34, 16896\n"
    "ds_read_b64_tr_b16 a[240:241], v5 offset:128\n"
    "ds_read_b64_tr_b16 a[242:243], v5 offset:384\n"
    "v_mfma_f32_16x16x32_bf16 v[48:51], a[164:167], v[136:139], v[48:51]\n"
    "buffer_load_dwordx4 v1, s[24:27], s32 offen offset:0  lds\n"
    "v_mfma_f32_16x16x32_bf16 v[48:51], a[168:171], v[140:143], v[48:51]\n"
    "ds_read_b64_tr_b16 v[196:197], v5 offset:640\n"
    "ds_read_b64_tr_b16 v[198:199], v5 offset:896\n"
    "v_mfma_f32_16x16x32_bf16 v[48:51], a[172:175], v[144:147], v[48:51]\n"
    "v_exp_f32 v8, v72\n"
    "v_mfma_f32_16x16x32_bf16 v[52:55], a[176:179], v[132:135], v[88:91]\n"
    "ds_read_b64_tr_b16 a[244:245], v5 offset:160\n"
    "ds_read_b64_tr_b16 a[246:247], v5 offset:416\n"
    "v_mfma_f32_16x16x32_bf16 v[52:55], a[180:183], v[136:139], v[52:55]\n"
    "v_exp_f32 v9, v73\n"
    "v_mfma_f32_16x16x32_bf16 v[52:55], a[184:187], v[140:143], v[52:55]\n"
    "s_add_u32 m0, s34, 16928\n"
    "ds_read_b64_tr_b16 v[200:201], v5 offset:672\n"
    "ds_read_b64_tr_b16 v[202:203], v5 offset:928\n"
    "v_mfma_f32_16x16x32_bf16 v[52:55], a[188:191], v[144:147], v[52:55]\n"
    "buffer_load_dwordx4 v1, s[24:27], s32 offen offset:1024  lds\n"
    "v_mfma_f32_16x16x32_bf16 v[56:59], a[192:195], v[132:135], v[88:91]\n"
    "ds_read_b64_tr_b16 a[248:249], v5 offset:192\n"
    "ds_read_b64_tr_b16 a[250:251], v5 offset:448\n"
    "v_mfma_f32_16x16x32_bf16 v[56:59], a[196:199], v[136:139], v[56:59]\n"
    "v_exp_f32 v10, v74\n"
    "v_mfma_f32_16x16x32_bf16 v[56:59], a[200:203], v[140:143], v[56:59]\n"
    "ds_read_b64_tr_b16 v[204:205], v5 offset:704\n"
    "ds_read_b64_tr_b16 v[206:207], v5 offset:960\n"
    "v_mfma_f32_16x16x32_bf16 v[56:59], a[204:207], v[144:147], v[56:59]\n"
    "v_exp_f32 v11, v75\n"
    "v_mfma_f32_16x16x32_bf16 v[60:63], a[208:211], v[132:135], v[88:91]\n"
    "ds_read_b64_tr_b16 a[252:253], v5 offset:224\n"
    "ds_read_b64_tr_b16 a[254:255], v5 offset:480\n"
    "v_mfma_f32_16x16x32_bf16 v[60:63], a[212:215], v[136:139], v[60:63]\n"
    "v_cvt_pk_bf16_f32 v42, v240, v241\n"
    "v_cvt_pk_bf16_f32 v43, v242, v243\n"
    "v_mfma_f32_16x16x32_bf16 v[60:63], a[216:219], v[140:143], v[60:63]\n"
    "s_add_u32 m0, s34, 16960\n"
    "ds_read_b64_tr_b16 v[208:209], v5 offset:736\n"
    "ds_read_b64_tr_b16 v[210:211], v5 offset:992\n"
    "v_mfma_f32_16x16x32_bf16 v[60:63], a[220:223], v[144:147], v[60:63]\n"
    "s_waitcnt lgkmcnt(15)\n"
    "buffer_load_dwordx4 v1, s[24:27], s32 offen offset:2048  lds\n"
    "v_mfma_f32_16x16x32_bf16 a[0:3], a[224:227], v[32:35], a[0:3]\n"
    "v_exp_f32 v240, v76\n"
    "v_mfma_f32_16x16x32_bf16 a[0:3], v[180:183], v[36:39], a[0:3]\n"
    "v_exp_f32 v241, v77\n"
    "v_mfma_f32_16x16x32_bf16 a[4:7], a[228:231], v[32:35], a[4:7]\n"
    "v_exp_f32 v242, v78\n"
    "v_mfma_f32_16x16x32_bf16 a[4:7], v[184:187], v[36:39], a[4:7]\n"
    "s_add_u32 m0, s34, 16992\n"
    "v_exp_f32 v243, v79\n"
    "v_mfma_f32_16x16x32_bf16 a[8:11], a[232:235], v[32:35], a[8:11]\n"
    "buffer_load_dwordx4 v1, s[24:27], s32 offen offset:3072  lds\n"
    "v_mfma_f32_16x16x32_bf16 a[8:11], v[188:191], v[36:39], a[8:11]\n"
    "v_cvt_pk_bf16_f32 v44, v8, v9\n"
    "v_cvt_pk_bf16_f32 v45, v10, v11\n"
    "v_mfma_f32_16x16x32_bf16 a[12:15], a[236:239], v[32:35], a[12:15]\n"
    "v_cvt_pk_bf16_f32 v46, v240, v241\n"
    "v_cvt_pk_bf16_f32 v47, v242, v243\n"
    "v_mfma_f32_16x16x32_bf16 a[12:15], v[192:195], v[36:39], a[12:15]\n"
    "s_waitcnt lgkmcnt(0)\n"
    "v_exp_f32 v8, v48\n"
    "v_mfma_f32_16x16x32_bf16 v[12:15], v[16:19], v[40:43], 0\n"
    "v_exp_f32 v9, v49\n"
    "v_mfma_f32_16x16x32_bf16 v[12:15], v[16:19], v[44:47], v[12:15]\n"
    "v_exp_f32 v10, v50\n"
    "v_mfma_f32_16x16x32_bf16 a[16:19], a[240:243], v[32:35], a[16:19]\n"
    "s_add_u32 s34, s36, s35\n"
    "v_exp_f32 v11, v51\n"
    "v_mfma_f32_16x16x32_bf16 a[16:19], v[196:199], v[36:39], a[16:19]\n"
    "v_exp_f32 v240, v52\n"
    "v_mfma_f32_16x16x32_bf16 a[20:23], a[244:247], v[32:35], a[20:23]\n"
    "s_add_u32 s36, s36, 33792\n"
    "v_exp_f32 v241, v53\n"
    "v_mfma_f32_16x16x32_bf16 a[20:23], v[200:203], v[36:39], a[20:23]\n"
    "v_exp_f32 v242, v54\n"
    "v_mfma_f32_16x16x32_bf16 a[24:27], a[248:251], v[32:35], a[24:27]\n"
    "s_sub_u32 s50, s36, 101376\n"
    "v_cmp_nge_f32 vcc, s40, v12\n"
    "v_mfma_f32_16x16x32_bf16 a[24:27], v[204:207], v[36:39], a[24:27]\n"
    "v_exp_f32 v243, v55\n"
    "v_mfma_f32_16x16x32_bf16 a[28:31], a[252:255], v[32:35], a[28:31]\n"
    "s_min_u32 s36, s36, s50\n"
    "v_cvt_pk_bf16_f32 v32, v8, v9\n"
    "v_cvt_pk_bf16_f32 v33, v10, v11\n"
    "v_mfma_f32_16x16x32_bf16 a[28:31], v[208:211], v[36:39], a[28:31]\n"
    "s_add_u32 s32, s32, 16384\n"
    "s_cbranch_vccnz LTR_41\n"
    "LRET_41:\n"
    "v_add_f32 v21, v21, v12\n"
    "v_mfma_f32_16x16x32_bf16 v[64:67], a[160:163], v[148:151], v[92:95]\n"
    "v_add_u32 v4, s36, v2\n"
    "v_mfma_f32_16x16x32_bf16 v[64:67], a[164:167], v[152:155], v[64:67]\n"
    "s_waitcnt vmcnt(11)\n"
    "s_barrier\n"
    "v_mfma_f32_16x16x32_bf16 v[64:67], a[168:171], v[156:159], v[64:67]\n"
    "v_exp_f32 v8, v56\n"
    "v_mfma_f32_16x16x32_bf16 v[64:67], a[172:175], v[160:163], v[64:67]\n"
    "v_exp_f32 v9, v57\n"
    "v_mfma_f32_16x16x32_bf16 v[68:71], a[176:179], v[148:151], v[92:95]\n"
    "v_exp_f32 v10, v58\n"
    "v_mfma_f32_16x16x32_bf16 v[68:71], a[180:183], v[152:155], v[68:71]\n"
    "ds_read_b128 a[160:163], v4 offset:0\n"
    "ds_read_b128 a[164:167], v4 offset:64\n"
    "v_mfma_f32_16x16x32_bf16 v[68:71], a[184:187], v[156:159], v[68:71]\n"
    "v_exp_f32 v11, v59\n"
    "v_mfma_f32_16x16x32_bf16 v[68:71], a[188:191], v[160:163], v[68:71]\n"
    "v_cvt_pk_bf16_f32 v34, v240, v241\n"
    "v_cvt_pk_bf16_f32 v35, v242, v243\n"
    "v_mfma_f32_16x16x32_bf16 v[72:75], a[192:195], v[148:151], v[92:95]\n"
    "ds_read_b128 a[168:171], v4 offset:128\n"
    "ds_read_b128 a[172:175], v4 offset:192\n"
    "v_mfma_f32_16x16x32_bf16 v[72:75], a[196:199], v[152:155], v[72:75]\n"
    "v_exp_f32 v240, v60\n"
    "v_mfma_f32_16x16x32_bf16 v[72:75], a[200:203], v[156:159], v[72:75]\n"
    "v_exp_f32 v241, v61\n"
    "v_mfma_f32_16x16x32_bf16 v[72:75], a[204:207], v[160:163], v[72:75]\n"
    "ds_read_b128 a[176:179], v4 offset:256\n"
    "ds_read_b128 a[180:183], v4 offset:320\n"
    "v_mfma_f32_16x16x32_bf16 v[76:79], a[208:211], v[148:151], v[92:95]\n"
    "v_exp_f32 v242, v62\n"
    "v_mfma_f32_16x16x32_bf16 v[76:79], a[212:215], v[152:155], v[76:79]\n"
    "v_exp_f32 v243, v63\n"
    "v_mfma_f32_16x16x32_bf16 v[76:79], a[216:219], v[156:159], v[76:79]\n"
    "ds_read_b128 a[184:187], v4 offset:384\n"
    "ds_read_b128 a[188:191], v4 offset:448\n"
    "v_mfma_f32_16x16x32_bf16 v[76:79], a[220:223], v[160:163], v[76:79]\n"
    "v_cvt_pk_bf16_f32 v36, v8, v9\n"
    "v_cvt_pk_bf16_f32 v37, v10, v11\n"
    "v_mfma_f32_16x16x32_bf16 a[32:35], a[224:227], v[40:43], a[32:35]\n"
    "v_cvt_pk_bf16_f32 v38, v240, v241\n"
    "v_cvt_pk_bf16_f32 v39, v242, v243\n"
    "v_mfma_f32_16x16x32_bf16 a[32:35], v[180:183], v[44:47], a[32:35]\n"
    "ds_read_b128 a[192:195], v4 offset:512\n"
    "ds_read_b128 a[196:199], v4 offset:576\n"
    "v_mfma_f32_16x16x32_bf16 v[12:15], v[16:19], v[32:35], 0\n"
    "v_exp_f32 v8, v64\n"
    "v_mfma_f32_16x16x32_bf16 v[12:15], v[16:19], v[36:39], v[12:15]\n"
    "ds_read_b128 a[200:203], v4 offset:640\n"
    "ds_read_b128 a[204:207], v4 offset:704\n"
    "v_mfma_f32_16x16x32_bf16 a[36:39], a[228:231], v[40:43], a[36:39]\n"
    "v_exp_f32 v9, v65\n"
    "v_mfma_f32_16x16x32_bf16 a[36:39], v[184:187], v[44:47], a[36:39]\n"
    "ds_read_b128 a[208:211], v4 offset:768\n"
    "ds_read_b128 a[212:215], v4 offset:832\n"
    "v_mfma_f32_16x16x32_bf16 a[40:43], a[232:235], v[40:43], a[40:43]\n"
    "v_exp_f32 v10, v66\n"
    "v_mfma_f32_16x16x32_bf16 a[40:43], v[188:191], v[44:47], a[40:43]\n"
    "ds_read_b128 a[216:219], v4 offset:896\n"
    "ds_read_b128 a[220:223], v4 offset:960\n"
    "v_mfma_f32_16x16x32_bf16 a[44:47], a[236:239], v[40:43], a[44:47]\n"
    "v_cmp_nge_f32 vcc, s40, v12\n"
    "v_mfma_f32_16x16x32_bf16 a[44:47], v[192:195], v[44:47], a[44:47]\n"
    "v_exp_f32 v11, v67\n"
    "v_mfma_f32_16x16x32_bf16 a[48:51], a[240:243], v[40:43], a[48:51]\n"
    "v_exp_f32 v240, v68\n"
    "v_mfma_f32_16x16x32_bf16 a[48:51], v[196:199], v[44:47], a[48:51]\n"
    "s_cbranch_vccnz LTR_42\n"
    "LRET_42:\n"
    "v_add_f32 v22, v22, v12\n"
    "v_mfma_f32_16x16x32_bf16 a[52:55], a[244:247], v[40:43], a[52:55]\n"
    "v_exp_f32 v241, v69\n"
    "v_mfma_f32_16x16x32_bf16 a[52:55], v[200:203], v[44:47], a[52:55]\n"
    "v_exp_f32 v242, v70\n"
    "v_mfma_f32_16x16x32_bf16 a[56:59], a[248:251], v[40:43], a[56:59]\n"
    "v_exp_f32 v243, v71\n"
    "v_mfma_f32_16x16x32_bf16 a[56:59], v[204:207], v[44:47], a[56:59]\n"
    "s_waitcnt vmcnt(9)\n"
    "v_lshlrev_b32 v31, 16, v212\n"
    "v_and_b32 v255, s42, v212\n"
    "v_mfma_f32_16x16x32_bf16 a[60:63], a[252:255], v[40:43], a[60:63]\n"
    "v_cvt_pk_bf16_f32 v40, v8, v9\n"
    "v_cvt_pk_bf16_f32 v41, v10, v11\n"
    "v_mfma_f32_16x16x32_bf16 a[60:63], v[208:211], v[44:47], a[60:63]\n"
    "s_waitcnt lgkmcnt(8)\n"
    "v_exp_f32 v8, v72\n"
    "v_mfma_f32_16x16x32_bf16 v[48:51], a[160:163], v[100:103], v[80:83]\n"
    "s_add_u32 m0, s34, 0\n"
    "v_exp_f32 v9, v73\n"
    "v_mfma_f32_16x16x32_bf16 v[48:51], a[164:167], v[104:107], v[48:51]\n"
    "buffer_load_dwordx4 v1, s[20:23], s32 offen offset:0  lds\n"
    "v_mfma_f32_16x16x32_bf16 v[48:51], a[168:171], v[108:111], v[48:51]\n"
    "v_exp_f32 v10, v74\n"
    "v_mfma_f32_16x16x32_bf16 v[48:51], a[172:175], v[112:115], v[48:51]\n"
    "v_exp_f32 v11, v75\n"
    "v_mfma_f32_16x16x32_bf16 v[52:55], a[176:179], v[100:103], v[80:83]\n"
    "v_cvt_pk_bf16_f32 v42, v240, v241\n"
    "v_cvt_pk_bf16_f32 v43, v242, v243\n"
    "v_mfma_f32_16x16x32_bf16 v[52:55], a[180:183], v[104:107], v[52:55]\n"
    "v_exp_f32 v240, v76\n"
    "v_mfma_f32_16x16x32_bf16 v[52:55], a[184:187], v[108:111], v[52:55]\n"
    "s_add_u32 m0, s34, 32\n"
    "v_exp_f32 v241, v77\n"
    "v_mfma_f32_16x16x32_bf16 v[52:55], a[188:191], v[112:115], v[52:55]\n"
    "s_waitcnt lgkmcnt(0)\n"
    "buffer_load_dwordx4 v1, s[20:23], s32 offen offset:1024  lds\n"
    "v_mfma_f32_16x16x32_bf16 v[56:59], a[192:195], v[100:103], v[80:83]\n"
    "v_exp_f32 v242, v78\n"
    "v_mfma_f32_16x16x32_bf16 v[56:59], a[196:199], v[104:107], v[56:59]\n"
    "v_exp_f32 v243, v79\n"
    "v_mfma_f32_16x16x32_bf16 v[56:59], a[200:203], v[108:111], v[56:59]\n"
    "v_cvt_pk_bf16_f32 v44, v8, v9\n"
    "v_cvt_pk_bf16_f32 v45, v10, v11\n"
    "v_mfma_f32_16x16x32_bf16 v[56:59], a[204:207], v[112:115], v[56:59]\n"
    "v_cvt_pk_bf16_f32 v46, v240, v241\n"
    "v_cvt_pk_bf16_f32 v47, v242, v243\n"
    "v_mfma_f32_16x16x32_bf16 v[12:15], v[16:19], v[40:43], 0\n"
    "v_exp_f32 v8, v48\n"
    "v_mfma_f32_16x16x32_bf16 v[12:15], v[16:19], v[44:47], v[12:15]\n"
    "v_exp_f32 v9, v49\n"
    "v_mfma_f32_16x16x32_bf16 v[60:63], a[208:211], v[100:103], v[80:83]\n"
    "s_add_u32 m0, s34, 64\n"
    "v_exp_f32 v10, v50\n"
    "v_mfma_f32_16x16x32_bf16 v[60:63], a[212:215], v[104:107], v[60:63]\n"
    "buffer_load_dwordx4 v1, s[20:23], s32 offen offset:2048  lds\n"
    "v_mfma_f32_16x16x32_bf16 v[60:63], a[216:219], v[108:111], v[60:63]\n"
    "v_exp_f32 v11, v51\n"
    "v_mfma_f32_16x16x32_bf16 v[60:63], a[220:223], v[112:115], v[60:63]\n"
    "v_exp_f32 v240, v52\n"
    "v_mfma_f32_16x16x32_bf16 a[64:67], a[224:227], v[32:35], a[64:67]\n"
    "v_cmp_nge_f32 vcc, s40, v12\n"
    "v_mfma_f32_16x16x32_bf16 a[64:67], v[180:183], v[36:39], a[64:67]\n"
    "v_exp_f32 v241, v53\n"
    "v_mfma_f32_16x16x32_bf16 a[68:71], a[228:231], v[32:35], a[68:71]\n"
    "s_add_u32 m0, s34, 96\n"
    "v_exp_f32 v242, v54\n"
    "v_mfma_f32_16x16x32_bf16 a[68:71], v[184:187], v[36:39], a[68:71]\n"
    "buffer_load_dwordx4 v1, s[20:23], s32 offen offset:3072  lds\n"
    "v_mfma_f32_16x16x32_bf16 a[72:75], a[232:235], v[32:35], a[72:75]\n"
    "s_cbranch_vccnz LTR_43\n"
    "LRET_43:\n"
    "v_add_f32 v23, v23, v12\n"
    "v_mfma_f32_16x16x32_bf16 a[72:75], v[188:191], v[36:39], a[72:75]\n"
    "v_exp_f32 v243, v55\n"
    "v_mfma_f32_16x16x32_bf16 a[76:79], a[236:239], v[32:35], a[76:79]\n"
    "v_mul_f32 v31, s41, v31\n"
    "v_mul_f32 v255, s41, v255\n"
    "v_mfma_f32_16x16x32_bf16 a[76:79], v[192:195], v[36:39], a[76:79]\n"
    "v_cvt_pk_bf16_f32 v232, v31, v255\n"
    "v_lshlrev_b32 v31, 16, v213\n"
    "v_mfma_f32_16x16x32_bf16 a[80:83], a[240:243], v[32:35], a[80:83]\n"
    "v_and_b32 v255, s42, v213\n"
    "v_mul_f32 v31, s41, v31\n"
    "v_mfma_f32_16x16x32_bf16 a[80:83], v[196:199], v[36:39], a[80:83]\n"
    "v_mul_f32 v255, s41, v255\n"
    "v_cvt_pk_bf16_f32 v233, v31, v255\n"
    "v_mfma_f32_16x16x32_bf16 a[84:87], a[244:247], v[32:35], a[84:87]\n"
    "v_lshlrev_b32 v31, 16, v214\n"
    "v_and_b32 v255, s42, v214\n"
    "v_mfma_f32_16x16x32_bf16 a[84:87], v[200:203], v[36:39], a[84:87]\n"
    "v_mul_f32 v31, s41, v31\n"
    "v_mul_f32 v255, s41, v255\n"
    "v_mfma_f32_16x16x32_bf16 a[88:91], a[248:251], v[32:35], a[88:91]\n"
    "v_cvt_pk_bf16_f32 v234, v31, v255\n"
    "v_lshlrev_b32 v31, 16, v215\n"
    "v_mfma_f32_16x16x32_bf16 a[88:91], v[204:207], v[36:39], a[88:91]\n"
    "v_and_b32 v255, s42, v215\n"
    "v_mul_f32 v31, s41, v31\n"
    "v_mfma_f32_16x16x32_bf16 a[92:95], a[252:255], v[32:35], a[92:95]\n"
    "v_cvt_pk_bf16_f32 v32, v8, v9\n"
    "v_cvt_pk_bf16_f32 v33, v10, v11\n"
    "v_mfma_f32_16x16x32_bf16 a[92:95], v[208:211], v[36:39], a[92:95]\n"
    "v_exp_f32 v8, v56\n"
    "v_mfma_f32_16x16x32_bf16 v[64:67], a[160:163], v[116:119], v[84:87]\n"
    "v_exp_f32 v9, v57\n"
    "v_mfma_f32_16x16x32_bf16 v[64:67], a[164:167], v[120:123], v[64:67]\n"
    "v_add_u32 v5, s36, v3\n"
    "v_mfma_f32_16x16x32_bf16 v[64:67], a[168:171], v[124:127], v[64:67]\n"
    "v_exp_f32 v10, v58\n"
    "v_mfma_f32_16x16x32_bf16 v[64:67], a[172:175], v[128:131], v[64:67]\n"
    "v_exp_f32 v11, v59\n"
    "v_mfma_f32_16x16x32_bf16 v[68:71], a[176:179], v[116:119], v[84:87]\n"
    "v_cvt_pk_bf16_f32 v34, v240, v241\n"
    "v_cvt_pk_bf16_f32 v35, v242, v243\n"
    "v_mfma_f32_16x16x32_bf16 v[68:71], a[180:183], v[120:123], v[68:71]\n"
    "v_exp_f32 v240, v60\n"
    "v_mfma_f32_16x16x32_bf16 v[68:71], a[184:187], v[124:127], v[68:71]\n"
    "v_exp_f32 v241, v61\n"
    "v_mfma_f32_16x16x32_bf16 v[68:71], a[188:191], v[128:131], v[68:71]\n"
    "v_exp_f32 v242, v62\n"
    "v_mfma_f32_16x16x32_bf16 v[72:75], a[192:195], v[116:119], v[84:87]\n"
    "v_exp_f32 v243, v63\n"
    "v_mfma_f32_16x16x32_bf16 v[72:75], a[196:199], v[120:123], v[72:75]\n"
    "v_cvt_pk_bf16_f32 v36, v8, v9\n"
    "v_cvt_pk_bf16_f32 v37, v10, v11\n"
    "v_mfma_f32_16x16x32_bf16 v[72:75], a[200:203], v[124:127], v[72:75]\n"
    "v_cvt_pk_bf16_f32 v38, v240, v241\n"
    "v_cvt_pk_bf16_f32 v39, v242, v243\n"
    "v_mfma_f32_16x16x32_bf16 v[72:75], a[204:207], v[128:131], v[72:75]\n"
    "v_exp_f32 v8, v64\n"
    "v_mfma_f32_16x16x32_bf16 v[12:15], v[16:19], v[32:35], 0\n"
    "v_exp_f32 v9, v65\n"
    "v_mfma_f32_16x16x32_bf16 v[12:15], v[16:19], v[36:39], v[12:15]\n"
    "v_exp_f32 v10, v66\n"
    "v_mfma_f32_16x16x32_bf16 v[76:79], a[208:211], v[116:119], v[84:87]\n"
    "v_exp_f32 v11, v67\n"
    "v_mfma_f32_16x16x32_bf16 v[76:79], a[212:215], v[120:123], v[76:79]\n"
    "v_exp_f32 v240, v68\n"
    "v_mfma_f32_16x16x32_bf16 v[76:79], a[216:219], v[124:127], v[76:79]\n"
    "v_exp_f32 v241, v69\n"
    "v_mfma_f32_16x16x32_bf16 v[76:79], a[220:223], v[128:131], v[76:79]\n"
    "v_exp_f32 v242, v70\n"
    "v_mfma_f32_16x16x32_bf16 a[96:99], a[224:227], v[40:43], a[96:99]\n"
    "ds_read_b64_tr_b16 a[224:225], v5 offset:0\n"
    "ds_read_b64_tr_b16 a[226:227], v5 offset:256\n"
    "v_mfma_f32_16x16x32_bf16 a[96:99], v[180:183], v[44:47], a[96:99]\n"
    "v_cmp_nge_f32 vcc, s40, v12\n"
    "v_mfma_f32_16x16x32_bf16 a[100:103], a[228:231], v[40:43], a[100:103]\n"
    "ds_read_b64_tr_b16 v[180:181], v5 offset:512\n"
    "ds_read_b64_tr_b16 v[182:183], v5 offset:768\n"
    "v_mfma_f32_16x16x32_bf16 a[100:103], v[184:187], v[44:47], a[100:103]\n"
    "v_exp_f32 v243, v71\n"
    "v_mfma_f32_16x16x32_bf16 a[104:107], a[232:235], v[40:43], a[104:107]\n"
    "ds_read_b64_tr_b16 a[228:229], v5 offset:32\n"
    "ds_read_b64_tr_b16 a[230:231], v5 offset:288\n"
    "v_mfma_f32_16x16x32_bf16 a[104:107], v[188:191], v[44:47], a[104:107]\n"
    "s_cbranch_vccnz LTR_44\n"
    "LRET_44:\n"
    "v_add_f32 v20, v20, v12\n"
    "v_mfma_f32_16x16x32_bf16 a[108:111], a[236:239], v[40:43], a[108:111]\n"
    "ds_read_b64_tr_b16 v[184:185], v5 offset:544\n"
    "ds_read_b64_tr_b16 v[186:187], v5 offset:800\n"
    "v_mfma_f32_16x16x32_bf16 a[108:111], v[192:195], v[44:47], a[108:111]\n"
    "v_mul_f32 v255, s41, v255\n"
    "v_cvt_pk_bf16_f32 v235, v31, v255\n"
    "v_mfma_f32_16x16x32_bf16 a[112:115], a[240:243], v[40:43], a[112:115]\n"
    "ds_read_b64_tr_b16 a[232:233], v5 offset:64\n"
    "ds_read_b64_tr_b16 a[234:235], v5 offset:320\n"
    "v_mfma_f32_16x16x32_bf16 a[112:115], v[196:199], v[44:47], a[112:115]\n"
    "buffer_load_dwordx4 v[212:215], v6, s[16:19], s53 offen offset:0 \n"
    "v_mfma_f32_16x16x32_bf16 a[116:119], a[244:247], v[40:43], a[116:119]\n"
    "ds_read_b64_tr_b16 v[188:189], v5 offset:576\n"
    "ds_read_b64_tr_b16 v[190:191], v5 offset:832\n"
    "v_mfma_f32_16x16x32_bf16 a[116:119], v[200:203], v[44:47], a[116:119]\n"
    "ds_write_b128 v228, v[232:235] offset:0\n"
    "v_mfma_f32_16x16x32_bf16 a[120:123], a[248:251], v[40:43], a[120:123]\n"
    "ds_read_b64_tr_b16 a[236:237], v5 offset:96\n"
    "ds_read_b64_tr_b16 a[238:239], v5 offset:352\n"
    "v_mfma_f32_16x16x32_bf16 a[120:123], v[204:207], v[44:47], a[120:123]\n"
    "s_waitcnt vmcnt(12)\n"
    "v_lshlrev_b32 v31, 16, v216\n"
    "v_and_b32 v255, s42, v216\n"
    "v_mfma_f32_16x16x32_bf16 a[124:127], a[252:255], v[40:43], a[124:127]\n"
    "ds_read_b64_tr_b16 v[192:193], v5 offset:608\n"
    "ds_read_b64_tr_b16 v[194:195], v5 offset:864\n"
    "v_mfma_f32_16x16x32_bf16 a[124:127], v[208:211], v[44:47], a[124:127]\n"
    "v_cvt_pk_bf16_f32 v40, v8, v9\n"
    "v_cvt_pk_bf16_f32 v41, v10, v11\n"
    "v_mfma_f32_16x16x32_bf16 v[48:51], a[160:163], v[132:135], v[88:91]\n"
    "s_add_u32 m0, s34, 16896\n"
    "ds_read_b64_tr_b16 a[240:241], v5 offset:128\n"
    "ds_read_b64_tr_b16 a[242:243], v5 offset:384\n"
    "v_mfma_f32_16x16x32_bf16 v[48:51], a[164:167], v[136:139], v[48:51]\n"
    "buffer_load_dwordx4 v1, s[24:27], s32 offen offset:0  lds\n"
    "v_mfma_f32_16x16x32_bf16 v[48:51], a[168:171], v[140:143], v[48:51]\n"
    "ds_read_b64_tr_b16 v[196:197], v5 offset:640\n"
    "ds_read_b64_tr_b16 v[198:199], v5 offset:896\n"
    "v_mfma_f32_16x16x32_bf16 v[48:51], a[172:175], v[144:147], v[48:51]\n"
    "v_exp_f32 v8, v72\n"
    "v_mfma_f32_16x16x32_bf16 v[52:55], a[176:179], v[132:135], v[88:91]\n"
    "ds_read_b64_tr_b16 a[244:245], v5 offset:160\n"
    "ds_read_b64_tr_b16 a[246:247], v5 offset:416\n"
    "v_mfma_f32_16x16x32_bf16 v[52:55], a[180:183], v[136:139], v[52:55]\n"
    "v_exp_f32 v9, v73\n"
    "v_mfma_f32_16x16x32_bf16 v[52:55], a[184:187], v[140:143], v[52:55]\n"
    "s_add_u32 m0, s34, 16928\n"
    "ds_read_b64_tr_b16 v[200:201], v5 offset:672\n"
    "ds_read_b64_tr_b16 v[202:203], v5 offset:928\n"
    "v_mfma_f32_16x16x32_bf16 v[52:55], a[188:191], v[144:147], v[52:55]\n"
    "buffer_load_dwordx4 v1, s[24:27], s32 offen offset:1024  lds\n"
    "v_mfma_f32_16x16x32_bf16 v[56:59], a[192:195], v[132:135], v[88:91]\n"
    "ds_read_b64_tr_b16 a[248:249], v5 offset:192\n"
    "ds_read_b64_tr_b16 a[250:251], v5 offset:448\n"
    "v_mfma_f32_16x16x32_bf16 v[56:59], a[196:199], v[136:139], v[56:59]\n"
    "v_exp_f32 v10, v74\n"
    "v_mfma_f32_16x16x32_bf16 v[56:59], a[200:203], v[140:143], v[56:59]\n"
    "ds_read_b64_tr_b16 v[204:205], v5 offset:704\n"
    "ds_read_b64_tr_b16 v[206:207], v5 offset:960\n"
    "v_mfma_f32_16x16x32_bf16 v[56:59], a[204:207], v[144:147], v[56:59]\n"
    "v_exp_f32 v11, v75\n"
    "v_mfma_f32_16x16x32_bf16 v[60:63], a[208:211], v[132:135], v[88:91]\n"
    "ds_read_b64_tr_b16 a[252:253], v5 offset:224\n"
    "ds_read_b64_tr_b16 a[254:255], v5 offset:480\n"
    "v_mfma_f32_16x16x32_bf16 v[60:63], a[212:215], v[136:139], v[60:63]\n"
    "v_cvt_pk_bf16_f32 v42, v240, v241\n"
    "v_cvt_pk_bf16_f32 v43, v242, v243\n"
    "v_mfma_f32_16x16x32_bf16 v[60:63], a[216:219], v[140:143], v[60:63]\n"
    "s_add_u32 m0, s34, 16960\n"
    "ds_read_b64_tr_b16 v[208:209], v5 offset:736\n"
    "ds_read_b64_tr_b16 v[210:211], v5 offset:992\n"
    "v_mfma_f32_16x16x32_bf16 v[60:63], a[220:223], v[144:147], v[60:63]\n"
    "s_waitcnt lgkmcnt(15)\n"
    "buffer_load_dwordx4 v1, s[24:27], s32 offen offset:2048  lds\n"
    "v_mfma_f32_16x16x32_bf16 a[0:3], a[224:227], v[32:35], a[0:3]\n"
    "v_exp_f32 v240, v76\n"
    "v_mfma_f32_16x16x32_bf16 a[0:3], v[180:183], v[36:39], a[0:3]\n"
    "v_exp_f32 v241, v77\n"
    "v_mfma_f32_16x16x32_bf16 a[4:7], a[228:231], v[32:35], a[4:7]\n"
    "v_exp_f32 v242, v78\n"
    "v_mfma_f32_16x16x32_bf16 a[4:7], v[184:187], v[36:39], a[4:7]\n"
    "s_add_u32 m0, s34, 16992\n"
    "v_exp_f32 v243, v79\n"
    "v_mfma_f32_16x16x32_bf16 a[8:11], a[232:235], v[32:35], a[8:11]\n"
    "buffer_load_dwordx4 v1, s[24:27], s32 offen offset:3072  lds\n"
    "v_mfma_f32_16x16x32_bf16 a[8:11], v[188:191], v[36:39], a[8:11]\n"
    "v_cvt_pk_bf16_f32 v44, v8, v9\n"
    "v_cvt_pk_bf16_f32 v45, v10, v11\n"
    "v_mfma_f32_16x16x32_bf16 a[12:15], a[236:239], v[32:35], a[12:15]\n"
    "v_cvt_pk_bf16_f32 v46, v240, v241\n"
    "v_cvt_pk_bf16_f32 v47, v242, v243\n"
    "v_mfma_f32_16x16x32_bf16 a[12:15], v[192:195], v[36:39], a[12:15]\n"
    "s_waitcnt lgkmcnt(0)\n"
    "v_exp_f32 v8, v48\n"
    "v_mfma_f32_16x16x32_bf16 v[12:15], v[16:19], v[40:43], 0\n"
    "v_exp_f32 v9, v49\n"
    "v_mfma_f32_16x16x32_bf16 v[12:15], v[16:19], v[44:47], v[12:15]\n"
    "v_exp_f32 v10, v50\n"
    "v_mfma_f32_16x16x32_bf16 a[16:19], a[240:243], v[32:35], a[16:19]\n"
    "s_add_u32 s34, s36, s35\n"
    "v_exp_f32 v11, v51\n"
    "v_mfma_f32_16x16x32_bf16 a[16:19], v[196:199], v[36:39], a[16:19]\n"
    "v_exp_f32 v240, v52\n"
    "v_mfma_f32_16x16x32_bf16 a[20:23], a[244:247], v[32:35], a[20:23]\n"
    "s_add_u32 s36, s36, 33792\n"
    "v_exp_f32 v241, v53\n"
    "v_mfma_f32_16x16x32_bf16 a[20:23], v[200:203], v[36:39], a[20:23]\n"
    "v_exp_f32 v242, v54\n"
    "v_mfma_f32_16x16x32_bf16 a[24:27], a[248:251], v[32:35], a[24:27]\n"
    "s_sub_u32 s50, s36, 101376\n"
    "v_cmp_nge_f32 vcc, s40, v12\n"
    "v_mfma_f32_16x16x32_bf16 a[24:27], v[204:207], v[36:39], a[24:27]\n"
    "v_exp_f32 v243, v55\n"
    "v_mfma_f32_16x16x32_bf16 a[28:31], a[252:255], v[32:35], a[28:31]\n"
    "s_min_u32 s36, s36, s50\n"
    "v_cvt_pk_bf16_f32 v32, v8, v9\n"
    "v_cvt_pk_bf16_f32 v33, v10, v11\n"
    "v_mfma_f32_16x16x32_bf16 a[28:31], v[208:211], v[36:39], a[28:31]\n"
    "s_add_u32 s32, s32, 16384\n"
    "s_cbranch_vccnz LTR_45\n"
    "LRET_45:\n"
    "v_add_f32 v21, v21, v12\n"
    "v_mfma_f32_16x16x32_bf16 v[64:67], a[160:163], v[148:151], v[92:95]\n"
    "v_add_u32 v4, s36, v2\n"
    "v_mfma_f32_16x16x32_bf16 v[64:67], a[164:167], v[152:155], v[64:67]\n"
    "s_waitcnt vmcnt(9)\n"
    "s_barrier\n"
    "v_mfma_f32_16x16x32_bf16 v[64:67], a[168:171], v[156:159], v[64:67]\n"
    "v_exp_f32 v8, v56\n"
    "v_mfma_f32_16x16x32_bf16 v[64:67], a[172:175], v[160:163], v[64:67]\n"
    "v_exp_f32 v9, v57\n"
    "v_mfma_f32_16x16x32_bf16 v[68:71], a[176:179], v[148:151], v[92:95]\n"
    "v_exp_f32 v10, v58\n"
    "v_mfma_f32_16x16x32_bf16 v[68:71], a[180:183], v[152:155], v[68:71]\n"
    "ds_read_b128 a[160:163], v4 offset:0\n"
    "ds_read_b128 a[164:167], v4 offset:64\n"
    "v_mfma_f32_16x16x32_bf16 v[68:71], a[184:187], v[156:159], v[68:71]\n"
    "v_exp_f32 v11, v59\n"
    "v_mfma_f32_16x16x32_bf16 v[68:71], a[188:191], v[160:163], v[68:71]\n"
    "v_cvt_pk_bf16_f32 v34, v240, v241\n"
    "v_cvt_pk_bf16_f32 v35, v242, v243\n"
    "v_mfma_f32_16x16x32_bf16 v[72:75], a[192:195], v[148:151], v[92:95]\n"
    "ds_read_b128 a[168:171], v4 offset:128\n"
    "ds_read_b128 a[172:175], v4 offset:192\n"
    "v_mfma_f32_16x16x32_bf16 v[72:75], a[196:199], v[152:155], v[72:75]\n"
    "v_exp_f32 v240, v60\n"
    "v_mfma_f32_16x16x32_bf16 v[72:75], a[200:203], v[156:159], v[72:75]\n"
    "v_exp_f32 v241, v61\n"
    "v_mfma_f32_16x16x32_bf16 v[72:75], a[204:207], v[160:163], v[72:75]\n"
    "ds_read_b128 a[176:179], v4 offset:256\n"
    "ds_read_b128 a[180:183], v4 offset:320\n"
    "v_mfma_f32_16x16x32_bf16 v[76:79], a[208:211], v[148:151], v[92:95]\n"
    "v_exp_f32 v242, v62\n"
    "v_mfma_f32_16x16x32_bf16 v[76:79], a[212:215], v[152:155], v[76:79]\n"
    "v_exp_f32 v243, v63\n"
    "v_mfma_f32_16x16x32_bf16 v[76:79], a[216:219], v[156:159], v[76:79]\n"
    "ds_read_b128 a[184:187], v4 offset:384\n"
    "ds_read_b128 a[188:191], v4 offset:448\n"
    "v_mfma_f32_16x16x32_bf16 v[76:79], a[220:223], v[160:163], v[76:79]\n"
    "v_cvt_pk_bf16_f32 v36, v8, v9\n"
    "v_cvt_pk_bf16_f32 v37, v10, v11\n"
    "v_mfma_f32_16x16x32_bf16 a[32:35], a[224:227], v[40:43], a[32:35]\n"
    "v_mul_f32 v31, s41, v31\n"
    "v_mul_f32 v255, s41, v255\n"
    "v_mfma_f32_16x16x32_bf16 a[32:35], v[180:183], v[44:47], a[32:35]\n"
    "v_cvt_pk_bf16_f32 v38, v240, v241\n"
    "v_cvt_pk_bf16_f32 v39, v242, v243\n"
    "v_mfma_f32_16x16x32_bf16 v[12:15], v[16:19], v[32:35], 0\n"
    "ds_read_b128 a[192:195], v4 offset:512\n"
    "ds_read_b128 a[196:199], v4 offset:576\n"
    "v_mfma_f32_16x16x32_bf16 v[12:15], v[16:19], v[36:39], v[12:15]\n"
    "v_cvt_pk_bf16_f32 v236, v31, v255\n"
    "v_lshlrev_b32 v31, 16, v217\n"
    "v_mfma_f32_16x16x32_bf16 a[36:39], a[228:231], v[40:43], a[36:39]\n"
    "v_and_b32 v255, s42, v217\n"
    "v_mul_f32 v31, s41, v31\n"
    "v_mfma_f32_16x16x32_bf16 a[36:39], v[184:187], v[44:47], a[36:39]\n"
    "ds_read_b128 a[200:203], v4 offset:640\n"
    "ds_read_b128 a[204:207], v4 offset:704\n"
    "v_mfma_f32_16x16x32_bf16 a[40:43], a[232:235], v[40:43], a[40:43]\n"
    "v_mul_f32 v255, s41, v255\n"
    "v_cvt_pk_bf16_f32 v237, v31, v255\n"
    "v_mfma_f32_16x16x32_bf16 a[40:43], v[188:191], v[44:47], a[40:43]\n"
    "v_lshlrev_b32 v31, 16, v218\n"
    "v_and_b32 v255, s42, v218\n"
    "v_mfma_f32_16x16x32_bf16 a[44:47], a[236:239], v[40:43], a[44:47]\n"
    "v_cmp_nge_f32 vcc, s40, v12\n"
    "v_mfma_f32_16x16x32_bf16 a[44:47], v[192:195], v[44:47], a[44:47]\n"
    "ds_read_b128 a[208:211], v4 offset:768\n"
    "ds_read_b128 a[212:215], v4 offset:832\n"
    "v_mfma_f32_16x16x32_bf16 a[48:51], a[240:243], v[40:43], a[48:51]\n"
    "v_mul_f32 v31, s41, v31\n"
    "v_mul_f32 v255, s41, v255\n"
    "v_mfma_f32_16x16x32_bf16 a[48:51], v[196:199], v[44:47], a[48:51]\n"
    "v_cvt_pk_bf16_f32 v238, v31, v255\n"
    "v_lshlrev_b32 v31, 16, v219\n"
    "v_mfma_f32_16x16x32_bf16 a[52:55], a[244:247], v[40:43], a[52:55]\n"
    "ds_read_b128 a[216:219], v4 offset:896\n"
    "ds_read_b128 a[220:223], v4 offset:960\n"
    "v_mfma_f32_16x16x32_bf16 a[52:55], v[200:203], v[44:47], a[52:55]\n"
    "v_and_b32 v255, s42, v219\n"
    "v_mul_f32 v31, s41, v31\n"
    "v_mfma_f32_16x16x32_bf16 a[56:59], a[248:251], v[40:43], a[56:59]\n"
    "s_cbranch_vccnz LTR_46\n"
    "LRET_46:\n"
    "v_add_f32 v22, v22, v12\n"
    "v_mfma_f32_16x16x32_bf16 a[56:59], v[204:207], v[44:47], a[56:59]\n"
    "v_mul_f32 v255, s41, v255\n"
    "v_cvt_pk_bf16_f32 v239, v31, v255\n"
    "v_mfma_f32_16x16x32_bf16 a[60:63], a[252:255], v[40:43], a[60:63]\n"
    "buffer_load_dwordx4 v[216:219], v6, s[16:19], s53 offen offset:64 \n"
    "v_mfma_f32_16x16x32_bf16 a[60:63], v[208:211], v[44:47], a[60:63]\n"
    "s_waitcnt lgkmcnt(8)\n"
    "v_exp_f32 v8, v64\n"
    "v_mfma_f32_16x16x32_bf16 v[48:51], a[160:163], v[100:103], v[80:83]\n"
    "ds_write_b128 v228, v[236:239] offset:1024\n"
    "s_waitcnt vmcnt(16)\n"
    "v_lshlrev_b32 v31, 16, v220\n"
    "v_and_b32 v255, s42, v220\n"
    "v_mfma_f32_16x16x32_bf16 v[48:51], a[164:167], v[104:107], v[48:51]\n"
    "v_mul_f32 v31, s41, v31\n"
    "v_mul_f32 v255, s41, v255\n"
    "v_cvt_pk_bf16_f32 v232, v31, v255\n"
    "v_lshlrev_b32 v31, 16, v221\n"
    "v_mfma_f32_16x16x32_bf16 v[48:51], a[168:171], v[108:111], v[48:51]\n"
    "v_exp_f32 v9, v65\n"
    "v_and_b32 v255, s42, v221\n"
    "v_mul_f32 v31, s41, v31\n"
    "v_mfma_f32_16x16x32_bf16 v[48:51], a[172:175], v[112:115], v[48:51]\n"
    "v_exp_f32 v10, v66\n"
    "v_mul_f32 v255, s41, v255\n"
    "v_cvt_pk_bf16_f32 v233, v31, v255\n"
    "v_mfma_f32_16x16x32_bf16 v[52:55], a[176:179], v[100:103], v[80:83]\n"
    "v_exp_f32 v11, v67\n"
    "v_lshlrev_b32 v31, 16, v222\n"
    "v_and_b32 v255, s42, v222\n"
    "v_mfma_f32_16x16x32_bf16 v[52:55], a[180:183], v[104:107], v[52:55]\n"
    "s_add_u32 m0, s34, 0\n"
    "v_exp_f32 v240, v68\n"
    "v_mul_f32 v31, s41, v31\n"
    "v_mul_f32 v255, s41, v255\n"
    "v_mfma_f32_16x16x32_bf16 v[52:55], a[184:187], v[108:111], v[52:55]\n"
    "buffer_load_dwordx4 v1, s[20:23], s32 offen offset:0  lds\n"
    "v_exp_f32 v241, v69\n"
    "v_mfma_f32_16x16x32_bf16 v[52:55], a[188:191], v[112:115], v[52:55]\n"
    "s_waitcnt lgkmcnt(1)\n"
    "v_cvt_pk_bf16_f32 v234, v31, v255\n"
    "v_lshlrev_b32 v31, 16, v223\n"
    "v_exp_f32 v242, v70\n"
    "v_mfma_f32_16x16x32_bf16 v[56:59], a[192:195], v[100:103], v[80:83]\n"
    "v_and_b32 v255, s42, v223\n"
    "v_mul_f32 v31, s41, v31\n"
    "v_exp_f32 v243, v71\n"
    "v_mfma_f32_16x16x32_bf16 v[56:59], a[196:199], v[104:107], v[56:59]\n"
    "v_mul_f32 v255, s41, v255\n"
    "v_cvt_pk_bf16_f32 v235, v31, v255\n"
    "v_cvt_pk_bf16_f32 v40, v8, v9\n"
    "v_cvt_pk_bf16_f32 v41, v10, v11\n"
    "v_mfma_f32_16x16x32_bf16 v[56:59], a[200:203], v[108:111], v[56:59]\n"
    "s_add_u32 m0, s34, 32\n"
    "buffer_load_dwordx4 v[220:223], v6, s[16:19], s53 offen offset:128 \n"
    "v_exp_f32 v8, v72\n"
    "v_mfma_f32_16x16x32_bf16 v[56:59], a[204:207], v[112:115], v[56:59]\n"
    "buffer_load_dwordx4 v1, s[20:23], s32 offen offset:1024  lds\n"
    "v_exp_f32 v9, v73\n"
    "v_mfma_f32_16x16x32_bf16 v[60:63], a[208:211], v[100:103], v[80:83]\n"
    "ds_write_b128 v228, v[232:235] offset:2048\n"
    "v_exp_f32 v10, v74\n"
    "v_mfma_f32_16x16x32_bf16 v[60:63], a[212:215], v[104:107], v[60:63]\n"
    "s_waitcnt vmcnt(17)\n"
    "v_lshlrev_b32 v31, 16, v224\n"
    "v_and_b32 v255, s42, v224\n"
    "v_exp_f32 v11, v75\n"
    "v_mfma_f32_16x16x32_bf16 v[60:63], a[216:219], v[108:111], v[60:63]\n"
    "v_mul_f32 v31, s41, v31\n"
    "v_mul_f32 v255, s41, v255\n"
    "v_cvt_pk_bf16_f32 v42, v240, v241\n"
    "v_cvt_pk_bf16_f32 v43, v242, v243\n"
    "v_mfma_f32_16x16x32_bf16 v[60:63], a[220:223], v[112:115], v[60:63]\n"
    "v_cvt_pk_bf16_f32 v236, v31, v255\n"
    "v_lshlrev_b32 v31, 16, v225\n"
    "v_exp_f32 v240, v76\n"
    "v_mfma_f32_16x16x32_bf16 a[64:67], a[224:227], v[32:35], a[64:67]\n"
    "v_and_b32 v255, s42, v225\n"
    "v_mul_f32 v31, s41, v31\n"
    "v_exp_f32 v241, v77\n"
    "v_mfma_f32_16x16x32_bf16 a[64:67], v[180:183], v[36:39], a[64:67]\n"
    "s_add_u32 m0, s34, 64\n"
    "v_mul_f32 v255, s41, v255\n"
    "v_cvt_pk_bf16_f32 v237, v31, v255\n"
    "v_exp_f32 v242, v78\n"
    "v_mfma_f32_16x16x32_bf16 a[68:71], a[228:231], v[32:35], a[68:71]\n"
    "buffer_load_dwordx4 v1, s[20:23], s32 offen offset:2048  lds\n"
    "v_lshlrev_b32 v31, 16, v226\n"
    "v_and_b32 v255, s42, v226\n"
    "v_mfma_f32_16x16x32_bf16 a[68:71], v[184:187], v[36:39], a[68:71]\n"
    "v_exp_f32 v243, v79\n"
    "v_mul_f32 v31, s41, v31\n"
    "v_mul_f32 v255, s41, v255\n"
    "v_mfma_f32_16x16x32_bf16 a[72:75], a[232:235], v[32:35], a[72:75]\n"
    "v_cvt_pk_bf16_f32 v44, v8, v9\n"
    "v_cvt_pk_bf16_f32 v45, v10, v11\n"
    "v_cvt_pk_bf16_f32 v238, v31, v255\n"
    "v_lshlrev_b32 v31, 16, v227\n"
    "v_mfma_f32_16x16x32_bf16 a[72:75], v[188:191], v[36:39], a[72:75]\n"
    "v_cvt_pk_bf16_f32 v46, v240, v241\n"
    "v_cvt_pk_bf16_f32 v47, v242, v243\n"
    "v_and_b32 v255, s42, v227\n"
    "v_mul_f32 v31, s41, v31\n"
    "v_mfma_f32_16x16x32_bf16 v[12:15], v[16:19], v[40:43], 0\n"
    "v_exp_f32 v8, v48\n"
    "v_mul_f32 v255, s41, v255\n"
    "v_cvt_pk_bf16_f32 v239, v31, v255\n"
    "v_mfma_f32_16x16x32_bf16 v[12:15], v[16:19], v[44:47], v[12:15]\n"
    "buffer_load_dwordx4 v[224:227], v6, s[16:19], s53 offen offset:192 \n"
    "v_exp_f32 v9, v49\n"
    "v_mfma_f32_16x16x32_bf16 a[76:79], a[236:239], v[32:35], a[76:79]\n"
    "s_add_u32 m0, s34, 96\n"
    "v_exp_f32 v10, v50\n"
    "v_mfma_f32_16x16x32_bf16 a[76:79], v[192:195], v[36:39], a[76:79]\n"
    "buffer_load_dwordx4 v1, s[20:23], s32 offen offset:3072  lds\n"
    "v_mfma_f32_16x16x32_bf16 a[80:83], a[240:243], v[32:35], a[80:83]\n"
    "v_exp_f32 v11, v51\n"
    "v_mfma_f32_16x16x32_bf16 a[80:83], v[196:199], v[36:39], a[80:83]\n"
    "ds_write_b128 v228, v[236:239] offset:3072\n"
    "v_mfma_f32_16x16x32_bf16 a[84:87], a[244:247], v[32:35], a[84:87]\n"
    "v_cmp_nge_f32 vcc, s40, v12\n"
    "v_mfma_f32_16x16x32_bf16 a[84:87], v[200:203], v[36:39], a[84:87]\n"
    "v_exp_f32 v240, v52\n"
    "v_mfma_f32_16x16x32_bf16 a[88:91], a[248:251], v[32:35], a[88:91]\n"
    "v_exp_f32 v241, v53\n"
    "v_mfma_f32_16x16x32_bf16 a[88:91], v[204:207], v[36:39], a[88:91]\n"
    "v_add_u32 v228, 4096, v228\n"
    "v_mfma_f32_16x16x32_bf16 a[92:95], a[252:255], v[32:35], a[92:95]\n"
    "v_exp_f32 v242, v54\n"
    "v_mfma_f32_16x16x32_bf16 a[92:95], v[208:211], v[36:39], a[92:95]\n"
    "s_cbranch_vccnz LTR_47\n"
    "LRET_47:\n"
    "v_add_f32 v23, v23, v12\n"
    "s_sub_u32 s37, s37, 1\n"
    "s_cmp_lg_u32 s37, 0\n"
    "s_cbranch_scc1 LBG4\n"
    "s_mov_b32 s37, 4\n"
    "s_branch LLOOP4\n"
    "LWARM4:\n"
    "s_waitcnt lgkmcnt(8)\n"
    "v_mfma_f32_16x16x32_bf16 v[64:67], a[160:163], v[116:119], 0\n"
    "v_mov_b32 v26, v25\n"
    "s_nop 1\n"
    "v_permlane16_swap_b32 v25, v26\n"
    "v_max_f32 v25, v25, v26\n"
    "v_mov_b32 v26, v25\n"
    "s_nop 1\n"
    "v_permlane32_swap_b32 v25, v26\n"
    "v_max_f32 v25, v25, v26\n"
    "v_sub_f32 v80, 0, v25\n"
    "v_sub_f32 v81, 0, v25\n"
    "v_sub_f32 v82, 0, v25\n"
    "v_sub_f32 v83, 0, v25\n"
    "v_sub_f32 v27, v48, v25\n"
    "v_sub_f32 v28, v49, v25\n"
    "v_sub_f32 v29, v50, v25\n"
    "v_sub_f32 v30, v51, v25\n"
    "v_exp_f32 v8, v27\n"
    "v_exp_f32 v9, v28\n"
    "v_exp_f32 v10, v29\n"
    "v_exp_f32 v11, v30\n"
    "v_cvt_pk_bf16_f32 v32, v8, v9\n"
    "v_cvt_pk_bf16_f32 v33, v10, v11\n"
    "v_sub_f32 v27, v52, v25\n"
    "v_sub_f32 v28, v53, v25\n"
    "v_sub_f32 v29, v54, v25\n"
    "v_sub_f32 v30, v55, v25\n"
    "v_exp_f32 v240, v27\n"
    "v_exp_f32 v241, v28\n"
    "v_exp_f32 v242, v29\n"
    "v_exp_f32 v243, v30\n"
    "v_cvt_pk_bf16_f32 v34, v240, v241\n"
    "v_cvt_pk_bf16_f32 v35, v242, v243\n"
    "v_sub_f32 v27, v56, v25\n"
    "v_sub_f32 v28, v57, v25\n"
    "v_sub_f32 v29, v58, v25\n"
    "v_sub_f32 v30, v59, v25\n"
    "v_exp_f32 v8, v27\n"
    "v_exp_f32 v9, v28\n"
    "v_exp_f32 v10, v29\n"
    "v_exp_f32 v11, v30\n"
    "v_cvt_pk_bf16_f32 v36, v8, v9\n"
    "v_cvt_pk_bf16_f32 v37, v10, v11\n"
    "v_sub_f32 v27, v60, v25\n"
    "v_sub_f32 v28, v61, v25\n"
    "v_sub_f32 v29, v62, v25\n"
    "v_sub_f32 v30, v63, v25\n"
    "v_exp_f32 v240, v27\n"
    "v_exp_f32 v241, v28\n"
    "v_exp_f32 v242, v29\n"
    "v_exp_f32 v243, v30\n"
    "v_cvt_pk_bf16_f32 v38, v240, v241\n"
    "v_cvt_pk_bf16_f32 v39, v242, v243\n"
    "v_mfma_f32_16x16x32_bf16 v[64:67], a[164:167], v[120:123], v[64:67]\n"
    "v_add_u32 v5, s36, v3\n"
    "v_mfma_f32_16x16x32_bf16 v[64:67], a[168:171], v[124:127], v[64:67]\n"
    "v_rcp_f32 v254, v22\n"
    "v_accvgpr_read_b32 v244, a64\n"
    "v_accvgpr_read_b32 v245, a65\n"
    "v_mul_f32 v244, v254, v244\n"
    "v_mul_f32 v245, v254, v245\n"
    "v_cvt_pk_bf16_f32 v246, v244, v245\n"
    "v_accvgpr_read_b32 v244, a66\n"
    "v_accvgpr_read_b32 v245, a67\n"
    "v_mul_f32 v244, v254, v244\n"
    "v_mul_f32 v245, v254, v245\n"
    "v_cvt_pk_bf16_f32 v247, v244, v245\n"
    "v_xor_b32 v31, 0, v231\n"
    "ds_write_b64 v31, v[246:247] offset:8192\n"
    "v_accvgpr_read_b32 v244, a68\n"
    "v_accvgpr_read_b32 v245, a69\n"
    "v_mul_f32 v244, v254, v244\n"
    "v_mul_f32 v245, v254, v245\n"
    "v_cvt_pk_bf16_f32 v248, v244, v245\n"
    "v_accvgpr_read_b32 v244, a70\n"
    "v_accvgpr_read_b32 v245, a71\n"
    "v_mul_f32 v244, v254, v244\n"
    "v_mul_f32 v245, v254, v245\n"
    "v_cvt_pk_bf16_f32 v249, v244, v245\n"
    "v_xor_b32 v255, 32, v231\n"
    "ds_write_b64 v255, v[248:249] offset:8192\n"
    "v_accvgpr_read_b32 v244, a72\n"
    "v_accvgpr_read_b32 v245, a73\n"
    "v_mul_f32 v244, v254, v244\n"
    "v_mul_f32 v245, v254, v245\n"
    "v_cvt_pk_bf16_f32 v250, v244, v245\n"
    "v_accvgpr_read_b32 v244, a74\n"
    "v_accvgpr_read_b32 v245, a75\n"
    "v_mul_f32 v244, v254, v244\n"
    "v_mul_f32 v245, v254, v245\n"
    "v_cvt_pk_bf16_f32 v251, v244, v245\n"
    "v_xor_b32 v31, 64, v231\n"
    "ds_write_b64 v31, v[250:251] offset:8192\n"
    "v_accvgpr_read_b32 v244, a76\n"
    "v_accvgpr_read_b32 v245, a77\n"
    "v_mul_f32 v244, v254, v244\n"
    "v_mfma_f32_16x16x32_bf16 v[64:67], a[172:175], v[128:131], v[64:67]\n"
    "v_mul_f32 v245, v254, v245\n"
    "v_cvt_pk_bf16_f32 v252, v244, v245\n"
    "v_accvgpr_read_b32 v244, a78\n"
    "v_accvgpr_read_b32 v245, a79\n"
    "v_mul_f32 v244, v254, v244\n"
    "v_mul_f32 v245, v254, v245\n"
    "v_cvt_pk_bf16_f32 v253, v244, v245\n"
    "v_xor_b32 v255, 96, v231\n"
    "v_mfma_f32_16x16x32_bf16 v[12:15], v[16:19], v[32:35], 0\n"
    "ds_write_b64 v255, v[252:253] offset:8192\n"
    "v_accvgpr_read_b32 v244, a80\n"
    "v_accvgpr_read_b32 v245, a81\n"
    "v_mul_f32 v244, v254, v244\n"
    "v_mul_f32 v245, v254, v245\n"
    "v_cvt_pk_bf16_f32 v246, v244, v245\n"
    "v_accvgpr_read_b32 v244, a82\n"
    "v_accvgpr_read_b32 v245, a83\n"
    "v_mfma_f32_16x16x32_bf16 v[12:15], v[16:19], v[36:39], v[12:15]\n"
    "v_mul_f32 v244, v254, v244\n"
    "v_mul_f32 v245, v254, v245\n"
    "v_cvt_pk_bf16_f32 v247, v244, v245\n"
    "v_xor_b32 v31, 128, v231\n"
    "ds_write_b64 v31, v[246:247] offset:8192\n"
    "v_accvgpr_read_b32 v244, a84\n"
    "v_accvgpr_read_b32 v245, a85\n"
    "v_mul_f32 v244, v254, v244\n"
    "v_mfma_f32_16x16x32_bf16 v[68:71], a[176:179], v[116:119], 0\n"
    "v_mul_f32 v245, v254, v245\n"
    "v_cvt_pk_bf16_f32 v248, v244, v245\n"
    "v_accvgpr_read_b32 v244, a86\n"
    "v_accvgpr_read_b32 v245, a87\n"
    "v_mul_f32 v244, v254, v244\n"
    "v_mul_f32 v245, v254, v245\n"
    "v_cvt_pk_bf16_f32 v249, v244, v245\n"
    "v_xor_b32 v255, 160, v231\n"
    "v_mfma_f32_16x16x32_bf16 v[68:71], a[180:183], v[120:123], v[68:71]\n"
    "ds_write_b64 v255, v[248:249] offset:8192\n"
    "v_accvgpr_read_b32 v244, a88\n"
    "v_accvgpr_read_b32 v245, a89\n"
    "v_mul_f32 v244, v254, v244\n"
    "v_mul_f32 v245, v254, v245\n"
    "v_cvt_pk_bf16_f32 v250, v244, v245\n"
    "v_accvgpr_read_b32 v244, a90\n"
    "v_accvgpr_read_b32 v245, a91\n"
    "v_mfma_f32_16x16x32_bf16 v[68:71], a[184:187], v[124:127], v[68:71]\n"
    "v_mul_f32 v244, v254, v244\n"
    "v_mul_f32 v245, v254, v245\n"
    "v_cvt_pk_bf16_f32 v251, v244, v245\n"
    "v_xor_b32 v31, 192, v231\n"
    "ds_write_b64 v31, v[250:251] offset:8192\n"
    "v_accvgpr_read_b32 v244, a92\n"
    "v_accvgpr_read_b32 v245, a93\n"
    "v_mul_f32 v244, v254, v244\n"
    "v_mfma_f32_16x16x32_bf16 v[68:71], a[188:191], v[128:131], v[68:71]\n"
    "v_mul_f32 v245, v254, v245\n"
    "v_cvt_pk_bf16_f32 v252, v244, v245\n"
    "v_accvgpr_read_b32 v244, a94\n"
    "v_accvgpr_read_b32 v245, a95\n"
    "v_mul_f32 v244, v254, v244\n"
    "v_mul_f32 v245, v254, v245\n"
    "v_cvt_pk_bf16_f32 v253, v244, v245\n"
    "v_xor_b32 v255, 224, v231\n"
    "v_mfma_f32_16x16x32_bf16 v[72:75], a[192:195], v[116:119], 0\n"
    "ds_write_b64 v255, v[252:253] offset:8192\n"
    "v_mov_b32 v20, v12\n"
    "v_mfma_f32_16x16x32_bf16 v[72:75], a[196:199], v[120:123], v[72:75]\n"
    "v_mfma_f32_16x16x32_bf16 v[72:75], a[200:203], v[124:127], v[72:75]\n"
    "v_mfma_f32_16x16x32_bf16 v[72:75], a[204:207], v[128:131], v[72:75]\n"
    "v_mfma_f32_16x16x32_bf16 v[76:79], a[208:211], v[116:119], 0\n"
    "v_max3_f32 v25, v64, v65, v66\n"
    "v_max3_f32 v26, v67, v68, v69\n"
    "v_mfma_f32_16x16x32_bf16 v[76:79], a[212:215], v[120:123], v[76:79]\n"
    "v_mfma_f32_16x16x32_bf16 v[76:79], a[216:219], v[124:127], v[76:79]\n"
    "v_mfma_f32_16x16x32_bf16 v[76:79], a[220:223], v[128:131], v[76:79]\n"
    "v_mfma_f32_16x16x32_bf16 a[96:99], a[224:227], v[40:43], a[96:99]\n"
    "ds_read_b64_tr_b16 a[224:225], v5 offset:0\n"
    "ds_read_b64_tr_b16 a[226:227], v5 offset:256\n"
    "v_max3_f32 v25, v25, v70, v71\n"
    "v_max3_f32 v26, v26, v72, v73\n"
    "v_mfma_f32_16x16x32_bf16 a[96:99], v[180:183], v[44:47], a[96:99]\n"
    "v_mfma_f32_16x16x32_bf16 a[100:103], a[228:231], v[40:43], a[100:103]\n"
    "ds_read_b64_tr_b16 v[180:181], v5 offset:512\n"
    "ds_read_b64_tr_b16 v[182:183], v5 offset:768\n"
    "v_mfma_f32_16x16x32_bf16 a[100:103], v[184:187], v[44:47], a[100:103]\n"
    "v_mfma_f32_16x16x32_bf16 a[104:107], a[232:235], v[40:43], a[104:107]\n"
    "ds_read_b64_tr_b16 a[228:229], v5 offset:32\n"
    "ds_read_b64_tr_b16 a[230:231], v5 offset:288\n"
    "v_max3_f32 v25, v25, v74, v75\n"
    "v_max3_f32 v26, v26, v76, v77\n"
    "v_max3_f32 v25, v25, v78, v79\n"
    "v_max_f32 v25, v25, v26\n"
    "v_mov_b32 v26, v25\n"
    "s_nop 1\n"
    "v_permlane16_swap_b32 v25, v26\n"
    "v_max_f32 v25, v25, v26\n"
    "v_mov_b32 v26, v25\n"
    "s_nop 1\n"
    "v_permlane32_swap_b32 v25, v26\n"
    "v_max_f32 v25, v25, v26\n"
    "v_sub_f32 v84, 0, v25\n"
    "v_sub_f32 v85, 0, v25\n"
    "v_sub_f32 v86, 0, v25\n"
    "v_sub_f32 v87, 0, v25\n"
    "v_sub_f32 v27, v64, v25\n"
    "v_sub_f32 v28, v65, v25\n"
    "v_sub_f32 v29, v66, v25\n"
    "v_sub_f32 v30, v67, v25\n"
    "v_exp_f32 v8, v27\n"
    "v_exp_f32 v9, v28\n"
    "v_exp_f32 v10, v29\n"
    "v_exp_f32 v11, v30\n"
    "v_mfma_f32_16x16x32_bf16 a[104:107], v[188:191], v[44:47], a[104:107]\n"
    "v_mfma_f32_16x16x32_bf16 a[108:111], a[236:239], v[40:43], a[108:111]\n"
    "ds_read_b64_tr_b16 v[184:185], v5 offset:544\n"
    "ds_read_b64_tr_b16 v[186:187], v5 offset:800\n"
    "v_mfma_f32_16x16x32_bf16 a[108:111], v[192:195], v[44:47], a[108:111]\n"
    "v_mfma_f32_16x16x32_bf16 a[112:115], a[240:243], v[40:43], a[112:115]\n"
    "ds_read_b64_tr_b16 a[232:233], v5 offset:64\n"
    "ds_read_b64_tr_b16 a[234:235], v5 offset:320\n"
    "v_mfma_f32_16x16x32_bf16 a[112:115], v[196:199], v[44:47], a[112:115]\n"
    "v_mfma_f32_16x16x32_bf16 a[116:119], a[244:247], v[40:43], a[116:119]\n"
    "ds_read_b64_tr_b16 v[188:189], v5 offset:576\n"
    "ds_read_b64_tr_b16 v[190:191], v5 offset:832\n"
    "v_mfma_f32_16x16x32_bf16 a[116:119], v[200:203], v[44:47], a[116:119]\n"
    "v_mfma_f32_16x16x32_bf16 a[120:123], a[248:251], v[40:43], a[120:123]\n"
    "ds_read_b64_tr_b16 a[236:237], v5 offset:96\n"
    "ds_read_b64_tr_b16 a[238:239], v5 offset:352\n"
    "v_mfma_f32_16x16x32_bf16 a[120:123], v[204:207], v[44:47], a[120:123]\n"
    "v_mfma_f32_16x16x32_bf16 a[124:127], a[252:255], v[40:43], a[124:127]\n"
    "ds_read_b64_tr_b16 v[192:193], v5 offset:608\n"
    "ds_read_b64_tr_b16 v[194:195], v5 offset:864\n"
    "v_cvt_pk_bf16_f32 v40, v8, v9\n"
    "v_cvt_pk_bf16_f32 v41, v10, v11\n"
    "v_sub_f32 v27, v68, v25\n"
    "v_sub_f32 v28, v69, v25\n"
    "v_sub_f32 v29, v70, v25\n"
    "v_sub_f32 v30, v71, v25\n"
    "v_exp_f32 v240, v27\n"
    "v_exp_f32 v241, v28\n"
    "v_exp_f32 v242, v29\n"
    "v_exp_f32 v243, v30\n"
    "v_cvt_pk_bf16_f32 v42, v240, v241\n"
    "v_cvt_pk_bf16_f32 v43, v242, v243\n"
    "v_sub_f32 v27, v72, v25\n"
    "v_sub_f32 v28, v73, v25\n"
    "v_sub_f32 v29, v74, v25\n"
    "v_sub_f32 v30, v75, v25\n"
    "v_exp_f32 v8, v27\n"
    "v_exp_f32 v9, v28\n"
    "v_exp_f32 v10, v29\n"
    "v_exp_f32 v11, v30\n"
    "v_mfma_f32_16x16x32_bf16 a[124:127], v[208:211], v[44:47], a[124:127]\n"
    "v_cvt_pk_bf16_f32 v44, v8, v9\n"
    "v_cvt_pk_bf16_f32 v45, v10, v11\n"
    "v_sub_f32 v27, v76, v25\n"
    "v_sub_f32 v28, v77, v25\n"
    "v_sub_f32 v29, v78, v25\n"
    "v_sub_f32 v30, v79, v25\n"
    "v_exp_f32 v240, v27\n"
    "v_exp_f32 v241, v28\n"
    "v_exp_f32 v242, v29\n"
    "v_exp_f32 v243, v30\n"
    "v_cvt_pk_bf16_f32 v46, v240, v241\n"
    "v_cvt_pk_bf16_f32 v47, v242, v243\n"
    "v_mfma_f32_16x16x32_bf16 v[12:15], v[16:19], v[40:43], 0\n"
    "ds_read_b64_tr_b16 a[240:241], v5 offset:128\n"
    "ds_read_b64_tr_b16 a[242:243], v5 offset:384\n"
    "v_mfma_f32_16x16x32_bf16 v[12:15], v[16:19], v[44:47], v[12:15]\n"
    "v_mfma_f32_16x16x32_bf16 v[48:51], a[160:163], v[132:135], 0\n"
    "s_add_u32 m0, s34, 16896\n"
    "ds_read_b64_tr_b16 v[196:197], v5 offset:640\n"
    "ds_read_b64_tr_b16 v[198:199], v5 offset:896\n"
    "v_mfma_f32_16x16x32_bf16 v[48:51], a[164:167], v[136:139], v[48:51]\n"
    "buffer_load_dwordx4 v1, s[24:27], s32 offen offset:0  lds\n"
    "v_mfma_f32_16x16x32_bf16 v[48:51], a[168:171], v[140:143], v[48:51]\n"
    "v_rcp_f32 v254, v23\n"
    "s_add_u32 s49, s45, 12288\n"
    "v_accvgpr_read_b32 v244, a96\n"
    "v_accvgpr_read_b32 v245, a97\n"
    "v_mul_f32 v244, v254, v244\n"
    "v_mul_f32 v245, v254, v245\n"
    "v_cvt_pk_bf16_f32 v246, v244, v245\n"
    "v_accvgpr_read_b32 v244, a98\n"
    "v_accvgpr_read_b32 v245, a99\n"
    "v_mul_f32 v244, v254, v244\n"
    "v_mul_f32 v245, v254, v245\n"
    "v_cvt_pk_bf16_f32 v247, v244, v245\n"
    "v_accvgpr_read_b32 v244, a100\n"
    "v_accvgpr_read_b32 v245, a101\n"
    "v_mul_f32 v244, v254, v244\n"
    "v_mul_f32 v245, v254, v245\n"
    "v_cvt_pk_bf16_f32 v248, v244, v245\n"
    "v_accvgpr_read_b32 v244, a102\n"
    "v_accvgpr_read_b32 v245, a103\n"
    "v_mul_f32 v244, v254, v244\n"
    "v_mul_f32 v245, v254, v245\n"
    "v_cvt_pk_bf16_f32 v249, v244, v245\n"
    "v_permlane16_swap_b32 v246, v248\n"
    "s_nop 0\n"
    "v_permlane16_swap_b32 v247, v249\n"
    "v_accvgpr_read_b32 v244, a104\n"
    "v_accvgpr_read_b32 v245, a105\n"
    "v_mul_f32 v244, v254, v244\n"
    "v_mul_f32 v245, v254, v245\n"
    "v_cvt_pk_bf16_f32 v250, v244, v245\n"
    "v_accvgpr_read_b32 v244, a106\n"
    "v_accvgpr_read_b32 v245, a107\n"
    "v_mul_f32 v244, v254, v244\n"
    "v_mul_f32 v245, v254, v245\n"
    "v_cvt_pk_bf16_f32 v251, v244, v245\n"
    "v_accvgpr_read_b32 v244, a108\n"
    "v_accvgpr_read_b32 v245, a109\n"
    "v_mul_f32 v244, v254, v244\n"
    "v_mul_f32 v245, v254, v245\n"
    "v_cvt_pk_bf16_f32 v252, v244, v245\n"
    "v_accvgpr_read_b32 v244, a110\n"
    "v_accvgpr_read_b32 v245, a111\n"
    "v_mul_f32 v244, v254, v244\n"
    "v_mul_f32 v245, v254, v245\n"
    "v_cvt_pk_bf16_f32 v253, v244, v245\n"
    "v_permlane16_swap_b32 v250, v252\n"
    "s_nop 0\n"
    "v_permlane16_swap_b32 v251, v253\n"
    "v_mov_b32 v244, v246\n"
    "v_mov_b32_dpp v246, v250 row_ror:8 row_mask:0xf bank_mask:0xc\n"
    "v_mov_b32 v245, v247\n"
    "v_mov_b32_dpp v247, v251 row_ror:8 row_mask:0xf bank_mask:0xc\n"
    "v_mov_b32_dpp v250, v244 row_ror:8 row_mask:0xf bank_mask:0x3\n"
    "v_mov_b32_dpp v251, v245 row_ror:8 row_mask:0xf bank_mask:0x3\n"
    "v_mov_b32 v244, v248\n"
    "v_mov_b32_dpp v248, v252 row_ror:8 row_mask:0xf bank_mask:0xc\n"
    "v_mov_b32 v245, v249\n"
    "v_mov_b32_dpp v249, v253 row_ror:8 row_mask:0xf bank_mask:0xc\n"
    "v_mov_b32_dpp v252, v244 row_ror:8 row_mask:0xf bank_mask:0x3\n"
    "v_mov_b32_dpp v253, v245 row_ror:8 row_mask:0xf bank_mask:0x3\n"
    "buffer_store_dwordx4 v[246:249], v7, s[28:31], s49 offen offset:0 nt\n"
    "buffer_store_dwordx4 v[250:253], v7, s[28:31], s49 offen offset:2048 nt\n"
    "v_accvgpr_read_b32 v244, a112\n"
    "v_accvgpr_read_b32 v245, a113\n"
    "v_mul_f32 v244, v254, v244\n"
    "v_mul_f32 v245, v254, v245\n"
    "v_cvt_pk_bf16_f32 v246, v244, v245\n"
    "v_accvgpr_read_b32 v244, a114\n"
    "v_mfma_f32_16x16x32_bf16 v[48:51], a[172:175], v[144:147], v[48:51]\n"
    "ds_read_b64_tr_b16 a[244:245], v5 offset:160\n"
    "ds_read_b64_tr_b16 a[246:247], v5 offset:416\n"
    "v_accvgpr_read_b32 v245, a115\n"
    "v_mul_f32 v244, v254, v244\n"
    "v_mul_f32 v245, v254, v245\n"
    "v_cvt_pk_bf16_f32 v247, v244, v245\n"
    "v_mfma_f32_16x16x32_bf16 v[52:55], a[176:179], v[132:135], 0\n"
    "v_accvgpr_read_b32 v244, a116\n"
    "v_accvgpr_read_b32 v245, a117\n"
    "v_mul_f32 v244, v254, v244\n"
    "v_mul_f32 v245, v254, v245\n"
    "v_cvt_pk_bf16_f32 v248, v244, v245\n"
    "v_accvgpr_read_b32 v244, a118\n"
    "v_mfma_f32_16x16x32_bf16 v[52:55], a[180:183], v[136:139], v[52:55]\n"
    "ds_read_b64_tr_b16 v[200:201], v5 offset:672\n"
    "ds_read_b64_tr_b16 v[202:203], v5 offset:928\n"
    "v_accvgpr_read_b32 v245, a119\n"
    "v_mul_f32 v244, v254, v244\n"
    "v_mul_f32 v245, v254, v245\n"
    "v_cvt_pk_bf16_f32 v249, v244, v245\n"
    "v_mfma_f32_16x16x32_bf16 v[52:55], a[184:187], v[140:143], v[52:55]\n"
    "v_permlane16_swap_b32 v246, v248\n"
    "v_permlane16_swap_b32 v247, v249\n"
    "v_accvgpr_read_b32 v244, a120\n"
    "v_accvgpr_read_b32 v245, a121\n"
    "v_mfma_f32_16x16x32_bf16 v[52:55], a[188:191], v[144:147], v[52:55]\n"
    "s_add_u32 m0, s34, 16928\n"
    "ds_read_b64_tr_b16 a[248:249], v5 offset:192\n"
    "ds_read_b64_tr_b16 a[250:251], v5 offset:448\n"
    "v_mul_f32 v244, v254, v244\n"
    "v_mul_f32 v245, v254, v245\n"
    "v_mfma_f32_16x16x32_bf16 v[56:59], a[192:195], v[132:135], 0\n"
    "buffer_load_dwordx4 v1, s[24:27], s32 offen offset:1024  lds\n"
    "v_cvt_pk_bf16_f32 v250, v244, v245\n"
    "v_accvgpr_read_b32 v244, a122\n"
    "v_mfma_f32_16x16x32_bf16 v[56:59], a[196:199], v[136:139], v[56:59]\n"
    "ds_read_b64_tr_b16 v[204:205], v5 offset:704\n"
    "ds_read_b64_tr_b16 v[206:207], v5 offset:960\n"
    "v_accvgpr_read_b32 v245, a123\n"
    "v_mul_f32 v244, v254, v244\n"
    "v_mfma_f32_16x16x32_bf16 v[56:59], a[200:203], v[140:143], v[56:59]\n"
    "v_mul_f32 v245, v254, v245\n"
    "v_cvt_pk_bf16_f32 v251, v244, v245\n"
    "v_accvgpr_read_b32 v244, a124\n"
    "v_accvgpr_read_b32 v245, a125\n"
    "v_mfma_f32_16x16x32_bf16 v[56:59], a[204:207], v[144:147], v[56:59]\n"
    "s_add_u32 m0, s34, 16960\n"
    "ds_read_b64_tr_b16 a[252:253], v5 offset:224\n"
    "ds_read_b64_tr_b16 a[254:255], v5 offset:480\n"
    "v_mul_f32 v244, v254, v244\n"
    "v_mul_f32 v245, v254, v245\n"
    "v_mfma_f32_16x16x32_bf16 v[60:63], a[208:211], v[132:135], 0\n"
    "buffer_load_dwordx4 v1, s[24:27], s32 offen offset:2048  lds\n"
    "v_max3_f32 v25, v48, v49, v50\n"
    "v_max3_f32 v26, v51, v52, v53\n"
    "v_mfma_f32_16x16x32_bf16 v[60:63], a[212:215], v[136:139], v[60:63]\n"
    "ds_read_b64_tr_b16 v[208:209], v5 offset:736\n"
    "ds_read_b64_tr_b16 v[210:211], v5 offset:992\n"
    "v_cvt_pk_bf16_f32 v252, v244, v245\n"
    "v_accvgpr_read_b32 v244, a126\n"
    "v_mfma_f32_16x16x32_bf16 v[60:63], a[216:219], v[140:143], v[60:63]\n"
    "v_accvgpr_read_b32 v245, a127\n"
    "v_mul_f32 v244, v254, v244\n"
    "v_mul_f32 v245, v254, v245\n"
    "v_cvt_pk_bf16_f32 v253, v244, v245\n"
    "v_mfma_f32_16x16x32_bf16 v[60:63], a[220:223], v[144:147], v[60:63]\n"
    "s_waitcnt lgkmcnt(15)\n"
    "s_add_u32 m0, s34, 16992\n"
    "v_permlane16_swap_b32 v250, v252\n"
    "v_permlane16_swap_b32 v251, v253\n"
    "v_mfma_f32_16x16x32_bf16 a[0:3], a[224:227], v[32:35], 0\n"
    "buffer_load_dwordx4 v1, s[24:27], s32 offen offset:3072  lds\n"
    "v_max3_f32 v25, v25, v54, v55\n"
    "v_max3_f32 v26, v26, v56, v57\n"
    "v_mfma_f32_16x16x32_bf16 a[0:3], v[180:183], v[36:39], a[0:3]\n"
    "v_mov_b32 v244, v246\n"
    "v_mov_b32_dpp v246, v250 row_ror:8 row_mask:0xf bank_mask:0xc\n"
    "v_mov_b32 v245, v247\n"
    "v_mov_b32_dpp v247, v251 row_ror:8 row_mask:0xf bank_mask:0xc\n"
    "v_mfma_f32_16x16x32_bf16 a[4:7], a[228:231], v[32:35], 0\n"
    "v_mov_b32_dpp v250, v244 row_ror:8 row_mask:0xf bank_mask:0x3\n"
    "v_mov_b32_dpp v251, v245 row_ror:8 row_mask:0xf bank_mask:0x3\n"
    "v_mov_b32 v244, v248\n"
    "v_mov_b32_dpp v248, v252 row_ror:8 row_mask:0xf bank_mask:0xc\n"
    "v_mfma_f32_16x16x32_bf16 a[4:7], v[184:187], v[36:39], a[4:7]\n"
    "v_mov_b32 v245, v249\n"
    "v_mov_b32_dpp v249, v253 row_ror:8 row_mask:0xf bank_mask:0xc\n"
    "v_mov_b32_dpp v252, v244 row_ror:8 row_mask:0xf bank_mask:0x3\n"
    "v_mov_b32_dpp v253, v245 row_ror:8 row_mask:0xf bank_mask:0x3\n"
    "v_mfma_f32_16x16x32_bf16 a[8:11], a[232:235], v[32:35], 0\n"
    "v_max3_f32 v25, v25, v58, v59\n"
    "v_max3_f32 v26, v26, v60, v61\n"
    "v_max3_f32 v25, v25, v62, v63\n"
    "v_max_f32 v25, v25, v26\n"
    "v_mov_b32 v26, v25\n"
    "s_nop 1\n"
    "v_permlane16_swap_b32 v25, v26\n"
    "v_max_f32 v25, v25, v26\n"
    "v_mov_b32 v26, v25\n"
    "s_nop 1\n"
    "v_permlane32_swap_b32 v25, v26\n"
    "v_max_f32 v25, v25, v26\n"
    "v_sub_f32 v88, 0, v25\n"
    "v_sub_f32 v89, 0, v25\n"
    "v_sub_f32 v90, 0, v25\n"
    "v_sub_f32 v91, 0, v25\n"
    "v_sub_f32 v27, v48, v25\n"
    "v_sub_f32 v28, v49, v25\n"
    "v_mfma_f32_16x16x32_bf16 a[8:11], v[188:191], v[36:39], a[8:11]\n"
    "v_sub_f32 v29, v50, v25\n"
    "v_sub_f32 v30, v51, v25\n"
    "v_exp_f32 v8, v27\n"
    "v_mfma_f32_16x16x32_bf16 a[12:15], a[236:239], v[32:35], 0\n"
    "v_exp_f32 v9, v28\n"
    "v_exp_f32 v10, v29\n"
    "v_mfma_f32_16x16x32_bf16 a[12:15], v[192:195], v[36:39], a[12:15]\n"
    "s_waitcnt lgkmcnt(0)\n"
    "buffer_store_dwordx4 v[246:249], v7, s[28:31], s49 offen offset:128 nt\n"
    "v_exp_f32 v11, v30\n"
    "v_mfma_f32_16x16x32_bf16 a[16:19], a[240:243], v[32:35], 0\n"
    "s_add_u32 s34, s36, s35\n"
    "v_mov_b32 v21, v12\n"
    "v_mfma_f32_16x16x32_bf16 a[16:19], v[196:199], v[36:39], a[16:19]\n"
    "buffer_store_dwordx4 v[250:253], v7, s[28:31], s49 offen offset:2176 nt\n"
    "v_mfma_f32_16x16x32_bf16 a[20:23], a[244:247], v[32:35], 0\n"
    "s_add_u32 s36, s36, 33792\n"
    "v_mfma_f32_16x16x32_bf16 a[20:23], v[200:203], v[36:39], a[20:23]\n"
    "v_mfma_f32_16x16x32_bf16 a[24:27], a[248:251], v[32:35], 0\n"
    "s_sub_u32 s50, s36, 101376\n"
    "v_mfma_f32_16x16x32_bf16 a[24:27], v[204:207], v[36:39], a[24:27]\n"
    "v_mfma_f32_16x16x32_bf16 a[28:31], a[252:255], v[32:35], 0\n"
    "s_min_u32 s36, s36, s50\n"
    "v_cvt_pk_bf16_f32 v32, v8, v9\n"
    "v_cvt_pk_bf16_f32 v33, v10, v11\n"
    "v_sub_f32 v27, v52, v25\n"
    "v_sub_f32 v28, v53, v25\n"
    "v_sub_f32 v29, v54, v25\n"
    "v_sub_f32 v30, v55, v25\n"
    "v_exp_f32 v240, v27\n"
    "v_exp_f32 v241, v28\n"
    "v_mfma_f32_16x16x32_bf16 a[28:31], v[208:211], v[36:39], a[28:31]\n"
    "s_add_u32 s32, s32, 16384\n"
    "v_exp_f32 v242, v29\n"
    "v_mfma_f32_16x16x32_bf16 v[64:67], a[160:163], v[148:151], 0\n"
    "v_exp_f32 v243, v30\n"
    "v_cvt_pk_bf16_f32 v34, v240, v241\n"
    "v_cvt_pk_bf16_f32 v35, v242, v243\n"
    "v_mfma_f32_16x16x32_bf16 v[64:67], a[164:167], v[152:155], v[64:67]\n"
    "v_add_u32 v4, s36, v2\n"
    "v_sub_f32 v27, v56, v25\n"
    "v_sub_f32 v28, v57, v25\n"
    "v_mfma_f32_16x16x32_bf16 v[64:67], a[168:171], v[156:159], v[64:67]\n"
    "s_waitcnt vmcnt(12)\n"
    "s_barrier\n"
    "v_sub_f32 v29, v58, v25\n"
    "v_sub_f32 v30, v59, v25\n"
    "v_mfma_f32_16x16x32_bf16 v[64:67], a[172:175], v[160:163], v[64:67]\n"
    "v_exp_f32 v8, v27\n"
    "v_exp_f32 v9, v28\n"
    "v_mfma_f32_16x16x32_bf16 v[68:71], a[176:179], v[148:151], 0\n"
    "v_exp_f32 v10, v29\n"
    "v_exp_f32 v11, v30\n"
    "v_mfma_f32_16x16x32_bf16 v[68:71], a[180:183], v[152:155], v[68:71]\n"
    "v_cvt_pk_bf16_f32 v36, v8, v9\n"
    "v_cvt_pk_bf16_f32 v37, v10, v11\n"
    "v_sub_f32 v27, v60, v25\n"
    "v_sub_f32 v28, v61, v25\n"
    "v_mfma_f32_16x16x32_bf16 v[68:71], a[184:187], v[156:159], v[68:71]\n"
    "v_sub_f32 v29, v62, v25\n"
    "v_sub_f32 v30, v63, v25\n"
    "v_exp_f32 v240, v27\n"
    "v_mfma_f32_16x16x32_bf16 v[68:71], a[188:191], v[160:163], v[68:71]\n"
    "v_exp_f32 v241, v28\n"
    "v_exp_f32 v242, v29\n"
    "v_mfma_f32_16x16x32_bf16 v[72:75], a[192:195], v[148:151], 0\n"
    "v_exp_f32 v243, v30\n"
    "v_cvt_pk_bf16_f32 v38, v240, v241\n"
    "v_cvt_pk_bf16_f32 v39, v242, v243\n"
    "v_mfma_f32_16x16x32_bf16 v[72:75], a[196:199], v[152:155], v[72:75]\n"
    "ds_read_b128 a[160:163], v4 offset:0\n"
    "ds_read_b128 a[164:167], v4 offset:64\n"
    "s_mov_b32 s61, s45\n"
    "v_lshrrev_b32 v31, 8, v1\n"
    "v_add_u32 v31, 0, v31\n"
    "v_mfma_f32_16x16x32_bf16 v[72:75], a[200:203], v[156:159], v[72:75]\n"
    "v_lshlrev_b32 v31, 4, v31\n"
    "v_xor_b32 v232, v229, v31\n"
    "v_add_u32 v232, 0, v232\n"
    "v_lshrrev_b32 v31, 8, v1\n"
    "v_mfma_f32_16x16x32_bf16 v[72:75], a[204:207], v[160:163], v[72:75]\n"
    "ds_read_b128 a[168:171], v4 offset:128\n"
    "ds_read_b128 a[172:175], v4 offset:192\n"
    "v_add_u32 v31, 4, v31\n"
    "v_lshlrev_b32 v31, 4, v31\n"
    "v_mfma_f32_16x16x32_bf16 v[12:15], v[16:19], v[32:35], 0\n"
    "v_xor_b32 v233, v229, v31\n"
    "v_add_u32 v233, 1024, v233\n"
    "v_lshrrev_b32 v31, 8, v1\n"
    "v_add_u32 v31, 8, v31\n"
    "v_mfma_f32_16x16x32_bf16 v[12:15], v[16:19], v[36:39], v[12:15]\n"
    "ds_read_b128 a[176:179], v4 offset:256\n"
    "ds_read_b128 a[180:183], v4 offset:320\n"
    "v_lshlrev_b32 v31, 4, v31\n"
    "v_xor_b32 v234, v229, v31\n"
    "v_mfma_f32_16x16x32_bf16 v[76:79], a[208:211], v[148:151], 0\n"
    "v_max3_f32 v25, v64, v65, v66\n"
    "v_max3_f32 v26, v67, v68, v69\n"
    "v_add_u32 v234, 2048, v234\n"
    "v_lshrrev_b32 v31, 8, v1\n"
    "v_mfma_f32_16x16x32_bf16 v[76:79], a[212:215], v[152:155], v[76:79]\n"
    "ds_read_b128 a[184:187], v4 offset:384\n"
    "ds_read_b128 a[188:191], v4 offset:448\n"
    "v_add_u32 v31, 12, v31\n"
    "v_lshlrev_b32 v31, 4, v31\n"
    "v_mfma_f32_16x16x32_bf16 v[76:79], a[216:219], v[156:159], v[76:79]\n"
    "v_xor_b32 v235, v229, v31\n"
    "v_add_u32 v235, 3072, v235\n"
    "v_mfma_f32_16x16x32_bf16 v[76:79], a[220:223], v[160:163], v[76:79]\n"
    "ds_read_b128 a[192:195], v4 offset:512\n"
    "ds_read_b128 a[196:199], v4 offset:576\n"
    "v_mfma_f32_16x16x32_bf16 a[32:35], a[224:227], v[40:43], 0\n"
    "v_max3_f32 v25, v25, v70, v71\n"
    "v_max3_f32 v26, v26, v72, v73\n"
    "v_mov_b32 v22, v12\n"
    "v_mfma_f32_16x16x32_bf16 a[32:35], v[180:183], v[44:47], a[32:35]\n"
    "ds_read_b128 a[200:203], v4 offset:640\n"
    "ds_read_b128 a[204:207], v4 offset:704\n"
    "v_mfma_f32_16x16x32_bf16 a[36:39], a[228:231], v[40:43], 0\n"
    "v_mfma_f32_16x16x32_bf16 a[36:39], v[184:187], v[44:47], a[36:39]\n"
    "ds_read_b128 a[208:211], v4 offset:768\n"
    "ds_read_b128 a[212:215], v4 offset:832\n"
    "v_mfma_f32_16x16x32_bf16 a[40:43], a[232:235], v[40:43], 0\n"
    "v_max3_f32 v25, v25, v74, v75\n"
    "v_max3_f32 v26, v26, v76, v77\n"
    "v_max3_f32 v25, v25, v78, v79\n"
    "v_max_f32 v25, v25, v26\n"
    "v_mov_b32 v26, v25\n"
    "v_mfma_f32_16x16x32_bf16 a[40:43], v[188:191], v[44:47], a[40:43]\n"
    "ds_read_b128 a[216:219], v4 offset:896\n"
    "ds_read_b128 a[220:223], v4 offset:960\n"
    "v_mfma_f32_16x16x32_bf16 a[44:47], a[236:239], v[40:43], 0\n"
    "v_permlane16_swap_b32 v25, v26\n"
    "v_mfma_f32_16x16x32_bf16 a[44:47], v[192:195], v[44:47], a[44:47]\n"
    "v_max_f32 v25, v25, v26\n"
    "v_mov_b32 v26, v25\n"
    "v_mfma_f32_16x16x32_bf16 a[48:51], a[240:243], v[40:43], 0\n"
    "v_mfma_f32_16x16x32_bf16 a[48:51], v[196:199], v[44:47], a[48:51]\n"
    "v_permlane32_swap_b32 v25, v26\n"
    "v_mfma_f32_16x16x32_bf16 a[52:55], a[244:247], v[40:43], 0\n"
    "v_max_f32 v25, v25, v26\n"
    "v_mfma_f32_16x16x32_bf16 a[52:55], v[200:203], v[44:47], a[52:55]\n"
    "v_sub_f32 v92, 0, v25\n"
    "v_sub_f32 v93, 0, v25\n"
    "v_mfma_f32_16x16x32_bf16 a[56:59], a[248:251], v[40:43], 0\n"
    "v_sub_f32 v94, 0, v25\n"
    "v_sub_f32 v95, 0, v25\n"
    "v_mfma_f32_16x16x32_bf16 a[56:59], v[204:207], v[44:47], a[56:59]\n"
    "v_sub_f32 v27, v64, v25\n"
    "v_sub_f32 v28, v65, v25\n"
    "v_mfma_f32_16x16x32_bf16 a[60:63], a[252:255], v[40:43], 0\n"
    "v_sub_f32 v29, v66, v25\n"
    "v_sub_f32 v30, v67, v25\n"
    "v_mfma_f32_16x16x32_bf16 a[60:63], v[208:211], v[44:47], a[60:63]\n"
    "s_waitcnt lgkmcnt(8)\n"
    "v_exp_f32 v8, v27\n"
    "v_mfma_f32_16x16x32_bf16 v[48:51], a[160:163], v[100:103], v[80:83]\n"
    "v_exp_f32 v9, v28\n"
    "v_exp_f32 v10, v29\n"
    "v_mfma_f32_16x16x32_bf16 v[48:51], a[164:167], v[104:107], v[48:51]\n"
    "v_exp_f32 v11, v30\n"
    "v_cvt_pk_bf16_f32 v40, v8, v9\n"
    "v_cvt_pk_bf16_f32 v41, v10, v11\n"
    "v_mfma_f32_16x16x32_bf16 v[48:51], a[168:171], v[108:111], v[48:51]\n"
    "v_sub_f32 v27, v68, v25\n"
    "v_sub_f32 v28, v69, v25\n"
    "v_sub_f32 v29, v70, v25\n"
    "v_sub_f32 v30, v71, v25\n"
    "v_mfma_f32_16x16x32_bf16 v[48:51], a[172:175], v[112:115], v[48:51]\n"
    "v_exp_f32 v240, v27\n"
    "v_exp_f32 v241, v28\n"
    "v_mfma_f32_16x16x32_bf16 v[52:55], a[176:179], v[100:103], v[80:83]\n"
    "s_add_u32 m0, s34, 0\n"
    "v_exp_f32 v242, v29\n"
    "v_exp_f32 v243, v30\n"
    "v_mfma_f32_16x16x32_bf16 v[52:55], a[180:183], v[104:107], v[52:55]\n"
    "buffer_load_dwordx4 v1, s[20:23], s32 offen offset:0  lds\n"
    "v_cvt_pk_bf16_f32 v42, v240, v241\n"
    "v_cvt_pk_bf16_f32 v43, v242, v243\n"
    "v_mfma_f32_16x16x32_bf16 v[52:55], a[184:187], v[108:111], v[52:55]\n"
    "v_sub_f32 v27, v72, v25\n"
    "v_sub_f32 v28, v73, v25\n"
    "v_sub_f32 v29, v74, v25\n"
    "v_sub_f32 v30, v75, v25\n"
    "v_mfma_f32_16x16x32_bf16 v[52:55], a[188:191], v[112:115], v[52:55]\n"
    "s_waitcnt lgkmcnt(0)\n"
    "s_add_u32 m0, s34, 32\n"
    "v_exp_f32 v8, v27\n"
    "v_exp_f32 v9, v28\n"
    "v_mfma_f32_16x16x32_bf16 v[56:59], a[192:195], v[100:103], v[80:83]\n"
    "buffer_load_dwordx4 v1, s[20:23], s32 offen offset:1024  lds\n"
    "v_exp_f32 v10, v29\n"
    "v_mfma_f32_16x16x32_bf16 v[56:59], a[196:199], v[104:107], v[56:59]\n"
    "v_exp_f32 v11, v30\n"
    "v_accvgpr_write_b32 a96, 0\n"
    "v_accvgpr_write_b32 a97, 0\n"
    "v_mfma_f32_16x16x32_bf16 v[56:59], a[200:203], v[108:111], v[56:59]\n"
    "v_cvt_pk_bf16_f32 v44, v8, v9\n"
    "v_cvt_pk_bf16_f32 v45, v10, v11\n"
    "v_accvgpr_write_b32 a98, 0\n"
    "v_accvgpr_write_b32 a99, 0\n"
    "v_mfma_f32_16x16x32_bf16 v[56:59], a[204:207], v[112:115], v[56:59]\n"
    "v_sub_f32 v27, v76, v25\n"
    "v_sub_f32 v28, v77, v25\n"
    "v_accvgpr_write_b32 a100, 0\n"
    "v_accvgpr_write_b32 a101, 0\n"
    "v_mfma_f32_16x16x32_bf16 v[60:63], a[208:211], v[100:103], v[80:83]\n"
    "v_sub_f32 v29, v78, v25\n"
    "v_sub_f32 v30, v79, v25\n"
    "v_accvgpr_write_b32 a102, 0\n"
    "v_accvgpr_write_b32 a103, 0\n"
    "v_mfma_f32_16x16x32_bf16 v[60:63], a[212:215], v[104:107], v[60:63]\n"
    "v_exp_f32 v240, v27\n"
    "v_accvgpr_write_b32 a104, 0\n"
    "v_accvgpr_write_b32 a105, 0\n"
    "v_mfma_f32_16x16x32_bf16 v[60:63], a[216:219], v[108:111], v[60:63]\n"
    "s_add_u32 m0, s34, 64\n"
    "v_exp_f32 v241, v28\n"
    "v_accvgpr_write_b32 a106, 0\n"
    "v_accvgpr_write_b32 a107, 0\n"
    "v_mfma_f32_16x16x32_bf16 v[60:63], a[220:223], v[112:115], v[60:63]\n"
    "buffer_load_dwordx4 v1, s[20:23], s32 offen offset:2048  lds\n"
    "v_exp_f32 v242, v29\n"
    "v_mfma_f32_16x16x32_bf16 a[64:67], a[224:227], v[32:35], 0\n"
    "v_accvgpr_write_b32 a108, 0\n"
    "v_accvgpr_write_b32 a109, 0\n"
    "v_exp_f32 v243, v30\n"
    "v_mfma_f32_16x16x32_bf16 a[64:67], v[180:183], v[36:39], a[64:67]\n"
    "v_accvgpr_write_b32 a110, 0\n"
    "v_accvgpr_write_b32 a111, 0\n"
    "v_cvt_pk_bf16_f32 v46, v240, v241\n"
    "v_cvt_pk_bf16_f32 v47, v242, v243\n"
    "v_mfma_f32_16x16x32_bf16 v[12:15], v[16:19], v[40:43], 0\n"
    "v_accvgpr_write_b32 a112, 0\n"
    "v_accvgpr_write_b32 a113, 0\n"
    "v_exp_f32 v8, v48\n"
    "v_mfma_f32_16x16x32_bf16 v[12:15], v[16:19], v[44:47], v[12:15]\n"
    "v_accvgpr_write_b32 a114, 0\n"
    "v_accvgpr_write_b32 a115, 0\n"
    "v_mfma_f32_16x16x32_bf16 a[68:71], a[228:231], v[32:35], 0\n"
    "v_exp_f32 v9, v49\n"
    "v_mfma_f32_16x16x32_bf16 a[68:71], v[184:187], v[36:39], a[68:71]\n"
    "v_accvgpr_write_b32 a116, 0\n"
    "v_accvgpr_write_b32 a117, 0\n"
    "v_mfma_f32_16x16x32_bf16 a[72:75], a[232:235], v[32:35], 0\n"
    "v_exp_f32 v10, v50\n"
    "v_mfma_f32_16x16x32_bf16 a[72:75], v[188:191], v[36:39], a[72:75]\n"
    "s_add_u32 m0, s34, 96\n"
    "v_accvgpr_write_b32 a118, 0\n"
    "v_accvgpr_write_b32 a119, 0\n"
    "v_mfma_f32_16x16x32_bf16 a[76:79], a[236:239], v[32:35], 0\n"
    "buffer_load_dwordx4 v1, s[20:23], s32 offen offset:3072  lds\n"
    "v_mfma_f32_16x16x32_bf16 a[76:79], v[192:195], v[36:39], a[76:79]\n"
    "v_exp_f32 v11, v51\n"
    "v_mfma_f32_16x16x32_bf16 a[80:83], a[240:243], v[32:35], 0\n"
    "v_accvgpr_write_b32 a120, 0\n"
    "v_accvgpr_write_b32 a121, 0\n"
    "v_mfma_f32_16x16x32_bf16 a[80:83], v[196:199], v[36:39], a[80:83]\n"
    "v_exp_f32 v240, v52\n"
    "v_mfma_f32_16x16x32_bf16 a[84:87], a[244:247], v[32:35], 0\n"
    "v_accvgpr_write_b32 a122, 0\n"
    "v_accvgpr_write_b32 a123, 0\n"
    "v_mfma_f32_16x16x32_bf16 a[84:87], v[200:203], v[36:39], a[84:87]\n"
    "v_exp_f32 v241, v53\n"
    "v_mfma_f32_16x16x32_bf16 a[88:91], a[248:251], v[32:35], 0\n"
    "v_accvgpr_write_b32 a124, 0\n"
    "v_accvgpr_write_b32 a125, 0\n"
    "v_mfma_f32_16x16x32_bf16 a[88:91], v[204:207], v[36:39], a[88:91]\n"
    "v_exp_f32 v242, v54\n"
    "v_mfma_f32_16x16x32_bf16 a[92:95], a[252:255], v[32:35], 0\n"
    "v_mov_b32 v23, v12\n"
    "v_mfma_f32_16x16x32_bf16 a[92:95], v[208:211], v[36:39], a[92:95]\n"
    "v_accvgpr_write_b32 a126, 0\n"
    "v_accvgpr_write_b32 a127, 0\n"
    "s_mov_b32 s37, 3\n"
    "LBH4:\n"
    "v_mfma_f32_16x16x32_bf16 v[64:67], a[160:163], v[116:119], v[84:87]\n"
    "v_exp_f32 v243, v55\n"
    "v_mfma_f32_16x16x32_bf16 v[64:67], a[164:167], v[120:123], v[64:67]\n"
    "v_cvt_pk_bf16_f32 v32, v8, v9\n"
    "v_cvt_pk_bf16_f32 v33, v10, v11\n"
    "v_mfma_f32_16x16x32_bf16 v[64:67], a[168:171], v[124:127], v[64:67]\n"
    "v_exp_f32 v8, v56\n"
    "v_mfma_f32_16x16x32_bf16 v[64:67], a[172:175], v[128:131], v[64:67]\n"
    "v_exp_f32 v9, v57\n"
    "v_mfma_f32_16x16x32_bf16 v[68:71], a[176:179], v[116:119], v[84:87]\n"
    "v_add_u32 v5, s36, v3\n"
    "v_mfma_f32_16x16x32_bf16 v[68:71], a[180:183], v[120:123], v[68:71]\n"
    "v_exp_f32 v10, v58\n"
    "v_mfma_f32_16x16x32_bf16 v[68:71], a[184:187], v[124:127], v[68:71]\n"
    "v_exp_f32 v11, v59\n"
    "v_mfma_f32_16x16x32_bf16 v[68:71], a[188:191], v[128:131], v[68:71]\n"
    "v_cvt_pk_bf16_f32 v34, v240, v241\n"
    "v_cvt_pk_bf16_f32 v35, v242, v243\n"
    "v_mfma_f32_16x16x32_bf16 v[72:75], a[192:195], v[116:119], v[84:87]\n"
    "v_exp_f32 v240, v60\n"
    "v_mfma_f32_16x16x32_bf16 v[72:75], a[196:199], v[120:123], v[72:75]\n"
    "v_exp_f32 v241, v61\n"
    "v_mfma_f32_16x16x32_bf16 v[72:75], a[200:203], v[124:127], v[72:75]\n"
    "v_exp_f32 v242, v62\n"
    "v_mfma_f32_16x16x32_bf16 v[72:75], a[204:207], v[128:131], v[72:75]\n"
    "v_exp_f32 v243, v63\n"
    "v_mfma_f32_16x16x32_bf16 v[76:79], a[208:211], v[116:119], v[84:87]\n"
    "v_cvt_pk_bf16_f32 v36, v8, v9\n"
    "v_cvt_pk_bf16_f32 v37, v10, v11\n"
    "v_mfma_f32_16x16x32_bf16 v[76:79], a[212:215], v[120:123], v[76:79]\n"
    "v_cvt_pk_bf16_f32 v38, v240, v241\n"
    "v_cvt_pk_bf16_f32 v39, v242, v243\n"
    "v_mfma_f32_16x16x32_bf16 v[76:79], a[216:219], v[124:127], v[76:79]\n"
    "v_exp_f32 v8, v64\n"
    "v_mfma_f32_16x16x32_bf16 v[76:79], a[220:223], v[128:131], v[76:79]\n"
    "v_exp_f32 v9, v65\n"
    "v_mfma_f32_16x16x32_bf16 v[12:15], v[16:19], v[32:35], 0\n"
    "v_exp_f32 v10, v66\n"
    "v_mfma_f32_16x16x32_bf16 v[12:15], v[16:19], v[36:39], v[12:15]\n"
    "v_exp_f32 v11, v67\n"
    "v_mfma_f32_16x16x32_bf16 a[96:99], a[224:227], v[40:43], a[96:99]\n"
    "ds_read_b64_tr_b16 a[224:225], v5 offset:0\n"
    "ds_read_b64_tr_b16 a[226:227], v5 offset:256\n"
    "v_mfma_f32_16x16x32_bf16 a[96:99], v[180:183], v[44:47], a[96:99]\n"
    "v_exp_f32 v240, v68\n"
    "v_mfma_f32_16x16x32_bf16 a[100:103], a[228:231], v[40:43], a[100:103]\n"
    "ds_read_b64_tr_b16 v[180:181], v5 offset:512\n"
    "ds_read_b64_tr_b16 v[182:183], v5 offset:768\n"
    "v_mfma_f32_16x16x32_bf16 a[100:103], v[184:187], v[44:47], a[100:103]\n"
    "v_exp_f32 v241, v69\n"
    "v_mfma_f32_16x16x32_bf16 a[104:107], a[232:235], v[40:43], a[104:107]\n"
    "ds_read_b64_tr_b16 a[228:229], v5 offset:32\n"
    "ds_read_b64_tr_b16 a[230:231], v5 offset:288\n"
    "v_mfma_f32_16x16x32_bf16 a[104:107], v[188:191], v[44:47], a[104:107]\n"
    "v_cmp_nge_f32 vcc, s40, v12\n"
    "v_mfma_f32_16x16x32_bf16 a[108:111], a[236:239], v[40:43], a[108:111]\n"
    "ds_read_b64_tr_b16 v[184:185], v5 offset:544\n"
    "ds_read_b64_tr_b16 v[186:187], v5 offset:800\n"
    "v_mfma_f32_16x16x32_bf16 a[108:111], v[192:195], v[44:47], a[108:111]\n"
    "v_exp_f32 v242, v70\n"
    "v_mfma_f32_16x16x32_bf16 a[112:115], a[240:243], v[40:43], a[112:115]\n"
    "ds_read_b64_tr_b16 a[232:233], v5 offset:64\n"
    "ds_read_b64_tr_b16 a[234:235], v5 offset:320\n"
    "v_mfma_f32_16x16x32_bf16 a[112:115], v[196:199], v[44:47], a[112:115]\n"
    "s_cbranch_vccnz LTR_48\n"
    "LRET_48:\n"
    "v_add_f32 v20, v20, v12\n"
    "v_mfma_f32_16x16x32_bf16 a[116:119], a[244:247], v[40:43], a[116:119]\n"
    "ds_read_b64_tr_b16 v[188:189], v5 offset:576\n"
    "ds_read_b64_tr_b16 v[190:191], v5 offset:832\n"
    "v_mfma_f32_16x16x32_bf16 a[116:119], v[200:203], v[44:47], a[116:119]\n"
    "v_exp_f32 v243, v71\n"
    "v_mfma_f32_16x16x32_bf16 a[120:123], a[248:251], v[40:43], a[120:123]\n"
    "ds_read_b64_tr_b16 a[236:237], v5 offset:96\n"
    "ds_read_b64_tr_b16 a[238:239], v5 offset:352\n"
    "v_mfma_f32_16x16x32_bf16 a[120:123], v[204:207], v[44:47], a[120:123]\n"
    "ds_read_b128 v[212:215], v232\n"
    "v_mfma_f32_16x16x32_bf16 a[124:127], a[252:255], v[40:43], a[124:127]\n"
    "ds_read_b64_tr_b16 v[192:193], v5 offset:608\n"
    "ds_read_b64_tr_b16 v[194:195], v5 offset:864\n"
    "v_mfma_f32_16x16x32_bf16 a[124:127], v[208:211], v[44:47], a[124:127]\n"
    "v_cvt_pk_bf16_f32 v40, v8, v9\n"
    "v_cvt_pk_bf16_f32 v41, v10, v11\n"
    "v_mfma_f32_16x16x32_bf16 v[48:51], a[160:163], v[132:135], v[88:91]\n"
    "s_add_u32 m0, s34, 16896\n"
    "ds_read_b64_tr_b16 a[240:241], v5 offset:128\n"
    "ds_read_b64_tr_b16 a[242:243], v5 offset:384\n"
    "v_mfma_f32_16x16x32_bf16 v[48:51], a[164:167], v[136:139], v[48:51]\n"
    "buffer_load_dwordx4 v1, s[24:27], s32 offen offset:0  lds\n"
    "v_mfma_f32_16x16x32_bf16 v[48:51], a[168:171], v[140:143], v[48:51]\n"
    "ds_read_b64_tr_b16 v[196:197], v5 offset:640\n"
    "ds_read_b64_tr_b16 v[198:199], v5 offset:896\n"
    "v_mfma_f32_16x16x32_bf16 v[48:51], a[172:175], v[144:147], v[48:51]\n"
    "v_exp_f32 v8, v72\n"
    "v_mfma_f32_16x16x32_bf16 v[52:55], a[176:179], v[132:135], v[88:91]\n"
    "ds_read_b64_tr_b16 a[244:245], v5 offset:160\n"
    "ds_read_b64_tr_b16 a[246:247], v5 offset:416\n"
    "v_mfma_f32_16x16x32_bf16 v[52:55], a[180:183], v[136:139], v[52:55]\n"
    "v_exp_f32 v9, v73\n"
    "v_mfma_f32_16x16x32_bf16 v[52:55], a[184:187], v[140:143], v[52:55]\n"
    "s_add_u32 m0, s34, 16928\n"
    "ds_read_b64_tr_b16 v[200:201], v5 offset:672\n"
    "ds_read_b64_tr_b16 v[202:203], v5 offset:928\n"
    "v_mfma_f32_16x16x32_bf16 v[52:55], a[188:191], v[144:147], v[52:55]\n"
    "buffer_load_dwordx4 v1, s[24:27], s32 offen offset:1024  lds\n"
    "v_mfma_f32_16x16x32_bf16 v[56:59], a[192:195], v[132:135], v[88:91]\n"
    "ds_read_b64_tr_b16 a[248:249], v5 offset:192\n"
    "ds_read_b64_tr_b16 a[250:251], v5 offset:448\n"
    "v_mfma_f32_16x16x32_bf16 v[56:59], a[196:199], v[136:139], v[56:59]\n"
    "v_exp_f32 v10, v74\n"
    "v_mfma_f32_16x16x32_bf16 v[56:59], a[200:203], v[140:143], v[56:59]\n"
    "ds_read_b64_tr_b16 v[204:205], v5 offset:704\n"
    "ds_read_b64_tr_b16 v[206:207], v5 offset:960\n"
    "v_mfma_f32_16x16x32_bf16 v[56:59], a[204:207], v[144:147], v[56:59]\n"
    "v_exp_f32 v11, v75\n"
    "v_mfma_f32_16x16x32_bf16 v[60:63], a[208:211], v[132:135], v[88:91]\n"
    "ds_read_b64_tr_b16 a[252:253], v5 offset:224\n"
    "ds_read_b64_tr_b16 a[254:255], v5 offset:480\n"
    "v_mfma_f32_16x16x32_bf16 v[60:63], a[212:215], v[136:139], v[60:63]\n"
    "v_cvt_pk_bf16_f32 v42, v240, v241\n"
    "v_cvt_pk_bf16_f32 v43, v242, v243\n"
    "v_mfma_f32_16x16x32_bf16 v[60:63], a[216:219], v[140:143], v[60:63]\n"
    "s_add_u32 m0, s34, 16960\n"
    "ds_read_b64_tr_b16 v[208:209], v5 offset:736\n"
    "ds_read_b64_tr_b16 v[210:211], v5 offset:992\n"
    "v_mfma_f32_16x16x32_bf16 v[60:63], a[220:223], v[144:147], v[60:63]\n"
    "s_waitcnt lgkmcnt(15)\n"
    "buffer_load_dwordx4 v1, s[24:27], s32 offen offset:2048  lds\n"
    "v_mfma_f32_16x16x32_bf16 a[0:3], a[224:227], v[32:35], a[0:3]\n"
    "v_exp_f32 v240, v76\n"
    "v_mfma_f32_16x16x32_bf16 a[0:3], v[180:183], v[36:39], a[0:3]\n"
    "v_exp_f32 v241, v77\n"
    "v_mfma_f32_16x16x32_bf16 a[4:7], a[228:231], v[32:35], a[4:7]\n"
    "v_exp_f32 v242, v78\n"
    "v_mfma_f32_16x16x32_bf16 a[4:7], v[184:187], v[36:39], a[4:7]\n"
    "s_add_u32 m0, s34, 16992\n"
    "v_exp_f32 v243, v79\n"
    "v_mfma_f32_16x16x32_bf16 a[8:11], a[232:235], v[32:35], a[8:11]\n"
    "buffer_load_dwordx4 v1, s[24:27], s32 offen offset:3072  lds\n"
    "v_mfma_f32_16x16x32_bf16 a[8:11], v[188:191], v[36:39], a[8:11]\n"
    "v_cvt_pk_bf16_f32 v44, v8, v9\n"
    "v_cvt_pk_bf16_f32 v45, v10, v11\n"
    "v_mfma_f32_16x16x32_bf16 a[12:15], a[236:239], v[32:35], a[12:15]\n"
    "v_cvt_pk_bf16_f32 v46, v240, v241\n"
    "v_cvt_pk_bf16_f32 v47, v242, v243\n"
    "v_mfma_f32_16x16x32_bf16 a[12:15], v[192:195], v[36:39], a[12:15]\n"
    "s_waitcnt lgkmcnt(0)\n"
    "v_exp_f32 v8, v48\n"
    "v_mfma_f32_16x16x32_bf16 v[12:15], v[16:19], v[40:43], 0\n"
    "v_exp_f32 v9, v49\n"
    "v_mfma_f32_16x16x32_bf16 v[12:15], v[16:19], v[44:47], v[12:15]\n"
    "v_exp_f32 v10, v50\n"
    "v_mfma_f32_16x16x32_bf16 a[16:19], a[240:243], v[32:35], a[16:19]\n"
    "s_add_u32 s34, s36, s35\n"
    "v_exp_f32 v11, v51\n"
    "v_mfma_f32_16x16x32_bf16 a[16:19], v[196:199], v[36:39], a[16:19]\n"
    "v_exp_f32 v240, v52\n"
    "v_mfma_f32_16x16x32_bf16 a[20:23], a[244:247], v[32:35], a[20:23]\n"
    "s_add_u32 s36, s36, 33792\n"
    "v_exp_f32 v241, v53\n"
    "v_mfma_f32_16x16x32_bf16 a[20:23], v[200:203], v[36:39], a[20:23]\n"
    "v_exp_f32 v242, v54\n"
    "v_mfma_f32_16x16x32_bf16 a[24:27], a[248:251], v[32:35], a[24:27]\n"
    "s_sub_u32 s50, s36, 101376\n"
    "v_cmp_nge_f32 vcc, s40, v12\n"
    "v_mfma_f32_16x16x32_bf16 a[24:27], v[204:207], v[36:39], a[24:27]\n"
    "v_exp_f32 v243, v55\n"
    "v_mfma_f32_16x16x32_bf16 a[28:31], a[252:255], v[32:35], a[28:31]\n"
    "s_min_u32 s36, s36, s50\n"
    "v_cvt_pk_bf16_f32 v32, v8, v9\n"
    "v_cvt_pk_bf16_f32 v33, v10, v11\n"
    "v_mfma_f32_16x16x32_bf16 a[28:31], v[208:211], v[36:39], a[28:31]\n"
    "s_add_u32 s32, s32, 16384\n"
    "s_cbranch_vccnz LTR_49\n"
    "LRET_49:\n"
    "v_add_f32 v21, v21, v12\n"
    "v_mfma_f32_16x16x32_bf16 v[64:67], a[160:163], v[148:151], v[92:95]\n"
    "v_add_u32 v4, s36, v2\n"
    "v_mfma_f32_16x16x32_bf16 v[64:67], a[164:167], v[152:155], v[64:67]\n"
    "s_waitcnt vmcnt(10)\n"
    "s_barrier\n"
    "v_mfma_f32_16x16x32_bf16 v[64:67], a[168:171], v[156:159], v[64:67]\n"
    "v_exp_f32 v8, v56\n"
    "v_mfma_f32_16x16x32_bf16 v[64:67], a[172:175], v[160:163], v[64:67]\n"
    "v_exp_f32 v9, v57\n"
    "v_mfma_f32_16x16x32_bf16 v[68:71], a[176:179], v[148:151], v[92:95]\n"
    "v_exp_f32 v10, v58\n"
    "v_mfma_f32_16x16x32_bf16 v[68:71], a[180:183], v[152:155], v[68:71]\n"
    "ds_read_b128 a[160:163], v4 offset:0\n"
    "ds_read_b128 a[164:167], v4 offset:64\n"
    "v_mfma_f32_16x16x32_bf16 v[68:71], a[184:187], v[156:159], v[68:71]\n"
    "v_exp_f32 v11, v59\n"
    "v_mfma_f32_16x16x32_bf16 v[68:71], a[188:191], v[160:163], v[68:71]\n"
    "v_cvt_pk_bf16_f32 v34, v240, v241\n"
    "v_cvt_pk_bf16_f32 v35, v242, v243\n"
    "v_mfma_f32_16x16x32_bf16 v[72:75], a[192:195], v[148:151], v[92:95]\n"
    "ds_read_b128 a[168:171], v4 offset:128\n"
    "ds_read_b128 a[172:175], v4 offset:192\n"
    "v_mfma_f32_16x16x32_bf16 v[72:75], a[196:199], v[152:155], v[72:75]\n"
    "v_exp_f32 v240, v60\n"
    "v_mfma_f32_16x16x32_bf16 v[72:75], a[200:203], v[156:159], v[72:75]\n"
    "v_exp_f32 v241, v61\n"
    "v_mfma_f32_16x16x32_bf16 v[72:75], a[204:207], v[160:163], v[72:75]\n"
    "ds_read_b128 a[176:179], v4 offset:256\n"
    "ds_read_b128 a[180:183], v4 offset:320\n"
    "v_mfma_f32_16x16x32_bf16 v[76:79], a[208:211], v[148:151], v[92:95]\n"
    "v_exp_f32 v242, v62\n"
    "v_mfma_f32_16x16x32_bf16 v[76:79], a[212:215], v[152:155], v[76:79]\n"
    "v_exp_f32 v243, v63\n"
    "v_mfma_f32_16x16x32_bf16 v[76:79], a[216:219], v[156:159], v[76:79]\n"
    "ds_read_b128 a[184:187], v4 offset:384\n"
    "ds_read_b128 a[188:191], v4 offset:448\n"
    "v_mfma_f32_16x16x32_bf16 v[76:79], a[220:223], v[160:163], v[76:79]\n"
    "v_cvt_pk_bf16_f32 v36, v8, v9\n"
    "v_cvt_pk_bf16_f32 v37, v10, v11\n"
    "v_mfma_f32_16x16x32_bf16 a[32:35], a[224:227], v[40:43], a[32:35]\n"
    "v_cvt_pk_bf16_f32 v38, v240, v241\n"
    "v_cvt_pk_bf16_f32 v39, v242, v243\n"
    "v_mfma_f32_16x16x32_bf16 a[32:35], v[180:183], v[44:47], a[32:35]\n"
    "ds_read_b128 a[192:195], v4 offset:512\n"
    "ds_read_b128 a[196:199], v4 offset:576\n"
    "v_mfma_f32_16x16x32_bf16 v[12:15], v[16:19], v[32:35], 0\n"
    "v_exp_f32 v8, v64\n"
    "v_mfma_f32_16x16x32_bf16 v[12:15], v[16:19], v[36:39], v[12:15]\n"
    "ds_read_b128 a[200:203], v4 offset:640\n"
    "ds_read_b128 a[204:207], v4 offset:704\n"
    "v_mfma_f32_16x16x32_bf16 a[36:39], a[228:231], v[40:43], a[36:39]\n"
    "v_exp_f32 v9, v65\n"
    "v_mfma_f32_16x16x32_bf16 a[36:39], v[184:187], v[44:47], a[36:39]\n"
    "ds_read_b128 a[208:211], v4 offset:768\n"
    "ds_read_b128 a[212:215], v4 offset:832\n"
    "v_mfma_f32_16x16x32_bf16 a[40:43], a[232:235], v[40:43], a[40:43]\n"
    "v_exp_f32 v10, v66\n"
    "v_mfma_f32_16x16x32_bf16 a[40:43], v[188:191], v[44:47], a[40:43]\n"
    "ds_read_b128 a[216:219], v4 offset:896\n"
    "ds_read_b128 a[220:223], v4 offset:960\n"
    "v_mfma_f32_16x16x32_bf16 a[44:47], a[236:239], v[40:43], a[44:47]\n"
    "v_cmp_nge_f32 vcc, s40, v12\n"
    "v_mfma_f32_16x16x32_bf16 a[44:47], v[192:195], v[44:47], a[44:47]\n"
    "v_exp_f32 v11, v67\n"
    "v_mfma_f32_16x16x32_bf16 a[48:51], a[240:243], v[40:43], a[48:51]\n"
    "v_exp_f32 v240, v68\n"
    "v_mfma_f32_16x16x32_bf16 a[48:51], v[196:199], v[44:47], a[48:51]\n"
    "s_cbranch_vccnz LTR_50\n"
    "LRET_50:\n"
    "v_add_f32 v22, v22, v12\n"
    "v_mfma_f32_16x16x32_bf16 a[52:55], a[244:247], v[40:43], a[52:55]\n"
    "v_exp_f32 v241, v69\n"
    "v_mfma_f32_16x16x32_bf16 a[52:55], v[200:203], v[44:47], a[52:55]\n"
    "v_exp_f32 v242, v70\n"
    "v_mfma_f32_16x16x32_bf16 a[56:59], a[248:251], v[40:43], a[56:59]\n"
    "v_exp_f32 v243, v71\n"
    "v_mfma_f32_16x16x32_bf16 a[56:59], v[204:207], v[44:47], a[56:59]\n"
    "v_add_u32 v232, 4096, v232\n"
    "v_mfma_f32_16x16x32_bf16 a[60:63], a[252:255], v[40:43], a[60:63]\n"
    "v_cvt_pk_bf16_f32 v40, v8, v9\n"
    "v_cvt_pk_bf16_f32 v41, v10, v11\n"
    "v_mfma_f32_16x16x32_bf16 a[60:63], v[208:211], v[44:47], a[60:63]\n"
    "s_waitcnt lgkmcnt(8)\n"
    "v_exp_f32 v8, v72\n"
    "v_mfma_f32_16x16x32_bf16 v[48:51], a[160:163], v[100:103], v[80:83]\n"
    "s_add_u32 m0, s34, 0\n"
    "v_exp_f32 v9, v73\n"
    "v_mfma_f32_16x16x32_bf16 v[48:51], a[164:167], v[104:107], v[48:51]\n"
    "buffer_load_dwordx4 v1, s[20:23], s32 offen offset:0  lds\n"
    "v_mfma_f32_16x16x32_bf16 v[48:51], a[168:171], v[108:111], v[48:51]\n"
    "v_exp_f32 v10, v74\n"
    "v_mfma_f32_16x16x32_bf16 v[48:51], a[172:175], v[112:115], v[48:51]\n"
    "v_exp_f32 v11, v75\n"
    "v_mfma_f32_16x16x32_bf16 v[52:55], a[176:179], v[100:103], v[80:83]\n"
    "v_cvt_pk_bf16_f32 v42, v240, v241\n"
    "v_cvt_pk_bf16_f32 v43, v242, v243\n"
    "v_mfma_f32_16x16x32_bf16 v[52:55], a[180:183], v[104:107], v[52:55]\n"
    "v_exp_f32 v240, v76\n"
    "v_mfma_f32_16x16x32_bf16 v[52:55], a[184:187], v[108:111], v[52:55]\n"
    "s_add_u32 m0, s34, 32\n"
    "v_exp_f32 v241, v77\n"
    "v_mfma_f32_16x16x32_bf16 v[52:55], a[188:191], v[112:115], v[52:55]\n"
    "s_waitcnt lgkmcnt(0)\n"
    "buffer_load_dwordx4 v1, s[20:23], s32 offen offset:1024  lds\n"
    "v_mfma_f32_16x16x32_bf16 v[56:59], a[192:195], v[100:103], v[80:83]\n"
    "v_exp_f32 v242, v78\n"
    "v_mfma_f32_16x16x32_bf16 v[56:59], a[196:199], v[104:107], v[56:59]\n"
    "v_exp_f32 v243, v79\n"
    "v_mfma_f32_16x16x32_bf16 v[56:59], a[200:203], v[108:111], v[56:59]\n"
    "v_cvt_pk_bf16_f32 v44, v8, v9\n"
    "v_cvt_pk_bf16_f32 v45, v10, v11\n"
    "v_mfma_f32_16x16x32_bf16 v[56:59], a[204:207], v[112:115], v[56:59]\n"
    "v_cvt_pk_bf16_f32 v46, v240, v241\n"
    "v_cvt_pk_bf16_f32 v47, v242, v243\n"
    "v_mfma_f32_16x16x32_bf16 v[12:15], v[16:19], v[40:43], 0\n"
    "v_exp_f32 v8, v48\n"
    "v_mfma_f32_16x16x32_bf16 v[12:15], v[16:19], v[44:47], v[12:15]\n"
    "v_exp_f32 v9, v49\n"
    "v_mfma_f32_16x16x32_bf16 v[60:63], a[208:211], v[100:103], v[80:83]\n"
    "s_add_u32 m0, s34, 64\n"
    "v_exp_f32 v10, v50\n"
    "v_mfma_f32_16x16x32_bf16 v[60:63], a[212:215], v[104:107], v[60:63]\n"
    "buffer_load_dwordx4 v1, s[20:23], s32 offen offset:2048  lds\n"
    "v_mfma_f32_16x16x32_bf16 v[60:63], a[216:219], v[108:111], v[60:63]\n"
    "v_exp_f32 v11, v51\n"
    "v_mfma_f32_16x16x32_bf16 v[60:63], a[220:223], v[112:115], v[60:63]\n"
    "v_exp_f32 v240, v52\n"
    "v_mfma_f32_16x16x32_bf16 a[64:67], a[224:227], v[32:35], a[64:67]\n"
    "v_cmp_nge_f32 vcc, s40, v12\n"
    "v_mfma_f32_16x16x32_bf16 a[64:67], v[180:183], v[36:39], a[64:67]\n"
    "v_exp_f32 v241, v53\n"
    "v_mfma_f32_16x16x32_bf16 a[68:71], a[228:231], v[32:35], a[68:71]\n"
    "s_add_u32 m0, s34, 96\n"
    "v_exp_f32 v242, v54\n"
    "v_mfma_f32_16x16x32_bf16 a[68:71], v[184:187], v[36:39], a[68:71]\n"
    "buffer_load_dwordx4 v1, s[20:23], s32 offen offset:3072  lds\n"
    "v_mfma_f32_16x16x32_bf16 a[72:75], a[232:235], v[32:35], a[72:75]\n"
    "s_cbranch_vccnz LTR_51\n"
    "LRET_51:\n"
    "v_add_f32 v23, v23, v12\n"
    "v_mfma_f32_16x16x32_bf16 a[72:75], v[188:191], v[36:39], a[72:75]\n"
    "v_exp_f32 v243, v55\n"
    "v_mfma_f32_16x16x32_bf16 a[76:79], a[236:239], v[32:35], a[76:79]\n"
    "s_waitcnt lgkmcnt(15)\n"
    "buffer_store_dwordx4 v[212:215], v1, s[28:31], s61 offen offset:0 nt\n"
    "v_mfma_f32_16x16x32_bf16 a[76:79], v[192:195], v[36:39], a[76:79]\n"
    "ds_read_b128 v[216:219], v233\n"
    "v_mfma_f32_16x16x32_bf16 a[80:83], a[240:243], v[32:35], a[80:83]\n"
    "v_mfma_f32_16x16x32_bf16 a[80:83], v[196:199], v[36:39], a[80:83]\n"
    "v_add_u32 v233, 4096, v233\n"
    "v_mfma_f32_16x16x32_bf16 a[84:87], a[244:247], v[32:35], a[84:87]\n"
    "v_mfma_f32_16x16x32_bf16 a[84:87], v[200:203], v[36:39], a[84:87]\n"
    "v_mfma_f32_16x16x32_bf16 a[88:91], a[248:251], v[32:35], a[88:91]\n"
    "v_mfma_f32_16x16x32_bf16 a[88:91], v[204:207], v[36:39], a[88:91]\n"
    "v_mfma_f32_16x16x32_bf16 a[92:95], a[252:255], v[32:35], a[92:95]\n"
    "v_cvt_pk_bf16_f32 v32, v8, v9\n"
    "v_cvt_pk_bf16_f32 v33, v10, v11\n"
    "v_mfma_f32_16x16x32_bf16 a[92:95], v[208:211], v[36:39], a[92:95]\n"
    "v_exp_f32 v8, v56\n"
    "v_mfma_f32_16x16x32_bf16 v[64:67], a[160:163], v[116:119], v[84:87]\n"
    "v_exp_f32 v9, v57\n"
    "v_mfma_f32_16x16x32_bf16 v[64:67], a[164:167], v[120:123], v[64:67]\n"
    "v_add_u32 v5, s36, v3\n"
    "v_mfma_f32_16x16x32_bf16 v[64:67], a[168:171], v[124:127], v[64:67]\n"
    "v_exp_f32 v10, v58\n"
    "v_mfma_f32_16x16x32_bf16 v[64:67], a[172:175], v[128:131], v[64:67]\n"
    "v_exp_f32 v11, v59\n"
    "v_mfma_f32_16x16x32_bf16 v[68:71], a[176:179], v[116:119], v[84:87]\n"
    "v_cvt_pk_bf16_f32 v34, v240, v241\n"
    "v_cvt_pk_bf16_f32 v35, v242, v243\n"
    "v_mfma_f32_16x16x32_bf16 v[68:71], a[180:183], v[120:123], v[68:71]\n"
    "v_exp_f32 v240, v60\n"
    "v_mfma_f32_16x16x32_bf16 v[68:71], a[184:187], v[124:127], v[68:71]\n"
    "v_exp_f32 v241, v61\n"
    "v_mfma_f32_16x16x32_bf16 v[68:71], a[188:191], v[128:131], v[68:71]\n"
    "v_exp_f32 v242, v62\n"
    "v_mfma_f32_16x16x32_bf16 v[72:75], a[192:195], v[116:119], v[84:87]\n"
    "v_exp_f32 v243, v63\n"
    "v_mfma_f32_16x16x32_bf16 v[72:75], a[196:199], v[120:123], v[72:75]\n"
    "v_cvt_pk_bf16_f32 v36, v8, v9\n"
    "v_cvt_pk_bf16_f32 v37, v10, v11\n"
    "v_mfma_f32_16x16x32_bf16 v[72:75], a[200:203], v[124:127], v[72:75]\n"
    "v_cvt_pk_bf16_f32 v38, v240, v241\n"
    "v_cvt_pk_bf16_f32 v39, v242, v243\n"
    "v_mfma_f32_16x16x32_bf16 v[72:75], a[204:207], v[128:131], v[72:75]\n"
    "v_exp_f32 v8, v64\n"
    "v_mfma_f32_16x16x32_bf16 v[12:15], v[16:19], v[32:35], 0\n"
    "v_exp_f32 v9, v65\n"
    "v_mfma_f32_16x16x32_bf16 v[12:15], v[16:19], v[36:39], v[12:15]\n"
    "v_exp_f32 v10, v66\n"
    "v_mfma_f32_16x16x32_bf16 v[76:79], a[208:211], v[116:119], v[84:87]\n"
    "v_exp_f32 v11, v67\n"
    "v_mfma_f32_16x16x32_bf16 v[76:79], a[212:215], v[120:123], v[76:79]\n"
    "v_exp_f32 v240, v68\n"
    "v_mfma_f32_16x16x32_bf16 v[76:79], a[216:219], v[124:127], v[76:79]\n"
    "v_exp_f32 v241, v69\n"
    "v_mfma_f32_16x16x32_bf16 v[76:79], a[220:223], v[128:131], v[76:79]\n"
    "v_exp_f32 v242, v70\n"
    "v_mfma_f32_16x16x32_bf16 a[96:99], a[224:227], v[40:43], a[96:99]\n"
    "ds_read_b64_tr_b16 a[224:225], v5 offset:0\n"
    "ds_read_b64_tr_b16 a[226:227], v5 offset:256\n"
    "v_mfma_f32_16x16x32_bf16 a[96:99], v[180:183], v[44:47], a[96:99]\n"
    "v_cmp_nge_f32 vcc, s40, v12\n"
    "v_mfma_f32_16x16x32_bf16 a[100:103], a[228:231], v[40:43], a[100:103]\n"
    "ds_read_b64_tr_b16 v[180:181], v5 offset:512\n"
    "ds_read_b64_tr_b16 v[182:183], v5 offset:768\n"
    "v_mfma_f32_16x16x32_bf16 a[100:103], v[184:187], v[44:47], a[100:103]\n"
    "v_exp_f32 v243, v71\n"
    "v_mfma_f32_16x16x32_bf16 a[104:107], a[232:235], v[40:43], a[104:107]\n"
    "ds_read_b64_tr_b16 a[228:229], v5 offset:32\n"
    "ds_read_b64_tr_b16 a[230:231], v5 offset:288\n"
    "v_mfma_f32_16x16x32_bf16 a[104:107], v[188:191], v[44:47], a[104:107]\n"
    "s_cbranch_vccnz LTR_52\n"
    "LRET_52:\n"
    "v_add_f32 v20, v20, v12\n"
    "v_mfma_f32_16x16x32_bf16 a[108:111], a[236:239], v[40:43], a[108:111]\n"
    "ds_read_b64_tr_b16 v[184:185], v5 offset:544\n"
    "ds_read_b64_tr_b16 v[186:187], v5 offset:800\n"
    "v_mfma_f32_16x16x32_bf16 a[108:111], v[192:195], v[44:47], a[108:111]\n"
    "s_waitcnt lgkmcnt(8)\n"
    "buffer_store_dwordx4 v[216:219], v1, s[28:31], s61 offen offset:1024 nt\n"
    "v_mfma_f32_16x16x32_bf16 a[112:115], a[240:243], v[40:43], a[112:115]\n"
    "ds_read_b64_tr_b16 a[232:233], v5 offset:64\n"
    "ds_read_b64_tr_b16 a[234:235], v5 offset:320\n"
    "v_mfma_f32_16x16x32_bf16 a[112:115], v[196:199], v[44:47], a[112:115]\n"
    "ds_read_b128 v[220:223], v234\n"
    "v_mfma_f32_16x16x32_bf16 a[116:119], a[244:247], v[40:43], a[116:119]\n"
    "ds_read_b64_tr_b16 v[188:189], v5 offset:576\n"
    "ds_read_b64_tr_b16 v[190:191], v5 offset:832\n"
    "v_mfma_f32_16x16x32_bf16 a[116:119], v[200:203], v[44:47], a[116:119]\n"
    "v_add_u32 v234, 4096, v234\n"
    "v_mfma_f32_16x16x32_bf16 a[120:123], a[248:251], v[40:43], a[120:123]\n"
    "ds_read_b64_tr_b16 a[236:237], v5 offset:96\n"
    "ds_read_b64_tr_b16 a[238:239], v5 offset:352\n"
    "v_mfma_f32_16x16x32_bf16 a[120:123], v[204:207], v[44:47], a[120:123]\n"
    "v_mfma_f32_16x16x32_bf16 a[124:127], a[252:255], v[40:43], a[124:127]\n"
    "ds_read_b64_tr_b16 v[192:193], v5 offset:608\n"
    "ds_read_b64_tr_b16 v[194:195], v5 offset:864\n"
    "v_mfma_f32_16x16x32_bf16 a[124:127], v[208:211], v[44:47], a[124:127]\n"
    "v_cvt_pk_bf16_f32 v40, v8, v9\n"
    "v_cvt_pk_bf16_f32 v41, v10, v11\n"
    "v_mfma_f32_16x16x32_bf16 v[48:51], a[160:163], v[132:135], v[88:91]\n"
    "s_add_u32 m0, s34, 16896\n"
    "ds_read_b64_tr_b16 a[240:241], v5 offset:128\n"
    "ds_read_b64_tr_b16 a[242:243], v5 offset:384\n"
    "v_mfma_f32_16x16x32_bf16 v[48:51], a[164:167], v[136:139], v[48:51]\n"
    "buffer_load_dwordx4 v1, s[24:27], s32 offen offset:0  lds\n"
    "v_mfma_f32_16x16x32_bf16 v[48:51], a[168:171], v[140:143], v[48:51]\n"
    "ds_read_b64_tr_b16 v[196:197], v5 offset:640\n"
    "ds_read_b64_tr_b16 v[198:199], v5 offset:896\n"
    "v_mfma_f32_16x16x32_bf16 v[48:51], a[172:175], v[144:147], v[48:51]\n"
    "v_exp_f32 v8, v72\n"
    "v_mfma_f32_16x16x32_bf16 v[52:55], a[176:179], v[132:135], v[88:91]\n"
    "ds_read_b64_tr_b16 a[244:245], v5 offset:160\n"
    "ds_read_b64_tr_b16 a[246:247], v5 offset:416\n"
    "v_mfma_f32_16x16x32_bf16 v[52:55], a[180:183], v[136:139], v[52:55]\n"
    "v_exp_f32 v9, v73\n"
    "v_mfma_f32_16x16x32_bf16 v[52:55], a[184:187], v[140:143], v[52:55]\n"
    "s_add_u32 m0, s34, 16928\n"
    "ds_read_b64_tr_b16 v[200:201], v5 offset:672\n"
    "ds_read_b64_tr_b16 v[202:203], v5 offset:928\n"
    "v_mfma_f32_16x16x32_bf16 v[52:55], a[188:191], v[144:147], v[52:55]\n"
    "buffer_load_dwordx4 v1, s[24:27], s32 offen offset:1024  lds\n"
    "v_mfma_f32_16x16x32_bf16 v[56:59], a[192:195], v[132:135], v[88:91]\n"
    "ds_read_b64_tr_b16 a[248:249], v5 offset:192\n"
    "ds_read_b64_tr_b16 a[250:251], v5 offset:448\n"
    "v_mfma_f32_16x16x32_bf16 v[56:59], a[196:199], v[136:139], v[56:59]\n"
    "v_exp_f32 v10, v74\n"
    "v_mfma_f32_16x16x32_bf16 v[56:59], a[200:203], v[140:143], v[56:59]\n"
    "ds_read_b64_tr_b16 v[204:205], v5 offset:704\n"
    "ds_read_b64_tr_b16 v[206:207], v5 offset:960\n"
    "v_mfma_f32_16x16x32_bf16 v[56:59], a[204:207], v[144:147], v[56:59]\n"
    "v_exp_f32 v11, v75\n"
    "v_mfma_f32_16x16x32_bf16 v[60:63], a[208:211], v[132:135], v[88:91]\n"
    "ds_read_b64_tr_b16 a[252:253], v5 offset:224\n"
    "ds_read_b64_tr_b16 a[254:255], v5 offset:480\n"
    "v_mfma_f32_16x16x32_bf16 v[60:63], a[212:215], v[136:139], v[60:63]\n"
    "v_cvt_pk_bf16_f32 v42, v240, v241\n"
    "v_cvt_pk_bf16_f32 v43, v242, v243\n"
    "v_mfma_f32_16x16x32_bf16 v[60:63], a[216:219], v[140:143], v[60:63]\n"
    "s_add_u32 m0, s34, 16960\n"
    "ds_read_b64_tr_b16 v[208:209], v5 offset:736\n"
    "ds_read_b64_tr_b16 v[210:211], v5 offset:992\n"
    "v_mfma_f32_16x16x32_bf16 v[60:63], a[220:223], v[144:147], v[60:63]\n"
    "s_waitcnt lgkmcnt(15)\n"
    "buffer_load_dwordx4 v1, s[24:27], s32 offen offset:2048  lds\n"
    "v_mfma_f32_16x16x32_bf16 a[0:3], a[224:227], v[32:35], a[0:3]\n"
    "v_exp_f32 v240, v76\n"
    "v_mfma_f32_16x16x32_bf16 a[0:3], v[180:183], v[36:39], a[0:3]\n"
    "v_exp_f32 v241, v77\n"
    "v_mfma_f32_16x16x32_bf16 a[4:7], a[228:231], v[32:35], a[4:7]\n"
    "v_exp_f32 v242, v78\n"
    "v_mfma_f32_16x16x32_bf16 a[4:7], v[184:187], v[36:39], a[4:7]\n"
    "s_add_u32 m0, s34, 16992\n"
    "v_exp_f32 v243, v79\n"
    "v_mfma_f32_16x16x32_bf16 a[8:11], a[232:235], v[32:35], a[8:11]\n"
    "buffer_load_dwordx4 v1, s[24:27], s32 offen offset:3072  lds\n"
    "v_mfma_f32_16x16x32_bf16 a[8:11], v[188:191], v[36:39], a[8:11]\n"
    "v_cvt_pk_bf16_f32 v44, v8, v9\n"
    "v_cvt_pk_bf16_f32 v45, v10, v11\n"
    "v_mfma_f32_16x16x32_bf16 a[12:15], a[236:239], v[32:35], a[12:15]\n"
    "v_cvt_pk_bf16_f32 v46, v240, v241\n"
    "v_cvt_pk_bf16_f32 v47, v242, v243\n"
    "v_mfma_f32_16x16x32_bf16 a[12:15], v[192:195], v[36:39], a[12:15]\n"
    "s_waitcnt lgkmcnt(0)\n"
    "v_exp_f32 v8, v48\n"
    "v_mfma_f32_16x16x32_bf16 v[12:15], v[16:19], v[40:43], 0\n"
    "v_exp_f32 v9, v49\n"
    "v_mfma_f32_16x16x32_bf16 v[12:15], v[16:19], v[44:47], v[12:15]\n"
    "v_exp_f32 v10, v50\n"
    "v_mfma_f32_16x16x32_bf16 a[16:19], a[240:243], v[32:35], a[16:19]\n"
    "s_add_u32 s34, s36, s35\n"
    "v_exp_f32 v11, v51\n"
    "v_mfma_f32_16x16x32_bf16 a[16:19], v[196:199], v[36:39], a[16:19]\n"
    "v_exp_f32 v240, v52\n"
    "v_mfma_f32_16x16x32_bf16 a[20:23], a[244:247], v[32:35], a[20:23]\n"
    "s_add_u32 s36, s36, 33792\n"
    "v_exp_f32 v241, v53\n"
    "v_mfma_f32_16x16x32_bf16 a[20:23], v[200:203], v[36:39], a[20:23]\n"
    "v_exp_f32 v242, v54\n"
    "v_mfma_f32_16x16x32_bf16 a[24:27], a[248:251], v[32:35], a[24:27]\n"
    "s_sub_u32 s50, s36, 101376\n"
    "v_cmp_nge_f32 vcc, s40, v12\n"
    "v_mfma_f32_16x16x32_bf16 a[24:27], v[204:207], v[36:39], a[24:27]\n"
    "v_exp_f32 v243, v55\n"
    "v_mfma_f32_16x16x32_bf16 a[28:31], a[252:255], v[32:35], a[28:31]\n"
    "s_min_u32 s36, s36, s50\n"
    "v_cvt_pk_bf16_f32 v32, v8, v9\n"
    "v_cvt_pk_bf16_f32 v33, v10, v11\n"
    "v_mfma_f32_16x16x32_bf16 a[28:31], v[208:211], v[36:39], a[28:31]\n"
    "s_add_u32 s32, s32, 16384\n"
    "s_cbranch_vccnz LTR_53\n"
    "LRET_53:\n"
    "v_add_f32 v21, v21, v12\n"
    "v_mfma_f32_16x16x32_bf16 v[64:67], a[160:163], v[148:151], v[92:95]\n"
    "v_add_u32 v4, s36, v2\n"
    "v_mfma_f32_16x16x32_bf16 v[64:67], a[164:167], v[152:155], v[64:67]\n"
    "s_waitcnt vmcnt(10)\n"
    "s_barrier\n"
    "v_mfma_f32_16x16x32_bf16 v[64:67], a[168:171], v[156:159], v[64:67]\n"
    "v_exp_f32 v8, v56\n"
    "v_mfma_f32_16x16x32_bf16 v[64:67], a[172:175], v[160:163], v[64:67]\n"
    "v_exp_f32 v9, v57\n"
    "v_mfma_f32_16x16x32_bf16 v[68:71], a[176:179], v[148:151], v[92:95]\n"
    "v_exp_f32 v10, v58\n"
    "v_mfma_f32_16x16x32_bf16 v[68:71], a[180:183], v[152:155], v[68:71]\n"
    "ds_read_b128 a[160:163], v4 offset:0\n"
    "ds_read_b128 a[164:167], v4 offset:64\n"
    "v_mfma_f32_16x16x32_bf16 v[68:71], a[184:187], v[156:159], v[68:71]\n"
    "v_exp_f32 v11, v59\n"
    "v_mfma_f32_16x16x32_bf16 v[68:71], a[188:191], v[160:163], v[68:71]\n"
    "v_cvt_pk_bf16_f32 v34, v240, v241\n"
    "v_cvt_pk_bf16_f32 v35, v242, v243\n"
    "v_mfma_f32_16x16x32_bf16 v[72:75], a[192:195], v[148:151], v[92:95]\n"
    "ds_read_b128 a[168:171], v4 offset:128\n"
    "ds_read_b128 a[172:175], v4 offset:192\n"
    "v_mfma_f32_16x16x32_bf16 v[72:75], a[196:199], v[152:155], v[72:75]\n"
    "v_exp_f32 v240, v60\n"
    "v_mfma_f32_16x16x32_bf16 v[72:75], a[200:203], v[156:159], v[72:75]\n"
    "v_exp_f32 v241, v61\n"
    "v_mfma_f32_16x16x32_bf16 v[72:75], a[204:207], v[160:163], v[72:75]\n"
    "ds_read_b128 a[176:179], v4 offset:256\n"
    "ds_read_b128 a[180:183], v4 offset:320\n"
    "v_mfma_f32_16x16x32_bf16 v[76:79], a[208:211], v[148:151], v[92:95]\n"
    "v_exp_f32 v242, v62\n"
    "v_mfma_f32_16x16x32_bf16 v[76:79], a[212:215], v[152:155], v[76:79]\n"
    "v_exp_f32 v243, v63\n"
    "v_mfma_f32_16x16x32_bf16 v[76:79], a[216:219], v[156:159], v[76:79]\n"
    "ds_read_b128 a[184:187], v4 offset:384\n"
    "ds_read_b128 a[188:191], v4 offset:448\n"
    "v_mfma_f32_16x16x32_bf16 v[76:79], a[220:223], v[160:163], v[76:79]\n"
    "v_cvt_pk_bf16_f32 v36, v8, v9\n"
    "v_cvt_pk_bf16_f32 v37, v10, v11\n"
    "v_mfma_f32_16x16x32_bf16 a[32:35], a[224:227], v[40:43], a[32:35]\n"
    "v_cvt_pk_bf16_f32 v38, v240, v241\n"
    "v_cvt_pk_bf16_f32 v39, v242, v243\n"
    "v_mfma_f32_16x16x32_bf16 a[32:35], v[180:183], v[44:47], a[32:35]\n"
    "ds_read_b128 a[192:195], v4 offset:512\n"
    "ds_read_b128 a[196:199], v4 offset:576\n"
    "v_mfma_f32_16x16x32_bf16 v[12:15], v[16:19], v[32:35], 0\n"
    "v_exp_f32 v8, v64\n"
    "v_mfma_f32_16x16x32_bf16 v[12:15], v[16:19], v[36:39], v[12:15]\n"
    "ds_read_b128 a[200:203], v4 offset:640\n"
    "ds_read_b128 a[204:207], v4 offset:704\n"
    "v_mfma_f32_16x16x32_bf16 a[36:39], a[228:231], v[40:43], a[36:39]\n"
    "v_exp_f32 v9, v65\n"
    "v_mfma_f32_16x16x32_bf16 a[36:39], v[184:187], v[44:47], a[36:39]\n"
    "ds_read_b128 a[208:211], v4 offset:768\n"
    "ds_read_b128 a[212:215], v4 offset:832\n"
    "v_mfma_f32_16x16x32_bf16 a[40:43], a[232:235], v[40:43], a[40:43]\n"
    "v_exp_f32 v10, v66\n"
    "v_mfma_f32_16x16x32_bf16 a[40:43], v[188:191], v[44:47], a[40:43]\n"
    "ds_read_b128 a[216:219], v4 offset:896\n"
    "ds_read_b128 a[220:223], v4 offset:960\n"
    "v_mfma_f32_16x16x32_bf16 a[44:47], a[236:239], v[40:43], a[44:47]\n"
    "v_cmp_nge_f32 vcc, s40, v12\n"
    "v_mfma_f32_16x16x32_bf16 a[44:47], v[192:195], v[44:47], a[44:47]\n"
    "v_exp_f32 v11, v67\n"
    "v_mfma_f32_16x16x32_bf16 a[48:51], a[240:243], v[40:43], a[48:51]\n"
    "v_exp_f32 v240, v68\n"
    "v_mfma_f32_16x16x32_bf16 a[48:51], v[196:199], v[44:47], a[48:51]\n"
    "s_cbranch_vccnz LTR_54\n"
    "LRET_54:\n"
    "v_add_f32 v22, v22, v12\n"
    "v_mfma_f32_16x16x32_bf16 a[52:55], a[244:247], v[40:43], a[52:55]\n"
    "v_exp_f32 v241, v69\n"
    "v_mfma_f32_16x16x32_bf16 a[52:55], v[200:203], v[44:47], a[52:55]\n"
    "v_exp_f32 v242, v70\n"
    "v_mfma_f32_16x16x32_bf16 a[56:59], a[248:251], v[40:43], a[56:59]\n"
    "v_exp_f32 v243, v71\n"
    "v_mfma_f32_16x16x32_bf16 a[56:59], v[204:207], v[44:47], a[56:59]\n"
    "s_waitcnt lgkmcnt(15)\n"
    "buffer_store_dwordx4 v[220:223], v1, s[28:31], s61 offen offset:2048 nt\n"
    "v_mfma_f32_16x16x32_bf16 a[60:63], a[252:255], v[40:43], a[60:63]\n"
    "v_cvt_pk_bf16_f32 v40, v8, v9\n"
    "v_cvt_pk_bf16_f32 v41, v10, v11\n"
    "v_mfma_f32_16x16x32_bf16 a[60:63], v[208:211], v[44:47], a[60:63]\n"
    "s_waitcnt lgkmcnt(8)\n"
    "v_exp_f32 v8, v72\n"
    "v_mfma_f32_16x16x32_bf16 v[48:51], a[160:163], v[100:103], v[80:83]\n"
    "s_add_u32 m0, s34, 0\n"
    "v_exp_f32 v9, v73\n"
    "v_mfma_f32_16x16x32_bf16 v[48:51], a[164:167], v[104:107], v[48:51]\n"
    "buffer_load_dwordx4 v1, s[20:23], s32 offen offset:0  lds\n"
    "v_mfma_f32_16x16x32_bf16 v[48:51], a[168:171], v[108:111], v[48:51]\n"
    "ds_read_b128 v[224:227], v235\n"
    "v_mfma_f32_16x16x32_bf16 v[48:51], a[172:175], v[112:115], v[48:51]\n"
    "v_exp_f32 v10, v74\n"
    "v_mfma_f32_16x16x32_bf16 v[52:55], a[176:179], v[100:103], v[80:83]\n"
    "v_exp_f32 v11, v75\n"
    "v_mfma_f32_16x16x32_bf16 v[52:55], a[180:183], v[104:107], v[52:55]\n"
    "v_add_u32 v235, 4096, v235\n"
    "v_mfma_f32_16x16x32_bf16 v[52:55], a[184:187], v[108:111], v[52:55]\n"
    "s_add_u32 m0, s34, 32\n"
    "v_cvt_pk_bf16_f32 v42, v240, v241\n"
    "v_cvt_pk_bf16_f32 v43, v242, v243\n"
    "v_mfma_f32_16x16x32_bf16 v[52:55], a[188:191], v[112:115], v[52:55]\n"
    "s_waitcnt lgkmcnt(1)\n"
    "buffer_load_dwordx4 v1, s[20:23], s32 offen offset:1024  lds\n"
    "v_mfma_f32_16x16x32_bf16 v[56:59], a[192:195], v[100:103], v[80:83]\n"
    "v_exp_f32 v240, v76\n"
    "v_mfma_f32_16x16x32_bf16 v[56:59], a[196:199], v[104:107], v[56:59]\n"
    "v_exp_f32 v241, v77\n"
    "v_mfma_f32_16x16x32_bf16 v[56:59], a[200:203], v[108:111], v[56:59]\n"
    "v_exp_f32 v242, v78\n"
    "v_mfma_f32_16x16x32_bf16 v[56:59], a[204:207], v[112:115], v[56:59]\n"
    "v_exp_f32 v243, v79\n"
    "v_mfma_f32_16x16x32_bf16 v[60:63], a[208:211], v[100:103], v[80:83]\n"
    "s_add_u32 m0, s34, 64\n"
    "v_cvt_pk_bf16_f32 v44, v8, v9\n"
    "v_cvt_pk_bf16_f32 v45, v10, v11\n"
    "v_mfma_f32_16x16x32_bf16 v[60:63], a[212:215], v[104:107], v[60:63]\n"
    "buffer_load_dwordx4 v1, s[20:23], s32 offen offset:2048  lds\n"
    "v_mfma_f32_16x16x32_bf16 v[60:63], a[216:219], v[108:111], v[60:63]\n"
    "v_cvt_pk_bf16_f32 v46, v240, v241\n"
    "v_cvt_pk_bf16_f32 v47, v242, v243\n"
    "v_mfma_f32_16x16x32_bf16 v[60:63], a[220:223], v[112:115], v[60:63]\n"
    "v_exp_f32 v8, v48\n"
    "v_mfma_f32_16x16x32_bf16 v[12:15], v[16:19], v[40:43], 0\n"
    "v_exp_f32 v9, v49\n"
    "v_mfma_f32_16x16x32_bf16 v[12:15], v[16:19], v[44:47], v[12:15]\n"
    "v_exp_f32 v10, v50\n"
    "v_mfma_f32_16x16x32_bf16 a[64:67], a[224:227], v[32:35], a[64:67]\n"
    "v_exp_f32 v11, v51\n"
    "v_mfma_f32_16x16x32_bf16 a[64:67], v[180:183], v[36:39], a[64:67]\n"
    "v_exp_f32 v240, v52\n"
    "v_mfma_f32_16x16x32_bf16 a[68:71], a[228:231], v[32:35], a[68:71]\n"
    "s_add_u32 m0, s34, 96\n"
    "s_waitcnt lgkmcnt(0)\n"
    "buffer_store_dwordx4 v[224:227], v1, s[28:31], s61 offen offset:3072 nt\n"
    "v_mfma_f32_16x16x32_bf16 a[68:71], v[184:187], v[36:39], a[68:71]\n"
    "buffer_load_dwordx4 v1, s[20:23], s32 offen offset:3072  lds\n"
    "v_mfma_f32_16x16x32_bf16 a[72:75], a[232:235], v[32:35], a[72:75]\n"
    "v_cmp_nge_f32 vcc, s40, v12\n"
    "v_mfma_f32_16x16x32_bf16 a[72:75], v[188:191], v[36:39], a[72:75]\n"
    "v_exp_f32 v241, v53\n"
    "v_mfma_f32_16x16x32_bf16 a[76:79], a[236:239], v[32:35], a[76:79]\n"
    "s_add_u32 s61, s61, 4096\n"
    "v_mfma_f32_16x16x32_bf16 a[76:79], v[192:195], v[36:39], a[76:79]\n"
    "v_exp_f32 v242, v54\n"
    "v_mfma_f32_16x16x32_bf16 a[80:83], a[240:243], v[32:35], a[80:83]\n"
    "s_cbranch_vccnz LTR_55\n"
    "LRET_55:\n"
    "v_add_f32 v23, v23, v12\n"
    "v_mfma_f32_16x16x32_bf16 a[80:83], v[196:199], v[36:39], a[80:83]\n"
    "v_mfma_f32_16x16x32_bf16 a[84:87], a[244:247], v[32:35], a[84:87]\n"
    "v_mfma_f32_16x16x32_bf16 a[84:87], v[200:203], v[36:39], a[84:87]\n"
    "v_mfma_f32_16x16x32_bf16 a[88:91], a[248:251], v[32:35], a[88:91]\n"
    "v_mfma_f32_16x16x32_bf16 a[88:91], v[204:207], v[36:39], a[88:91]\n"
    "v_mfma_f32_16x16x32_bf16 a[92:95], a[252:255], v[32:35], a[92:95]\n"
    "v_mfma_f32_16x16x32_bf16 a[92:95], v[208:211], v[36:39], a[92:95]\n"
    "s_sub_u32 s37, s37, 1\n"
    "s_cmp_lg_u32 s37, 0\n"
    "s_cbranch_scc1 LBH4\n"
    "s_mov_b32 s37, 4\n"
    "LLOOP4:\n"
    "v_mfma_f32_16x16x32_bf16 v[64:67], a[160:163], v[116:119], v[84:87]\n"
    "v_exp_f32 v243, v55\n"
    "v_mfma_f32_16x16x32_bf16 v[64:67], a[164:167], v[120:123], v[64:67]\n"
    "v_cvt_pk_bf16_f32 v32, v8, v9\n"
    "v_cvt_pk_bf16_f32 v33, v10, v11\n"
    "v_mfma_f32_16x16x32_bf16 v[64:67], a[168:171], v[124:127], v[64:67]\n"
    "v_exp_f32 v8, v56\n"
    "v_mfma_f32_16x16x32_bf16 v[64:67], a[172:175], v[128:131], v[64:67]\n"
    "v_exp_f32 v9, v57\n"
    "v_mfma_f32_16x16x32_bf16 v[68:71], a[176:179], v[116:119], v[84:87]\n"
    "v_add_u32 v5, s36, v3\n"
    "v_mfma_f32_16x16x32_bf16 v[68:71], a[180:183], v[120:123], v[68:71]\n"
    "v_exp_f32 v10, v58\n"
    "v_mfma_f32_16x16x32_bf16 v[68:71], a[184:187], v[124:127], v[68:71]\n"
    "v_exp_f32 v11, v59\n"
    "v_mfma_f32_16x16x32_bf16 v[68:71], a[188:191], v[128:131], v[68:71]\n"
    "v_cvt_pk_bf16_f32 v34, v240, v241\n"
    "v_cvt_pk_bf16_f32 v35, v242, v243\n"
    "v_mfma_f32_16x16x32_bf16 v[72:75], a[192:195], v[116:119], v[84:87]\n"
    "v_exp_f32 v240, v60\n"
    "v_mfma_f32_16x16x32_bf16 v[72:75], a[196:199], v[120:123], v[72:75]\n"
    "v_exp_f32 v241, v61\n"
    "v_mfma_f32_16x16x32_bf16 v[72:75], a[200:203], v[124:127], v[72:75]\n"
    "v_exp_f32 v242, v62\n"
    "v_mfma_f32_16x16x32_bf16 v[72:75], a[204:207], v[128:131], v[72:75]\n"
    "v_exp_f32 v243, v63\n"
    "v_mfma_f32_16x16x32_bf16 v[76:79], a[208:211], v[116:119], v[84:87]\n"
    "v_cvt_pk_bf16_f32 v36, v8, v9\n"
    "v_cvt_pk_bf16_f32 v37, v10, v11\n"
    "v_mfma_f32_16x16x32_bf16 v[76:79], a[212:215], v[120:123], v[76:79]\n"
    "v_cvt_pk_bf16_f32 v38, v240, v241\n"
    "v_cvt_pk_bf16_f32 v39, v242, v243\n"
    "v_mfma_f32_16x16x32_bf16 v[76:79], a[216:219], v[124:127], v[76:79]\n"
    "v_exp_f32 v8, v64\n"
    "v_mfma_f32_16x16x32_bf16 v[76:79], a[220:223], v[128:131], v[76:79]\n"
    "v_exp_f32 v9, v65\n"
    "v_mfma_f32_16x16x32_bf16 v[12:15], v[16:19], v[32:35], 0\n"
    "v_exp_f32 v10, v66\n"
    "v_mfma_f32_16x16x32_bf16 v[12:15], v[16:19], v[36:39], v[12:15]\n"
    "v_exp_f32 v11, v67\n"
    "v_mfma_f32_16x16x32_bf16 a[96:99], a[224:227], v[40:43], a[96:99]\n"
    "ds_read_b64_tr_b16 a[224:225], v5 offset:0\n"
    "ds_read_b64_tr_b16 a[226:227], v5 offset:256\n"
    "v_mfma_f32_16x16x32_bf16 a[96:99], v[180:183], v[44:47], a[96:99]\n"
    "v_exp_f32 v240, v68\n"
    "v_mfma_f32_16x16x32_bf16 a[100:103], a[228:231], v[40:43], a[100:103]\n"
    "ds_read_b64_tr_b16 v[180:181], v5 offset:512\n"
    "ds_read_b64_tr_b16 v[182:183], v5 offset:768\n"
    "v_mfma_f32_16x16x32_bf16 a[100:103], v[184:187], v[44:47], a[100:103]\n"
    "v_exp_f32 v241, v69\n"
    "v_mfma_f32_16x16x32_bf16 a[104:107], a[232:235], v[40:43], a[104:107]\n"
    "ds_read_b64_tr_b16 a[228:229], v5 offset:32\n"
    "ds_read_b64_tr_b16 a[230:231], v5 offset:288\n"
    "v_mfma_f32_16x16x32_bf16 a[104:107], v[188:191], v[44:47], a[104:107]\n"
    "v_cmp_nge_f32 vcc, s40, v12\n"
    "v_mfma_f32_16x16x32_bf16 a[108:111], a[236:239], v[40:43], a[108:111]\n"
    "ds_read_b64_tr_b16 v[184:185], v5 offset:544\n"
    "ds_read_b64_tr_b16 v[186:187], v5 offset:800\n"
    "v_mfma_f32_16x16x32_bf16 a[108:111], v[192:195], v[44:47], a[108:111]\n"
    "v_exp_f32 v242, v70\n"
    "v_mfma_f32_16x16x32_bf16 a[112:115], a[240:243], v[40:43], a[112:115]\n"
    "ds_read_b64_tr_b16 a[232:233], v5 offset:64\n"
    "ds_read_b64_tr_b16 a[234:235], v5 offset:320\n"
    "v_mfma_f32_16x16x32_bf16 a[112:115], v[196:199], v[44:47], a[112:115]\n"
    "s_cbranch_vccnz LTR_56\n"
    "LRET_56:\n"
    "v_add_f32 v20, v20, v12\n"
    "v_mfma_f32_16x16x32_bf16 a[116:119], a[244:247], v[40:43], a[116:119]\n"
    "ds_read_b64_tr_b16 v[188:189], v5 offset:576\n"
    "ds_read_b64_tr_b16 v[190:191], v5 offset:832\n"
    "v_mfma_f32_16x16x32_bf16 a[116:119], v[200:203], v[44:47], a[116:119]\n"
    "v_exp_f32 v243, v71\n"
    "v_mfma_f32_16x16x32_bf16 a[120:123], a[248:251], v[40:43], a[120:123]\n"
    "ds_read_b64_tr_b16 a[236:237], v5 offset:96\n"
    "ds_read_b64_tr_b16 a[238:239], v5 offset:352\n"
    "v_mfma_f32_16x16x32_bf16 a[120:123], v[204:207], v[44:47], a[120:123]\n"
    "v_mfma_f32_16x16x32_bf16 a[124:127], a[252:255], v[40:43], a[124:127]\n"
    "ds_read_b64_tr_b16 v[192:193], v5 offset:608\n"
    "ds_read_b64_tr_b16 v[194:195], v5 offset:864\n"
    "v_mfma_f32_16x16x32_bf16 a[124:127], v[208:211], v[44:47], a[124:127]\n"
    "v_cvt_pk_bf16_f32 v40, v8, v9\n"
    "v_cvt_pk_bf16_f32 v41, v10, v11\n"
    "v_mfma_f32_16x16x32_bf16 v[48:51], a[160:163], v[132:135], v[88:91]\n"
    "s_add_u32 m0, s34, 16896\n"
    "ds_read_b64_tr_b16 a[240:241], v5 offset:128\n"
    "ds_read_b64_tr_b16 a[242:243], v5 offset:384\n"
    "v_mfma_f32_16x16x32_bf16 v[48:51], a[164:167], v[136:139], v[48:51]\n"
    "buffer_load_dwordx4 v1, s[24:27], s32 offen offset:0  lds\n"
    "v_mfma_f32_16x16x32_bf16 v[48:51], a[168:171], v[140:143], v[48:51]\n"
    "ds_read_b64_tr_b16 v[196:197], v5 offset:640\n"
    "ds_read_b64_tr_b16 v[198:199], v5 offset:896\n"
    "v_mfma_f32_16x16x32_bf16 v[48:51], a[172:175], v[144:147], v[48:51]\n"
    "v_exp_f32 v8, v72\n"
    "v_mfma_f32_16x16x32_bf16 v[52:55], a[176:179], v[132:135], v[88:91]\n"
    "ds_read_b64_tr_b16 a[244:245], v5 offset:160\n"
    "ds_read_b64_tr_b16 a[246:247], v5 offset:416\n"
    "v_mfma_f32_16x16x32_bf16 v[52:55], a[180:183], v[136:139], v[52:55]\n"
    "v_exp_f32 v9, v73\n"
    "v_mfma_f32_16x16x32_bf16 v[52:55], a[184:187], v[140:143], v[52:55]\n"
    "s_add_u32 m0, s34, 16928\n"
    "ds_read_b64_tr_b16 v[200:201], v5 offset:672\n"
    "ds_read_b64_tr_b16 v[202:203], v5 offset:928\n"
    "v_mfma_f32_16x16x32_bf16 v[52:55], a[188:191], v[144:147], v[52:55]\n"
    "buffer_load_dwordx4 v1, s[24:27], s32 offen offset:1024  lds\n"
    "v_mfma_f32_16x16x32_bf16 v[56:59], a[192:195], v[132:135], v[88:91]\n"
    "ds_read_b64_tr_b16 a[248:249], v5 offset:192\n"
    "ds_read_b64_tr_b16 a[250:251], v5 offset:448\n"
    "v_mfma_f32_16x16x32_bf16 v[56:59], a[196:199], v[136:139], v[56:59]\n"
    "v_exp_f32 v10, v74\n"
    "v_mfma_f32_16x16x32_bf16 v[56:59], a[200:203], v[140:143], v[56:59]\n"
    "ds_read_b64_tr_b16 v[204:205], v5 offset:704\n"
    "ds_read_b64_tr_b16 v[206:207], v5 offset:960\n"
    "v_mfma_f32_16x16x32_bf16 v[56:59], a[204:207], v[144:147], v[56:59]\n"
    "v_exp_f32 v11, v75\n"
    "v_mfma_f32_16x16x32_bf16 v[60:63], a[208:211], v[132:135], v[88:91]\n"
    "ds_read_b64_tr_b16 a[252:253], v5 offset:224\n"
    "ds_read_b64_tr_b16 a[254:255], v5 offset:480\n"
    "v_mfma_f32_16x16x32_bf16 v[60:63], a[212:215], v[136:139], v[60:63]\n"
    "v_cvt_pk_bf16_f32 v42, v240, v241\n"
    "v_cvt_pk_bf16_f32 v43, v242, v243\n"
    "v_mfma_f32_16x16x32_bf16 v[60:63], a[216:219], v[140:143], v[60:63]\n"
    "s_add_u32 m0, s34, 16960\n"
    "ds_read_b64_tr_b16 v[208:209], v5 offset:736\n"
    "ds_read_b64_tr_b16 v[210:211], v5 offset:992\n"
    "v_mfma_f32_16x16x32_bf16 v[60:63], a[220:223], v[144:147], v[60:63]\n"
    "s_waitcnt lgkmcnt(15)\n"
    "buffer_load_dwordx4 v1, s[24:27], s32 offen offset:2048  lds\n"
    "v_mfma_f32_16x16x32_bf16 a[0:3], a[224:227], v[32:35], a[0:3]\n"
    "v_exp_f32 v240, v76\n"
    "v_mfma_f32_16x16x32_bf16 a[0:3], v[180:183], v[36:39], a[0:3]\n"
    "v_exp_f32 v241, v77\n"
    "v_mfma_f32_16x16x32_bf16 a[4:7], a[228:231], v[32:35], a[4:7]\n"
    "v_exp_f32 v242, v78\n"
    "v_mfma_f32_16x16x32_bf16 a[4:7], v[184:187], v[36:39], a[4:7]\n"
    "s_add_u32 m0, s34, 16992\n"
    "v_exp_f32 v243, v79\n"
    "v_mfma_f32_16x16x32_bf16 a[8:11], a[232:235], v[32:35], a[8:11]\n"
    "buffer_load_dwordx4 v1, s[24:27], s32 offen offset:3072  lds\n"
    "v_mfma_f32_16x16x32_bf16 a[8:11], v[188:191], v[36:39], a[8:11]\n"
    "v_cvt_pk_bf16_f32 v44, v8, v9\n"
    "v_cvt_pk_bf16_f32 v45, v10, v11\n"
    "v_mfma_f32_16x16x32_bf16 a[12:15], a[236:239], v[32:35], a[12:15]\n"
    "v_cvt_pk_bf16_f32 v46, v240, v241\n"
    "v_cvt_pk_bf16_f32 v47, v242, v243\n"
    "v_mfma_f32_16x16x32_bf16 a[12:15], v[192:195], v[36:39], a[12:15]\n"
    "s_waitcnt lgkmcnt(0)\n"
    "v_exp_f32 v8, v48\n"
    "v_mfma_f32_16x16x32_bf16 v[12:15], v[16:19], v[40:43], 0\n"
    "v_exp_f32 v9, v49\n"
    "v_mfma_f32_16x16x32_bf16 v[12:15], v[16:19], v[44:47], v[12:15]\n"
    "v_exp_f32 v10, v50\n"
    "v_mfma_f32_16x16x32_bf16 a[16:19], a[240:243], v[32:35], a[16:19]\n"
    "s_add_u32 s34, s36, s35\n"
    "v_exp_f32 v11, v51\n"
    "v_mfma_f32_16x16x32_bf16 a[16:19], v[196:199], v[36:39], a[16:19]\n"
    "v_exp_f32 v240, v52\n"
    "v_mfma_f32_16x16x32_bf16 a[20:23], a[244:247], v[32:35], a[20:23]\n"
    "s_add_u32 s36, s36, 33792\n"
    "v_exp_f32 v241, v53\n"
    "v_mfma_f32_16x16x32_bf16 a[20:23], v[200:203], v[36:39], a[20:23]\n"
    "v_exp_f32 v242, v54\n"
    "v_mfma_f32_16x16x32_bf16 a[24:27], a[248:251], v[32:35], a[24:27]\n"
    "s_sub_u32 s50, s36, 101376\n"
    "v_cmp_nge_f32 vcc, s40, v12\n"
    "v_mfma_f32_16x16x32_bf16 a[24:27], v[204:207], v[36:39], a[24:27]\n"
    "v_exp_f32 v243, v55\n"
    "v_mfma_f32_16x16x32_bf16 a[28:31], a[252:255], v[32:35], a[28:31]\n"
    "s_min_u32 s36, s36, s50\n"
    "v_cvt_pk_bf16_f32 v32, v8, v9\n"
    "v_cvt_pk_bf16_f32 v33, v10, v11\n"
    "v_mfma_f32_16x16x32_bf16 a[28:31], v[208:211], v[36:39], a[28:31]\n"
    "s_add_u32 s32, s32, 16384\n"
    "s_cbranch_vccnz LTR_57\n"
    "LRET_57:\n"
    "v_add_f32 v21, v21, v12\n"
    "v_mfma_f32_16x16x32_bf16 v[64:67], a[160:163], v[148:151], v[92:95]\n"
    "v_add_u32 v4, s36, v2\n"
    "v_mfma_f32_16x16x32_bf16 v[64:67], a[164:167], v[152:155], v[64:67]\n"
    "s_waitcnt vmcnt(8)\n"
    "s_barrier\n"
    "v_mfma_f32_16x16x32_bf16 v[64:67], a[168:171], v[156:159], v[64:67]\n"
    "v_exp_f32 v8, v56\n"
    "v_mfma_f32_16x16x32_bf16 v[64:67], a[172:175], v[160:163], v[64:67]\n"
    "v_exp_f32 v9, v57\n"
    "v_mfma_f32_16x16x32_bf16 v[68:71], a[176:179], v[148:151], v[92:95]\n"
    "v_exp_f32 v10, v58\n"
    "v_mfma_f32_16x16x32_bf16 v[68:71], a[180:183], v[152:155], v[68:71]\n"
    "ds_read_b128 a[160:163], v4 offset:0\n"
    "ds_read_b128 a[164:167], v4 offset:64\n"
    "v_mfma_f32_16x16x32_bf16 v[68:71], a[184:187], v[156:159], v[68:71]\n"
    "v_exp_f32 v11, v59\n"
    "v_mfma_f32_16x16x32_bf16 v[68:71], a[188:191], v[160:163], v[68:71]\n"
    "v_cvt_pk_bf16_f32 v34, v240, v241\n"
    "v_cvt_pk_bf16_f32 v35, v242, v243\n"
    "v_mfma_f32_16x16x32_bf16 v[72:75], a[192:195], v[148:151], v[92:95]\n"
    "ds_read_b128 a[168:171], v4 offset:128\n"
    "ds_read_b128 a[172:175], v4 offset:192\n"
    "v_mfma_f32_16x16x32_bf16 v[72:75], a[196:199], v[152:155], v[72:75]\n"
    "v_exp_f32 v240, v60\n"
    "v_mfma_f32_16x16x32_bf16 v[72:75], a[200:203], v[156:159], v[72:75]\n"
    "v_exp_f32 v241, v61\n"
    "v_mfma_f32_16x16x32_bf16 v[72:75], a[204:207], v[160:163], v[72:75]\n"
    "ds_read_b128 a[176:179], v4 offset:256\n"
    "ds_read_b128 a[180:183], v4 offset:320\n"
    "v_mfma_f32_16x16x32_bf16 v[76:79], a[208:211], v[148:151], v[92:95]\n"
    "v_exp_f32 v242, v62\n"
    "v_mfma_f32_16x16x32_bf16 v[76:79], a[212:215], v[152:155], v[76:79]\n"
    "v_exp_f32 v243, v63\n"
    "v_mfma_f32_16x16x32_bf16 v[76:79], a[216:219], v[156:159], v[76:79]\n"
    "ds_read_b128 a[184:187], v4 offset:384\n"
    "ds_read_b128 a[188:191], v4 offset:448\n"
    "v_mfma_f32_16x16x32_bf16 v[76:79], a[220:223], v[160:163], v[76:79]\n"
    "v_cvt_pk_bf16_f32 v36, v8, v9\n"
    "v_cvt_pk_bf16_f32 v37, v10, v11\n"
    "v_mfma_f32_16x16x32_bf16 a[32:35], a[224:227], v[40:43], a[32:35]\n"
    "v_cvt_pk_bf16_f32 v38, v240, v241\n"
    "v_cvt_pk_bf16_f32 v39, v242, v243\n"
    "v_mfma_f32_16x16x32_bf16 a[32:35], v[180:183], v[44:47], a[32:35]\n"
    "ds_read_b128 a[192:195], v4 offset:512\n"
    "ds_read_b128 a[196:199], v4 offset:576\n"
    "v_mfma_f32_16x16x32_bf16 v[12:15], v[16:19], v[32:35], 0\n"
    "v_exp_f32 v8, v64\n"
    "v_mfma_f32_16x16x32_bf16 v[12:15], v[16:19], v[36:39], v[12:15]\n"
    "ds_read_b128 a[200:203], v4 offset:640\n"
    "ds_read_b128 a[204:207], v4 offset:704\n"
    "v_mfma_f32_16x16x32_bf16 a[36:39], a[228:231], v[40:43], a[36:39]\n"
    "v_exp_f32 v9, v65\n"
    "v_mfma_f32_16x16x32_bf16 a[36:39], v[184:187], v[44:47], a[36:39]\n"
    "ds_read_b128 a[208:211], v4 offset:768\n"
    "ds_read_b128 a[212:215], v4 offset:832\n"
    "v_mfma_f32_16x16x32_bf16 a[40:43], a[232:235], v[40:43], a[40:43]\n"
    "v_exp_f32 v10, v66\n"
    "v_mfma_f32_16x16x32_bf16 a[40:43], v[188:191], v[44:47], a[40:43]\n"
    "ds_read_b128 a[216:219], v4 offset:896\n"
    "ds_read_b128 a[220:223], v4 offset:960\n"
    "v_mfma_f32_16x16x32_bf16 a[44:47], a[236:239], v[40:43], a[44:47]\n"
    "v_cmp_nge_f32 vcc, s40, v12\n"
    "v_mfma_f32_16x16x32_bf16 a[44:47], v[192:195], v[44:47], a[44:47]\n"
    "v_exp_f32 v11, v67\n"
    "v_mfma_f32_16x16x32_bf16 a[48:51], a[240:243], v[40:43], a[48:51]\n"
    "v_exp_f32 v240, v68\n"
    "v_mfma_f32_16x16x32_bf16 a[48:51], v[196:199], v[44:47], a[48:51]\n"
    "s_cbranch_vccnz LTR_58\n"
    "LRET_58:\n"
    "v_add_f32 v22, v22, v12\n"
    "v_mfma_f32_16x16x32_bf16 a[52:55], a[244:247], v[40:43], a[52:55]\n"
    "v_exp_f32 v241, v69\n"
    "v_mfma_f32_16x16x32_bf16 a[52:55], v[200:203], v[44:47], a[52:55]\n"
    "v_exp_f32 v242, v70\n"
    "v_mfma_f32_16x16x32_bf16 a[56:59], a[248:251], v[40:43], a[56:59]\n"
    "v_exp_f32 v243, v71\n"
    "v_mfma_f32_16x16x32_bf16 a[56:59], v[204:207], v[44:47], a[56:59]\n"
    "v_mfma_f32_16x16x32_bf16 a[60:63], a[252:255], v[40:43], a[60:63]\n"
    "v_cvt_pk_bf16_f32 v40, v8, v9\n"
    "v_cvt_pk_bf16_f32 v41, v10, v11\n"
    "v_mfma_f32_16x16x32_bf16 a[60:63], v[208:211], v[44:47], a[60:63]\n"
    "s_waitcnt lgkmcnt(8)\n"
    "v_exp_f32 v8, v72\n"
    "v_mfma_f32_16x16x32_bf16 v[48:51], a[160:163], v[100:103], v[80:83]\n"
    "s_add_u32 m0, s34, 0\n"
    "v_exp_f32 v9, v73\n"
    "v_mfma_f32_16x16x32_bf16 v[48:51], a[164:167], v[104:107], v[48:51]\n"
    "buffer_load_dwordx4 v1, s[20:23], s32 offen offset:0  lds\n"
    "v_mfma_f32_16x16x32_bf16 v[48:51], a[168:171], v[108:111], v[48:51]\n"
    "v_exp_f32 v10, v74\n"
    "v_mfma_f32_16x16x32_bf16 v[48:51], a[172:175], v[112:115], v[48:51]\n"
    "v_exp_f32 v11, v75\n"
    "v_mfma_f32_16x16x32_bf16 v[52:55], a[176:179], v[100:103], v[80:83]\n"
    "v_cvt_pk_bf16_f32 v42, v240, v241\n"
    "v_cvt_pk_bf16_f32 v43, v242, v243\n"
    "v_mfma_f32_16x16x32_bf16 v[52:55], a[180:183], v[104:107], v[52:55]\n"
    "v_exp_f32 v240, v76\n"
    "v_mfma_f32_16x16x32_bf16 v[52:55], a[184:187], v[108:111], v[52:55]\n"
    "s_add_u32 m0, s34, 32\n"
    "v_exp_f32 v241, v77\n"
    "v_mfma_f32_16x16x32_bf16 v[52:55], a[188:191], v[112:115], v[52:55]\n"
    "s_waitcnt lgkmcnt(0)\n"
    "buffer_load_dwordx4 v1, s[20:23], s32 offen offset:1024  lds\n"
    "v_mfma_f32_16x16x32_bf16 v[56:59], a[192:195], v[100:103], v[80:83]\n"
    "v_exp_f32 v242, v78\n"
    "v_mfma_f32_16x16x32_bf16 v[56:59], a[196:199], v[104:107], v[56:59]\n"
    "v_exp_f32 v243, v79\n"
    "v_mfma_f32_16x16x32_bf16 v[56:59], a[200:203], v[108:111], v[56:59]\n"
    "v_cvt_pk_bf16_f32 v44, v8, v9\n"
    "v_cvt_pk_bf16_f32 v45, v10, v11\n"
    "v_mfma_f32_16x16x32_bf16 v[56:59], a[204:207], v[112:115], v[56:59]\n"
    "v_cvt_pk_bf16_f32 v46, v240, v241\n"
    "v_cvt_pk_bf16_f32 v47, v242, v243\n"
    "v_mfma_f32_16x16x32_bf16 v[12:15], v[16:19], v[40:43], 0\n"
    "v_exp_f32 v8, v48\n"
    "v_mfma_f32_16x16x32_bf16 v[12:15], v[16:19], v[44:47], v[12:15]\n"
    "v_exp_f32 v9, v49\n"
    "v_mfma_f32_16x16x32_bf16 v[60:63], a[208:211], v[100:103], v[80:83]\n"
    "s_add_u32 m0, s34, 64\n"
    "v_exp_f32 v10, v50\n"
    "v_mfma_f32_16x16x32_bf16 v[60:63], a[212:215], v[104:107], v[60:63]\n"
    "buffer_load_dwordx4 v1, s[20:23], s32 offen offset:2048  lds\n"
    "v_mfma_f32_16x16x32_bf16 v[60:63], a[216:219], v[108:111], v[60:63]\n"
    "v_exp_f32 v11, v51\n"
    "v_mfma_f32_16x16x32_bf16 v[60:63], a[220:223], v[112:115], v[60:63]\n"
    "v_exp_f32 v240, v52\n"
    "v_mfma_f32_16x16x32_bf16 a[64:67], a[224:227], v[32:35], a[64:67]\n"
    "v_cmp_nge_f32 vcc, s40, v12\n"
    "v_mfma_f32_16x16x32_bf16 a[64:67], v[180:183], v[36:39], a[64:67]\n"
    "v_exp_f32 v241, v53\n"
    "v_mfma_f32_16x16x32_bf16 a[68:71], a[228:231], v[32:35], a[68:71]\n"
    "s_add_u32 m0, s34, 96\n"
    "v_exp_f32 v242, v54\n"
    "v_mfma_f32_16x16x32_bf16 a[68:71], v[184:187], v[36:39], a[68:71]\n"
    "buffer_load_dwordx4 v1, s[20:23], s32 offen offset:3072  lds\n"
    "v_mfma_f32_16x16x32_bf16 a[72:75], a[232:235], v[32:35], a[72:75]\n"
    "s_cbranch_vccnz LTR_59\n"
    "LRET_59:\n"
    "v_add_f32 v23, v23, v12\n"
    "v_mfma_f32_16x16x32_bf16 a[72:75], v[188:191], v[36:39], a[72:75]\n"
    "v_exp_f32 v243, v55\n"
    "v_mfma_f32_16x16x32_bf16 a[76:79], a[236:239], v[32:35], a[76:79]\n"
    "v_mfma_f32_16x16x32_bf16 a[76:79], v[192:195], v[36:39], a[76:79]\n"
    "v_mfma_f32_16x16x32_bf16 a[80:83], a[240:243], v[32:35], a[80:83]\n"
    "v_mfma_f32_16x16x32_bf16 a[80:83], v[196:199], v[36:39], a[80:83]\n"
    "v_mfma_f32_16x16x32_bf16 a[84:87], a[244:247], v[32:35], a[84:87]\n"
    "v_mfma_f32_16x16x32_bf16 a[84:87], v[200:203], v[36:39], a[84:87]\n"
    "v_mfma_f32_16x16x32_bf16 a[88:91], a[248:251], v[32:35], a[88:91]\n"
    "v_mfma_f32_16x16x32_bf16 a[88:91], v[204:207], v[36:39], a[88:91]\n"
    "v_mfma_f32_16x16x32_bf16 a[92:95], a[252:255], v[32:35], a[92:95]\n"
    "v_cvt_pk_bf16_f32 v32, v8, v9\n"
    "v_cvt_pk_bf16_f32 v33, v10, v11\n"
    "v_mfma_f32_16x16x32_bf16 a[92:95], v[208:211], v[36:39], a[92:95]\n"
    "v_exp_f32 v8, v56\n"
    "v_mfma_f32_16x16x32_bf16 v[64:67], a[160:163], v[116:119], v[84:87]\n"
    "v_exp_f32 v9, v57\n"
    "v_mfma_f32_16x16x32_bf16 v[64:67], a[164:167], v[120:123], v[64:67]\n"
    "v_add_u32 v5, s36, v3\n"
    "v_mfma_f32_16x16x32_bf16 v[64:67], a[168:171], v[124:127], v[64:67]\n"
    "v_exp_f32 v10, v58\n"
    "v_mfma_f32_16x16x32_bf16 v[64:67], a[172:175], v[128:131], v[64:67]\n"
    "v_exp_f32 v11, v59\n"
    "v_mfma_f32_16x16x32_bf16 v[68:71], a[176:179], v[116:119], v[84:87]\n"
    "v_cvt_pk_bf16_f32 v34, v240, v241\n"
    "v_cvt_pk_bf16_f32 v35, v242, v243\n"
    "v_mfma_f32_16x16x32_bf16 v[68:71], a[180:183], v[120:123], v[68:71]\n"
    "v_exp_f32 v240, v60\n"
    "v_mfma_f32_16x16x32_bf16 v[68:71], a[184:187], v[124:127], v[68:71]\n"
    "v_exp_f32 v241, v61\n"
    "v_mfma_f32_16x16x32_bf16 v[68:71], a[188:191], v[128:131], v[68:71]\n"
    "v_exp_f32 v242, v62\n"
    "v_mfma_f32_16x16x32_bf16 v[72:75], a[192:195], v[116:119], v[84:87]\n"
    "v_exp_f32 v243, v63\n"
    "v_mfma_f32_16x16x32_bf16 v[72:75], a[196:199], v[120:123], v[72:75]\n"
    "v_cvt_pk_bf16_f32 v36, v8, v9\n"
    "v_cvt_pk_bf16_f32 v37, v10, v11\n"
    "v_mfma_f32_16x16x32_bf16 v[72:75], a[200:203], v[124:127], v[72:75]\n"
    "v_cvt_pk_bf16_f32 v38, v240, v241\n"
    "v_cvt_pk_bf16_f32 v39, v242, v243\n"
    "v_mfma_f32_16x16x32_bf16 v[72:75], a[204:207], v[128:131], v[72:75]\n"
    "v_exp_f32 v8, v64\n"
    "v_mfma_f32_16x16x32_bf16 v[12:15], v[16:19], v[32:35], 0\n"
    "v_exp_f32 v9, v65\n"
    "v_mfma_f32_16x16x32_bf16 v[12:15], v[16:19], v[36:39], v[12:15]\n"
    "v_exp_f32 v10, v66\n"
    "v_mfma_f32_16x16x32_bf16 v[76:79], a[208:211], v[116:119], v[84:87]\n"
    "v_exp_f32 v11, v67\n"
    "v_mfma_f32_16x16x32_bf16 v[76:79], a[212:215], v[120:123], v[76:79]\n"
    "v_exp_f32 v240, v68\n"
    "v_mfma_f32_16x16x32_bf16 v[76:79], a[216:219], v[124:127], v[76:79]\n"
    "v_exp_f32 v241, v69\n"
    "v_mfma_f32_16x16x32_bf16 v[76:79], a[220:223], v[128:131], v[76:79]\n"
    "v_exp_f32 v242, v70\n"
    "v_mfma_f32_16x16x32_bf16 a[96:99], a[224:227], v[40:43], a[96:99]\n"
    "ds_read_b64_tr_b16 a[224:225], v5 offset:0\n"
    "ds_read_b64_tr_b16 a[226:227], v5 offset:256\n"
    "v_mfma_f32_16x16x32_bf16 a[96:99], v[180:183], v[44:47], a[96:99]\n"
    "v_cmp_nge_f32 vcc, s40, v12\n"
    "v_mfma_f32_16x16x32_bf16 a[100:103], a[228:231], v[40:43], a[100:103]\n"
    "ds_read_b64_tr_b16 v[180:181], v5 offset:512\n"
    "ds_read_b64_tr_b16 v[182:183], v5 offset:768\n"
    "v_mfma_f32_16x16x32_bf16 a[100:103], v[184:187], v[44:47], a[100:103]\n"
    "v_exp_f32 v243, v71\n"
    "v_mfma_f32_16x16x32_bf16 a[104:107], a[232:235], v[40:43], a[104:107]\n"
    "ds_read_b64_tr_b16 a[228:229], v5 offset:32\n"
    "ds_read_b64_tr_b16 a[230:231], v5 offset:288\n"
    "v_mfma_f32_16x16x32_bf16 a[104:107], v[188:191], v[44:47], a[104:107]\n"
    "s_cbranch_vccnz LTR_60\n"
    "LRET_60:\n"
    "v_add_f32 v20, v20, v12\n"
    "v_mfma_f32_16x16x32_bf16 a[108:111], a[236:239], v[40:43], a[108:111]\n"
    "ds_read_b64_tr_b16 v[184:185], v5 offset:544\n"
    "ds_read_b64_tr_b16 v[186:187], v5 offset:800\n"
    "v_mfma_f32_16x16x32_bf16 a[108:111], v[192:195], v[44:47], a[108:111]\n"
    "v_mfma_f32_16x16x32_bf16 a[112:115], a[240:243], v[40:43], a[112:115]\n"
    "ds_read_b64_tr_b16 a[232:233], v5 offset:64\n"
    "ds_read_b64_tr_b16 a[234:235], v5 offset:320\n"
    "v_mfma_f32_16x16x32_bf16 a[112:115], v[196:199], v[44:47], a[112:115]\n"
    "v_mfma_f32_16x16x32_bf16 a[116:119], a[244:247], v[40:43], a[116:119]\n"
    "ds_read_b64_tr_b16 v[188:189], v5 offset:576\n"
    "ds_read_b64_tr_b16 v[190:191], v5 offset:832\n"
    "v_mfma_f32_16x16x32_bf16 a[116:119], v[200:203], v[44:47], a[116:119]\n"
    "v_mfma_f32_16x16x32_bf16 a[120:123], a[248:251], v[40:43], a[120:123]\n"
    "ds_read_b64_tr_b16 a[236:237], v5 offset:96\n"
    "ds_read_b64_tr_b16 a[238:239], v5 offset:352\n"
    "v_mfma_f32_16x16x32_bf16 a[120:123], v[204:207], v[44:47], a[120:123]\n"
    "v_mfma_f32_16x16x32_bf16 a[124:127], a[252:255], v[40:43], a[124:127]\n"
    "ds_read_b64_tr_b16 v[192:193], v5 offset:608\n"
    "ds_read_b64_tr_b16 v[194:195], v5 offset:864\n"
    "v_mfma_f32_16x16x32_bf16 a[124:127], v[208:211], v[44:47], a[124:127]\n"
    "v_cvt_pk_bf16_f32 v40, v8, v9\n"
    "v_cvt_pk_bf16_f32 v41, v10, v11\n"
    "v_mfma_f32_16x16x32_bf16 v[48:51], a[160:163], v[132:135], v[88:91]\n"
    "s_add_u32 m0, s34, 16896\n"
    "ds_read_b64_tr_b16 a[240:241], v5 offset:128\n"
    "ds_read_b64_tr_b16 a[242:243], v5 offset:384\n"
    "v_mfma_f32_16x16x32_bf16 v[48:51], a[164:167], v[136:139], v[48:51]\n"
    "buffer_load_dwordx4 v1, s[24:27], s32 offen offset:0  lds\n"
    "v_mfma_f32_16x16x32_bf16 v[48:51], a[168:171], v[140:143], v[48:51]\n"
    "ds_read_b64_tr_b16 v[196:197], v5 offset:640\n"
    "ds_read_b64_tr_b16 v[198:199], v5 offset:896\n"
    "v_mfma_f32_16x16x32_bf16 v[48:51], a[172:175], v[144:147], v[48:51]\n"
    "v_exp_f32 v8, v72\n"
    "v_mfma_f32_16x16x32_bf16 v[52:55], a[176:179], v[132:135], v[88:91]\n"
    "ds_read_b64_tr_b16 a[244:245], v5 offset:160\n"
    "ds_read_b64_tr_b16 a[246:247], v5 offset:416\n"
    "v_mfma_f32_16x16x32_bf16 v[52:55], a[180:183], v[136:139], v[52:55]\n"
    "v_exp_f32 v9, v73\n"
    "v_mfma_f32_16x16x32_bf16 v[52:55], a[184:187], v[140:143], v[52:55]\n"
    "s_add_u32 m0, s34, 16928\n"
    "ds_read_b64_tr_b16 v[200:201], v5 offset:672\n"
    "ds_read_b64_tr_b16 v[202:203], v5 offset:928\n"
    "v_mfma_f32_16x16x32_bf16 v[52:55], a[188:191], v[144:147], v[52:55]\n"
    "buffer_load_dwordx4 v1, s[24:27], s32 offen offset:1024  lds\n"
    "v_mfma_f32_16x16x32_bf16 v[56:59], a[192:195], v[132:135], v[88:91]\n"
    "ds_read_b64_tr_b16 a[248:249], v5 offset:192\n"
    "ds_read_b64_tr_b16 a[250:251], v5 offset:448\n"
    "v_mfma_f32_16x16x32_bf16 v[56:59], a[196:199], v[136:139], v[56:59]\n"
    "v_exp_f32 v10, v74\n"
    "v_mfma_f32_16x16x32_bf16 v[56:59], a[200:203], v[140:143], v[56:59]\n"
    "ds_read_b64_tr_b16 v[204:205], v5 offset:704\n"
    "ds_read_b64_tr_b16 v[206:207], v5 offset:960\n"
    "v_mfma_f32_16x16x32_bf16 v[56:59], a[204:207], v[144:147], v[56:59]\n"
    "v_exp_f32 v11, v75\n"
    "v_mfma_f32_16x16x32_bf16 v[60:63], a[208:211], v[132:135], v[88:91]\n"
    "ds_read_b64_tr_b16 a[252:253], v5 offset:224\n"
    "ds_read_b64_tr_b16 a[254:255], v5 offset:480\n"
    "v_mfma_f32_16x16x32_bf16 v[60:63], a[212:215], v[136:139], v[60:63]\n"
    "v_cvt_pk_bf16_f32 v42, v240, v241\n"
    "v_cvt_pk_bf16_f32 v43, v242, v243\n"
    "v_mfma_f32_16x16x32_bf16 v[60:63], a[216:219], v[140:143], v[60:63]\n"
    "s_add_u32 m0, s34, 16960\n"
    "ds_read_b64_tr_b16 v[208:209], v5 offset:736\n"
    "ds_read_b64_tr_b16 v[210:211], v5 offset:992\n"
    "v_mfma_f32_16x16x32_bf16 v[60:63], a[220:223], v[144:147], v[60:63]\n"
    "s_waitcnt lgkmcnt(15)\n"
    "buffer_load_dwordx4 v1, s[24:27], s32 offen offset:2048  lds\n"
    "v_mfma_f32_16x16x32_bf16 a[0:3], a[224:227], v[32:35], a[0:3]\n"
    "v_exp_f32 v240, v76\n"
    "v_mfma_f32_16x16x32_bf16 a[0:3], v[180:183], v[36:39], a[0:3]\n"
    "v_exp_f32 v241, v77\n"
    "v_mfma_f32_16x16x32_bf16 a[4:7], a[228:231], v[32:35], a[4:7]\n"
    "v_exp_f32 v242, v78\n"
    "v_mfma_f32_16x16x32_bf16 a[4:7], v[184:187], v[36:39], a[4:7]\n"
    "s_add_u32 m0, s34, 16992\n"
    "v_exp_f32 v243, v79\n"
    "v_mfma_f32_16x16x32_bf16 a[8:11], a[232:235], v[32:35], a[8:11]\n"
    "buffer_load_dwordx4 v1, s[24:27], s32 offen offset:3072  lds\n"
    "v_mfma_f32_16x16x32_bf16 a[8:11], v[188:191], v[36:39], a[8:11]\n"
    "v_cvt_pk_bf16_f32 v44, v8, v9\n"
    "v_cvt_pk_bf16_f32 v45, v10, v11\n"
    "v_mfma_f32_16x16x32_bf16 a[12:15], a[236:239], v[32:35], a[12:15]\n"
    "v_cvt_pk_bf16_f32 v46, v240, v241\n"
    "v_cvt_pk_bf16_f32 v47, v242, v243\n"
    "v_mfma_f32_16x16x32_bf16 a[12:15], v[192:195], v[36:39], a[12:15]\n"
    "s_waitcnt lgkmcnt(0)\n"
    "v_exp_f32 v8, v48\n"
    "v_mfma_f32_16x16x32_bf16 v[12:15], v[16:19], v[40:43], 0\n"
    "v_exp_f32 v9, v49\n"
    "v_mfma_f32_16x16x32_bf16 v[12:15], v[16:19], v[44:47], v[12:15]\n"
    "v_exp_f32 v10, v50\n"
    "v_mfma_f32_16x16x32_bf16 a[16:19], a[240:243], v[32:35], a[16:19]\n"
    "s_add_u32 s34, s36, s35\n"
    "v_exp_f32 v11, v51\n"
    "v_mfma_f32_16x16x32_bf16 a[16:19], v[196:199], v[36:39], a[16:19]\n"
    "v_exp_f32 v240, v52\n"
    "v_mfma_f32_16x16x32_bf16 a[20:23], a[244:247], v[32:35], a[20:23]\n"
    "s_add_u32 s36, s36, 33792\n"
    "v_exp_f32 v241, v53\n"
    "v_mfma_f32_16x16x32_bf16 a[20:23], v[200:203], v[36:39], a[20:23]\n"
    "v_exp_f32 v242, v54\n"
    "v_mfma_f32_16x16x32_bf16 a[24:27], a[248:251], v[32:35], a[24:27]\n"
    "s_sub_u32 s50, s36, 101376\n"
    "v_cmp_nge_f32 vcc, s40, v12\n"
    "v_mfma_f32_16x16x32_bf16 a[24:27], v[204:207], v[36:39], a[24:27]\n"
    "v_exp_f32 v243, v55\n"
    "v_mfma_f32_16x16x32_bf16 a[28:31], a[252:255], v[32:35], a[28:31]\n"
    "s_min_u32 s36, s36, s50\n"
    "v_cvt_pk_bf16_f32 v32, v8, v9\n"
    "v_cvt_pk_bf16_f32 v33, v10, v11\n"
    "v_mfma_f32_16x16x32_bf16 a[28:31], v[208:211], v[36:39], a[28:31]\n"
    "s_add_u32 s32, s32, 16384\n"
    "s_cbranch_vccnz LTR_61\n"
    "LRET_61:\n"
    "v_add_f32 v21, v21, v12\n"
    "v_mfma_f32_16x16x32_bf16 v[64:67], a[160:163], v[148:151], v[92:95]\n"
    "v_add_u32 v4, s36, v2\n"
    "v_mfma_f32_16x16x32_bf16 v[64:67], a[164:167], v[152:155], v[64:67]\n"
    "s_waitcnt vmcnt(8)\n"
    "s_barrier\n"
    "v_mfma_f32_16x16x32_bf16 v[64:67], a[168:171], v[156:159], v[64:67]\n"
    "v_exp_f32 v8, v56\n"
    "v_mfma_f32_16x16x32_bf16 v[64:67], a[172:175], v[160:163], v[64:67]\n"
    "v_exp_f32 v9, v57\n"
    "v_mfma_f32_16x16x32_bf16 v[68:71], a[176:179], v[148:151], v[92:95]\n"
    "v_exp_f32 v10, v58\n"
    "v_mfma_f32_16x16x32_bf16 v[68:71], a[180:183], v[152:155], v[68:71]\n"
    "ds_read_b128 a[160:163], v4 offset:0\n"
    "ds_read_b128 a[164:167], v4 offset:64\n"
    "v_mfma_f32_16x16x32_bf16 v[68:71], a[184:187], v[156:159], v[68:71]\n"
    "v_exp_f32 v11, v59\n"
    "v_mfma_f32_16x16x32_bf16 v[68:71], a[188:191], v[160:163], v[68:71]\n"
    "v_cvt_pk_bf16_f32 v34, v240, v241\n"
    "v_cvt_pk_bf16_f32 v35, v242, v243\n"
    "v_mfma_f32_16x16x32_bf16 v[72:75], a[192:195], v[148:151], v[92:95]\n"
    "ds_read_b128 a[168:171], v4 offset:128\n"
    "ds_read_b128 a[172:175], v4 offset:192\n"
    "v_mfma_f32_16x16x32_bf16 v[72:75], a[196:199], v[152:155], v[72:75]\n"
    "v_exp_f32 v240, v60\n"
    "v_mfma_f32_16x16x32_bf16 v[72:75], a[200:203], v[156:159], v[72:75]\n"
    "v_exp_f32 v241, v61\n"
    "v_mfma_f32_16x16x32_bf16 v[72:75], a[204:207], v[160:163], v[72:75]\n"
    "ds_read_b128 a[176:179], v4 offset:256\n"
    "ds_read_b128 a[180:183], v4 offset:320\n"
    "v_mfma_f32_16x16x32_bf16 v[76:79], a[208:211], v[148:151], v[92:95]\n"
    "v_exp_f32 v242, v62\n"
    "v_mfma_f32_16x16x32_bf16 v[76:79], a[212:215], v[152:155], v[76:79]\n"
    "v_exp_f32 v243, v63\n"
    "v_mfma_f32_16x16x32_bf16 v[76:79], a[216:219], v[156:159], v[76:79]\n"
    "ds_read_b128 a[184:187], v4 offset:384\n"
    "ds_read_b128 a[188:191], v4 offset:448\n"
    "v_mfma_f32_16x16x32_bf16 v[76:79], a[220:223], v[160:163], v[76:79]\n"
    "v_cvt_pk_bf16_f32 v36, v8, v9\n"
    "v_cvt_pk_bf16_f32 v37, v10, v11\n"
    "v_mfma_f32_16x16x32_bf16 a[32:35], a[224:227], v[40:43], a[32:35]\n"
    "v_cvt_pk_bf16_f32 v38, v240, v241\n"
    "v_cvt_pk_bf16_f32 v39, v242, v243\n"
    "v_mfma_f32_16x16x32_bf16 a[32:35], v[180:183], v[44:47], a[32:35]\n"
    "ds_read_b128 a[192:195], v4 offset:512\n"
    "ds_read_b128 a[196:199], v4 offset:576\n"
    "v_mfma_f32_16x16x32_bf16 v[12:15], v[16:19], v[32:35], 0\n"
    "v_exp_f32 v8, v64\n"
    "v_mfma_f32_16x16x32_bf16 v[12:15], v[16:19], v[36:39], v[12:15]\n"
    "ds_read_b128 a[200:203], v4 offset:640\n"
    "ds_read_b128 a[204:207], v4 offset:704\n"
    "v_mfma_f32_16x16x32_bf16 a[36:39], a[228:231], v[40:43], a[36:39]\n"
    "v_exp_f32 v9, v65\n"
    "v_mfma_f32_16x16x32_bf16 a[36:39], v[184:187], v[44:47], a[36:39]\n"
    "ds_read_b128 a[208:211], v4 offset:768\n"
    "ds_read_b128 a[212:215], v4 offset:832\n"
    "v_mfma_f32_16x16x32_bf16 a[40:43], a[232:235], v[40:43], a[40:43]\n"
    "v_exp_f32 v10, v66\n"
    "v_mfma_f32_16x16x32_bf16 a[40:43], v[188:191], v[44:47], a[40:43]\n"
    "ds_read_b128 a[216:219], v4 offset:896\n"
    "ds_read_b128 a[220:223], v4 offset:960\n"
    "v_mfma_f32_16x16x32_bf16 a[44:47], a[236:239], v[40:43], a[44:47]\n"
    "v_cmp_nge_f32 vcc, s40, v12\n"
    "v_mfma_f32_16x16x32_bf16 a[44:47], v[192:195], v[44:47], a[44:47]\n"
    "v_exp_f32 v11, v67\n"
    "v_mfma_f32_16x16x32_bf16 a[48:51], a[240:243], v[40:43], a[48:51]\n"
    "v_exp_f32 v240, v68\n"
    "v_mfma_f32_16x16x32_bf16 a[48:51], v[196:199], v[44:47], a[48:51]\n"
    "s_cbranch_vccnz LTR_62\n"
    "LRET_62:\n"
    "v_add_f32 v22, v22, v12\n"
    "v_mfma_f32_16x16x32_bf16 a[52:55], a[244:247], v[40:43], a[52:55]\n"
    "v_exp_f32 v241, v69\n"
    "v_mfma_f32_16x16x32_bf16 a[52:55], v[200:203], v[44:47], a[52:55]\n"
    "v_exp_f32 v242, v70\n"
    "v_mfma_f32_16x16x32_bf16 a[56:59], a[248:251], v[40:43], a[56:59]\n"
    "v_exp_f32 v243, v71\n"
    "v_mfma_f32_16x16x32_bf16 a[56:59], v[204:207], v[44:47], a[56:59]\n"
    "v_mfma_f32_16x16x32_bf16 a[60:63], a[252:255], v[40:43], a[60:63]\n"
    "v_cvt_pk_bf16_f32 v40, v8, v9\n"
    "v_cvt_pk_bf16_f32 v41, v10, v11\n"
    "v_mfma_f32_16x16x32_bf16 a[60:63], v[208:211], v[44:47], a[60:63]\n"
    "s_waitcnt lgkmcnt(8)\n"
    "v_exp_f32 v8, v72\n"
    "v_mfma_f32_16x16x32_bf16 v[48:51], a[160:163], v[100:103], v[80:83]\n"
    "s_add_u32 m0, s34, 0\n"
    "v_exp_f32 v9, v73\n"
    "v_mfma_f32_16x16x32_bf16 v[48:51], a[164:167], v[104:107], v[48:51]\n"
    "buffer_load_dwordx4 v1, s[20:23], s32 offen offset:0  lds\n"
    "v_mfma_f32_16x16x32_bf16 v[48:51], a[168:171], v[108:111], v[48:51]\n"
    "v_exp_f32 v10, v74\n"
    "v_mfma_f32_16x16x32_bf16 v[48:51], a[172:175], v[112:115], v[48:51]\n"
    "v_exp_f32 v11, v75\n"
    "v_mfma_f32_16x16x32_bf16 v[52:55], a[176:179], v[100:103], v[80:83]\n"
    "v_cvt_pk_bf16_f32 v42, v240, v241\n"
    "v_cvt_pk_bf16_f32 v43, v242, v243\n"
    "v_mfma_f32_16x16x32_bf16 v[52:55], a[180:183], v[104:107], v[52:55]\n"
    "v_exp_f32 v240, v76\n"
    "v_mfma_f32_16x16x32_bf16 v[52:55], a[184:187], v[108:111], v[52:55]\n"
    "s_add_u32 m0, s34, 32\n"
    "v_exp_f32 v241, v77\n"
    "v_mfma_f32_16x16x32_bf16 v[52:55], a[188:191], v[112:115], v[52:55]\n"
    "s_waitcnt lgkmcnt(0)\n"
    "buffer_load_dwordx4 v1, s[20:23], s32 offen offset:1024  lds\n"
    "v_mfma_f32_16x16x32_bf16 v[56:59], a[192:195], v[100:103], v[80:83]\n"
    "v_exp_f32 v242, v78\n"
    "v_mfma_f32_16x16x32_bf16 v[56:59], a[196:199], v[104:107], v[56:59]\n"
    "v_exp_f32 v243, v79\n"
    "v_mfma_f32_16x16x32_bf16 v[56:59], a[200:203], v[108:111], v[56:59]\n"
    "v_cvt_pk_bf16_f32 v44, v8, v9\n"
    "v_cvt_pk_bf16_f32 v45, v10, v11\n"
    "v_mfma_f32_16x16x32_bf16 v[56:59], a[204:207], v[112:115], v[56:59]\n"
    "v_cvt_pk_bf16_f32 v46, v240, v241\n"
    "v_cvt_pk_bf16_f32 v47, v242, v243\n"
    "v_mfma_f32_16x16x32_bf16 v[12:15], v[16:19], v[40:43], 0\n"
    "v_exp_f32 v8, v48\n"
    "v_mfma_f32_16x16x32_bf16 v[12:15], v[16:19], v[44:47], v[12:15]\n"
    "v_exp_f32 v9, v49\n"
    "v_mfma_f32_16x16x32_bf16 v[60:63], a[208:211], v[100:103], v[80:83]\n"
    "s_add_u32 m0, s34, 64\n"
    "v_exp_f32 v10, v50\n"
    "v_mfma_f32_16x16x32_bf16 v[60:63], a[212:215], v[104:107], v[60:63]\n"
    "buffer_load_dwordx4 v1, s[20:23], s32 offen offset:2048  lds\n"
    "v_mfma_f32_16x16x32_bf16 v[60:63], a[216:219], v[108:111], v[60:63]\n"
    "v_exp_f32 v11, v51\n"
    "v_mfma_f32_16x16x32_bf16 v[60:63], a[220:223], v[112:115], v[60:63]\n"
    "v_exp_f32 v240, v52\n"
    "v_mfma_f32_16x16x32_bf16 a[64:67], a[224:227], v[32:35], a[64:67]\n"
    "v_cmp_nge_f32 vcc, s40, v12\n"
    "v_mfma_f32_16x16x32_bf16 a[64:67], v[180:183], v[36:39], a[64:67]\n"
    "v_exp_f32 v241, v53\n"
    "v_mfma_f32_16x16x32_bf16 a[68:71], a[228:231], v[32:35], a[68:71]\n"
    "s_add_u32 m0, s34, 96\n"
    "v_exp_f32 v242, v54\n"
    "v_mfma_f32_16x16x32_bf16 a[68:71], v[184:187], v[36:39], a[68:71]\n"
    "buffer_load_dwordx4 v1, s[20:23], s32 offen offset:3072  lds\n"
    "v_mfma_f32_16x16x32_bf16 a[72:75], a[232:235], v[32:35], a[72:75]\n"
    "s_cbranch_vccnz LTR_63\n"
    "LRET_63:\n"
    "v_add_f32 v23, v23, v12\n"
    "v_mfma_f32_16x16x32_bf16 a[72:75], v[188:191], v[36:39], a[72:75]\n"
    "v_mfma_f32_16x16x32_bf16 a[76:79], a[236:239], v[32:35], a[76:79]\n"
    "v_mfma_f32_16x16x32_bf16 a[76:79], v[192:195], v[36:39], a[76:79]\n"
    "v_mfma_f32_16x16x32_bf16 a[80:83], a[240:243], v[32:35], a[80:83]\n"
    "v_mfma_f32_16x16x32_bf16 a[80:83], v[196:199], v[36:39], a[80:83]\n"
    "v_mfma_f32_16x16x32_bf16 a[84:87], a[244:247], v[32:35], a[84:87]\n"
    "v_mfma_f32_16x16x32_bf16 a[84:87], v[200:203], v[36:39], a[84:87]\n"
    "v_mfma_f32_16x16x32_bf16 a[88:91], a[248:251], v[32:35], a[88:91]\n"
    "v_mfma_f32_16x16x32_bf16 a[88:91], v[204:207], v[36:39], a[88:91]\n"
    "v_mfma_f32_16x16x32_bf16 a[92:95], a[252:255], v[32:35], a[92:95]\n"
    "v_mfma_f32_16x16x32_bf16 a[92:95], v[208:211], v[36:39], a[92:95]\n"
    "s_sub_u32 s37, s37, 1\n"
    "s_cmp_lg_u32 s37, 0\n"
    "s_cbranch_scc1 LLOOP4\n"
    "s_cmp_eq_u32 s38, 1\n"
    "s_cbranch_scc1 LFINAL4\n"
    "v_mfma_f32_16x16x32_bf16 v[64:67], a[160:163], v[116:119], v[84:87]\n"
    "v_exp_f32 v243, v55\n"
    "v_mfma_f32_16x16x32_bf16 v[64:67], a[164:167], v[120:123], v[64:67]\n"
    "v_cvt_pk_bf16_f32 v32, v8, v9\n"
    "v_cvt_pk_bf16_f32 v33, v10, v11\n"
    "v_mfma_f32_16x16x32_bf16 v[64:67], a[168:171], v[124:127], v[64:67]\n"
    "v_exp_f32 v8, v56\n"
    "v_mfma_f32_16x16x32_bf16 v[64:67], a[172:175], v[128:131], v[64:67]\n"
    "v_exp_f32 v9, v57\n"
    "v_mfma_f32_16x16x32_bf16 v[68:71], a[176:179], v[116:119], v[84:87]\n"
    "v_add_u32 v5, s36, v3\n"
    "v_mfma_f32_16x16x32_bf16 v[68:71], a[180:183], v[120:123], v[68:71]\n"
    "v_exp_f32 v10, v58\n"
    "v_mfma_f32_16x16x32_bf16 v[68:71], a[184:187], v[124:127], v[68:71]\n"
    "v_exp_f32 v11, v59\n"
    "v_mfma_f32_16x16x32_bf16 v[68:71], a[188:191], v[128:131], v[68:71]\n"
    "v_cvt_pk_bf16_f32 v34, v240, v241\n"
    "v_cvt_pk_bf16_f32 v35, v242, v243\n"
    "v_mfma_f32_16x16x32_bf16 v[72:75], a[192:195], v[116:119], v[84:87]\n"
    "v_exp_f32 v240, v60\n"
    "v_mfma_f32_16x16x32_bf16 v[72:75], a[196:199], v[120:123], v[72:75]\n"
    "v_exp_f32 v241, v61\n"
    "v_mfma_f32_16x16x32_bf16 v[72:75], a[200:203], v[124:127], v[72:75]\n"
    "v_exp_f32 v242, v62\n"
    "v_mfma_f32_16x16x32_bf16 v[72:75], a[204:207], v[128:131], v[72:75]\n"
    "v_exp_f32 v243, v63\n"
    "v_mfma_f32_16x16x32_bf16 v[76:79], a[208:211], v[116:119], v[84:87]\n"
    "v_cvt_pk_bf16_f32 v36, v8, v9\n"
    "v_cvt_pk_bf16_f32 v37, v10, v11\n"
    "v_mfma_f32_16x16x32_bf16 v[76:79], a[212:215], v[120:123], v[76:79]\n"
    "v_cvt_pk_bf16_f32 v38, v240, v241\n"
    "v_cvt_pk_bf16_f32 v39, v242, v243\n"
    "v_mfma_f32_16x16x32_bf16 v[76:79], a[216:219], v[124:127], v[76:79]\n"
    "s_waitcnt vmcnt(63)\n"
    "v_lshlrev_b32 v31, 16, v212\n"
    "v_and_b32 v255, s42, v212\n"
    "v_mfma_f32_16x16x32_bf16 v[76:79], a[220:223], v[128:131], v[76:79]\n"
    "v_mul_f32 v31, s41, v31\n"
    "v_mul_f32 v255, s41, v255\n"
    "v_mfma_f32_16x16x32_bf16 v[12:15], v[16:19], v[32:35], 0\n"
    "v_cvt_pk_bf16_f32 v100, v31, v255\n"
    "v_lshlrev_b32 v31, 16, v213\n"
    "v_mfma_f32_16x16x32_bf16 v[12:15], v[16:19], v[36:39], v[12:15]\n"
    "v_and_b32 v255, s42, v213\n"
    "v_mul_f32 v31, s41, v31\n"
    "v_mfma_f32_16x16x32_bf16 a[96:99], a[224:227], v[40:43], a[96:99]\n"
    "ds_read_b64_tr_b16 a[224:225], v5 offset:0\n"
    "ds_read_b64_tr_b16 a[226:227], v5 offset:256\n"
    "v_mfma_f32_16x16x32_bf16 a[96:99], v[180:183], v[44:47], a[96:99]\n"
    "v_mul_f32 v255, s41, v255\n"
    "v_cvt_pk_bf16_f32 v101, v31, v255\n"
    "v_mfma_f32_16x16x32_bf16 a[100:103], a[228:231], v[40:43], a[100:103]\n"
    "ds_read_b64_tr_b16 v[180:181], v5 offset:512\n"
    "ds_read_b64_tr_b16 v[182:183], v5 offset:768\n"
    "v_mfma_f32_16x16x32_bf16 a[100:103], v[184:187], v[44:47], a[100:103]\n"
    "v_lshlrev_b32 v31, 16, v214\n"
    "v_and_b32 v255, s42, v214\n"
    "v_mfma_f32_16x16x32_bf16 a[104:107], a[232:235], v[40:43], a[104:107]\n"
    "ds_read_b64_tr_b16 a[228:229], v5 offset:32\n"
    "ds_read_b64_tr_b16 a[230:231], v5 offset:288\n"
    "v_mfma_f32_16x16x32_bf16 a[104:107], v[188:191], v[44:47], a[104:107]\n"
    "v_cmp_nge_f32 vcc, s40, v12\n"
    "v_mfma_f32_16x16x32_bf16 a[108:111], a[236:239], v[40:43], a[108:111]\n"
    "ds_read_b64_tr_b16 v[184:185], v5 offset:544\n"
    "ds_read_b64_tr_b16 v[186:187], v5 offset:800\n"
    "v_mfma_f32_16x16x32_bf16 a[108:111], v[192:195], v[44:47], a[108:111]\n"
    "ds_read_b128 v[116:119], v229 offset:0\n"
    "ds_read_b128 v[120:123], v229 offset:1024\n"
    "v_mfma_f32_16x16x32_bf16 a[112:115], a[240:243], v[40:43], a[112:115]\n"
    "ds_read_b64_tr_b16 a[232:233], v5 offset:64\n"
    "ds_read_b64_tr_b16 a[234:235], v5 offset:320\n"
    "v_mfma_f32_16x16x32_bf16 a[112:115], v[196:199], v[44:47], a[112:115]\n"
    "v_mul_f32 v31, s41, v31\n"
    "v_mul_f32 v255, s41, v255\n"
    "v_mfma_f32_16x16x32_bf16 a[116:119], a[244:247], v[40:43], a[116:119]\n"
    "ds_read_b64_tr_b16 v[188:189], v5 offset:576\n"
    "ds_read_b64_tr_b16 v[190:191], v5 offset:832\n"
    "v_mfma_f32_16x16x32_bf16 a[116:119], v[200:203], v[44:47], a[116:119]\n"
    "s_cbranch_vccnz LTR_64\n"
    "LRET_64:\n"
    "v_add_f32 v20, v20, v12\n"
    "v_mfma_f32_16x16x32_bf16 a[120:123], a[248:251], v[40:43], a[120:123]\n"
    "ds_read_b64_tr_b16 a[236:237], v5 offset:96\n"
    "ds_read_b64_tr_b16 a[238:239], v5 offset:352\n"
    "v_mfma_f32_16x16x32_bf16 a[120:123], v[204:207], v[44:47], a[120:123]\n"
    "v_cvt_pk_bf16_f32 v102, v31, v255\n"
    "v_lshlrev_b32 v31, 16, v215\n"
    "v_mfma_f32_16x16x32_bf16 a[124:127], a[252:255], v[40:43], a[124:127]\n"
    "ds_read_b64_tr_b16 v[192:193], v5 offset:608\n"
    "ds_read_b64_tr_b16 v[194:195], v5 offset:864\n"
    "v_mfma_f32_16x16x32_bf16 a[124:127], v[208:211], v[44:47], a[124:127]\n"
    "ds_read_b128 v[124:127], v229 offset:2048\n"
    "ds_read_b128 v[128:131], v229 offset:3072\n"
    "v_mfma_f32_16x16x32_bf16 v[48:51], a[160:163], v[132:135], v[88:91]\n"
    "ds_read_b64_tr_b16 a[240:241], v5 offset:128\n"
    "ds_read_b64_tr_b16 a[242:243], v5 offset:384\n"
    "v_and_b32 v255, s42, v215\n"
    "v_mul_f32 v31, s41, v31\n"
    "v_mul_f32 v255, s41, v255\n"
    "v_cvt_pk_bf16_f32 v103, v31, v255\n"
    "v_mfma_f32_16x16x32_bf16 v[48:51], a[164:167], v[136:139], v[48:51]\n"
    "v_lshlrev_b32 v31, 16, v216\n"
    "v_and_b32 v255, s42, v216\n"
    "v_mul_f32 v31, s41, v31\n"
    "v_mul_f32 v255, s41, v255\n"
    "v_cvt_pk_bf16_f32 v104, v31, v255\n"
    "v_lshlrev_b32 v31, 16, v217\n"
    "v_mfma_f32_16x16x32_bf16 v[48:51], a[168:171], v[140:143], v[48:51]\n"
    "ds_read_b64_tr_b16 v[196:197], v5 offset:640\n"
    "ds_read_b64_tr_b16 v[198:199], v5 offset:896\n"
    "v_exp_f32 v8, v64\n"
    "v_and_b32 v255, s42, v217\n"
    "v_mul_f32 v31, s41, v31\n"
    "v_mfma_f32_16x16x32_bf16 v[48:51], a[172:175], v[144:147], v[48:51]\n"
    "v_exp_f32 v9, v65\n"
    "v_mul_f32 v255, s41, v255\n"
    "v_cvt_pk_bf16_f32 v105, v31, v255\n"
    "v_exp_f32 v10, v66\n"
    "v_mfma_f32_16x16x32_bf16 v[52:55], a[176:179], v[132:135], v[88:91]\n"
    "s_add_u32 m0, s34, 16896\n"
    "ds_read_b64_tr_b16 a[244:245], v5 offset:160\n"
    "ds_read_b64_tr_b16 a[246:247], v5 offset:416\n"
    "v_lshlrev_b32 v31, 16, v218\n"
    "v_and_b32 v255, s42, v218\n"
    "v_exp_f32 v11, v67\n"
    "v_mfma_f32_16x16x32_bf16 v[52:55], a[180:183], v[136:139], v[52:55]\n"
    "buffer_load_dwordx4 v1, s[24:27], s32 offen offset:0  lds\n"
    "v_mul_f32 v31, s41, v31\n"
    "v_mul_f32 v255, s41, v255\n"
    "v_exp_f32 v240, v68\n"
    "v_mfma_f32_16x16x32_bf16 v[52:55], a[184:187], v[140:143], v[52:55]\n"
    "ds_read_b64_tr_b16 v[200:201], v5 offset:672\n"
    "ds_read_b64_tr_b16 v[202:203], v5 offset:928\n"
    "v_cvt_pk_bf16_f32 v106, v31, v255\n"
    "v_lshlrev_b32 v31, 16, v219\n"
    "v_exp_f32 v241, v69\n"
    "v_mfma_f32_16x16x32_bf16 v[52:55], a[188:191], v[144:147], v[52:55]\n"
    "v_and_b32 v255, s42, v219\n"
    "v_mul_f32 v31, s41, v31\n"
    "v_exp_f32 v242, v70\n"
    "v_mul_f32 v255, s41, v255\n"
    "v_cvt_pk_bf16_f32 v107, v31, v255\n"
    "v_mfma_f32_16x16x32_bf16 v[56:59], a[192:195], v[132:135], v[88:91]\n"
    "s_add_u32 m0, s34, 16928\n"
    "ds_read_b64_tr_b16 a[248:249], v5 offset:192\n"
    "ds_read_b64_tr_b16 a[250:251], v5 offset:448\n"
    "v_exp_f32 v243, v71\n"
    "v_lshlrev_b32 v31, 16, v220\n"
    "v_and_b32 v255, s42, v220\n"
    "v_mfma_f32_16x16x32_bf16 v[56:59], a[196:199], v[136:139], v[56:59]\n"
    "buffer_load_dwordx4 v1, s[24:27], s32 offen offset:1024  lds\n"
    "v_cvt_pk_bf16_f32 v40, v8, v9\n"
    "v_cvt_pk_bf16_f32 v41, v10, v11\n"
    "v_mul_f32 v31, s41, v31\n"
    "v_mul_f32 v255, s41, v255\n"
    "v_mfma_f32_16x16x32_bf16 v[56:59], a[200:203], v[140:143], v[56:59]\n"
    "ds_read_b64_tr_b16 v[204:205], v5 offset:704\n"
    "ds_read_b64_tr_b16 v[206:207], v5 offset:960\n"
    "v_exp_f32 v8, v72\n"
    "v_cvt_pk_bf16_f32 v108, v31, v255\n"
    "v_lshlrev_b32 v31, 16, v221\n"
    "v_mfma_f32_16x16x32_bf16 v[56:59], a[204:207], v[144:147], v[56:59]\n"
    "v_exp_f32 v9, v73\n"
    "v_and_b32 v255, s42, v221\n"
    "v_mul_f32 v31, s41, v31\n"
    "v_exp_f32 v10, v74\n"
    "v_mfma_f32_16x16x32_bf16 v[60:63], a[208:211], v[132:135], v[88:91]\n"
    "ds_read_b64_tr_b16 a[252:253], v5 offset:224\n"
    "ds_read_b64_tr_b16 a[254:255], v5 offset:480\n"
    "v_mul_f32 v255, s41, v255\n"
    "v_cvt_pk_bf16_f32 v109, v31, v255\n"
    "v_exp_f32 v11, v75\n"
    "v_mfma_f32_16x16x32_bf16 v[60:63], a[212:215], v[136:139], v[60:63]\n"
    "s_add_u32 m0, s34, 16960\n"
    "v_lshlrev_b32 v31, 16, v222\n"
    "v_and_b32 v255, s42, v222\n"
    "v_cvt_pk_bf16_f32 v42, v240, v241\n"
    "v_cvt_pk_bf16_f32 v43, v242, v243\n"
    "v_mul_f32 v31, s41, v31\n"
    "v_mul_f32 v255, s41, v255\n"
    "v_mfma_f32_16x16x32_bf16 v[60:63], a[216:219], v[140:143], v[60:63]\n"
    "ds_read_b64_tr_b16 v[208:209], v5 offset:736\n"
    "ds_read_b64_tr_b16 v[210:211], v5 offset:992\n"
    "buffer_load_dwordx4 v1, s[24:27], s32 offen offset:2048  lds\n"
    "v_exp_f32 v240, v76\n"
    "v_mfma_f32_16x16x32_bf16 v[60:63], a[220:223], v[144:147], v[60:63]\n"
    "s_waitcnt lgkmcnt(15)\n"
    "v_cvt_pk_bf16_f32 v110, v31, v255\n"
    "v_lshlrev_b32 v31, 16, v223\n"
    "v_exp_f32 v241, v77\n"
    "v_and_b32 v255, s42, v223\n"
    "v_mul_f32 v31, s41, v31\n"
    "v_mfma_f32_16x16x32_bf16 a[0:3], a[224:227], v[32:35], a[0:3]\n"
    "s_add_u32 m0, s34, 16992\n"
    "v_exp_f32 v242, v78\n"
    "v_mul_f32 v255, s41, v255\n"
    "v_cvt_pk_bf16_f32 v111, v31, v255\n"
    "v_exp_f32 v243, v79\n"
    "v_mfma_f32_16x16x32_bf16 a[0:3], v[180:183], v[36:39], a[0:3]\n"
    "buffer_load_dwordx4 v1, s[24:27], s32 offen offset:3072  lds\n"
    "v_lshlrev_b32 v31, 16, v224\n"
    "v_and_b32 v255, s42, v224\n"
    "v_cvt_pk_bf16_f32 v44, v8, v9\n"
    "v_cvt_pk_bf16_f32 v45, v10, v11\n"
    "v_mfma_f32_16x16x32_bf16 a[4:7], a[228:231], v[32:35], a[4:7]\n"
    "v_mul_f32 v31, s41, v31\n"
    "v_mul_f32 v255, s41, v255\n"
    "v_cvt_pk_bf16_f32 v46, v240, v241\n"
    "v_cvt_pk_bf16_f32 v47, v242, v243\n"
    "v_mfma_f32_16x16x32_bf16 a[4:7], v[184:187], v[36:39], a[4:7]\n"
    "v_cvt_pk_bf16_f32 v112, v31, v255\n"
    "v_lshlrev_b32 v31, 16, v225\n"
    "v_and_b32 v255, s42, v225\n"
    "v_mul_f32 v31, s41, v31\n"
    "v_mfma_f32_16x16x32_bf16 v[12:15], v[16:19], v[40:43], 0\n"
    "v_mul_f32 v255, s41, v255\n"
    "v_cvt_pk_bf16_f32 v113, v31, v255\n"
    "v_lshlrev_b32 v31, 16, v226\n"
    "v_and_b32 v255, s42, v226\n"
    "v_mfma_f32_16x16x32_bf16 v[12:15], v[16:19], v[44:47], v[12:15]\n"
    "v_mul_f32 v31, s41, v31\n"
    "v_mul_f32 v255, s41, v255\n"
    "v_cvt_pk_bf16_f32 v114, v31, v255\n"
    "v_lshlrev_b32 v31, 16, v227\n"
    "v_mfma_f32_16x16x32_bf16 a[8:11], a[232:235], v[32:35], a[8:11]\n"
    "ds_read_b128 v[132:135], v229 offset:4096\n"
    "ds_read_b128 v[136:139], v229 offset:5120\n"
    "v_and_b32 v255, s42, v227\n"
    "v_mul_f32 v31, s41, v31\n"
    "v_mfma_f32_16x16x32_bf16 a[8:11], v[188:191], v[36:39], a[8:11]\n"
    "v_mul_f32 v255, s41, v255\n"
    "v_cvt_pk_bf16_f32 v115, v31, v255\n"
    "v_exp_f32 v8, v48\n"
    "v_mfma_f32_16x16x32_bf16 a[12:15], a[236:239], v[32:35], a[12:15]\n"
    "ds_read_b128 v[140:143], v229 offset:6144\n"
    "ds_read_b128 v[144:147], v229 offset:7168\n"
    "v_exp_f32 v9, v49\n"
    "v_exp_f32 v10, v50\n"
    "v_mfma_f32_16x16x32_bf16 a[12:15], v[192:195], v[36:39], a[12:15]\n"
    "s_waitcnt lgkmcnt(4)\n"
    "v_exp_f32 v11, v51\n"
    "v_exp_f32 v240, v52\n"
    "v_exp_f32 v241, v53\n"
    "v_mfma_f32_16x16x32_bf16 a[16:19], a[240:243], v[32:35], a[16:19]\n"
    "s_add_u32 s34, s36, s35\n"
    "v_cmp_nge_f32 vcc, s40, v12\n"
    "v_exp_f32 v242, v54\n"
    "v_exp_f32 v243, v55\n"
    "v_mfma_f32_16x16x32_bf16 a[16:19], v[196:199], v[36:39], a[16:19]\n"
    "v_mfma_f32_16x16x32_bf16 a[20:23], a[244:247], v[32:35], a[20:23]\n"
    "s_add_u32 s36, s36, 33792\n"
    "v_mfma_f32_16x16x32_bf16 a[20:23], v[200:203], v[36:39], a[20:23]\n"
    "s_cbranch_vccnz LTR_65\n"
    "LRET_65:\n"
    "v_add_f32 v21, v21, v12\n"
    "v_mfma_f32_16x16x32_bf16 a[24:27], a[248:251], v[32:35], a[24:27]\n"
    "s_sub_u32 s50, s36, 101376\n"
    "v_mfma_f32_16x16x32_bf16 a[24:27], v[204:207], v[36:39], a[24:27]\n"
    "v_mfma_f32_16x16x32_bf16 a[28:31], a[252:255], v[32:35], a[28:31]\n"
    "s_min_u32 s36, s36, s50\n"
    "v_cvt_pk_bf16_f32 v32, v8, v9\n"
    "v_cvt_pk_bf16_f32 v33, v10, v11\n"
    "v_exp_f32 v8, v56\n"
    "v_exp_f32 v9, v57\n"
    "v_exp_f32 v10, v58\n"
    "v_exp_f32 v11, v59\n"
    "v_cvt_pk_bf16_f32 v34, v240, v241\n"
    "v_cvt_pk_bf16_f32 v35, v242, v243\n"
    "v_exp_f32 v240, v60\n"
    "v_exp_f32 v241, v61\n"
    "v_exp_f32 v242, v62\n"
    "v_mfma_f32_16x16x32_bf16 a[28:31], v[208:211], v[36:39], a[28:31]\n"
    "s_add_u32 s32, s32, 16384\n"
    "v_exp_f32 v243, v63\n"
    "v_cvt_pk_bf16_f32 v36, v8, v9\n"
    "v_cvt_pk_bf16_f32 v37, v10, v11\n"
    "v_cvt_pk_bf16_f32 v38, v240, v241\n"
    "v_cvt_pk_bf16_f32 v39, v242, v243\n"
    "v_mfma_f32_16x16x32_bf16 v[12:15], v[16:19], v[32:35], 0\n"
    "s_nop 0\n"
    "v_mfma_f32_16x16x32_bf16 v[12:15], v[16:19], v[36:39], v[12:15]\n"
    "v_mfma_f32_16x16x32_bf16 v[64:67], a[160:163], v[148:151], v[92:95]\n"
    "v_add_u32 v4, s36, v2\n"
    "s_waitcnt vmcnt(8)\n"
    "s_barrier\n"
    "v_mfma_f32_16x16x32_bf16 v[64:67], a[164:167], v[152:155], v[64:67]\n"
    "ds_read_b128 a[160:163], v4 offset:0\n"
    "ds_read_b128 a[164:167], v4 offset:64\n"
    "v_mfma_f32_16x16x32_bf16 v[64:67], a[168:171], v[156:159], v[64:67]\n"
    "v_rcp_f32 v254, v20\n"
    "v_accvgpr_read_b32 v244, a0\n"
    "v_accvgpr_read_b32 v245, a1\n"
    "v_mul_f32 v244, v254, v244\n"
    "v_mul_f32 v245, v254, v245\n"
    "v_cvt_pk_bf16_f32 v246, v244, v245\n"
    "v_accvgpr_read_b32 v244, a2\n"
    "v_accvgpr_read_b32 v245, a3\n"
    "v_mul_f32 v244, v254, v244\n"
    "v_mul_f32 v245, v254, v245\n"
    "v_cvt_pk_bf16_f32 v247, v244, v245\n"
    "v_xor_b32 v31, 0, v231\n"
    "ds_write_b64 v31, v[246:247] offset:0\n"
    "v_accvgpr_read_b32 v244, a4\n"
    "v_accvgpr_read_b32 v245, a5\n"
    "v_mul_f32 v244, v254, v244\n"
    "v_mul_f32 v245, v254, v245\n"
    "v_cvt_pk_bf16_f32 v248, v244, v245\n"
    "v_accvgpr_read_b32 v244, a6\n"
    "v_accvgpr_read_b32 v245, a7\n"
    "v_mul_f32 v244, v254, v244\n"
    "v_mul_f32 v245, v254, v245\n"
    "v_cvt_pk_bf16_f32 v249, v244, v245\n"
    "v_xor_b32 v255, 32, v231\n"
    "ds_write_b64 v255, v[248:249] offset:0\n"
    "v_accvgpr_read_b32 v244, a8\n"
    "v_accvgpr_read_b32 v245, a9\n"
    "v_mul_f32 v244, v254, v244\n"
    "v_mul_f32 v245, v254, v245\n"
    "v_cvt_pk_bf16_f32 v250, v244, v245\n"
    "v_accvgpr_read_b32 v244, a10\n"
    "v_accvgpr_read_b32 v245, a11\n"
    "v_mul_f32 v244, v254, v244\n"
    "v_mul_f32 v245, v254, v245\n"
    "v_cvt_pk_bf16_f32 v251, v244, v245\n"
    "v_xor_b32 v31, 64, v231\n"
    "ds_write_b64 v31, v[250:251] offset:0\n"
    "v_accvgpr_read_b32 v244, a12\n"
    "v_accvgpr_read_b32 v245, a13\n"
    "v_mul_f32 v244, v254, v244\n"
    "v_mfma_f32_16x16x32_bf16 v[64:67], a[172:175], v[160:163], v[64:67]\n"
    "v_mul_f32 v245, v254, v245\n"
    "v_cvt_pk_bf16_f32 v252, v244, v245\n"
    "v_accvgpr_read_b32 v244, a14\n"
    "v_accvgpr_read_b32 v245, a15\n"
    "v_mfma_f32_16x16x32_bf16 v[68:71], a[176:179], v[148:151], v[92:95]\n"
    "v_mul_f32 v244, v254, v244\n"
    "v_mul_f32 v245, v254, v245\n"
    "v_cvt_pk_bf16_f32 v253, v244, v245\n"
    "v_xor_b32 v255, 96, v231\n"
    "v_mfma_f32_16x16x32_bf16 v[68:71], a[180:183], v[152:155], v[68:71]\n"
    "ds_write_b64 v255, v[252:253] offset:0\n"
    "v_accvgpr_read_b32 v244, a16\n"
    "v_accvgpr_read_b32 v245, a17\n"
    "v_mul_f32 v244, v254, v244\n"
    "v_mfma_f32_16x16x32_bf16 v[68:71], a[184:187], v[156:159], v[68:71]\n"
    "v_mul_f32 v245, v254, v245\n"
    "v_cvt_pk_bf16_f32 v246, v244, v245\n"
    "v_accvgpr_read_b32 v244, a18\n"
    "v_accvgpr_read_b32 v245, a19\n"
    "v_mfma_f32_16x16x32_bf16 v[68:71], a[188:191], v[160:163], v[68:71]\n"
    "v_mul_f32 v244, v254, v244\n"
    "v_mul_f32 v245, v254, v245\n"
    "v_cvt_pk_bf16_f32 v247, v244, v245\n"
    "v_xor_b32 v31, 128, v231\n"
    "v_mfma_f32_16x16x32_bf16 v[72:75], a[192:195], v[148:151], v[92:95]\n"
    "ds_write_b64 v31, v[246:247] offset:0\n"
    "v_accvgpr_read_b32 v244, a20\n"
    "v_accvgpr_read_b32 v245, a21\n"
    "v_mul_f32 v244, v254, v244\n"
    "v_mfma_f32_16x16x32_bf16 v[72:75], a[196:199], v[152:155], v[72:75]\n"
    "v_mul_f32 v245, v254, v245\n"
    "v_cvt_pk_bf16_f32 v248, v244, v245\n"
    "v_accvgpr_read_b32 v244, a22\n"
    "v_accvgpr_read_b32 v245, a23\n"
    "v_mfma_f32_16x16x32_bf16 v[72:75], a[200:203], v[156:159], v[72:75]\n"
    "ds_read_b128 a[168:171], v4 offset:128\n"
    "ds_read_b128 a[172:175], v4 offset:192\n"
    "v_mul_f32 v244, v254, v244\n"
    "v_mul_f32 v245, v254, v245\n"
    "v_mfma_f32_16x16x32_bf16 v[72:75], a[204:207], v[160:163], v[72:75]\n"
    "v_cvt_pk_bf16_f32 v249, v244, v245\n"
    "v_xor_b32 v255, 160, v231\n"
    "ds_write_b64 v255, v[248:249] offset:0\n"
    "v_accvgpr_read_b32 v244, a24\n"
    "v_mfma_f32_16x16x32_bf16 v[76:79], a[208:211], v[148:151], v[92:95]\n"
    "ds_read_b128 a[176:179], v4 offset:256\n"
    "ds_read_b128 a[180:183], v4 offset:320\n"
    "v_accvgpr_read_b32 v245, a25\n"
    "v_mul_f32 v244, v254, v244\n"
    "v_mfma_f32_16x16x32_bf16 v[76:79], a[212:215], v[152:155], v[76:79]\n"
    "v_mul_f32 v245, v254, v245\n"
    "v_cvt_pk_bf16_f32 v250, v244, v245\n"
    "v_accvgpr_read_b32 v244, a26\n"
    "v_accvgpr_read_b32 v245, a27\n"
    "v_mfma_f32_16x16x32_bf16 v[76:79], a[216:219], v[156:159], v[76:79]\n"
    "ds_read_b128 a[184:187], v4 offset:384\n"
    "ds_read_b128 a[188:191], v4 offset:448\n"
    "v_mul_f32 v244, v254, v244\n"
    "v_mul_f32 v245, v254, v245\n"
    "v_mfma_f32_16x16x32_bf16 v[76:79], a[220:223], v[160:163], v[76:79]\n"
    "v_cvt_pk_bf16_f32 v251, v244, v245\n"
    "v_xor_b32 v31, 192, v231\n"
    "ds_write_b64 v31, v[250:251] offset:0\n"
    "v_accvgpr_read_b32 v244, a28\n"
    "v_mfma_f32_16x16x32_bf16 a[32:35], a[224:227], v[40:43], a[32:35]\n"
    "ds_read_b128 a[192:195], v4 offset:512\n"
    "ds_read_b128 a[196:199], v4 offset:576\n"
    "v_accvgpr_read_b32 v245, a29\n"
    "v_mul_f32 v244, v254, v244\n"
    "v_mfma_f32_16x16x32_bf16 a[32:35], v[180:183], v[44:47], a[32:35]\n"
    "v_mul_f32 v245, v254, v245\n"
    "v_cvt_pk_bf16_f32 v252, v244, v245\n"
    "v_cmp_nge_f32 vcc, s40, v12\n"
    "v_mfma_f32_16x16x32_bf16 a[36:39], a[228:231], v[40:43], a[36:39]\n"
    "ds_read_b128 a[200:203], v4 offset:640\n"
    "ds_read_b128 a[204:207], v4 offset:704\n"
    "v_accvgpr_read_b32 v244, a30\n"
    "v_accvgpr_read_b32 v245, a31\n"
    "v_mfma_f32_16x16x32_bf16 a[36:39], v[184:187], v[44:47], a[36:39]\n"
    "ds_read_b128 v[148:151], v229 offset:8192\n"
    "ds_read_b128 v[152:155], v229 offset:9216\n"
    "v_mul_f32 v244, v254, v244\n"
    "v_mul_f32 v245, v254, v245\n"
    "v_mfma_f32_16x16x32_bf16 a[40:43], a[232:235], v[40:43], a[40:43]\n"
    "ds_read_b128 a[208:211], v4 offset:768\n"
    "ds_read_b128 a[212:215], v4 offset:832\n"
    "v_cvt_pk_bf16_f32 v253, v244, v245\n"
    "v_xor_b32 v255, 224, v231\n"
    "v_mfma_f32_16x16x32_bf16 a[40:43], v[188:191], v[44:47], a[40:43]\n"
    "s_cbranch_vccnz LTR_66\n"
    "LRET_66:\n"
    "v_add_f32 v22, v22, v12\n"
    "ds_write_b64 v255, v[252:253] offset:0\n"
    "v_mfma_f32_16x16x32_bf16 a[44:47], a[236:239], v[40:43], a[44:47]\n"
    "ds_read_b128 a[216:219], v4 offset:896\n"
    "ds_read_b128 a[220:223], v4 offset:960\n"
    "ds_read_b128 v[156:159], v229 offset:10240\n"
    "ds_read_b128 v[160:163], v229 offset:11264\n"
    "v_mfma_f32_16x16x32_bf16 a[44:47], v[192:195], v[44:47], a[44:47]\n"
    "v_exp_f32 v8, v64\n"
    "v_exp_f32 v9, v65\n"
    "v_mfma_f32_16x16x32_bf16 a[48:51], a[240:243], v[40:43], a[48:51]\n"
    "v_exp_f32 v10, v66\n"
    "v_exp_f32 v11, v67\n"
    "v_exp_f32 v240, v68\n"
    "v_mfma_f32_16x16x32_bf16 a[48:51], v[196:199], v[44:47], a[48:51]\n"
    "v_exp_f32 v241, v69\n"
    "v_exp_f32 v242, v70\n"
    "v_exp_f32 v243, v71\n"
    "v_mfma_f32_16x16x32_bf16 a[52:55], a[244:247], v[40:43], a[52:55]\n"
    "v_mfma_f32_16x16x32_bf16 a[52:55], v[200:203], v[44:47], a[52:55]\n"
    "v_mfma_f32_16x16x32_bf16 a[56:59], a[248:251], v[40:43], a[56:59]\n"
    "v_mfma_f32_16x16x32_bf16 a[56:59], v[204:207], v[44:47], a[56:59]\n"
    "v_mfma_f32_16x16x32_bf16 a[60:63], a[252:255], v[40:43], a[60:63]\n"
    "v_cvt_pk_bf16_f32 v40, v8, v9\n"
    "v_cvt_pk_bf16_f32 v41, v10, v11\n"
    "v_exp_f32 v8, v72\n"
    "v_exp_f32 v9, v73\n"
    "v_exp_f32 v10, v74\n"
    "v_exp_f32 v11, v75\n"
    "v_cvt_pk_bf16_f32 v42, v240, v241\n"
    "v_cvt_pk_bf16_f32 v43, v242, v243\n"
    "v_exp_f32 v240, v76\n"
    "v_exp_f32 v241, v77\n"
    "v_exp_f32 v242, v78\n"
    "v_mfma_f32_16x16x32_bf16 a[60:63], v[208:211], v[44:47], a[60:63]\n"
    "s_waitcnt lgkmcnt(14)\n"
    "v_exp_f32 v243, v79\n"
    "v_cvt_pk_bf16_f32 v44, v8, v9\n"
    "v_cvt_pk_bf16_f32 v45, v10, v11\n"
    "v_cvt_pk_bf16_f32 v46, v240, v241\n"
    "v_cvt_pk_bf16_f32 v47, v242, v243\n"
    "v_mfma_f32_16x16x32_bf16 v[12:15], v[16:19], v[40:43], 0\n"
    "s_nop 0\n"
    "v_mfma_f32_16x16x32_bf16 v[12:15], v[16:19], v[44:47], v[12:15]\n"
    "v_mfma_f32_16x16x32_bf16 v[48:51], a[160:163], v[100:103], 0\n"
    "s_add_u32 m0, s34, 0\n"
    "v_mfma_f32_16x16x32_bf16 v[48:51], a[164:167], v[104:107], v[48:51]\n"
    "buffer_load_dwordx4 v1, s[20:23], s32 offen offset:0  lds\n"
    "v_mfma_f32_16x16x32_bf16 v[48:51], a[168:171], v[108:111], v[48:51]\n"
    "v_rcp_f32 v254, v21\n"
    "v_accvgpr_read_b32 v244, a32\n"
    "v_accvgpr_read_b32 v245, a33\n"
    "v_mul_f32 v244, v254, v244\n"
    "v_mul_f32 v245, v254, v245\n"
    "v_cvt_pk_bf16_f32 v246, v244, v245\n"
    "v_accvgpr_read_b32 v244, a34\n"
    "v_accvgpr_read_b32 v245, a35\n"
    "v_mul_f32 v244, v254, v244\n"
    "v_mul_f32 v245, v254, v245\n"
    "v_cvt_pk_bf16_f32 v247, v244, v245\n"
    "v_xor_b32 v31, 0, v231\n"
    "ds_write_b64 v31, v[246:247] offset:4096\n"
    "v_accvgpr_read_b32 v244, a36\n"
    "v_accvgpr_read_b32 v245, a37\n"
    "v_mul_f32 v244, v254, v244\n"
    "v_mul_f32 v245, v254, v245\n"
    "v_cvt_pk_bf16_f32 v248, v244, v245\n"
    "v_accvgpr_read_b32 v244, a38\n"
    "v_accvgpr_read_b32 v245, a39\n"
    "v_mul_f32 v244, v254, v244\n"
    "v_mul_f32 v245, v254, v245\n"
    "v_cvt_pk_bf16_f32 v249, v244, v245\n"
    "v_xor_b32 v255, 32, v231\n"
    "ds_write_b64 v255, v[248:249] offset:4096\n"
    "v_accvgpr_read_b32 v244, a40\n"
    "v_accvgpr_read_b32 v245, a41\n"
    "v_mul_f32 v244, v254, v244\n"
    "v_mul_f32 v245, v254, v245\n"
    "v_cvt_pk_bf16_f32 v250, v244, v245\n"
    "v_accvgpr_read_b32 v244, a42\n"
    "v_accvgpr_read_b32 v245, a43\n"
    "v_mul_f32 v244, v254, v244\n"
    "v_mul_f32 v245, v254, v245\n"
    "v_cvt_pk_bf16_f32 v251, v244, v245\n"
    "v_xor_b32 v31, 64, v231\n"
    "ds_write_b64 v31, v[250:251] offset:4096\n"
    "v_accvgpr_read_b32 v244, a44\n"
    "v_accvgpr_read_b32 v245, a45\n"
    "v_mul_f32 v244, v254, v244\n"
    "v_mfma_f32_16x16x32_bf16 v[48:51], a[172:175], v[112:115], v[48:51]\n"
    "v_mul_f32 v245, v254, v245\n"
    "v_cvt_pk_bf16_f32 v252, v244, v245\n"
    "v_accvgpr_read_b32 v244, a46\n"
    "v_accvgpr_read_b32 v245, a47\n"
    "v_mfma_f32_16x16x32_bf16 v[52:55], a[176:179], v[100:103], 0\n"
    "v_mul_f32 v244, v254, v244\n"
    "v_mul_f32 v245, v254, v245\n"
    "v_cvt_pk_bf16_f32 v253, v244, v245\n"
    "v_xor_b32 v255, 96, v231\n"
    "v_mfma_f32_16x16x32_bf16 v[52:55], a[180:183], v[104:107], v[52:55]\n"
    "ds_write_b64 v255, v[252:253] offset:4096\n"
    "v_accvgpr_read_b32 v244, a48\n"
    "v_accvgpr_read_b32 v245, a49\n"
    "v_mul_f32 v244, v254, v244\n"
    "v_mfma_f32_16x16x32_bf16 v[52:55], a[184:187], v[108:111], v[52:55]\n"
    "v_mul_f32 v245, v254, v245\n"
    "v_cvt_pk_bf16_f32 v246, v244, v245\n"
    "v_accvgpr_read_b32 v244, a50\n"
    "v_accvgpr_read_b32 v245, a51\n"
    "v_mfma_f32_16x16x32_bf16 v[52:55], a[188:191], v[112:115], v[52:55]\n"
    "s_waitcnt lgkmcnt(6)\n"
    "v_mul_f32 v244, v254, v244\n"
    "v_mul_f32 v245, v254, v245\n"
    "v_cvt_pk_bf16_f32 v247, v244, v245\n"
    "v_xor_b32 v31, 128, v231\n"
    "v_mfma_f32_16x16x32_bf16 v[56:59], a[192:195], v[100:103], 0\n"
    "s_add_u32 m0, s34, 32\n"
    "ds_write_b64 v31, v[246:247] offset:4096\n"
    "v_accvgpr_read_b32 v244, a52\n"
    "v_accvgpr_read_b32 v245, a53\n"
    "v_mul_f32 v244, v254, v244\n"
    "v_mfma_f32_16x16x32_bf16 v[56:59], a[196:199], v[104:107], v[56:59]\n"
    "buffer_load_dwordx4 v1, s[20:23], s32 offen offset:1024  lds\n"
    "v_mul_f32 v245, v254, v245\n"
    "v_cvt_pk_bf16_f32 v248, v244, v245\n"
    "v_mfma_f32_16x16x32_bf16 v[56:59], a[200:203], v[108:111], v[56:59]\n"
    "v_accvgpr_read_b32 v244, a54\n"
    "v_accvgpr_read_b32 v245, a55\n"
    "v_mul_f32 v244, v254, v244\n"
    "v_mul_f32 v245, v254, v245\n"
    "v_mfma_f32_16x16x32_bf16 v[56:59], a[204:207], v[112:115], v[56:59]\n"
    "v_cvt_pk_bf16_f32 v249, v244, v245\n"
    "v_xor_b32 v255, 160, v231\n"
    "ds_write_b64 v255, v[248:249] offset:4096\n"
    "v_accvgpr_read_b32 v244, a56\n"
    "v_mfma_f32_16x16x32_bf16 v[60:63], a[208:211], v[100:103], 0\n"
    "v_accvgpr_read_b32 v245, a57\n"
    "v_mul_f32 v244, v254, v244\n"
    "v_mfma_f32_16x16x32_bf16 v[60:63], a[212:215], v[104:107], v[60:63]\n"
    "s_add_u32 m0, s34, 64\n"
    "v_mul_f32 v245, v254, v245\n"
    "v_cvt_pk_bf16_f32 v250, v244, v245\n"
    "v_mfma_f32_16x16x32_bf16 v[60:63], a[216:219], v[108:111], v[60:63]\n"
    "buffer_load_dwordx4 v1, s[20:23], s32 offen offset:2048  lds\n"
    "v_mfma_f32_16x16x32_bf16 v[60:63], a[220:223], v[112:115], v[60:63]\n"
    "v_accvgpr_read_b32 v244, a58\n"
    "v_accvgpr_read_b32 v245, a59\n"
    "v_mfma_f32_16x16x32_bf16 a[64:67], a[224:227], v[32:35], a[64:67]\n"
    "v_mul_f32 v244, v254, v244\n"
    "v_mul_f32 v245, v254, v245\n"
    "v_mfma_f32_16x16x32_bf16 a[64:67], v[180:183], v[36:39], a[64:67]\n"
    "v_cvt_pk_bf16_f32 v251, v244, v245\n"
    "v_xor_b32 v31, 192, v231\n"
    "v_mfma_f32_16x16x32_bf16 a[68:71], a[228:231], v[32:35], a[68:71]\n"
    "ds_write_b64 v31, v[250:251] offset:4096\n"
    "v_accvgpr_read_b32 v244, a60\n"
    "v_mfma_f32_16x16x32_bf16 a[68:71], v[184:187], v[36:39], a[68:71]\n"
    "v_accvgpr_read_b32 v245, a61\n"
    "v_mul_f32 v244, v254, v244\n"
    "v_mfma_f32_16x16x32_bf16 a[72:75], a[232:235], v[32:35], a[72:75]\n"
    "s_add_u32 m0, s34, 96\n"
    "v_mul_f32 v245, v254, v245\n"
    "v_cvt_pk_bf16_f32 v252, v244, v245\n"
    "v_mfma_f32_16x16x32_bf16 a[72:75], v[188:191], v[36:39], a[72:75]\n"
    "buffer_load_dwordx4 v1, s[20:23], s32 offen offset:3072  lds\n"
    "v_mfma_f32_16x16x32_bf16 a[76:79], a[236:239], v[32:35], a[76:79]\n"
    "v_max3_f32 v25, v48, v49, v50\n"
    "v_max3_f32 v26, v51, v52, v53\n"
    "v_mfma_f32_16x16x32_bf16 a[76:79], v[192:195], v[36:39], a[76:79]\n"
    "v_cmp_nge_f32 vcc, s40, v12\n"
    "v_mfma_f32_16x16x32_bf16 a[80:83], a[240:243], v[32:35], a[80:83]\n"
    "v_accvgpr_read_b32 v244, a62\n"
    "v_accvgpr_read_b32 v245, a63\n"
    "v_mfma_f32_16x16x32_bf16 a[80:83], v[196:199], v[36:39], a[80:83]\n"
    "v_max3_f32 v25, v25, v54, v55\n"
    "v_max3_f32 v26, v26, v56, v57\n"
    "v_mfma_f32_16x16x32_bf16 a[84:87], a[244:247], v[32:35], a[84:87]\n"
    "v_mul_f32 v244, v254, v244\n"
    "v_mul_f32 v245, v254, v245\n"
    "v_mfma_f32_16x16x32_bf16 a[84:87], v[200:203], v[36:39], a[84:87]\n"
    "v_max3_f32 v25, v25, v58, v59\n"
    "v_max3_f32 v26, v26, v60, v61\n"
    "v_mfma_f32_16x16x32_bf16 a[88:91], a[248:251], v[32:35], a[88:91]\n"
    "v_cvt_pk_bf16_f32 v253, v244, v245\n"
    "v_xor_b32 v255, 224, v231\n"
    "v_mfma_f32_16x16x32_bf16 a[88:91], v[204:207], v[36:39], a[88:91]\n"
    "v_max3_f32 v25, v25, v62, v63\n"
    "v_max_f32 v25, v25, v26\n"
    "v_mfma_f32_16x16x32_bf16 a[92:95], a[252:255], v[32:35], a[92:95]\n"
    "s_cbranch_vccnz LTR_67\n"
    "LRET_67:\n"
    "v_add_f32 v23, v23, v12\n"
    "v_mfma_f32_16x16x32_bf16 a[92:95], v[208:211], v[36:39], a[92:95]\n"
    "ds_write_b64 v255, v[252:253] offset:4096\n"
    "s_mov_b32 s38, 1\n"
    "s_mov_b32 s45, s43\n"
    "s_mov_b32 s43, s44\n"
    "s_branch LWARM4\n"
    "LFINAL4:\n"
    "v_mfma_f32_16x16x32_bf16 v[64:67], a[160:163], v[116:119], v[84:87]\n"
    "v_exp_f32 v243, v55\n"
    "v_mfma_f32_16x16x32_bf16 v[64:67], a[164:167], v[120:123], v[64:67]\n"
    "v_cvt_pk_bf16_f32 v32, v8, v9\n"
    "v_cvt_pk_bf16_f32 v33, v10, v11\n"
    "v_mfma_f32_16x16x32_bf16 v[64:67], a[168:171], v[124:127], v[64:67]\n"
    "v_exp_f32 v8, v56\n"
    "v_mfma_f32_16x16x32_bf16 v[64:67], a[172:175], v[128:131], v[64:67]\n"
    "v_exp_f32 v9, v57\n"
    "v_mfma_f32_16x16x32_bf16 v[68:71], a[176:179], v[116:119], v[84:87]\n"
    "v_add_u32 v5, s36, v3\n"
    "v_mfma_f32_16x16x32_bf16 v[68:71], a[180:183], v[120:123], v[68:71]\n"
    "v_exp_f32 v10, v58\n"
    "v_mfma_f32_16x16x32_bf16 v[68:71], a[184:187], v[124:127], v[68:71]\n"
    "v_exp_f32 v11, v59\n"
    "v_mfma_f32_16x16x32_bf16 v[68:71], a[188:191], v[128:131], v[68:71]\n"
    "v_cvt_pk_bf16_f32 v34, v240, v241\n"
    "v_cvt_pk_bf16_f32 v35, v242, v243\n"
    "v_mfma_f32_16x16x32_bf16 v[72:75], a[192:195], v[116:119], v[84:87]\n"
    "v_exp_f32 v240, v60\n"
    "v_mfma_f32_16x16x32_bf16 v[72:75], a[196:199], v[120:123], v[72:75]\n"
    "v_exp_f32 v241, v61\n"
    "v_mfma_f32_16x16x32_bf16 v[72:75], a[200:203], v[124:127], v[72:75]\n"
    "v_exp_f32 v242, v62\n"
    "v_mfma_f32_16x16x32_bf16 v[72:75], a[204:207], v[128:131], v[72:75]\n"
    "v_exp_f32 v243, v63\n"
    "v_mfma_f32_16x16x32_bf16 v[76:79], a[208:211], v[116:119], v[84:87]\n"
    "v_cvt_pk_bf16_f32 v36, v8, v9\n"
    "v_cvt_pk_bf16_f32 v37, v10, v11\n"
    "v_mfma_f32_16x16x32_bf16 v[76:79], a[212:215], v[120:123], v[76:79]\n"
    "v_cvt_pk_bf16_f32 v38, v240, v241\n"
    "v_cvt_pk_bf16_f32 v39, v242, v243\n"
    "v_mfma_f32_16x16x32_bf16 v[76:79], a[216:219], v[124:127], v[76:79]\n"
    "v_exp_f32 v8, v64\n"
    "v_mfma_f32_16x16x32_bf16 v[76:79], a[220:223], v[128:131], v[76:79]\n"
    "v_exp_f32 v9, v65\n"
    "v_mfma_f32_16x16x32_bf16 v[12:15], v[16:19], v[32:35], 0\n"
    "v_exp_f32 v10, v66\n"
    "v_mfma_f32_16x16x32_bf16 v[12:15], v[16:19], v[36:39], v[12:15]\n"
    "v_exp_f32 v11, v67\n"
    "v_mfma_f32_16x16x32_bf16 a[96:99], a[224:227], v[40:43], a[96:99]\n"
    "ds_read_b64_tr_b16 a[224:225], v5 offset:0\n"
    "ds_read_b64_tr_b16 a[226:227], v5 offset:256\n"
    "v_mfma_f32_16x16x32_bf16 a[96:99], v[180:183], v[44:47], a[96:99]\n"
    "v_exp_f32 v240, v68\n"
    "v_mfma_f32_16x16x32_bf16 a[100:103], a[228:231], v[40:43], a[100:103]\n"
    "ds_read_b64_tr_b16 v[180:181], v5 offset:512\n"
    "ds_read_b64_tr_b16 v[182:183], v5 offset:768\n"
    "v_mfma_f32_16x16x32_bf16 a[100:103], v[184:187], v[44:47], a[100:103]\n"
    "v_exp_f32 v241, v69\n"
    "v_mfma_f32_16x16x32_bf16 a[104:107], a[232:235], v[40:43], a[104:107]\n"
    "ds_read_b64_tr_b16 a[228:229], v5 offset:32\n"
    "ds_read_b64_tr_b16 a[230:231], v5 offset:288\n"
    "v_mfma_f32_16x16x32_bf16 a[104:107], v[188:191], v[44:47], a[104:107]\n"
    "v_cmp_nge_f32 vcc, s40, v12\n"
    "v_mfma_f32_16x16x32_bf16 a[108:111], a[236:239], v[40:43], a[108:111]\n"
    "ds_read_b64_tr_b16 v[184:185], v5 offset:544\n"
    "ds_read_b64_tr_b16 v[186:187], v5 offset:800\n"
    "v_mfma_f32_16x16x32_bf16 a[108:111], v[192:195], v[44:47], a[108:111]\n"
    "v_exp_f32 v242, v70\n"
    "v_mfma_f32_16x16x32_bf16 a[112:115], a[240:243], v[40:43], a[112:115]\n"
    "ds_read_b64_tr_b16 a[232:233], v5 offset:64\n"
    "ds_read_b64_tr_b16 a[234:235], v5 offset:320\n"
    "v_mfma_f32_16x16x32_bf16 a[112:115], v[196:199], v[44:47], a[112:115]\n"
    "s_cbranch_vccnz LTR_68\n"
    "LRET_68:\n"
    "v_add_f32 v20, v20, v12\n"
    "v_mfma_f32_16x16x32_bf16 a[116:119], a[244:247], v[40:43], a[116:119]\n"
    "ds_read_b64_tr_b16 v[188:189], v5 offset:576\n"
    "ds_read_b64_tr_b16 v[190:191], v5 offset:832\n"
    "v_mfma_f32_16x16x32_bf16 a[116:119], v[200:203], v[44:47], a[116:119]\n"
    "v_exp_f32 v243, v71\n"
    "v_mfma_f32_16x16x32_bf16 a[120:123], a[248:251], v[40:43], a[120:123]\n"
    "ds_read_b64_tr_b16 a[236:237], v5 offset:96\n"
    "ds_read_b64_tr_b16 a[238:239], v5 offset:352\n"
    "v_mfma_f32_16x16x32_bf16 a[120:123], v[204:207], v[44:47], a[120:123]\n"
    "v_mfma_f32_16x16x32_bf16 a[124:127], a[252:255], v[40:43], a[124:127]\n"
    "ds_read_b64_tr_b16 v[192:193], v5 offset:608\n"
    "ds_read_b64_tr_b16 v[194:195], v5 offset:864\n"
    "v_mfma_f32_16x16x32_bf16 a[124:127], v[208:211], v[44:47], a[124:127]\n"
    "v_cvt_pk_bf16_f32 v40, v8, v9\n"
    "v_cvt_pk_bf16_f32 v41, v10, v11\n"
    "v_mfma_f32_16x16x32_bf16 v[48:51], a[160:163], v[132:135], v[88:91]\n"
    "ds_read_b64_tr_b16 a[240:241], v5 offset:128\n"
    "ds_read_b64_tr_b16 a[242:243], v5 offset:384\n"
    "v_exp_f32 v8, v72\n"
    "v_exp_f32 v9, v73\n"
    "v_mfma_f32_16x16x32_bf16 v[48:51], a[164:167], v[136:139], v[48:51]\n"
    "v_exp_f32 v10, v74\n"
    "v_exp_f32 v11, v75\n"
    "v_cvt_pk_bf16_f32 v42, v240, v241\n"
    "v_cvt_pk_bf16_f32 v43, v242, v243\n"
    "v_mfma_f32_16x16x32_bf16 v[48:51], a[168:171], v[140:143], v[48:51]\n"
    "ds_read_b64_tr_b16 v[196:197], v5 offset:640\n"
    "ds_read_b64_tr_b16 v[198:199], v5 offset:896\n"
    "v_exp_f32 v240, v76\n"
    "v_exp_f32 v241, v77\n"
    "v_mfma_f32_16x16x32_bf16 v[48:51], a[172:175], v[144:147], v[48:51]\n"
    "v_exp_f32 v242, v78\n"
    "v_exp_f32 v243, v79\n"
    "v_cvt_pk_bf16_f32 v44, v8, v9\n"
    "v_cvt_pk_bf16_f32 v45, v10, v11\n"
    "v_mfma_f32_16x16x32_bf16 v[52:55], a[176:179], v[132:135], v[88:91]\n"
    "ds_read_b64_tr_b16 a[244:245], v5 offset:160\n"
    "ds_read_b64_tr_b16 a[246:247], v5 offset:416\n"
    "v_cvt_pk_bf16_f32 v46, v240, v241\n"
    "v_cvt_pk_bf16_f32 v47, v242, v243\n"
    "v_mfma_f32_16x16x32_bf16 v[52:55], a[180:183], v[136:139], v[52:55]\n"
    "v_mfma_f32_16x16x32_bf16 v[52:55], a[184:187], v[140:143], v[52:55]\n"
    "ds_read_b64_tr_b16 v[200:201], v5 offset:672\n"
    "ds_read_b64_tr_b16 v[202:203], v5 offset:928\n"
    "v_mfma_f32_16x16x32_bf16 v[52:55], a[188:191], v[144:147], v[52:55]\n"
    "v_mfma_f32_16x16x32_bf16 v[12:15], v[16:19], v[40:43], 0\n"
    "ds_read_b64_tr_b16 a[248:249], v5 offset:192\n"
    "ds_read_b64_tr_b16 a[250:251], v5 offset:448\n"
    "v_mfma_f32_16x16x32_bf16 v[12:15], v[16:19], v[44:47], v[12:15]\n"
    "v_mfma_f32_16x16x32_bf16 v[56:59], a[192:195], v[132:135], v[88:91]\n"
    "ds_read_b64_tr_b16 v[204:205], v5 offset:704\n"
    "ds_read_b64_tr_b16 v[206:207], v5 offset:960\n"
    "v_exp_f32 v8, v48\n"
    "v_exp_f32 v9, v49\n"
    "v_mfma_f32_16x16x32_bf16 v[56:59], a[196:199], v[136:139], v[56:59]\n"
    "v_exp_f32 v10, v50\n"
    "v_exp_f32 v11, v51\n"
    "v_mfma_f32_16x16x32_bf16 v[56:59], a[200:203], v[140:143], v[56:59]\n"
    "ds_read_b64_tr_b16 a[252:253], v5 offset:224\n"
    "ds_read_b64_tr_b16 a[254:255], v5 offset:480\n"
    "v_mfma_f32_16x16x32_bf16 v[56:59], a[204:207], v[144:147], v[56:59]\n"
    "v_mfma_f32_16x16x32_bf16 v[60:63], a[208:211], v[132:135], v[88:91]\n"
    "ds_read_b64_tr_b16 v[208:209], v5 offset:736\n"
    "ds_read_b64_tr_b16 v[210:211], v5 offset:992\n"
    "v_cmp_nge_f32 vcc, s40, v12\n"
    "v_exp_f32 v240, v52\n"
    "v_mfma_f32_16x16x32_bf16 v[60:63], a[212:215], v[136:139], v[60:63]\n"
    "v_exp_f32 v241, v53\n"
    "v_exp_f32 v242, v54\n"
    "v_exp_f32 v243, v55\n"
    "v_mfma_f32_16x16x32_bf16 v[60:63], a[216:219], v[140:143], v[60:63]\n"
    "v_mfma_f32_16x16x32_bf16 v[60:63], a[220:223], v[144:147], v[60:63]\n"
    "s_waitcnt lgkmcnt(15)\n"
    "s_cbranch_vccnz LTR_69\n"
    "LRET_69:\n"
    "v_add_f32 v21, v21, v12\n"
    "v_mfma_f32_16x16x32_bf16 a[0:3], a[224:227], v[32:35], a[0:3]\n"
    "v_mfma_f32_16x16x32_bf16 a[0:3], v[180:183], v[36:39], a[0:3]\n"
    "v_mfma_f32_16x16x32_bf16 a[4:7], a[228:231], v[32:35], a[4:7]\n"
    "v_mfma_f32_16x16x32_bf16 a[4:7], v[184:187], v[36:39], a[4:7]\n"
    "v_mfma_f32_16x16x32_bf16 a[8:11], a[232:235], v[32:35], a[8:11]\n"
    "v_mfma_f32_16x16x32_bf16 a[8:11], v[188:191], v[36:39], a[8:11]\n"
    "v_mfma_f32_16x16x32_bf16 a[12:15], a[236:239], v[32:35], a[12:15]\n"
    "v_mfma_f32_16x16x32_bf16 a[12:15], v[192:195], v[36:39], a[12:15]\n"
    "s_waitcnt lgkmcnt(0)\n"
    "v_mfma_f32_16x16x32_bf16 a[16:19], a[240:243], v[32:35], a[16:19]\n"
    "v_mfma_f32_16x16x32_bf16 a[16:19], v[196:199], v[36:39], a[16:19]\n"
    "v_mfma_f32_16x16x32_bf16 a[20:23], a[244:247], v[32:35], a[20:23]\n"
    "v_mfma_f32_16x16x32_bf16 a[20:23], v[200:203], v[36:39], a[20:23]\n"
    "v_mfma_f32_16x16x32_bf16 a[24:27], a[248:251], v[32:35], a[24:27]\n"
    "v_mfma_f32_16x16x32_bf16 a[24:27], v[204:207], v[36:39], a[24:27]\n"
    "v_mfma_f32_16x16x32_bf16 a[28:31], a[252:255], v[32:35], a[28:31]\n"
    "v_cvt_pk_bf16_f32 v32, v8, v9\n"
    "v_cvt_pk_bf16_f32 v33, v10, v11\n"
    "v_exp_f32 v8, v56\n"
    "v_exp_f32 v9, v57\n"
    "v_exp_f32 v10, v58\n"
    "v_exp_f32 v11, v59\n"
    "v_cvt_pk_bf16_f32 v34, v240, v241\n"
    "v_cvt_pk_bf16_f32 v35, v242, v243\n"
    "v_exp_f32 v240, v60\n"
    "v_exp_f32 v241, v61\n"
    "v_exp_f32 v242, v62\n"
    "v_exp_f32 v243, v63\n"
    "v_mfma_f32_16x16x32_bf16 a[28:31], v[208:211], v[36:39], a[28:31]\n"
    "v_cvt_pk_bf16_f32 v36, v8, v9\n"
    "v_cvt_pk_bf16_f32 v37, v10, v11\n"
    "v_cvt_pk_bf16_f32 v38, v240, v241\n"
    "v_cvt_pk_bf16_f32 v39, v242, v243\n"
    "v_mfma_f32_16x16x32_bf16 v[12:15], v[16:19], v[32:35], 0\n"
    "s_nop 0\n"
    "v_mfma_f32_16x16x32_bf16 v[12:15], v[16:19], v[36:39], v[12:15]\n"
    "v_mfma_f32_16x16x32_bf16 v[64:67], a[160:163], v[148:151], v[92:95]\n"
    "v_mfma_f32_16x16x32_bf16 v[64:67], a[164:167], v[152:155], v[64:67]\n"
    "v_mfma_f32_16x16x32_bf16 v[64:67], a[168:171], v[156:159], v[64:67]\n"
    "v_rcp_f32 v254, v20\n"
    "s_add_u32 s49, s43, 0\n"
    "v_accvgpr_read_b32 v244, a0\n"
    "v_accvgpr_read_b32 v245, a1\n"
    "v_mul_f32 v244, v254, v244\n"
    "v_mul_f32 v245, v254, v245\n"
    "v_cvt_pk_bf16_f32 v246, v244, v245\n"
    "v_accvgpr_read_b32 v244, a2\n"
    "v_accvgpr_read_b32 v245, a3\n"
    "v_mul_f32 v244, v254, v244\n"
    "v_mul_f32 v245, v254, v245\n"
    "v_cvt_pk_bf16_f32 v247, v244, v245\n"
    "v_accvgpr_read_b32 v244, a4\n"
    "v_accvgpr_read_b32 v245, a5\n"
    "v_mul_f32 v244, v254, v244\n"
    "v_mul_f32 v245, v254, v245\n"
    "v_cvt_pk_bf16_f32 v248, v244, v245\n"
    "v_accvgpr_read_b32 v244, a6\n"
    "v_accvgpr_read_b32 v245, a7\n"
    "v_mul_f32 v244, v254, v244\n"
    "v_mul_f32 v245, v254, v245\n"
    "v_cvt_pk_bf16_f32 v249, v244, v245\n"
    "v_permlane16_swap_b32 v246, v248\n"
    "s_nop 0\n"
    "v_permlane16_swap_b32 v247, v249\n"
    "v_accvgpr_read_b32 v244, a8\n"
    "v_accvgpr_read_b32 v245, a9\n"
    "v_mul_f32 v244, v254, v244\n"
    "v_mul_f32 v245, v254, v245\n"
    "v_cvt_pk_bf16_f32 v250, v244, v245\n"
    "v_accvgpr_read_b32 v244, a10\n"
    "v_accvgpr_read_b32 v245, a11\n"
    "v_mul_f32 v244, v254, v244\n"
    "v_mul_f32 v245, v254, v245\n"
    "v_cvt_pk_bf16_f32 v251, v244, v245\n"
    "v_accvgpr_read_b32 v244, a12\n"
    "v_accvgpr_read_b32 v245, a13\n"
    "v_mul_f32 v244, v254, v244\n"
    "v_mul_f32 v245, v254, v245\n"
    "v_cvt_pk_bf16_f32 v252, v244, v245\n"
    "v_accvgpr_read_b32 v244, a14\n"
    "v_accvgpr_read_b32 v245, a15\n"
    "v_mul_f32 v244, v254, v244\n"
    "v_mul_f32 v245, v254, v245\n"
    "v_cvt_pk_bf16_f32 v253, v244, v245\n"
    "v_permlane16_swap_b32 v250, v252\n"
    "s_nop 0\n"
    "v_permlane16_swap_b32 v251, v253\n"
    "v_mov_b32 v244, v246\n"
    "v_mov_b32_dpp v246, v250 row_ror:8 row_mask:0xf bank_mask:0xc\n"
    "v_mov_b32 v245, v247\n"
    "v_mov_b32_dpp v247, v251 row_ror:8 row_mask:0xf bank_mask:0xc\n"
    "v_mov_b32_dpp v250, v244 row_ror:8 row_mask:0xf bank_mask:0x3\n"
    "v_mov_b32_dpp v251, v245 row_ror:8 row_mask:0xf bank_mask:0x3\n"
    "v_mov_b32 v244, v248\n"
    "v_mov_b32_dpp v248, v252 row_ror:8 row_mask:0xf bank_mask:0xc\n"
    "v_mov_b32 v245, v249\n"
    "v_mov_b32_dpp v249, v253 row_ror:8 row_mask:0xf bank_mask:0xc\n"
    "v_mov_b32_dpp v252, v244 row_ror:8 row_mask:0xf bank_mask:0x3\n"
    "v_mov_b32_dpp v253, v245 row_ror:8 row_mask:0xf bank_mask:0x3\n"
    "buffer_store_dwordx4 v[246:249], v7, s[28:31], s49 offen offset:0 nt\n"
    "buffer_store_dwordx4 v[250:253], v7, s[28:31], s49 offen offset:2048 nt\n"
    "v_accvgpr_read_b32 v244, a16\n"
    "v_accvgpr_read_b32 v245, a17\n"
    "v_mul_f32 v244, v254, v244\n"
    "v_mul_f32 v245, v254, v245\n"
    "v_cvt_pk_bf16_f32 v246, v244, v245\n"
    "v_accvgpr_read_b32 v244, a18\n"
    "v_mfma_f32_16x16x32_bf16 v[64:67], a[172:175], v[160:163], v[64:67]\n"
    "v_accvgpr_read_b32 v245, a19\n"
    "v_mul_f32 v244, v254, v244\n"
    "v_mul_f32 v245, v254, v245\n"
    "v_cvt_pk_bf16_f32 v247, v244, v245\n"
    "v_accvgpr_read_b32 v244, a20\n"
    "v_accvgpr_read_b32 v245, a21\n"
    "v_mul_f32 v244, v254, v244\n"
    "v_mul_f32 v245, v254, v245\n"
    "v_cvt_pk_bf16_f32 v248, v244, v245\n"
    "v_accvgpr_read_b32 v244, a22\n"
    "v_mfma_f32_16x16x32_bf16 v[68:71], a[176:179], v[148:151], v[92:95]\n"
    "v_accvgpr_read_b32 v245, a23\n"
    "v_mul_f32 v244, v254, v244\n"
    "v_mul_f32 v245, v254, v245\n"
    "v_cvt_pk_bf16_f32 v249, v244, v245\n"
    "v_permlane16_swap_b32 v246, v248\n"
    "s_nop 0\n"
    "v_permlane16_swap_b32 v247, v249\n"
    "v_accvgpr_read_b32 v244, a24\n"
    "v_accvgpr_read_b32 v245, a25\n"
    "v_mfma_f32_16x16x32_bf16 v[68:71], a[180:183], v[152:155], v[68:71]\n"
    "v_mul_f32 v244, v254, v244\n"
    "v_mul_f32 v245, v254, v245\n"
    "v_cvt_pk_bf16_f32 v250, v244, v245\n"
    "v_accvgpr_read_b32 v244, a26\n"
    "v_accvgpr_read_b32 v245, a27\n"
    "v_mul_f32 v244, v254, v244\n"
    "v_mul_f32 v245, v254, v245\n"
    "v_cvt_pk_bf16_f32 v251, v244, v245\n"
    "v_accvgpr_read_b32 v244, a28\n"
    "v_accvgpr_read_b32 v245, a29\n"
    "v_mfma_f32_16x16x32_bf16 v[68:71], a[184:187], v[156:159], v[68:71]\n"
    "v_mul_f32 v244, v254, v244\n"
    "v_mul_f32 v245, v254, v245\n"
    "v_cvt_pk_bf16_f32 v252, v244, v245\n"
    "v_accvgpr_read_b32 v244, a30\n"
    "v_accvgpr_read_b32 v245, a31\n"
    "v_mul_f32 v244, v254, v244\n"
    "v_mul_f32 v245, v254, v245\n"
    "v_cvt_pk_bf16_f32 v253, v244, v245\n"
    "v_permlane16_swap_b32 v250, v252\n"
    "v_mfma_f32_16x16x32_bf16 v[68:71], a[188:191], v[160:163], v[68:71]\n"
    "v_permlane16_swap_b32 v251, v253\n"
    "v_mov_b32 v244, v246\n"
    "v_mov_b32_dpp v246, v250 row_ror:8 row_mask:0xf bank_mask:0xc\n"
    "v_mov_b32 v245, v247\n"
    "v_mov_b32_dpp v247, v251 row_ror:8 row_mask:0xf bank_mask:0xc\n"
    "v_mov_b32_dpp v250, v244 row_ror:8 row_mask:0xf bank_mask:0x3\n"
    "v_mov_b32_dpp v251, v245 row_ror:8 row_mask:0xf bank_mask:0x3\n"
    "v_mov_b32 v244, v248\n"
    "v_mov_b32_dpp v248, v252 row_ror:8 row_mask:0xf bank_mask:0xc\n"
    "v_mfma_f32_16x16x32_bf16 v[72:75], a[192:195], v[148:151], v[92:95]\n"
    "v_exp_f32 v8, v64\n"
    "v_exp_f32 v9, v65\n"
    "v_exp_f32 v10, v66\n"
    "v_exp_f32 v11, v67\n"
    "v_cmp_nge_f32 vcc, s40, v12\n"
    "v_mfma_f32_16x16x32_bf16 v[72:75], a[196:199], v[152:155], v[72:75]\n"
    "v_mov_b32 v245, v249\n"
    "v_mov_b32_dpp v249, v253 row_ror:8 row_mask:0xf bank_mask:0xc\n"
    "v_mov_b32_dpp v252, v244 row_ror:8 row_mask:0xf bank_mask:0x3\n"
    "v_mov_b32_dpp v253, v245 row_ror:8 row_mask:0xf bank_mask:0x3\n"
    "buffer_store_dwordx4 v[246:249], v7, s[28:31], s49 offen offset:128 nt\n"
    "buffer_store_dwordx4 v[250:253], v7, s[28:31], s49 offen offset:2176 nt\n"
    "v_mfma_f32_16x16x32_bf16 v[72:75], a[200:203], v[156:159], v[72:75]\n"
    "v_mfma_f32_16x16x32_bf16 v[72:75], a[204:207], v[160:163], v[72:75]\n"
    "s_cbranch_vccnz LTR_70\n"
    "LRET_70:\n"
    "v_add_f32 v22, v22, v12\n"
    "v_mfma_f32_16x16x32_bf16 v[76:79], a[208:211], v[148:151], v[92:95]\n"
    "v_exp_f32 v240, v68\n"
    "v_exp_f32 v241, v69\n"
    "v_exp_f32 v242, v70\n"
    "v_exp_f32 v243, v71\n"
    "v_mfma_f32_16x16x32_bf16 v[76:79], a[212:215], v[152:155], v[76:79]\n"
    "v_mfma_f32_16x16x32_bf16 v[76:79], a[216:219], v[156:159], v[76:79]\n"
    "v_mfma_f32_16x16x32_bf16 v[76:79], a[220:223], v[160:163], v[76:79]\n"
    "v_mfma_f32_16x16x32_bf16 a[32:35], a[224:227], v[40:43], a[32:35]\n"
    "v_mfma_f32_16x16x32_bf16 a[32:35], v[180:183], v[44:47], a[32:35]\n"
    "v_mfma_f32_16x16x32_bf16 a[36:39], a[228:231], v[40:43], a[36:39]\n"
    "v_mfma_f32_16x16x32_bf16 a[36:39], v[184:187], v[44:47], a[36:39]\n"
    "v_mfma_f32_16x16x32_bf16 a[40:43], a[232:235], v[40:43], a[40:43]\n"
    "v_mfma_f32_16x16x32_bf16 a[40:43], v[188:191], v[44:47], a[40:43]\n"
    "v_mfma_f32_16x16x32_bf16 a[44:47], a[236:239], v[40:43], a[44:47]\n"
    "v_mfma_f32_16x16x32_bf16 a[44:47], v[192:195], v[44:47], a[44:47]\n"
    "v_mfma_f32_16x16x32_bf16 a[48:51], a[240:243], v[40:43], a[48:51]\n"
    "v_mfma_f32_16x16x32_bf16 a[48:51], v[196:199], v[44:47], a[48:51]\n"
    "v_mfma_f32_16x16x32_bf16 a[52:55], a[244:247], v[40:43], a[52:55]\n"
    "v_mfma_f32_16x16x32_bf16 a[52:55], v[200:203], v[44:47], a[52:55]\n"
    "v_mfma_f32_16x16x32_bf16 a[56:59], a[248:251], v[40:43], a[56:59]\n"
    "v_mfma_f32_16x16x32_bf16 a[56:59], v[204:207], v[44:47], a[56:59]\n"
    "v_mfma_f32_16x16x32_bf16 a[60:63], a[252:255], v[40:43], a[60:63]\n"
    "v_cvt_pk_bf16_f32 v40, v8, v9\n"
    "v_cvt_pk_bf16_f32 v41, v10, v11\n"
    "v_exp_f32 v8, v72\n"
    "v_exp_f32 v9, v73\n"
    "v_exp_f32 v10, v74\n"
    "v_exp_f32 v11, v75\n"
    "v_cvt_pk_bf16_f32 v42, v240, v241\n"
    "v_cvt_pk_bf16_f32 v43, v242, v243\n"
    "v_exp_f32 v240, v76\n"
    "v_exp_f32 v241, v77\n"
    "v_exp_f32 v242, v78\n"
    "v_exp_f32 v243, v79\n"
    "v_mfma_f32_16x16x32_bf16 a[60:63], v[208:211], v[44:47], a[60:63]\n"
    "v_cvt_pk_bf16_f32 v44, v8, v9\n"
    "v_cvt_pk_bf16_f32 v45, v10, v11\n"
    "v_cvt_pk_bf16_f32 v46, v240, v241\n"
    "v_cvt_pk_bf16_f32 v47, v242, v243\n"
    "v_mfma_f32_16x16x32_bf16 v[12:15], v[16:19], v[40:43], 0\n"
    "s_nop 0\n"
    "v_mfma_f32_16x16x32_bf16 v[12:15], v[16:19], v[44:47], v[12:15]\n"
    "v_mfma_f32_16x16x32_bf16 a[64:67], a[224:227], v[32:35], a[64:67]\n"
    "v_mfma_f32_16x16x32_bf16 a[64:67], v[180:183], v[36:39], a[64:67]\n"
    "v_mfma_f32_16x16x32_bf16 a[68:71], a[228:231], v[32:35], a[68:71]\n"
    "v_rcp_f32 v254, v21\n"
    "s_add_u32 s49, s43, 4096\n"
    "v_accvgpr_read_b32 v244, a32\n"
    "v_accvgpr_read_b32 v245, a33\n"
    "v_mul_f32 v244, v254, v244\n"
    "v_mul_f32 v245, v254, v245\n"
    "v_cvt_pk_bf16_f32 v246, v244, v245\n"
    "v_accvgpr_read_b32 v244, a34\n"
    "v_accvgpr_read_b32 v245, a35\n"
    "v_mul_f32 v244, v254, v244\n"
    "v_mul_f32 v245, v254, v245\n"
    "v_cvt_pk_bf16_f32 v247, v244, v245\n"
    "v_accvgpr_read_b32 v244, a36\n"
    "v_accvgpr_read_b32 v245, a37\n"
    "v_mul_f32 v244, v254, v244\n"
    "v_mul_f32 v245, v254, v245\n"
    "v_cvt_pk_bf16_f32 v248, v244, v245\n"
    "v_accvgpr_read_b32 v244, a38\n"
    "v_accvgpr_read_b32 v245, a39\n"
    "v_mul_f32 v244, v254, v244\n"
    "v_mul_f32 v245, v254, v245\n"
    "v_cvt_pk_bf16_f32 v249, v244, v245\n"
    "v_permlane16_swap_b32 v246, v248\n"
    "s_nop 0\n"
    "v_permlane16_swap_b32 v247, v249\n"
    "v_accvgpr_read_b32 v244, a40\n"
    "v_accvgpr_read_b32 v245, a41\n"
    "v_mul_f32 v244, v254, v244\n"
    "v_mul_f32 v245, v254, v245\n"
    "v_cvt_pk_bf16_f32 v250, v244, v245\n"
    "v_accvgpr_read_b32 v244, a42\n"
    "v_accvgpr_read_b32 v245, a43\n"
    "v_mul_f32 v244, v254, v244\n"
    "v_mul_f32 v245, v254, v245\n"
    "v_cvt_pk_bf16_f32 v251, v244, v245\n"
    "v_accvgpr_read_b32 v244, a44\n"
    "v_accvgpr_read_b32 v245, a45\n"
    "v_mul_f32 v244, v254, v244\n"
    "v_mul_f32 v245, v254, v245\n"
    "v_cvt_pk_bf16_f32 v252, v244, v245\n"
    "v_accvgpr_read_b32 v244, a46\n"
    "v_accvgpr_read_b32 v245, a47\n"
    "v_mul_f32 v244, v254, v244\n"
    "v_mul_f32 v245, v254, v245\n"
    "v_cvt_pk_bf16_f32 v253, v244, v245\n"
    "v_permlane16_swap_b32 v250, v252\n"
    "s_nop 0\n"
    "v_permlane16_swap_b32 v251, v253\n"
    "v_mov_b32 v244, v246\n"
    "v_mov_b32_dpp v246, v250 row_ror:8 row_mask:0xf bank_mask:0xc\n"
    "v_mov_b32 v245, v247\n"
    "v_mov_b32_dpp v247, v251 row_ror:8 row_mask:0xf bank_mask:0xc\n"
    "v_mov_b32_dpp v250, v244 row_ror:8 row_mask:0xf bank_mask:0x3\n"
    "v_mov_b32_dpp v251, v245 row_ror:8 row_mask:0xf bank_mask:0x3\n"
    "v_mov_b32 v244, v248\n"
    "v_mov_b32_dpp v248, v252 row_ror:8 row_mask:0xf bank_mask:0xc\n"
    "v_mov_b32 v245, v249\n"
    "v_mov_b32_dpp v249, v253 row_ror:8 row_mask:0xf bank_mask:0xc\n"
    "v_mov_b32_dpp v252, v244 row_ror:8 row_mask:0xf bank_mask:0x3\n"
    "v_mov_b32_dpp v253, v245 row_ror:8 row_mask:0xf bank_mask:0x3\n"
    "buffer_store_dwordx4 v[246:249], v7, s[28:31], s49 offen offset:0 nt\n"
    "buffer_store_dwordx4 v[250:253], v7, s[28:31], s49 offen offset:2048 nt\n"
    "v_accvgpr_read_b32 v244, a48\n"
    "v_accvgpr_read_b32 v245, a49\n"
    "v_mul_f32 v244, v254, v244\n"
    "v_mul_f32 v245, v254, v245\n"
    "v_cvt_pk_bf16_f32 v246, v244, v245\n"
    "v_accvgpr_read_b32 v244, a50\n"
    "v_accvgpr_read_b32 v245, a51\n"
    "v_mul_f32 v244, v254, v244\n"
    "v_mul_f32 v245, v254, v245\n"
    "v_cvt_pk_bf16_f32 v247, v244, v245\n"
    "v_accvgpr_read_b32 v244, a52\n"
    "v_accvgpr_read_b32 v245, a53\n"
    "v_mul_f32 v244, v254, v244\n"
    "v_mul_f32 v245, v254, v245\n"
    "v_cvt_pk_bf16_f32 v248, v244, v245\n"
    "v_accvgpr_read_b32 v244, a54\n"
    "v_accvgpr_read_b32 v245, a55\n"
    "v_mul_f32 v244, v254, v244\n"
    "v_mul_f32 v245, v254, v245\n"
    "v_cvt_pk_bf16_f32 v249, v244, v245\n"
    "v_permlane16_swap_b32 v246, v248\n"
    "s_nop 0\n"
    "v_permlane16_swap_b32 v247, v249\n"
    "v_accvgpr_read_b32 v244, a56\n"
    "v_accvgpr_read_b32 v245, a57\n"
    "v_mul_f32 v244, v254, v244\n"
    "v_mul_f32 v245, v254, v245\n"
    "v_cvt_pk_bf16_f32 v250, v244, v245\n"
    "v_accvgpr_read_b32 v244, a58\n"
    "v_accvgpr_read_b32 v245, a59\n"
    "v_mul_f32 v244, v254, v244\n"
    "v_mul_f32 v245, v254, v245\n"
    "v_cvt_pk_bf16_f32 v251, v244, v245\n"
    "v_accvgpr_read_b32 v244, a60\n"
    "v_accvgpr_read_b32 v245, a61\n"
    "v_mul_f32 v244, v254, v244\n"
    "v_mul_f32 v245, v254, v245\n"
    "v_mfma_f32_16x16x32_bf16 a[68:71], v[184:187], v[36:39], a[68:71]\n"
    "v_cvt_pk_bf16_f32 v252, v244, v245\n"
    "v_accvgpr_read_b32 v244, a62\n"
    "v_accvgpr_read_b32 v245, a63\n"
    "v_mul_f32 v244, v254, v244\n"
    "v_mul_f32 v245, v254, v245\n"
    "v_cvt_pk_bf16_f32 v253, v244, v245\n"
    "v_permlane16_swap_b32 v250, v252\n"
    "s_nop 0\n"
    "v_permlane16_swap_b32 v251, v253\n"
    "v_mfma_f32_16x16x32_bf16 a[72:75], a[232:235], v[32:35], a[72:75]\n"
    "v_mov_b32 v244, v246\n"
    "v_mov_b32_dpp v246, v250 row_ror:8 row_mask:0xf bank_mask:0xc\n"
    "v_mov_b32 v245, v247\n"
    "v_mov_b32_dpp v247, v251 row_ror:8 row_mask:0xf bank_mask:0xc\n"
    "v_mov_b32_dpp v250, v244 row_ror:8 row_mask:0xf bank_mask:0x3\n"
    "v_mov_b32_dpp v251, v245 row_ror:8 row_mask:0xf bank_mask:0x3\n"
    "v_mov_b32 v244, v248\n"
    "v_mov_b32_dpp v248, v252 row_ror:8 row_mask:0xf bank_mask:0xc\n"
    "v_cmp_nge_f32 vcc, s40, v12\n"
    "v_mfma_f32_16x16x32_bf16 a[72:75], v[188:191], v[36:39], a[72:75]\n"
    "v_mov_b32 v245, v249\n"
    "v_mov_b32_dpp v249, v253 row_ror:8 row_mask:0xf bank_mask:0xc\n"
    "v_mov_b32_dpp v252, v244 row_ror:8 row_mask:0xf bank_mask:0x3\n"
    "v_mov_b32_dpp v253, v245 row_ror:8 row_mask:0xf bank_mask:0x3\n"
    "buffer_store_dwordx4 v[246:249], v7, s[28:31], s49 offen offset:128 nt\n"
    "buffer_store_dwordx4 v[250:253], v7, s[28:31], s49 offen offset:2176 nt\n"
    "v_mfma_f32_16x16x32_bf16 a[76:79], a[236:239], v[32:35], a[76:79]\n"
    "v_mfma_f32_16x16x32_bf16 a[76:79], v[192:195], v[36:39], a[76:79]\n"
    "s_cbranch_vccnz LTR_71\n"
    "LRET_71:\n"
    "v_add_f32 v23, v23, v12\n"
    "v_mfma_f32_16x16x32_bf16 a[80:83], a[240:243], v[32:35], a[80:83]\n"
    "v_mfma_f32_16x16x32_bf16 a[80:83], v[196:199], v[36:39], a[80:83]\n"
    "v_mfma_f32_16x16x32_bf16 a[84:87], a[244:247], v[32:35], a[84:87]\n"
    "v_mfma_f32_16x16x32_bf16 a[84:87], v[200:203], v[36:39], a[84:87]\n"
    "v_mfma_f32_16x16x32_bf16 a[88:91], a[248:251], v[32:35], a[88:91]\n"
    "v_mfma_f32_16x16x32_bf16 a[88:91], v[204:207], v[36:39], a[88:91]\n"
    "v_mfma_f32_16x16x32_bf16 a[92:95], a[252:255], v[32:35], a[92:95]\n"
    "v_mfma_f32_16x16x32_bf16 a[92:95], v[208:211], v[36:39], a[92:95]\n"
    "v_mfma_f32_16x16x32_bf16 a[96:99], a[224:227], v[40:43], a[96:99]\n"
    "v_mfma_f32_16x16x32_bf16 a[96:99], v[180:183], v[44:47], a[96:99]\n"
    "v_mfma_f32_16x16x32_bf16 a[100:103], a[228:231], v[40:43], a[100:103]\n"
    "v_rcp_f32 v254, v22\n"
    "s_add_u32 s49, s43, 8192\n"
    "v_accvgpr_read_b32 v244, a64\n"
    "v_accvgpr_read_b32 v245, a65\n"
    "v_mul_f32 v244, v254, v244\n"
    "v_mul_f32 v245, v254, v245\n"
    "v_cvt_pk_bf16_f32 v246, v244, v245\n"
    "v_accvgpr_read_b32 v244, a66\n"
    "v_accvgpr_read_b32 v245, a67\n"
    "v_mul_f32 v244, v254, v244\n"
    "v_mul_f32 v245, v254, v245\n"
    "v_cvt_pk_bf16_f32 v247, v244, v245\n"
    "v_accvgpr_read_b32 v244, a68\n"
    "v_accvgpr_read_b32 v245, a69\n"
    "v_mul_f32 v244, v254, v244\n"
    "v_mul_f32 v245, v254, v245\n"
    "v_cvt_pk_bf16_f32 v248, v244, v245\n"
    "v_accvgpr_read_b32 v244, a70\n"
    "v_accvgpr_read_b32 v245, a71\n"
    "v_mul_f32 v244, v254, v244\n"
    "v_mul_f32 v245, v254, v245\n"
    "v_cvt_pk_bf16_f32 v249, v244, v245\n"
    "v_permlane16_swap_b32 v246, v248\n"
    "s_nop 0\n"
    "v_permlane16_swap_b32 v247, v249\n"
    "v_accvgpr_read_b32 v244, a72\n"
    "v_accvgpr_read_b32 v245, a73\n"
    "v_mul_f32 v244, v254, v244\n"
    "v_mul_f32 v245, v254, v245\n"
    "v_cvt_pk_bf16_f32 v250, v244, v245\n"
    "v_accvgpr_read_b32 v244, a74\n"
    "v_accvgpr_read_b32 v245, a75\n"
    "v_mul_f32 v244, v254, v244\n"
    "v_mul_f32 v245, v254, v245\n"
    "v_cvt_pk_bf16_f32 v251, v244, v245\n"
    "v_accvgpr_read_b32 v244, a76\n"
    "v_accvgpr_read_b32 v245, a77\n"
    "v_mul_f32 v244, v254, v244\n"
    "v_mul_f32 v245, v254, v245\n"
    "v_cvt_pk_bf16_f32 v252, v244, v245\n"
    "v_accvgpr_read_b32 v244, a78\n"
    "v_accvgpr_read_b32 v245, a79\n"
    "v_mul_f32 v244, v254, v244\n"
    "v_mul_f32 v245, v254, v245\n"
    "v_cvt_pk_bf16_f32 v253, v244, v245\n"
    "v_permlane16_swap_b32 v250, v252\n"
    "s_nop 0\n"
    "v_permlane16_swap_b32 v251, v253\n"
    "v_mov_b32 v244, v246\n"
    "v_mov_b32_dpp v246, v250 row_ror:8 row_mask:0xf bank_mask:0xc\n"
    "v_mov_b32 v245, v247\n"
    "v_mov_b32_dpp v247, v251 row_ror:8 row_mask:0xf bank_mask:0xc\n"
    "v_mov_b32_dpp v250, v244 row_ror:8 row_mask:0xf bank_mask:0x3\n"
    "v_mov_b32_dpp v251, v245 row_ror:8 row_mask:0xf bank_mask:0x3\n"
    "v_mov_b32 v244, v248\n"
    "v_mov_b32_dpp v248, v252 row_ror:8 row_mask:0xf bank_mask:0xc\n"
    "v_mov_b32 v245, v249\n"
    "v_mov_b32_dpp v249, v253 row_ror:8 row_mask:0xf bank_mask:0xc\n"
    "v_mov_b32_dpp v252, v244 row_ror:8 row_mask:0xf bank_mask:0x3\n"
    "v_mov_b32_dpp v253, v245 row_ror:8 row_mask:0xf bank_mask:0x3\n"
    "buffer_store_dwordx4 v[246:249], v7, s[28:31], s49 offen offset:0 nt\n"
    "buffer_store_dwordx4 v[250:253], v7, s[28:31], s49 offen offset:2048 nt\n"
    "v_accvgpr_read_b32 v244, a80\n"
    "v_accvgpr_read_b32 v245, a81\n"
    "v_mul_f32 v244, v254, v244\n"
    "v_mul_f32 v245, v254, v245\n"
    "v_cvt_pk_bf16_f32 v246, v244, v245\n"
    "v_accvgpr_read_b32 v244, a82\n"
    "v_accvgpr_read_b32 v245, a83\n"
    "v_mul_f32 v244, v254, v244\n"
    "v_mul_f32 v245, v254, v245\n"
    "v_cvt_pk_bf16_f32 v247, v244, v245\n"
    "v_accvgpr_read_b32 v244, a84\n"
    "v_accvgpr_read_b32 v245, a85\n"
    "v_mul_f32 v244, v254, v244\n"
    "v_mul_f32 v245, v254, v245\n"
    "v_cvt_pk_bf16_f32 v248, v244, v245\n"
    "v_accvgpr_read_b32 v244, a86\n"
    "v_accvgpr_read_b32 v245, a87\n"
    "v_mul_f32 v244, v254, v244\n"
    "v_mul_f32 v245, v254, v245\n"
    "v_cvt_pk_bf16_f32 v249, v244, v245\n"
    "v_permlane16_swap_b32 v246, v248\n"
    "s_nop 0\n"
    "v_permlane16_swap_b32 v247, v249\n"
    "v_accvgpr_read_b32 v244, a88\n"
    "v_accvgpr_read_b32 v245, a89\n"
    "v_mul_f32 v244, v254, v244\n"
    "v_mul_f32 v245, v254, v245\n"
    "v_cvt_pk_bf16_f32 v250, v244, v245\n"
    "v_accvgpr_read_b32 v244, a90\n"
    "v_accvgpr_read_b32 v245, a91\n"
    "v_mul_f32 v244, v254, v244\n"
    "v_mul_f32 v245, v254, v245\n"
    "v_cvt_pk_bf16_f32 v251, v244, v245\n"
    "v_accvgpr_read_b32 v244, a92\n"
    "v_accvgpr_read_b32 v245, a93\n"
    "v_mul_f32 v244, v254, v244\n"
    "v_mul_f32 v245, v254, v245\n"
    "v_mfma_f32_16x16x32_bf16 a[100:103], v[184:187], v[44:47], a[100:103]\n"
    "v_cvt_pk_bf16_f32 v252, v244, v245\n"
    "v_accvgpr_read_b32 v244, a94\n"
    "v_mfma_f32_16x16x32_bf16 a[104:107], a[232:235], v[40:43], a[104:107]\n"
    "v_accvgpr_read_b32 v245, a95\n"
    "v_mul_f32 v244, v254, v244\n"
    "v_mfma_f32_16x16x32_bf16 a[104:107], v[188:191], v[44:47], a[104:107]\n"
    "v_mul_f32 v245, v254, v245\n"
    "v_cvt_pk_bf16_f32 v253, v244, v245\n"
    "v_mfma_f32_16x16x32_bf16 a[108:111], a[236:239], v[40:43], a[108:111]\n"
    "v_permlane16_swap_b32 v250, v252\n"
    "v_mfma_f32_16x16x32_bf16 a[108:111], v[192:195], v[44:47], a[108:111]\n"
    "v_permlane16_swap_b32 v251, v253\n"
    "v_mfma_f32_16x16x32_bf16 a[112:115], a[240:243], v[40:43], a[112:115]\n"
    "v_mov_b32 v244, v246\n"
    "v_mov_b32_dpp v246, v250 row_ror:8 row_mask:0xf bank_mask:0xc\n"
    "v_mfma_f32_16x16x32_bf16 a[112:115], v[196:199], v[44:47], a[112:115]\n"
    "v_mov_b32 v245, v247\n"
    "v_mov_b32_dpp v247, v251 row_ror:8 row_mask:0xf bank_mask:0xc\n"
    "v_mfma_f32_16x16x32_bf16 a[116:119], a[244:247], v[40:43], a[116:119]\n"
    "v_mov_b32_dpp v250, v244 row_ror:8 row_mask:0xf bank_mask:0x3\n"
    "v_mov_b32_dpp v251, v245 row_ror:8 row_mask:0xf bank_mask:0x3\n"
    "v_mfma_f32_16x16x32_bf16 a[116:119], v[200:203], v[44:47], a[116:119]\n"
    "v_mov_b32 v244, v248\n"
    "v_mov_b32_dpp v248, v252 row_ror:8 row_mask:0xf bank_mask:0xc\n"
    "v_mfma_f32_16x16x32_bf16 a[120:123], a[248:251], v[40:43], a[120:123]\n"
    "v_mov_b32 v245, v249\n"
    "v_mov_b32_dpp v249, v253 row_ror:8 row_mask:0xf bank_mask:0xc\n"
    "v_mfma_f32_16x16x32_bf16 a[120:123], v[204:207], v[44:47], a[120:123]\n"
    "v_mov_b32_dpp v252, v244 row_ror:8 row_mask:0xf bank_mask:0x3\n"
    "v_mov_b32_dpp v253, v245 row_ror:8 row_mask:0xf bank_mask:0x3\n"
    "v_mfma_f32_16x16x32_bf16 a[124:127], a[252:255], v[40:43], a[124:127]\n"
    "buffer_store_dwordx4 v[246:249], v7, s[28:31], s49 offen offset:128 nt\n"
    "v_mfma_f32_16x16x32_bf16 a[124:127], v[208:211], v[44:47], a[124:127]\n"
    "buffer_store_dwordx4 v[250:253], v7, s[28:31], s49 offen offset:2176 nt\n"
    "v_rcp_f32 v254, v23\n"
    "s_add_u32 s49, s43, 12288\n"
    "v_accvgpr_read_b32 v244, a96\n"
    "v_accvgpr_read_b32 v245, a97\n"
    "v_mul_f32 v244, v254, v244\n"
    "v_mul_f32 v245, v254, v245\n"
    "v_cvt_pk_bf16_f32 v246, v244, v245\n"
    "v_accvgpr_read_b32 v244, a98\n"
    "v_accvgpr_read_b32 v245, a99\n"
    "v_mul_f32 v244, v254, v244\n"
    "v_mul_f32 v245, v254, v245\n"
    "v_cvt_pk_bf16_f32 v247, v244, v245\n"
    "v_accvgpr_read_b32 v244, a100\n"
    "v_accvgpr_read_b32 v245, a101\n"
    "v_mul_f32 v244, v254, v244\n"
    "v_mul_f32 v245, v254, v245\n"
    "v_cvt_pk_bf16_f32 v248, v244, v245\n"
    "v_accvgpr_read_b32 v244, a102\n"
    "v_accvgpr_read_b32 v245, a103\n"
    "v_mul_f32 v244, v254, v244\n"
    "v_mul_f32 v245, v254, v245\n"
    "v_cvt_pk_bf16_f32 v249, v244, v245\n"
    "v_permlane16_swap_b32 v246, v248\n"
    "s_nop 0\n"
    "v_permlane16_swap_b32 v247, v249\n"
    "v_accvgpr_read_b32 v244, a104\n"
    "v_accvgpr_read_b32 v245, a105\n"
    "v_mul_f32 v244, v254, v244\n"
    "v_mul_f32 v245, v254, v245\n"
    "v_cvt_pk_bf16_f32 v250, v244, v245\n"
    "v_accvgpr_read_b32 v244, a106\n"
    "v_accvgpr_read_b32 v245, a107\n"
    "v_mul_f32 v244, v254, v244\n"
    "v_mul_f32 v245, v254, v245\n"
    "v_cvt_pk_bf16_f32 v251, v244, v245\n"
    "v_accvgpr_read_b32 v244, a108\n"
    "v_accvgpr_read_b32 v245, a109\n"
    "v_mul_f32 v244, v254, v244\n"
    "v_mul_f32 v245, v254, v245\n"
    "v_cvt_pk_bf16_f32 v252, v244, v245\n"
    "v_accvgpr_read_b32 v244, a110\n"
    "v_accvgpr_read_b32 v245, a111\n"
    "v_mul_f32 v244, v254, v244\n"
    "v_mul_f32 v245, v254, v245\n"
    "v_cvt_pk_bf16_f32 v253, v244, v245\n"
    "v_permlane16_swap_b32 v250, v252\n"
    "s_nop 0\n"
    "v_permlane16_swap_b32 v251, v253\n"
    "v_mov_b32 v244, v246\n"
    "v_mov_b32_dpp v246, v250 row_ror:8 row_mask:0xf bank_mask:0xc\n"
    "v_mov_b32 v245, v247\n"
    "v_mov_b32_dpp v247, v251 row_ror:8 row_mask:0xf bank_mask:0xc\n"
    "v_mov_b32_dpp v250, v244 row_ror:8 row_mask:0xf bank_mask:0x3\n"
    "v_mov_b32_dpp v251, v245 row_ror:8 row_mask:0xf bank_mask:0x3\n"
    "v_mov_b32 v244, v248\n"
    "v_mov_b32_dpp v248, v252 row_ror:8 row_mask:0xf bank_mask:0xc\n"
    "v_mov_b32 v245, v249\n"
    "v_mov_b32_dpp v249, v253 row_ror:8 row_mask:0xf bank_mask:0xc\n"
    "v_mov_b32_dpp v252, v244 row_ror:8 row_mask:0xf bank_mask:0x3\n"
    "v_mov_b32_dpp v253, v245 row_ror:8 row_mask:0xf bank_mask:0x3\n"
    "buffer_store_dwordx4 v[246:249], v7, s[28:31], s49 offen offset:0 nt\n"
    "buffer_store_dwordx4 v[250:253], v7, s[28:31], s49 offen offset:2048 nt\n"
    "v_accvgpr_read_b32 v244, a112\n"
    "v_accvgpr_read_b32 v245, a113\n"
    "v_mul_f32 v244, v254, v244\n"
    "v_mul_f32 v245, v254, v245\n"
    "v_cvt_pk_bf16_f32 v246, v244, v245\n"
    "v_accvgpr_read_b32 v244, a114\n"
    "v_accvgpr_read_b32 v245, a115\n"
    "v_mul_f32 v244, v254, v244\n"
    "v_mul_f32 v245, v254, v245\n"
    "v_cvt_pk_bf16_f32 v247, v244, v245\n"
    "v_accvgpr_read_b32 v244, a116\n"
    "v_accvgpr_read_b32 v245, a117\n"
    "v_mul_f32 v244, v254, v244\n"
    "v_mul_f32 v245, v254, v245\n"
    "v_cvt_pk_bf16_f32 v248, v244, v245\n"
    "v_accvgpr_read_b32 v244, a118\n"
    "v_accvgpr_read_b32 v245, a119\n"
    "v_mul_f32 v244, v254, v244\n"
    "v_mul_f32 v245, v254, v245\n"
    "v_cvt_pk_bf16_f32 v249, v244, v245\n"
    "v_permlane16_swap_b32 v246, v248\n"
    "s_nop 0\n"
    "v_permlane16_swap_b32 v247, v249\n"
    "v_accvgpr_read_b32 v244, a120\n"
    "v_accvgpr_read_b32 v245, a121\n"
    "v_mul_f32 v244, v254, v244\n"
    "v_mul_f32 v245, v254, v245\n"
    "v_cvt_pk_bf16_f32 v250, v244, v245\n"
    "v_accvgpr_read_b32 v244, a122\n"
    "v_accvgpr_read_b32 v245, a123\n"
    "v_mul_f32 v244, v254, v244\n"
    "v_mul_f32 v245, v254, v245\n"
    "v_cvt_pk_bf16_f32 v251, v244, v245\n"
    "v_accvgpr_read_b32 v244, a124\n"
    "v_accvgpr_read_b32 v245, a125\n"
    "v_mul_f32 v244, v254, v244\n"
    "v_mul_f32 v245, v254, v245\n"
    "v_cvt_pk_bf16_f32 v252, v244, v245\n"
    "v_accvgpr_read_b32 v244, a126\n"
    "v_accvgpr_read_b32 v245, a127\n"
    "v_mul_f32 v244, v254, v244\n"
    "v_mul_f32 v245, v254, v245\n"
    "v_cvt_pk_bf16_f32 v253, v244, v245\n"
    "v_permlane16_swap_b32 v250, v252\n"
    "s_nop 0\n"
    "v_permlane16_swap_b32 v251, v253\n"
    "v_mov_b32 v244, v246\n"
    "v_mov_b32_dpp v246, v250 row_ror:8 row_mask:0xf bank_mask:0xc\n"
    "v_mov_b32 v245, v247\n"
    "v_mov_b32_dpp v247, v251 row_ror:8 row_mask:0xf bank_mask:0xc\n"
    "v_mov_b32_dpp v250, v244 row_ror:8 row_mask:0xf bank_mask:0x3\n"
    "v_mov_b32_dpp v251, v245 row_ror:8 row_mask:0xf bank_mask:0x3\n"
    "v_mov_b32 v244, v248\n"
    "v_mov_b32_dpp v248, v252 row_ror:8 row_mask:0xf bank_mask:0xc\n"
    "v_mov_b32 v245, v249\n"
    "v_mov_b32_dpp v249, v253 row_ror:8 row_mask:0xf bank_mask:0xc\n"
    "v_mov_b32_dpp v252, v244 row_ror:8 row_mask:0xf bank_mask:0x3\n"
    "v_mov_b32_dpp v253, v245 row_ror:8 row_mask:0xf bank_mask:0x3\n"
    "buffer_store_dwordx4 v[246:249], v7, s[28:31], s49 offen offset:128 nt\n"
    "buffer_store_dwordx4 v[250:253], v7, s[28:31], s49 offen offset:2176 nt\n"
    "s_branch LEND\n"
    "LHOTEND:\n"
    "LTR_0:\n"
    "s_getpc_b64 s[46:47]\n"
    "s_branch LSLOW_0_1_1\n"
    "s_branch LRET_0\n"
    "LTR_1:\n"
    "s_getpc_b64 s[46:47]\n"
    "s_branch LSLOW_1_0_0\n"
    "s_branch LRET_1\n"
    "LTR_2:\n"
    "s_getpc_b64 s[46:47]\n"
    "s_branch LSLOW_2_1_1\n"
    "s_branch LRET_2\n"
    "LTR_3:\n"
    "s_getpc_b64 s[46:47]\n"
    "s_branch LSLOW_3_0_0\n"
    "s_branch LRET_3\n"
    "LTR_4:\n"
    "s_getpc_b64 s[46:47]\n"
    "s_branch LSLOW_4_1_1\n"
    "s_branch LRET_4\n"
    "LTR_5:\n"
    "s_getpc_b64 s[46:47]\n"
    "s_branch LSLOW_0_0_0\n"
    "s_branch LRET_5\n"
    "LTR_6:\n"
    "s_getpc_b64 s[46:47]\n"
    "s_branch LSLOW_1_1_1\n"
    "s_branch LRET_6\n"
    "LTR_7:\n"
    "s_getpc_b64 s[46:47]\n"
    "s_branch LSLOW_2_0_0\n"
    "s_branch LRET_7\n"
    "LTR_8:\n"
    "s_getpc_b64 s[46:47]\n"
    "s_branch LSLOW_3_1_1\n"
    "s_branch LRET_8\n"
    "LTR_9:\n"
    "s_getpc_b64 s[46:47]\n"
    "s_branch LSLOW_4_0_0\n"
    "s_branch LRET_9\n"
    "LTR_10:\n"
    "s_getpc_b64 s[46:47]\n"
    "s_branch LSLOW_0_1_1\n"
    "s_branch LRET_10\n"
    "LTR_11:\n"
    "s_getpc_b64 s[46:47]\n"
    "s_branch LSLOW_1_0_0\n"
    "s_branch LRET_11\n"
    "LTR_12:\n"
    "s_getpc_b64 s[46:47]\n"
    "s_branch LSLOW_2_1_1\n"
    "s_branch LRET_12\n"
    "LTR_13:\n"
    "s_getpc_b64 s[46:47]\n"
    "s_branch LSLOW_3_0_0\n"
    "s_branch LRET_13\n"
    "LTR_14:\n"
    "s_getpc_b64 s[46:47]\n"
    "s_branch LSLOW_4_1_1\n"
    "s_branch LRET_14\n"
    "LTR_15:\n"
    "s_getpc_b64 s[46:47]\n"
    "s_branch LSLOW_0_0_0\n"
    "s_branch LRET_15\n"
    "LTR_16:\n"
    "s_getpc_b64 s[46:47]\n"
    "s_branch LSLOW_1_1_1\n"
    "s_branch LRET_16\n"
    "LTR_17:\n"
    "s_getpc_b64 s[46:47]\n"
    "s_branch LSLOW_2_0_0\n"
    "s_branch LRET_17\n"
    "LTR_18:\n"
    "s_getpc_b64 s[46:47]\n"
    "s_branch LSLOW_3_1_1\n"
    "s_branch LRET_18\n"
    "LTR_19:\n"
    "s_getpc_b64 s[46:47]\n"
    "s_branch LSLOW_4_0_0\n"
    "s_branch LRET_19\n"
    "LTR_20:\n"
    "s_getpc_b64 s[46:47]\n"
    "s_branch LSLOW_0_1_1\n"
    "s_branch LRET_20\n"
    "LTR_21:\n"
    "s_getpc_b64 s[46:47]\n"
    "s_branch LSLOW_1_0_0\n"
    "s_branch LRET_21\n"
    "LTR_22:\n"
    "s_getpc_b64 s[46:47]\n"
    "s_branch LSLOW_2_1_1\n"
    "s_branch LRET_22\n"
    "LTR_23:\n"
    "s_getpc_b64 s[46:47]\n"
    "s_branch LSLOW_3_0_0\n"
    "s_branch LRET_23\n"
    "LTR_24:\n"
    "s_getpc_b64 s[46:47]\n"
    "s_branch LSLOW_4_1_1\n"
    "s_branch LRET_24\n"
    "LTR_25:\n"
    "s_getpc_b64 s[46:47]\n"
    "s_branch LSLOW_0_0_0\n"
    "s_branch LRET_25\n"
    "LTR_26:\n"
    "s_getpc_b64 s[46:47]\n"
    "s_branch LSLOW_1_1_1\n"
    "s_branch LRET_26\n"
    "LTR_27:\n"
    "s_getpc_b64 s[46:47]\n"
    "s_branch LSLOW_2_0_0\n"
    "s_branch LRET_27\n"
    "LTR_28:\n"
    "s_getpc_b64 s[46:47]\n"
    "s_branch LSLOW_3_1_1\n"
    "s_branch LRET_28\n"
    "LTR_29:\n"
    "s_getpc_b64 s[46:47]\n"
    "s_branch LSLOW_4_0_0\n"
    "s_branch LRET_29\n"
    "LTR_30:\n"
    "s_getpc_b64 s[46:47]\n"
    "s_branch LSLOW_0_1_1\n"
    "s_branch LRET_30\n"
    "LTR_31:\n"
    "s_getpc_b64 s[46:47]\n"
    "s_branch LSLOW_1_0_0\n"
    "s_branch LRET_31\n"
    "LTR_32:\n"
    "s_getpc_b64 s[46:47]\n"
    "s_branch LSLOW_2_1_1\n"
    "s_branch LRET_32\n"
    "LTR_33:\n"
    "s_getpc_b64 s[46:47]\n"
    "s_branch LSLOW_3_0_0\n"
    "s_branch LRET_33\n"
    "LTR_34:\n"
    "s_getpc_b64 s[46:47]\n"
    "s_branch LSLOW_4_1_1\n"
    "s_branch LRET_34\n"
    "LTR_35:\n"
    "s_getpc_b64 s[46:47]\n"
    "s_branch LSLOW_0_1_1\n"
    "s_branch LRET_35\n"
    "LTR_36:\n"
    "s_getpc_b64 s[46:47]\n"
    "s_branch LSLOW_1_0_0\n"
    "s_branch LRET_36\n"
    "LTR_37:\n"
    "s_getpc_b64 s[46:47]\n"
    "s_branch LSLOW_2_1_1\n"
    "s_branch LRET_37\n"
    "LTR_38:\n"
    "s_getpc_b64 s[46:47]\n"
    "s_branch LSLOW_3_0_0\n"
    "s_branch LRET_38\n"
    "LTR_39:\n"
    "s_getpc_b64 s[46:47]\n"
    "s_branch LSLOW_4_1_1\n"
    "s_branch LRET_39\n"
    "LTR_40:\n"
    "s_getpc_b64 s[46:47]\n"
    "s_branch LSLOW_0_0_0\n"
    "s_branch LRET_40\n"
    "LTR_41:\n"
    "s_getpc_b64 s[46:47]\n"
    "s_branch LSLOW_1_1_1\n"
    "s_branch LRET_41\n"
    "LTR_42:\n"
    "s_getpc_b64 s[46:47]\n"
    "s_branch LSLOW_2_0_0\n"
    "s_branch LRET_42\n"
    "LTR_43:\n"
    "s_getpc_b64 s[46:47]\n"
    "s_branch LSLOW_3_1_1\n"
    "s_branch LRET_43\n"
    "LTR_44:\n"
    "s_getpc_b64 s[46:47]\n"
    "s_branch LSLOW_0_0_0\n"
    "s_branch LRET_44\n"
    "LTR_45:\n"
    "s_getpc_b64 s[46:47]\n"
    "s_branch LSLOW_1_1_1\n"
    "s_branch LRET_45\n"
    "LTR_46:\n"
    "s_getpc_b64 s[46:47]\n"
    "s_branch LSLOW_2_0_0\n"
    "s_branch LRET_46\n"
    "LTR_47:\n"
    "s_getpc_b64 s[46:47]\n"
    "s_branch LSLOW_3_1_1\n"
    "s_branch LRET_47\n"
    "LTR_48:\n"
    "s_getpc_b64 s[46:47]\n"
    "s_branch LSLOW_0_0_0\n"
    "s_branch LRET_48\n"
    "LTR_49:\n"
    "s_getpc_b64 s[46:47]\n"
    "s_branch LSLOW_1_1_1\n"
    "s_branch LRET_49\n"
    "LTR_50:\n"
    "s_getpc_b64 s[46:47]\n"
    "s_branch LSLOW_2_0_0\n"
    "s_branch LRET_50\n"
    "LTR_51:\n"
    "s_getpc_b64 s[46:47]\n"
    "s_branch LSLOW_3_1_1\n"
    "s_branch LRET_51\n"
    "LTR_52:\n"
    "s_getpc_b64 s[46:47]\n"
    "s_branch LSLOW_0_0_0\n"
    "s_branch LRET_52\n"
    "LTR_53:\n"
    "s_getpc_b64 s[46:47]\n"
    "s_branch LSLOW_1_1_1\n"
    "s_branch LRET_53\n"
    "LTR_54:\n"
    "s_getpc_b64 s[46:47]\n"
    "s_branch LSLOW_2_0_0\n"
    "s_branch LRET_54\n"
    "LTR_55:\n"
    "s_getpc_b64 s[46:47]\n"
    "s_branch LSLOW_3_1_1\n"
    "s_branch LRET_55\n"
    "LTR_56:\n"
    "s_getpc_b64 s[46:47]\n"
    "s_branch LSLOW_0_0_0\n"
    "s_branch LRET_56\n"
    "LTR_57:\n"
    "s_getpc_b64 s[46:47]\n"
    "s_branch LSLOW_1_1_1\n"
    "s_branch LRET_57\n"
    "LTR_58:\n"
    "s_getpc_b64 s[46:47]\n"
    "s_branch LSLOW_2_0_0\n"
    "s_branch LRET_58\n"
    "LTR_59:\n"
    "s_getpc_b64 s[46:47]\n"
    "s_branch LSLOW_3_1_1\n"
    "s_branch LRET_59\n"
    "LTR_60:\n"
    "s_getpc_b64 s[46:47]\n"
    "s_branch LSLOW_0_0_0\n"
    "s_branch LRET_60\n"
    "LTR_61:\n"
    "s_getpc_b64 s[46:47]\n"
    "s_branch LSLOW_1_1_1\n"
    "s_branch LRET_61\n"
    "LTR_62:\n"
    "s_getpc_b64 s[46:47]\n"
    "s_branch LSLOW_2_0_0\n"
    "s_branch LRET_62\n"
    "LTR_63:\n"
    "s_getpc_b64 s[46:47]\n"
    "s_branch LSLOW_3_1_1\n"
    "s_branch LRET_63\n"
    "LTR_64:\n"
    "s_getpc_b64 s[46:47]\n"
    "s_branch LSLOW_0_0_0\n"
    "s_branch LRET_64\n"
    "LTR_65:\n"
    "s_getpc_b64 s[46:47]\n"
    "s_branch LSLOW_1_1_1\n"
    "s_branch LRET_65\n"
    "LTR_66:\n"
    "s_getpc_b64 s[46:47]\n"
    "s_branch LSLOW_2_0_0\n"
    "s_branch LRET_66\n"
    "LTR_67:\n"
    "s_getpc_b64 s[46:47]\n"
    "s_branch LSLOW_3_1_1\n"
    "s_branch LRET_67\n"
    "LTR_68:\n"
    "s_getpc_b64 s[46:47]\n"
    "s_branch LSLOW_0_0_0\n"
    "s_branch LRET_68\n"
    "LTR_69:\n"
    "s_getpc_b64 s[46:47]\n"
    "s_branch LSLOW_1_1_1\n"
    "s_branch LRET_69\n"
    "LTR_70:\n"
    "s_getpc_b64 s[46:47]\n"
    "s_branch LSLOW_2_0_0\n"
    "s_branch LRET_70\n"
    "LTR_71:\n"
    "s_getpc_b64 s[46:47]\n"
    "s_branch LSLOW_3_1_1\n"
    "s_branch LRET_71\n"
    "LSLOW_0_1_1:\n"
    "s_nop 15\n"
    "v_max3_f32 v12, v64, v65, 0\n"
    "v_max3_f32 v12, v12, v66, v67\n"
    "v_max3_f32 v12, v12, v68, v69\n"
    "v_max3_f32 v12, v12, v70, v71\n"
    "v_max3_f32 v12, v12, v72, v73\n"
    "v_max3_f32 v12, v12, v74, v75\n"
    "v_max3_f32 v12, v12, v76, v77\n"
    "v_max3_f32 v12, v12, v78, v79\n"
    "v_mov_b32 v13, v12\n"
    "s_nop 1\n"
    "v_permlane16_swap_b32 v12, v13\n"
    "v_max_f32 v12, v12, v13\n"
    "v_mov_b32 v13, v12\n"
    "s_nop 1\n"
    "v_permlane32_swap_b32 v12, v13\n"
    "v_max_f32 v12, v12, v13\n"
    "v_exp_f32 v13, -v12\n"
    "v_accvgpr_read_b32 v14, a0\n"
    "v_accvgpr_read_b32 v15, a1\n"
    "v_mul_f32 v14, v13, v14\n"
    "v_mul_f32 v15, v13, v15\n"
    "v_accvgpr_write_b32 a0, v14\n"
    "v_accvgpr_write_b32 a1, v15\n"
    "v_accvgpr_read_b32 v14, a2\n"
    "v_accvgpr_read_b32 v15, a3\n"
    "v_mul_f32 v14, v13, v14\n"
    "v_mul_f32 v15, v13, v15\n"
    "v_accvgpr_write_b32 a2, v14\n"
    "v_accvgpr_write_b32 a3, v15\n"
    "v_accvgpr_read_b32 v14, a4\n"
    "v_accvgpr_read_b32 v15, a5\n"
    "v_mul_f32 v14, v13, v14\n"
    "v_mul_f32 v15, v13, v15\n"
    "v_accvgpr_write_b32 a4, v14\n"
    "v_accvgpr_write_b32 a5, v15\n"
    "v_accvgpr_read_b32 v14, a6\n"
    "v_accvgpr_read_b32 v15, a7\n"
    "v_mul_f32 v14, v13, v14\n"
    "v_mul_f32 v15, v13, v15\n"
    "v_accvgpr_write_b32 a6, v14\n"
    "v_accvgpr_write_b32 a7, v15\n"
    "v_accvgpr_read_b32 v14, a8\n"
    "v_accvgpr_read_b32 v15, a9\n"
    "v_mul_f32 v14, v13, v14\n"
    "v_mul_f32 v15, v13, v15\n"
    "v_accvgpr_write_b32 a8, v14\n"
    "v_accvgpr_write_b32 a9, v15\n"
    "v_accvgpr_read_b32 v14, a10\n"
    "v_accvgpr_read_b32 v15, a11\n"
    "v_mul_f32 v14, v13, v14\n"
    "v_mul_f32 v15, v13, v15\n"
    "v_accvgpr_write_b32 a10, v14\n"
    "v_accvgpr_write_b32 a11, v15\n"
    "v_accvgpr_read_b32 v14, a12\n"
    "v_accvgpr_read_b32 v15, a13\n"
    "v_mul_f32 v14, v13, v14\n"
    "v_mul_f32 v15, v13, v15\n"
    "v_accvgpr_write_b32 a12, v14\n"
    "v_accvgpr_write_b32 a13, v15\n"
    "v_accvgpr_read_b32 v14, a14\n"
    "v_accvgpr_read_b32 v15, a15\n"
    "v_mul_f32 v14, v13, v14\n"
    "v_mul_f32 v15, v13, v15\n"
    "v_accvgpr_write_b32 a14, v14\n"
    "v_accvgpr_write_b32 a15, v15\n"
    "v_accvgpr_read_b32 v14, a16\n"
    "v_accvgpr_read_b32 v15, a17\n"
    "v_mul_f32 v14, v13, v14\n"
    "v_mul_f32 v15, v13, v15\n"
    "v_accvgpr_write_b32 a16, v14\n"
    "v_accvgpr_write_b32 a17, v15\n"
    "v_accvgpr_read_b32 v14, a18\n"
    "v_accvgpr_read_b32 v15, a19\n"
    "v_mul_f32 v14, v13, v14\n"
    "v_mul_f32 v15, v13, v15\n"
    "v_accvgpr_write_b32 a18, v14\n"
    "v_accvgpr_write_b32 a19, v15\n"
    "v_accvgpr_read_b32 v14, a20\n"
    "v_accvgpr_read_b32 v15, a21\n"
    "v_mul_f32 v14, v13, v14\n"
    "v_mul_f32 v15, v13, v15\n"
    "v_accvgpr_write_b32 a20, v14\n"
    "v_accvgpr_write_b32 a21, v15\n"
    "v_accvgpr_read_b32 v14, a22\n"
    "v_accvgpr_read_b32 v15, a23\n"
    "v_mul_f32 v14, v13, v14\n"
    "v_mul_f32 v15, v13, v15\n"
    "v_accvgpr_write_b32 a22, v14\n"
    "v_accvgpr_write_b32 a23, v15\n"
    "v_accvgpr_read_b32 v14, a24\n"
    "v_accvgpr_read_b32 v15, a25\n"
    "v_mul_f32 v14, v13, v14\n"
    "v_mul_f32 v15, v13, v15\n"
    "v_accvgpr_write_b32 a24, v14\n"
    "v_accvgpr_write_b32 a25, v15\n"
    "v_accvgpr_read_b32 v14, a26\n"
    "v_accvgpr_read_b32 v15, a27\n"
    "v_mul_f32 v14, v13, v14\n"
    "v_mul_f32 v15, v13, v15\n"
    "v_accvgpr_write_b32 a26, v14\n"
    "v_accvgpr_write_b32 a27, v15\n"
    "v_accvgpr_read_b32 v14, a28\n"
    "v_accvgpr_read_b32 v15, a29\n"
    "v_mul_f32 v14, v13, v14\n"
    "v_mul_f32 v15, v13, v15\n"
    "v_accvgpr_write_b32 a28, v14\n"
    "v_accvgpr_write_b32 a29, v15\n"
    "v_accvgpr_read_b32 v14, a30\n"
    "v_accvgpr_read_b32 v15, a31\n"
    "v_mul_f32 v14, v13, v14\n"
    "v_mul_f32 v15, v13, v15\n"
    "v_accvgpr_write_b32 a30, v14\n"
    "v_accvgpr_write_b32 a31, v15\n"
    "v_mul_f32 v20, v13, v20\n"
    "v_sub_f32 v80, v80, v12\n"
    "v_sub_f32 v81, v81, v12\n"
    "v_sub_f32 v82, v82, v12\n"
    "v_sub_f32 v83, v83, v12\n"
    "v_sub_f32 v14, v64, v12\n"
    "v_sub_f32 v15, v65, v12\n"
    "v_exp_f32 v14, v14\n"
    "v_exp_f32 v15, v15\n"
    "s_nop 0\n"
    "v_cvt_pk_bf16_f32 v40, v14, v15\n"
    "v_sub_f32 v14, v66, v12\n"
    "v_sub_f32 v15, v67, v12\n"
    "v_exp_f32 v14, v14\n"
    "v_exp_f32 v15, v15\n"
    "s_nop 0\n"
    "v_cvt_pk_bf16_f32 v41, v14, v15\n"
    "v_sub_f32 v14, v68, v12\n"
    "v_sub_f32 v15, v69, v12\n"
    "v_exp_f32 v14, v14\n"
    "v_exp_f32 v15, v15\n"
    "s_nop 0\n"
    "v_cvt_pk_bf16_f32 v42, v14, v15\n"
    "v_sub_f32 v14, v70, v12\n"
    "v_sub_f32 v15, v71, v12\n"
    "v_exp_f32 v14, v14\n"
    "v_exp_f32 v15, v15\n"
    "s_nop 0\n"
    "v_cvt_pk_bf16_f32 v43, v14, v15\n"
    "v_sub_f32 v14, v72, v12\n"
    "v_sub_f32 v15, v73, v12\n"
    "v_exp_f32 v14, v14\n"
    "v_exp_f32 v15, v15\n"
    "s_nop 0\n"
    "v_cvt_pk_bf16_f32 v44, v14, v15\n"
    "v_sub_f32 v14, v74, v12\n"
    "v_sub_f32 v15, v75, v12\n"
    "v_exp_f32 v14, v14\n"
    "v_exp_f32 v15, v15\n"
    "s_nop 0\n"
    "v_cvt_pk_bf16_f32 v45, v14, v15\n"
    "v_sub_f32 v14, v76, v12\n"
    "v_sub_f32 v15, v77, v12\n"
    "v_exp_f32 v14, v14\n"
    "v_exp_f32 v15, v15\n"
    "s_nop 0\n"
    "v_cvt_pk_bf16_f32 v46, v14, v15\n"
    "v_sub_f32 v14, v78, v12\n"
    "v_sub_f32 v15, v79, v12\n"
    "v_exp_f32 v14, v14\n"
    "v_exp_f32 v15, v15\n"
    "s_nop 0\n"
    "v_cvt_pk_bf16_f32 v47, v14, v15\n"
    "v_mfma_f32_16x16x32_bf16 v[12:15], v[16:19], v[40:43], 0\n"
    "s_nop 0\n"
    "v_mfma_f32_16x16x32_bf16 v[12:15], v[16:19], v[44:47], v[12:15]\n"
    "s_nop 11\n"
    "s_add_u32 s46, s46, 4\n"
    "s_addc_u32 s47, s47, 0\n"
    "s_setpc_b64 s[46:47]\n"
    "LSLOW_1_0_0:\n"
    "s_nop 15\n"
    "v_max3_f32 v12, v48, v49, 0\n"
    "v_max3_f32 v12, v12, v50, v51\n"
    "v_max3_f32 v12, v12, v52, v53\n"
    "v_max3_f32 v12, v12, v54, v55\n"
    "v_max3_f32 v12, v12, v56, v57\n"
    "v_max3_f32 v12, v12, v58, v59\n"
    "v_max3_f32 v12, v12, v60, v61\n"
    "v_max3_f32 v12, v12, v62, v63\n"
    "v_mov_b32 v13, v12\n"
    "s_nop 1\n"
    "v_permlane16_swap_b32 v12, v13\n"
    "v_max_f32 v12, v12, v13\n"
    "v_mov_b32 v13, v12\n"
    "s_nop 1\n"
    "v_permlane32_swap_b32 v12, v13\n"
    "v_max_f32 v12, v12, v13\n"
    "v_exp_f32 v13, -v12\n"
    "v_accvgpr_read_b32 v14, a32\n"
    "v_accvgpr_read_b32 v15, a33\n"
    "v_mul_f32 v14, v13, v14\n"
    "v_mul_f32 v15, v13, v15\n"
    "v_accvgpr_write_b32 a32, v14\n"
    "v_accvgpr_write_b32 a33, v15\n"
    "v_accvgpr_read_b32 v14, a34\n"
    "v_accvgpr_read_b32 v15, a35\n"
    "v_mul_f32 v14, v13, v14\n"
    "v_mul_f32 v15, v13, v15\n"
    "v_accvgpr_write_b32 a34, v14\n"
    "v_accvgpr_write_b32 a35, v15\n"
    "v_accvgpr_read_b32 v14, a36\n"
    "v_accvgpr_read_b32 v15, a37\n"
    "v_mul_f32 v14, v13, v14\n"
    "v_mul_f32 v15, v13, v15\n"
    "v_accvgpr_write_b32 a36, v14\n"
    "v_accvgpr_write_b32 a37, v15\n"
    "v_accvgpr_read_b32 v14, a38\n"
    "v_accvgpr_read_b32 v15, a39\n"
    "v_mul_f32 v14, v13, v14\n"
    "v_mul_f32 v15, v13, v15\n"
    "v_accvgpr_write_b32 a38, v14\n"
    "v_accvgpr_write_b32 a39, v15\n"
    "v_accvgpr_read_b32 v14, a40\n"
    "v_accvgpr_read_b32 v15, a41\n"
    "v_mul_f32 v14, v13, v14\n"
    "v_mul_f32 v15, v13, v15\n"
    "v_accvgpr_write_b32 a40, v14\n"
    "v_accvgpr_write_b32 a41, v15\n"
    "v_accvgpr_read_b32 v14, a42\n"
    "v_accvgpr_read_b32 v15, a43\n"
    "v_mul_f32 v14, v13, v14\n"
    "v_mul_f32 v15, v13, v15\n"
    "v_accvgpr_write_b32 a42, v14\n"
    "v_accvgpr_write_b32 a43, v15\n"
    "v_accvgpr_read_b32 v14, a44\n"
    "v_accvgpr_read_b32 v15, a45\n"
    "v_mul_f32 v14, v13, v14\n"
    "v_mul_f32 v15, v13, v15\n"
    "v_accvgpr_write_b32 a44, v14\n"
    "v_accvgpr_write_b32 a45, v15\n"
    "v_accvgpr_read_b32 v14, a46\n"
    "v_accvgpr_read_b32 v15, a47\n"
    "v_mul_f32 v14, v13, v14\n"
    "v_mul_f32 v15, v13, v15\n"
    "v_accvgpr_write_b32 a46, v14\n"
    "v_accvgpr_write_b32 a47, v15\n"
    "v_accvgpr_read_b32 v14, a48\n"
    "v_accvgpr_read_b32 v15, a49\n"
    "v_mul_f32 v14, v13, v14\n"
    "v_mul_f32 v15, v13, v15\n"
    "v_accvgpr_write_b32 a48, v14\n"
    "v_accvgpr_write_b32 a49, v15\n"
    "v_accvgpr_read_b32 v14, a50\n"
    "v_accvgpr_read_b32 v15, a51\n"
    "v_mul_f32 v14, v13, v14\n"
    "v_mul_f32 v15, v13, v15\n"
    "v_accvgpr_write_b32 a50, v14\n"
    "v_accvgpr_write_b32 a51, v15\n"
    "v_accvgpr_read_b32 v14, a52\n"
    "v_accvgpr_read_b32 v15, a53\n"
    "v_mul_f32 v14, v13, v14\n"
    "v_mul_f32 v15, v13, v15\n"
    "v_accvgpr_write_b32 a52, v14\n"
    "v_accvgpr_write_b32 a53, v15\n"
    "v_accvgpr_read_b32 v14, a54\n"
    "v_accvgpr_read_b32 v15, a55\n"
    "v_mul_f32 v14, v13, v14\n"
    "v_mul_f32 v15, v13, v15\n"
    "v_accvgpr_write_b32 a54, v14\n"
    "v_accvgpr_write_b32 a55, v15\n"
    "v_accvgpr_read_b32 v14, a56\n"
    "v_accvgpr_read_b32 v15, a57\n"
    "v_mul_f32 v14, v13, v14\n"
    "v_mul_f32 v15, v13, v15\n"
    "v_accvgpr_write_b32 a56, v14\n"
    "v_accvgpr_write_b32 a57, v15\n"
    "v_accvgpr_read_b32 v14, a58\n"
    "v_accvgpr_read_b32 v15, a59\n"
    "v_mul_f32 v14, v13, v14\n"
    "v_mul_f32 v15, v13, v15\n"
    "v_accvgpr_write_b32 a58, v14\n"
    "v_accvgpr_write_b32 a59, v15\n"
    "v_accvgpr_read_b32 v14, a60\n"
    "v_accvgpr_read_b32 v15, a61\n"
    "v_mul_f32 v14, v13, v14\n"
    "v_mul_f32 v15, v13, v15\n"
    "v_accvgpr_write_b32 a60, v14\n"
    "v_accvgpr_write_b32 a61, v15\n"
    "v_accvgpr_read_b32 v14, a62\n"
    "v_accvgpr_read_b32 v15, a63\n"
    "v_mul_f32 v14, v13, v14\n"
    "v_mul_f32 v15, v13, v15\n"
    "v_accvgpr_write_b32 a62, v14\n"
    "v_accvgpr_write_b32 a63, v15\n"
    "v_mul_f32 v21, v13, v21\n"
    "v_sub_f32 v84, v84, v12\n"
    "v_sub_f32 v85, v85, v12\n"
    "v_sub_f32 v86, v86, v12\n"
    "v_sub_f32 v87, v87, v12\n"
    "v_sub_f32 v14, v48, v12\n"
    "v_sub_f32 v15, v49, v12\n"
    "v_exp_f32 v14, v14\n"
    "v_exp_f32 v15, v15\n"
    "s_nop 0\n"
    "v_cvt_pk_bf16_f32 v32, v14, v15\n"
    "v_sub_f32 v14, v50, v12\n"
    "v_sub_f32 v15, v51, v12\n"
    "v_exp_f32 v14, v14\n"
    "v_exp_f32 v15, v15\n"
    "s_nop 0\n"
    "v_cvt_pk_bf16_f32 v33, v14, v15\n"
    "v_sub_f32 v14, v52, v12\n"
    "v_sub_f32 v15, v53, v12\n"
    "v_exp_f32 v14, v14\n"
    "v_exp_f32 v15, v15\n"
    "s_nop 0\n"
    "v_cvt_pk_bf16_f32 v34, v14, v15\n"
    "v_sub_f32 v14, v54, v12\n"
    "v_sub_f32 v15, v55, v12\n"
    "v_exp_f32 v14, v14\n"
    "v_exp_f32 v15, v15\n"
    "s_nop 0\n"
    "v_cvt_pk_bf16_f32 v35, v14, v15\n"
    "v_sub_f32 v14, v56, v12\n"
    "v_sub_f32 v15, v57, v12\n"
    "v_exp_f32 v14, v14\n"
    "v_exp_f32 v15, v15\n"
    "s_nop 0\n"
    "v_cvt_pk_bf16_f32 v36, v14, v15\n"
    "v_sub_f32 v14, v58, v12\n"
    "v_sub_f32 v15, v59, v12\n"
    "v_exp_f32 v14, v14\n"
    "v_exp_f32 v15, v15\n"
    "s_nop 0\n"
    "v_cvt_pk_bf16_f32 v37, v14, v15\n"
    "v_sub_f32 v14, v60, v12\n"
    "v_sub_f32 v15, v61, v12\n"
    "v_exp_f32 v14, v14\n"
    "v_exp_f32 v15, v15\n"
    "s_nop 0\n"
    "v_cvt_pk_bf16_f32 v38, v14, v15\n"
    "v_sub_f32 v14, v62, v12\n"
    "v_sub_f32 v15, v63, v12\n"
    "v_exp_f32 v14, v14\n"
    "v_exp_f32 v15, v15\n"
    "s_nop 0\n"
    "v_cvt_pk_bf16_f32 v39, v14, v15\n"
    "v_mfma_f32_16x16x32_bf16 v[12:15], v[16:19], v[32:35], 0\n"
    "s_nop 0\n"
    "v_mfma_f32_16x16x32_bf16 v[12:15], v[16:19], v[36:39], v[12:15]\n"
    "s_nop 11\n"
    "s_add_u32 s46, s46, 4\n"
    "s_addc_u32 s47, s47, 0\n"
    "s_setpc_b64 s[46:47]\n"
    "LSLOW_2_1_1:\n"
    "s_nop 15\n"
    "v_max3_f32 v12, v64, v65, 0\n"
    "v_max3_f32 v12, v12, v66, v67\n"
    "v_max3_f32 v12, v12, v68, v69\n"
    "v_max3_f32 v12, v12, v70, v71\n"
    "v_max3_f32 v12, v12, v72, v73\n"
    "v_max3_f32 v12, v12, v74, v75\n"
    "v_max3_f32 v12, v12, v76, v77\n"
    "v_max3_f32 v12, v12, v78, v79\n"
    "v_mov_b32 v13, v12\n"
    "s_nop 1\n"
    "v_permlane16_swap_b32 v12, v13\n"
    "v_max_f32 v12, v12, v13\n"
    "v_mov_b32 v13, v12\n"
    "s_nop 1\n"
    "v_permlane32_swap_b32 v12, v13\n"
    "v_max_f32 v12, v12, v13\n"
    "v_exp_f32 v13, -v12\n"
    "v_accvgpr_read_b32 v14, a64\n"
    "v_accvgpr_read_b32 v15, a65\n"
    "v_mul_f32 v14, v13, v14\n"
    "v_mul_f32 v15, v13, v15\n"
    "v_accvgpr_write_b32 a64, v14\n"
    "v_accvgpr_write_b32 a65, v15\n"
    "v_accvgpr_read_b32 v14, a66\n"
    "v_accvgpr_read_b32 v15, a67\n"
    "v_mul_f32 v14, v13, v14\n"
    "v_mul_f32 v15, v13, v15\n"
    "v_accvgpr_write_b32 a66, v14\n"
    "v_accvgpr_write_b32 a67, v15\n"
    "v_accvgpr_read_b32 v14, a68\n"
    "v_accvgpr_read_b32 v15, a69\n"
    "v_mul_f32 v14, v13, v14\n"
    "v_mul_f32 v15, v13, v15\n"
    "v_accvgpr_write_b32 a68, v14\n"
    "v_accvgpr_write_b32 a69, v15\n"
    "v_accvgpr_read_b32 v14, a70\n"
    "v_accvgpr_read_b32 v15, a71\n"
    "v_mul_f32 v14, v13, v14\n"
    "v_mul_f32 v15, v13, v15\n"
    "v_accvgpr_write_b32 a70, v14\n"
    "v_accvgpr_write_b32 a71, v15\n"
    "v_accvgpr_read_b32 v14, a72\n"
    "v_accvgpr_read_b32 v15, a73\n"
    "v_mul_f32 v14, v13, v14\n"
    "v_mul_f32 v15, v13, v15\n"
    "v_accvgpr_write_b32 a72, v14\n"
    "v_accvgpr_write_b32 a73, v15\n"
    "v_accvgpr_read_b32 v14, a74\n"
    "v_accvgpr_read_b32 v15, a75\n"
    "v_mul_f32 v14, v13, v14\n"
    "v_mul_f32 v15, v13, v15\n"
    "v_accvgpr_write_b32 a74, v14\n"
    "v_accvgpr_write_b32 a75, v15\n"
    "v_accvgpr_read_b32 v14, a76\n"
    "v_accvgpr_read_b32 v15, a77\n"
    "v_mul_f32 v14, v13, v14\n"
    "v_mul_f32 v15, v13, v15\n"
    "v_accvgpr_write_b32 a76, v14\n"
    "v_accvgpr_write_b32 a77, v15\n"
    "v_accvgpr_read_b32 v14, a78\n"
    "v_accvgpr_read_b32 v15, a79\n"
    "v_mul_f32 v14, v13, v14\n"
    "v_mul_f32 v15, v13, v15\n"
    "v_accvgpr_write_b32 a78, v14\n"
    "v_accvgpr_write_b32 a79, v15\n"
    "v_accvgpr_read_b32 v14, a80\n"
    "v_accvgpr_read_b32 v15, a81\n"
    "v_mul_f32 v14, v13, v14\n"
    "v_mul_f32 v15, v13, v15\n"
    "v_accvgpr_write_b32 a80, v14\n"
    "v_accvgpr_write_b32 a81, v15\n"
    "v_accvgpr_read_b32 v14, a82\n"
    "v_accvgpr_read_b32 v15, a83\n"
    "v_mul_f32 v14, v13, v14\n"
    "v_mul_f32 v15, v13, v15\n"
    "v_accvgpr_write_b32 a82, v14\n"
    "v_accvgpr_write_b32 a83, v15\n"
    "v_accvgpr_read_b32 v14, a84\n"
    "v_accvgpr_read_b32 v15, a85\n"
    "v_mul_f32 v14, v13, v14\n"
    "v_mul_f32 v15, v13, v15\n"
    "v_accvgpr_write_b32 a84, v14\n"
    "v_accvgpr_write_b32 a85, v15\n"
    "v_accvgpr_read_b32 v14, a86\n"
    "v_accvgpr_read_b32 v15, a87\n"
    "v_mul_f32 v14, v13, v14\n"
    "v_mul_f32 v15, v13, v15\n"
    "v_accvgpr_write_b32 a86, v14\n"
    "v_accvgpr_write_b32 a87, v15\n"
    "v_accvgpr_read_b32 v14, a88\n"
    "v_accvgpr_read_b32 v15, a89\n"
    "v_mul_f32 v14, v13, v14\n"
    "v_mul_f32 v15, v13, v15\n"
    "v_accvgpr_write_b32 a88, v14\n"
    "v_accvgpr_write_b32 a89, v15\n"
    "v_accvgpr_read_b32 v14, a90\n"
    "v_accvgpr_read_b32 v15, a91\n"
    "v_mul_f32 v14, v13, v14\n"
    "v_mul_f32 v15, v13, v15\n"
    "v_accvgpr_write_b32 a90, v14\n"
    "v_accvgpr_write_b32 a91, v15\n"
    "v_accvgpr_read_b32 v14, a92\n"
    "v_accvgpr_read_b32 v15, a93\n"
    "v_mul_f32 v14, v13, v14\n"
    "v_mul_f32 v15, v13, v15\n"
    "v_accvgpr_write_b32 a92, v14\n"
    "v_accvgpr_write_b32 a93, v15\n"
    "v_accvgpr_read_b32 v14, a94\n"
    "v_accvgpr_read_b32 v15, a95\n"
    "v_mul_f32 v14, v13, v14\n"
    "v_mul_f32 v15, v13, v15\n"
    "v_accvgpr_write_b32 a94, v14\n"
    "v_accvgpr_write_b32 a95, v15\n"
    "v_mul_f32 v22, v13, v22\n"
    "v_sub_f32 v88, v88, v12\n"
    "v_sub_f32 v89, v89, v12\n"
    "v_sub_f32 v90, v90, v12\n"
    "v_sub_f32 v91, v91, v12\n"
    "v_sub_f32 v14, v64, v12\n"
    "v_sub_f32 v15, v65, v12\n"
    "v_exp_f32 v14, v14\n"
    "v_exp_f32 v15, v15\n"
    "s_nop 0\n"
    "v_cvt_pk_bf16_f32 v40, v14, v15\n"
    "v_sub_f32 v14, v66, v12\n"
    "v_sub_f32 v15, v67, v12\n"
    "v_exp_f32 v14, v14\n"
    "v_exp_f32 v15, v15\n"
    "s_nop 0\n"
    "v_cvt_pk_bf16_f32 v41, v14, v15\n"
    "v_sub_f32 v14, v68, v12\n"
    "v_sub_f32 v15, v69, v12\n"
    "v_exp_f32 v14, v14\n"
    "v_exp_f32 v15, v15\n"
    "s_nop 0\n"
    "v_cvt_pk_bf16_f32 v42, v14, v15\n"
    "v_sub_f32 v14, v70, v12\n"
    "v_sub_f32 v15, v71, v12\n"
    "v_exp_f32 v14, v14\n"
    "v_exp_f32 v15, v15\n"
    "s_nop 0\n"
    "v_cvt_pk_bf16_f32 v43, v14, v15\n"
    "v_sub_f32 v14, v72, v12\n"
    "v_sub_f32 v15, v73, v12\n"
    "v_exp_f32 v14, v14\n"
    "v_exp_f32 v15, v15\n"
    "s_nop 0\n"
    "v_cvt_pk_bf16_f32 v44, v14, v15\n"
    "v_sub_f32 v14, v74, v12\n"
    "v_sub_f32 v15, v75, v12\n"
    "v_exp_f32 v14, v14\n"
    "v_exp_f32 v15, v15\n"
    "s_nop 0\n"
    "v_cvt_pk_bf16_f32 v45, v14, v15\n"
    "v_sub_f32 v14, v76, v12\n"
    "v_sub_f32 v15, v77, v12\n"
    "v_exp_f32 v14, v14\n"
    "v_exp_f32 v15, v15\n"
    "s_nop 0\n"
    "v_cvt_pk_bf16_f32 v46, v14, v15\n"
    "v_sub_f32 v14, v78, v12\n"
    "v_sub_f32 v15, v79, v12\n"
    "v_exp_f32 v14, v14\n"
    "v_exp_f32 v15, v15\n"
    "s_nop 0\n"
    "v_cvt_pk_bf16_f32 v47, v14, v15\n"
    "v_mfma_f32_16x16x32_bf16 v[12:15], v[16:19], v[40:43], 0\n"
    "s_nop 0\n"
    "v_mfma_f32_16x16x32_bf16 v[12:15], v[16:19], v[44:47], v[12:15]\n"
    "s_nop 11\n"
    "s_add_u32 s46, s46, 4\n"
    "s_addc_u32 s47, s47, 0\n"
    "s_setpc_b64 s[46:47]\n"
    "LSLOW_3_0_0:\n"
    "s_nop 15\n"
    "v_max3_f32 v12, v48, v49, 0\n"
    "v_max3_f32 v12, v12, v50, v51\n"
    "v_max3_f32 v12, v12, v52, v53\n"
    "v_max3_f32 v12, v12, v54, v55\n"
    "v_max3_f32 v12, v12, v56, v57\n"
    "v_max3_f32 v12, v12, v58, v59\n"
    "v_max3_f32 v12, v12, v60, v61\n"
    "v_max3_f32 v12, v12, v62, v63\n"
    "v_mov_b32 v13, v12\n"
    "s_nop 1\n"
    "v_permlane16_swap_b32 v12, v13\n"
    "v_max_f32 v12, v12, v13\n"
    "v_mov_b32 v13, v12\n"
    "s_nop 1\n"
    "v_permlane32_swap_b32 v12, v13\n"
    "v_max_f32 v12, v12, v13\n"
    "v_exp_f32 v13, -v12\n"
    "v_accvgpr_read_b32 v14, a96\n"
    "v_accvgpr_read_b32 v15, a97\n"
    "v_mul_f32 v14, v13, v14\n"
    "v_mul_f32 v15, v13, v15\n"
    "v_accvgpr_write_b32 a96, v14\n"
    "v_accvgpr_write_b32 a97, v15\n"
    "v_accvgpr_read_b32 v14, a98\n"
    "v_accvgpr_read_b32 v15, a99\n"
    "v_mul_f32 v14, v13, v14\n"
    "v_mul_f32 v15, v13, v15\n"
    "v_accvgpr_write_b32 a98, v14\n"
    "v_accvgpr_write_b32 a99, v15\n"
    "v_accvgpr_read_b32 v14, a100\n"
    "v_accvgpr_read_b32 v15, a101\n"
    "v_mul_f32 v14, v13, v14\n"
    "v_mul_f32 v15, v13, v15\n"
    "v_accvgpr_write_b32 a100, v14\n"
    "v_accvgpr_write_b32 a101, v15\n"
    "v_accvgpr_read_b32 v14, a102\n"
    "v_accvgpr_read_b32 v15, a103\n"
    "v_mul_f32 v14, v13, v14\n"
    "v_mul_f32 v15, v13, v15\n"
    "v_accvgpr_write_b32 a102, v14\n"
    "v_accvgpr_write_b32 a103, v15\n"
    "v_accvgpr_read_b32 v14, a104\n"
    "v_accvgpr_read_b32 v15, a105\n"
    "v_mul_f32 v14, v13, v14\n"
    "v_mul_f32 v15, v13, v15\n"
    "v_accvgpr_write_b32 a104, v14\n"
    "v_accvgpr_write_b32 a105, v15\n"
    "v_accvgpr_read_b32 v14, a106\n"
    "v_accvgpr_read_b32 v15, a107\n"
    "v_mul_f32 v14, v13, v14\n"
    "v_mul_f32 v15, v13, v15\n"
    "v_accvgpr_write_b32 a106, v14\n"
    "v_accvgpr_write_b32 a107, v15\n"
    "v_accvgpr_read_b32 v14, a108\n"
    "v_accvgpr_read_b32 v15, a109\n"
    "v_mul_f32 v14, v13, v14\n"
    "v_mul_f32 v15, v13, v15\n"
    "v_accvgpr_write_b32 a108, v14\n"
    "v_accvgpr_write_b32 a109, v15\n"
    "v_accvgpr_read_b32 v14, a110\n"
    "v_accvgpr_read_b32 v15, a111\n"
    "v_mul_f32 v14, v13, v14\n"
    "v_mul_f32 v15, v13, v15\n"
    "v_accvgpr_write_b32 a110, v14\n"
    "v_accvgpr_write_b32 a111, v15\n"
    "v_accvgpr_read_b32 v14, a112\n"
    "v_accvgpr_read_b32 v15, a113\n"
    "v_mul_f32 v14, v13, v14\n"
    "v_mul_f32 v15, v13, v15\n"
    "v_accvgpr_write_b32 a112, v14\n"
    "v_accvgpr_write_b32 a113, v15\n"
    "v_accvgpr_read_b32 v14, a114\n"
    "v_accvgpr_read_b32 v15, a115\n"
    "v_mul_f32 v14, v13, v14\n"
    "v_mul_f32 v15, v13, v15\n"
    "v_accvgpr_write_b32 a114, v14\n"
    "v_accvgpr_write_b32 a115, v15\n"
    "v_accvgpr_read_b32 v14, a116\n"
    "v_accvgpr_read_b32 v15, a117\n"
    "v_mul_f32 v14, v13, v14\n"
    "v_mul_f32 v15, v13, v15\n"
    "v_accvgpr_write_b32 a116, v14\n"
    "v_accvgpr_write_b32 a117, v15\n"
    "v_accvgpr_read_b32 v14, a118\n"
    "v_accvgpr_read_b32 v15, a119\n"
    "v_mul_f32 v14, v13, v14\n"
    "v_mul_f32 v15, v13, v15\n"
    "v_accvgpr_write_b32 a118, v14\n"
    "v_accvgpr_write_b32 a119, v15\n"
    "v_accvgpr_read_b32 v14, a120\n"
    "v_accvgpr_read_b32 v15, a121\n"
    "v_mul_f32 v14, v13, v14\n"
    "v_mul_f32 v15, v13, v15\n"
    "v_accvgpr_write_b32 a120, v14\n"
    "v_accvgpr_write_b32 a121, v15\n"
    "v_accvgpr_read_b32 v14, a122\n"
    "v_accvgpr_read_b32 v15, a123\n"
    "v_mul_f32 v14, v13, v14\n"
    "v_mul_f32 v15, v13, v15\n"
    "v_accvgpr_write_b32 a122, v14\n"
    "v_accvgpr_write_b32 a123, v15\n"
    "v_accvgpr_read_b32 v14, a124\n"
    "v_accvgpr_read_b32 v15, a125\n"
    "v_mul_f32 v14, v13, v14\n"
    "v_mul_f32 v15, v13, v15\n"
    "v_accvgpr_write_b32 a124, v14\n"
    "v_accvgpr_write_b32 a125, v15\n"
    "v_accvgpr_read_b32 v14, a126\n"
    "v_accvgpr_read_b32 v15, a127\n"
    "v_mul_f32 v14, v13, v14\n"
    "v_mul_f32 v15, v13, v15\n"
    "v_accvgpr_write_b32 a126, v14\n"
    "v_accvgpr_write_b32 a127, v15\n"
    "v_mul_f32 v23, v13, v23\n"
    "v_sub_f32 v92, v92, v12\n"
    "v_sub_f32 v93, v93, v12\n"
    "v_sub_f32 v94, v94, v12\n"
    "v_sub_f32 v95, v95, v12\n"
    "v_sub_f32 v14, v48, v12\n"
    "v_sub_f32 v15, v49, v12\n"
    "v_exp_f32 v14, v14\n"
    "v_exp_f32 v15, v15\n"
    "s_nop 0\n"
    "v_cvt_pk_bf16_f32 v32, v14, v15\n"
    "v_sub_f32 v14, v50, v12\n"
    "v_sub_f32 v15, v51, v12\n"
    "v_exp_f32 v14, v14\n"
    "v_exp_f32 v15, v15\n"
    "s_nop 0\n"
    "v_cvt_pk_bf16_f32 v33, v14, v15\n"
    "v_sub_f32 v14, v52, v12\n"
    "v_sub_f32 v15, v53, v12\n"
    "v_exp_f32 v14, v14\n"
    "v_exp_f32 v15, v15\n"
    "s_nop 0\n"
    "v_cvt_pk_bf16_f32 v34, v14, v15\n"
    "v_sub_f32 v14, v54, v12\n"
    "v_sub_f32 v15, v55, v12\n"
    "v_exp_f32 v14, v14\n"
    "v_exp_f32 v15, v15\n"
    "s_nop 0\n"
    "v_cvt_pk_bf16_f32 v35, v14, v15\n"
    "v_sub_f32 v14, v56, v12\n"
    "v_sub_f32 v15, v57, v12\n"
    "v_exp_f32 v14, v14\n"
    "v_exp_f32 v15, v15\n"
    "s_nop 0\n"
    "v_cvt_pk_bf16_f32 v36, v14, v15\n"
    "v_sub_f32 v14, v58, v12\n"
    "v_sub_f32 v15, v59, v12\n"
    "v_exp_f32 v14, v14\n"
    "v_exp_f32 v15, v15\n"
    "s_nop 0\n"
    "v_cvt_pk_bf16_f32 v37, v14, v15\n"
    "v_sub_f32 v14, v60, v12\n"
    "v_sub_f32 v15, v61, v12\n"
    "v_exp_f32 v14, v14\n"
    "v_exp_f32 v15, v15\n"
    "s_nop 0\n"
    "v_cvt_pk_bf16_f32 v38, v14, v15\n"
    "v_sub_f32 v14, v62, v12\n"
    "v_sub_f32 v15, v63, v12\n"
    "v_exp_f32 v14, v14\n"
    "v_exp_f32 v15, v15\n"
    "s_nop 0\n"
    "v_cvt_pk_bf16_f32 v39, v14, v15\n"
    "v_mfma_f32_16x16x32_bf16 v[12:15], v[16:19], v[32:35], 0\n"
    "s_nop 0\n"
    "v_mfma_f32_16x16x32_bf16 v[12:15], v[16:19], v[36:39], v[12:15]\n"
    "s_nop 11\n"
    "s_add_u32 s46, s46, 4\n"
    "s_addc_u32 s47, s47, 0\n"
    "s_setpc_b64 s[46:47]\n"
    "LSLOW_4_1_1:\n"
    "s_nop 15\n"
    "v_max3_f32 v12, v64, v65, 0\n"
    "v_max3_f32 v12, v12, v66, v67\n"
    "v_max3_f32 v12, v12, v68, v69\n"
    "v_max3_f32 v12, v12, v70, v71\n"
    "v_max3_f32 v12, v12, v72, v73\n"
    "v_max3_f32 v12, v12, v74, v75\n"
    "v_max3_f32 v12, v12, v76, v77\n"
    "v_max3_f32 v12, v12, v78, v79\n"
    "v_mov_b32 v13, v12\n"
    "s_nop 1\n"
    "v_permlane16_swap_b32 v12, v13\n"
    "v_max_f32 v12, v12, v13\n"
    "v_mov_b32 v13, v12\n"
    "s_nop 1\n"
    "v_permlane32_swap_b32 v12, v13\n"
    "v_max_f32 v12, v12, v13\n"
    "v_exp_f32 v13, -v12\n"
    "v_accvgpr_read_b32 v14, a128\n"
    "v_accvgpr_read_b32 v15, a129\n"
    "v_mul_f32 v14, v13, v14\n"
    "v_mul_f32 v15, v13, v15\n"
    "v_accvgpr_write_b32 a128, v14\n"
    "v_accvgpr_write_b32 a129, v15\n"
    "v_accvgpr_read_b32 v14, a130\n"
    "v_accvgpr_read_b32 v15, a131\n"
    "v_mul_f32 v14, v13, v14\n"
    "v_mul_f32 v15, v13, v15\n"
    "v_accvgpr_write_b32 a130, v14\n"
    "v_accvgpr_write_b32 a131, v15\n"
    "v_accvgpr_read_b32 v14, a132\n"
    "v_accvgpr_read_b32 v15, a133\n"
    "v_mul_f32 v14, v13, v14\n"
    "v_mul_f32 v15, v13, v15\n"
    "v_accvgpr_write_b32 a132, v14\n"
    "v_accvgpr_write_b32 a133, v15\n"
    "v_accvgpr_read_b32 v14, a134\n"
    "v_accvgpr_read_b32 v15, a135\n"
    "v_mul_f32 v14, v13, v14\n"
    "v_mul_f32 v15, v13, v15\n"
    "v_accvgpr_write_b32 a134, v14\n"
    "v_accvgpr_write_b32 a135, v15\n"
    "v_accvgpr_read_b32 v14, a136\n"
    "v_accvgpr_read_b32 v15, a137\n"
    "v_mul_f32 v14, v13, v14\n"
    "v_mul_f32 v15, v13, v15\n"
    "v_accvgpr_write_b32 a136, v14\n"
    "v_accvgpr_write_b32 a137, v15\n"
    "v_accvgpr_read_b32 v14, a138\n"
    "v_accvgpr_read_b32 v15, a139\n"
    "v_mul_f32 v14, v13, v14\n"
    "v_mul_f32 v15, v13, v15\n"
    "v_accvgpr_write_b32 a138, v14\n"
    "v_accvgpr_write_b32 a139, v15\n"
    "v_accvgpr_read_b32 v14, a140\n"
    "v_accvgpr_read_b32 v15, a141\n"
    "v_mul_f32 v14, v13, v14\n"
    "v_mul_f32 v15, v13, v15\n"
    "v_accvgpr_write_b32 a140, v14\n"
    "v_accvgpr_write_b32 a141, v15\n"
    "v_accvgpr_read_b32 v14, a142\n"
    "v_accvgpr_read_b32 v15, a143\n"
    "v_mul_f32 v14, v13, v14\n"
    "v_mul_f32 v15, v13, v15\n"
    "v_accvgpr_write_b32 a142, v14\n"
    "v_accvgpr_write_b32 a143, v15\n"
    "v_accvgpr_read_b32 v14, a144\n"
    "v_accvgpr_read_b32 v15, a145\n"
    "v_mul_f32 v14, v13, v14\n"
    "v_mul_f32 v15, v13, v15\n"
    "v_accvgpr_write_b32 a144, v14\n"
    "v_accvgpr_write_b32 a145, v15\n"
    "v_accvgpr_read_b32 v14, a146\n"
    "v_accvgpr_read_b32 v15, a147\n"
    "v_mul_f32 v14, v13, v14\n"
    "v_mul_f32 v15, v13, v15\n"
    "v_accvgpr_write_b32 a146, v14\n"
    "v_accvgpr_write_b32 a147, v15\n"
    "v_accvgpr_read_b32 v14, a148\n"
    "v_accvgpr_read_b32 v15, a149\n"
    "v_mul_f32 v14, v13, v14\n"
    "v_mul_f32 v15, v13, v15\n"
    "v_accvgpr_write_b32 a148, v14\n"
    "v_accvgpr_write_b32 a149, v15\n"
    "v_accvgpr_read_b32 v14, a150\n"
    "v_accvgpr_read_b32 v15, a151\n"
    "v_mul_f32 v14, v13, v14\n"
    "v_mul_f32 v15, v13, v15\n"
    "v_accvgpr_write_b32 a150, v14\n"
    "v_accvgpr_write_b32 a151, v15\n"
    "v_accvgpr_read_b32 v14, a152\n"
    "v_accvgpr_read_b32 v15, a153\n"
    "v_mul_f32 v14, v13, v14\n"
    "v_mul_f32 v15, v13, v15\n"
    "v_accvgpr_write_b32 a152, v14\n"
    "v_accvgpr_write_b32 a153, v15\n"
    "v_accvgpr_read_b32 v14, a154\n"
    "v_accvgpr_read_b32 v15, a155\n"
    "v_mul_f32 v14, v13, v14\n"
    "v_mul_f32 v15, v13, v15\n"
    "v_accvgpr_write_b32 a154, v14\n"
    "v_accvgpr_write_b32 a155, v15\n"
    "v_accvgpr_read_b32 v14, a156\n"
    "v_accvgpr_read_b32 v15, a157\n"
    "v_mul_f32 v14, v13, v14\n"
    "v_mul_f32 v15, v13, v15\n"
    "v_accvgpr_write_b32 a156, v14\n"
    "v_accvgpr_write_b32 a157, v15\n"
    "v_accvgpr_read_b32 v14, a158\n"
    "v_accvgpr_read_b32 v15, a159\n"
    "v_mul_f32 v14, v13, v14\n"
    "v_mul_f32 v15, v13, v15\n"
    "v_accvgpr_write_b32 a158, v14\n"
    "v_accvgpr_write_b32 a159, v15\n"
    "v_mul_f32 v24, v13, v24\n"
    "v_sub_f32 v96, v96, v12\n"
    "v_sub_f32 v97, v97, v12\n"
    "v_sub_f32 v98, v98, v12\n"
    "v_sub_f32 v99, v99, v12\n"
    "v_sub_f32 v14, v64, v12\n"
    "v_sub_f32 v15, v65, v12\n"
    "v_exp_f32 v14, v14\n"
    "v_exp_f32 v15, v15\n"
    "s_nop 0\n"
    "v_cvt_pk_bf16_f32 v40, v14, v15\n"
    "v_sub_f32 v14, v66, v12\n"
    "v_sub_f32 v15, v67, v12\n"
    "v_exp_f32 v14, v14\n"
    "v_exp_f32 v15, v15\n"
    "s_nop 0\n"
    "v_cvt_pk_bf16_f32 v41, v14, v15\n"
    "v_sub_f32 v14, v68, v12\n"
    "v_sub_f32 v15, v69, v12\n"
    "v_exp_f32 v14, v14\n"
    "v_exp_f32 v15, v15\n"
    "s_nop 0\n"
    "v_cvt_pk_bf16_f32 v42, v14, v15\n"
    "v_sub_f32 v14, v70, v12\n"
    "v_sub_f32 v15, v71, v12\n"
    "v_exp_f32 v14, v14\n"
    "v_exp_f32 v15, v15\n"
    "s_nop 0\n"
    "v_cvt_pk_bf16_f32 v43, v14, v15\n"
    "v_sub_f32 v14, v72, v12\n"
    "v_sub_f32 v15, v73, v12\n"
    "v_exp_f32 v14, v14\n"
    "v_exp_f32 v15, v15\n"
    "s_nop 0\n"
    "v_cvt_pk_bf16_f32 v44, v14, v15\n"
    "v_sub_f32 v14, v74, v12\n"
    "v_sub_f32 v15, v75, v12\n"
    "v_exp_f32 v14, v14\n"
    "v_exp_f32 v15, v15\n"
    "s_nop 0\n"
    "v_cvt_pk_bf16_f32 v45, v14, v15\n"
    "v_sub_f32 v14, v76, v12\n"
    "v_sub_f32 v15, v77, v12\n"
    "v_exp_f32 v14, v14\n"
    "v_exp_f32 v15, v15\n"
    "s_nop 0\n"
    "v_cvt_pk_bf16_f32 v46, v14, v15\n"
    "v_sub_f32 v14, v78, v12\n"
    "v_sub_f32 v15, v79, v12\n"
    "v_exp_f32 v14, v14\n"
    "v_exp_f32 v15, v15\n"
    "s_nop 0\n"
    "v_cvt_pk_bf16_f32 v47, v14, v15\n"
    "v_mfma_f32_16x16x32_bf16 v[12:15], v[16:19], v[40:43], 0\n"
    "s_nop 0\n"
    "v_mfma_f32_16x16x32_bf16 v[12:15], v[16:19], v[44:47], v[12:15]\n"
    "s_nop 11\n"
    "s_add_u32 s46, s46, 4\n"
    "s_addc_u32 s47, s47, 0\n"
    "s_setpc_b64 s[46:47]\n"
    "LSLOW_0_0_0:\n"
    "s_nop 15\n"
    "v_max3_f32 v12, v48, v49, 0\n"
    "v_max3_f32 v12, v12, v50, v51\n"
    "v_max3_f32 v12, v12, v52, v53\n"
    "v_max3_f32 v12, v12, v54, v55\n"
    "v_max3_f32 v12, v12, v56, v57\n"
    "v_max3_f32 v12, v12, v58, v59\n"
    "v_max3_f32 v12, v12, v60, v61\n"
    "v_max3_f32 v12, v12, v62, v63\n"
    "v_mov_b32 v13, v12\n"
    "s_nop 1\n"
    "v_permlane16_swap_b32 v12, v13\n"
    "v_max_f32 v12, v12, v13\n"
    "v_mov_b32 v13, v12\n"
    "s_nop 1\n"
    "v_permlane32_swap_b32 v12, v13\n"
    "v_max_f32 v12, v12, v13\n"
    "v_exp_f32 v13, -v12\n"
    "v_accvgpr_read_b32 v14, a0\n"
    "v_accvgpr_read_b32 v15, a1\n"
    "v_mul_f32 v14, v13, v14\n"
    "v_mul_f32 v15, v13, v15\n"
    "v_accvgpr_write_b32 a0, v14\n"
    "v_accvgpr_write_b32 a1, v15\n"
    "v_accvgpr_read_b32 v14, a2\n"
    "v_accvgpr_read_b32 v15, a3\n"
    "v_mul_f32 v14, v13, v14\n"
    "v_mul_f32 v15, v13, v15\n"
    "v_accvgpr_write_b32 a2, v14\n"
    "v_accvgpr_write_b32 a3, v15\n"
    "v_accvgpr_read_b32 v14, a4\n"
    "v_accvgpr_read_b32 v15, a5\n"
    "v_mul_f32 v14, v13, v14\n"
    "v_mul_f32 v15, v13, v15\n"
    "v_accvgpr_write_b32 a4, v14\n"
    "v_accvgpr_write_b32 a5, v15\n"
    "v_accvgpr_read_b32 v14, a6\n"
    "v_accvgpr_read_b32 v15, a7\n"
    "v_mul_f32 v14, v13, v14\n"
    "v_mul_f32 v15, v13, v15\n"
    "v_accvgpr_write_b32 a6, v14\n"
    "v_accvgpr_write_b32 a7, v15\n"
    "v_accvgpr_read_b32 v14, a8\n"
    "v_accvgpr_read_b32 v15, a9\n"
    "v_mul_f32 v14, v13, v14\n"
    "v_mul_f32 v15, v13, v15\n"
    "v_accvgpr_write_b32 a8, v14\n"
    "v_accvgpr_write_b32 a9, v15\n"
    "v_accvgpr_read_b32 v14, a10\n"
    "v_accvgpr_read_b32 v15, a11\n"
    "v_mul_f32 v14, v13, v14\n"
    "v_mul_f32 v15, v13, v15\n"
    "v_accvgpr_write_b32 a10, v14\n"
    "v_accvgpr_write_b32 a11, v15\n"
    "v_accvgpr_read_b32 v14, a12\n"
    "v_accvgpr_read_b32 v15, a13\n"
    "v_mul_f32 v14, v13, v14\n"
    "v_mul_f32 v15, v13, v15\n"
    "v_accvgpr_write_b32 a12, v14\n"
    "v_accvgpr_write_b32 a13, v15\n"
    "v_accvgpr_read_b32 v14, a14\n"
    "v_accvgpr_read_b32 v15, a15\n"
    "v_mul_f32 v14, v13, v14\n"
    "v_mul_f32 v15, v13, v15\n"
    "v_accvgpr_write_b32 a14, v14\n"
    "v_accvgpr_write_b32 a15, v15\n"
    "v_accvgpr_read_b32 v14, a16\n"
    "v_accvgpr_read_b32 v15, a17\n"
    "v_mul_f32 v14, v13, v14\n"
    "v_mul_f32 v15, v13, v15\n"
    "v_accvgpr_write_b32 a16, v14\n"
    "v_accvgpr_write_b32 a17, v15\n"
    "v_accvgpr_read_b32 v14, a18\n"
    "v_accvgpr_read_b32 v15, a19\n"
    "v_mul_f32 v14, v13, v14\n"
    "v_mul_f32 v15, v13, v15\n"
    "v_accvgpr_write_b32 a18, v14\n"
    "v_accvgpr_write_b32 a19, v15\n"
    "v_accvgpr_read_b32 v14, a20\n"
    "v_accvgpr_read_b32 v15, a21\n"
    "v_mul_f32 v14, v13, v14\n"
    "v_mul_f32 v15, v13, v15\n"
    "v_accvgpr_write_b32 a20, v14\n"
    "v_accvgpr_write_b32 a21, v15\n"
    "v_accvgpr_read_b32 v14, a22\n"
    "v_accvgpr_read_b32 v15, a23\n"
    "v_mul_f32 v14, v13, v14\n"
    "v_mul_f32 v15, v13, v15\n"
    "v_accvgpr_write_b32 a22, v14\n"
    "v_accvgpr_write_b32 a23, v15\n"
    "v_accvgpr_read_b32 v14, a24\n"
    "v_accvgpr_read_b32 v15, a25\n"
    "v_mul_f32 v14, v13, v14\n"
    "v_mul_f32 v15, v13, v15\n"
    "v_accvgpr_write_b32 a24, v14\n"
    "v_accvgpr_write_b32 a25, v15\n"
    "v_accvgpr_read_b32 v14, a26\n"
    "v_accvgpr_read_b32 v15, a27\n"
    "v_mul_f32 v14, v13, v14\n"
    "v_mul_f32 v15, v13, v15\n"
    "v_accvgpr_write_b32 a26, v14\n"
    "v_accvgpr_write_b32 a27, v15\n"
    "v_accvgpr_read_b32 v14, a28\n"
    "v_accvgpr_read_b32 v15, a29\n"
    "v_mul_f32 v14, v13, v14\n"
    "v_mul_f32 v15, v13, v15\n"
    "v_accvgpr_write_b32 a28, v14\n"
    "v_accvgpr_write_b32 a29, v15\n"
    "v_accvgpr_read_b32 v14, a30\n"
    "v_accvgpr_read_b32 v15, a31\n"
    "v_mul_f32 v14, v13, v14\n"
    "v_mul_f32 v15, v13, v15\n"
    "v_accvgpr_write_b32 a30, v14\n"
    "v_accvgpr_write_b32 a31, v15\n"
    "v_mul_f32 v20, v13, v20\n"
    "v_sub_f32 v80, v80, v12\n"
    "v_sub_f32 v81, v81, v12\n"
    "v_sub_f32 v82, v82, v12\n"
    "v_sub_f32 v83, v83, v12\n"
    "v_sub_f32 v14, v48, v12\n"
    "v_sub_f32 v15, v49, v12\n"
    "v_exp_f32 v14, v14\n"
    "v_exp_f32 v15, v15\n"
    "s_nop 0\n"
    "v_cvt_pk_bf16_f32 v32, v14, v15\n"
    "v_sub_f32 v14, v50, v12\n"
    "v_sub_f32 v15, v51, v12\n"
    "v_exp_f32 v14, v14\n"
    "v_exp_f32 v15, v15\n"
    "s_nop 0\n"
    "v_cvt_pk_bf16_f32 v33, v14, v15\n"
    "v_sub_f32 v14, v52, v12\n"
    "v_sub_f32 v15, v53, v12\n"
    "v_exp_f32 v14, v14\n"
    "v_exp_f32 v15, v15\n"
    "s_nop 0\n"
    "v_cvt_pk_bf16_f32 v34, v14, v15\n"
    "v_sub_f32 v14, v54, v12\n"
    "v_sub_f32 v15, v55, v12\n"
    "v_exp_f32 v14, v14\n"
    "v_exp_f32 v15, v15\n"
    "s_nop 0\n"
    "v_cvt_pk_bf16_f32 v35, v14, v15\n"
    "v_sub_f32 v14, v56, v12\n"
    "v_sub_f32 v15, v57, v12\n"
    "v_exp_f32 v14, v14\n"
    "v_exp_f32 v15, v15\n"
    "s_nop 0\n"
    "v_cvt_pk_bf16_f32 v36, v14, v15\n"
    "v_sub_f32 v14, v58, v12\n"
    "v_sub_f32 v15, v59, v12\n"
    "v_exp_f32 v14, v14\n"
    "v_exp_f32 v15, v15\n"
    "s_nop 0\n"
    "v_cvt_pk_bf16_f32 v37, v14, v15\n"
    "v_sub_f32 v14, v60, v12\n"
    "v_sub_f32 v15, v61, v12\n"
    "v_exp_f32 v14, v14\n"
    "v_exp_f32 v15, v15\n"
    "s_nop 0\n"
    "v_cvt_pk_bf16_f32 v38, v14, v15\n"
    "v_sub_f32 v14, v62, v12\n"
    "v_sub_f32 v15, v63, v12\n"
    "v_exp_f32 v14, v14\n"
    "v_exp_f32 v15, v15\n"
    "s_nop 0\n"
    "v_cvt_pk_bf16_f32 v39, v14, v15\n"
    "v_mfma_f32_16x16x32_bf16 v[12:15], v[16:19], v[32:35], 0\n"
    "s_nop 0\n"
    "v_mfma_f32_16x16x32_bf16 v[12:15], v[16:19], v[36:39], v[12:15]\n"
    "s_nop 11\n"
    "s_add_u32 s46, s46, 4\n"
    "s_addc_u32 s47, s47, 0\n"
    "s_setpc_b64 s[46:47]\n"
    "LSLOW_1_1_1:\n"
    "s_nop 15\n"
    "v_max3_f32 v12, v64, v65, 0\n"
    "v_max3_f32 v12, v12, v66, v67\n"
    "v_max3_f32 v12, v12, v68, v69\n"
    "v_max3_f32 v12, v12, v70, v71\n"
    "v_max3_f32 v12, v12, v72, v73\n"
    "v_max3_f32 v12, v12, v74, v75\n"
    "v_max3_f32 v12, v12, v76, v77\n"
    "v_max3_f32 v12, v12, v78, v79\n"
    "v_mov_b32 v13, v12\n"
    "s_nop 1\n"
    "v_permlane16_swap_b32 v12, v13\n"
    "v_max_f32 v12, v12, v13\n"
    "v_mov_b32 v13, v12\n"
    "s_nop 1\n"
    "v_permlane32_swap_b32 v12, v13\n"
    "v_max_f32 v12, v12, v13\n"
    "v_exp_f32 v13, -v12\n"
    "v_accvgpr_read_b32 v14, a32\n"
    "v_accvgpr_read_b32 v15, a33\n"
    "v_mul_f32 v14, v13, v14\n"
    "v_mul_f32 v15, v13, v15\n"
    "v_accvgpr_write_b32 a32, v14\n"
    "v_accvgpr_write_b32 a33, v15\n"
    "v_accvgpr_read_b32 v14, a34\n"
    "v_accvgpr_read_b32 v15, a35\n"
    "v_mul_f32 v14, v13, v14\n"
    "v_mul_f32 v15, v13, v15\n"
    "v_accvgpr_write_b32 a34, v14\n"
    "v_accvgpr_write_b32 a35, v15\n"
    "v_accvgpr_read_b32 v14, a36\n"
    "v_accvgpr_read_b32 v15, a37\n"
    "v_mul_f32 v14, v13, v14\n"
    "v_mul_f32 v15, v13, v15\n"
    "v_accvgpr_write_b32 a36, v14\n"
    "v_accvgpr_write_b32 a37, v15\n"
    "v_accvgpr_read_b32 v14, a38\n"
    "v_accvgpr_read_b32 v15, a39\n"
    "v_mul_f32 v14, v13, v14\n"
    "v_mul_f32 v15, v13, v15\n"
    "v_accvgpr_write_b32 a38, v14\n"
    "v_accvgpr_write_b32 a39, v15\n"
    "v_accvgpr_read_b32 v14, a40\n"
    "v_accvgpr_read_b32 v15, a41\n"
    "v_mul_f32 v14, v13, v14\n"
    "v_mul_f32 v15, v13, v15\n"
    "v_accvgpr_write_b32 a40, v14\n"
    "v_accvgpr_write_b32 a41, v15\n"
    "v_accvgpr_read_b32 v14, a42\n"
    "v_accvgpr_read_b32 v15, a43\n"
    "v_mul_f32 v14, v13, v14\n"
    "v_mul_f32 v15, v13, v15\n"
    "v_accvgpr_write_b32 a42, v14\n"
    "v_accvgpr_write_b32 a43, v15\n"
    "v_accvgpr_read_b32 v14, a44\n"
    "v_accvgpr_read_b32 v15, a45\n"
    "v_mul_f32 v14, v13, v14\n"
    "v_mul_f32 v15, v13, v15\n"
    "v_accvgpr_write_b32 a44, v14\n"
    "v_accvgpr_write_b32 a45, v15\n"
    "v_accvgpr_read_b32 v14, a46\n"
    "v_accvgpr_read_b32 v15, a47\n"
    "v_mul_f32 v14, v13, v14\n"
    "v_mul_f32 v15, v13, v15\n"
    "v_accvgpr_write_b32 a46, v14\n"
    "v_accvgpr_write_b32 a47, v15\n"
    "v_accvgpr_read_b32 v14, a48\n"
    "v_accvgpr_read_b32 v15, a49\n"
    "v_mul_f32 v14, v13, v14\n"
    "v_mul_f32 v15, v13, v15\n"
    "v_accvgpr_write_b32 a48, v14\n"
    "v_accvgpr_write_b32 a49, v15\n"
    "v_accvgpr_read_b32 v14, a50\n"
    "v_accvgpr_read_b32 v15, a51\n"
    "v_mul_f32 v14, v13, v14\n"
    "v_mul_f32 v15, v13, v15\n"
    "v_accvgpr_write_b32 a50, v14\n"
    "v_accvgpr_write_b32 a51, v15\n"
    "v_accvgpr_read_b32 v14, a52\n"
    "v_accvgpr_read_b32 v15, a53\n"
    "v_mul_f32 v14, v13, v14\n"
    "v_mul_f32 v15, v13, v15\n"
    "v_accvgpr_write_b32 a52, v14\n"
    "v_accvgpr_write_b32 a53, v15\n"
    "v_accvgpr_read_b32 v14, a54\n"
    "v_accvgpr_read_b32 v15, a55\n"
    "v_mul_f32 v14, v13, v14\n"
    "v_mul_f32 v15, v13, v15\n"
    "v_accvgpr_write_b32 a54, v14\n"
    "v_accvgpr_write_b32 a55, v15\n"
    "v_accvgpr_read_b32 v14, a56\n"
    "v_accvgpr_read_b32 v15, a57\n"
    "v_mul_f32 v14, v13, v14\n"
    "v_mul_f32 v15, v13, v15\n"
    "v_accvgpr_write_b32 a56, v14\n"
    "v_accvgpr_write_b32 a57, v15\n"
    "v_accvgpr_read_b32 v14, a58\n"
    "v_accvgpr_read_b32 v15, a59\n"
    "v_mul_f32 v14, v13, v14\n"
    "v_mul_f32 v15, v13, v15\n"
    "v_accvgpr_write_b32 a58, v14\n"
    "v_accvgpr_write_b32 a59, v15\n"
    "v_accvgpr_read_b32 v14, a60\n"
    "v_accvgpr_read_b32 v15, a61\n"
    "v_mul_f32 v14, v13, v14\n"
    "v_mul_f32 v15, v13, v15\n"
    "v_accvgpr_write_b32 a60, v14\n"
    "v_accvgpr_write_b32 a61, v15\n"
    "v_accvgpr_read_b32 v14, a62\n"
    "v_accvgpr_read_b32 v15, a63\n"
    "v_mul_f32 v14, v13, v14\n"
    "v_mul_f32 v15, v13, v15\n"
    "v_accvgpr_write_b32 a62, v14\n"
    "v_accvgpr_write_b32 a63, v15\n"
    "v_mul_f32 v21, v13, v21\n"
    "v_sub_f32 v84, v84, v12\n"
    "v_sub_f32 v85, v85, v12\n"
    "v_sub_f32 v86, v86, v12\n"
    "v_sub_f32 v87, v87, v12\n"
    "v_sub_f32 v14, v64, v12\n"
    "v_sub_f32 v15, v65, v12\n"
    "v_exp_f32 v14, v14\n"
    "v_exp_f32 v15, v15\n"
    "s_nop 0\n"
    "v_cvt_pk_bf16_f32 v40, v14, v15\n"
    "v_sub_f32 v14, v66, v12\n"
    "v_sub_f32 v15, v67, v12\n"
    "v_exp_f32 v14, v14\n"
    "v_exp_f32 v15, v15\n"
    "s_nop 0\n"
    "v_cvt_pk_bf16_f32 v41, v14, v15\n"
    "v_sub_f32 v14, v68, v12\n"
    "v_sub_f32 v15, v69, v12\n"
    "v_exp_f32 v14, v14\n"
    "v_exp_f32 v15, v15\n"
    "s_nop 0\n"
    "v_cvt_pk_bf16_f32 v42, v14, v15\n"
    "v_sub_f32 v14, v70, v12\n"
    "v_sub_f32 v15, v71, v12\n"
    "v_exp_f32 v14, v14\n"
    "v_exp_f32 v15, v15\n"
    "s_nop 0\n"
    "v_cvt_pk_bf16_f32 v43, v14, v15\n"
    "v_sub_f32 v14, v72, v12\n"
    "v_sub_f32 v15, v73, v12\n"
    "v_exp_f32 v14, v14\n"
    "v_exp_f32 v15, v15\n"
    "s_nop 0\n"
    "v_cvt_pk_bf16_f32 v44, v14, v15\n"
    "v_sub_f32 v14, v74, v12\n"
    "v_sub_f32 v15, v75, v12\n"
    "v_exp_f32 v14, v14\n"
    "v_exp_f32 v15, v15\n"
    "s_nop 0\n"
    "v_cvt_pk_bf16_f32 v45, v14, v15\n"
    "v_sub_f32 v14, v76, v12\n"
    "v_sub_f32 v15, v77, v12\n"
    "v_exp_f32 v14, v14\n"
    "v_exp_f32 v15, v15\n"
    "s_nop 0\n"
    "v_cvt_pk_bf16_f32 v46, v14, v15\n"
    "v_sub_f32 v14, v78, v12\n"
    "v_sub_f32 v15, v79, v12\n"
    "v_exp_f32 v14, v14\n"
    "v_exp_f32 v15, v15\n"
    "s_nop 0\n"
    "v_cvt_pk_bf16_f32 v47, v14, v15\n"
    "v_mfma_f32_16x16x32_bf16 v[12:15], v[16:19], v[40:43], 0\n"
    "s_nop 0\n"
    "v_mfma_f32_16x16x32_bf16 v[12:15], v[16:19], v[44:47], v[12:15]\n"
    "s_nop 11\n"
    "s_add_u32 s46, s46, 4\n"
    "s_addc_u32 s47, s47, 0\n"
    "s_setpc_b64 s[46:47]\n"
    "LSLOW_2_0_0:\n"
    "s_nop 15\n"
    "v_max3_f32 v12, v48, v49, 0\n"
    "v_max3_f32 v12, v12, v50, v51\n"
    "v_max3_f32 v12, v12, v52, v53\n"
    "v_max3_f32 v12, v12, v54, v55\n"
    "v_max3_f32 v12, v12, v56, v57\n"
    "v_max3_f32 v12, v12, v58, v59\n"
    "v_max3_f32 v12, v12, v60, v61\n"
    "v_max3_f32 v12, v12, v62, v63\n"
    "v_mov_b32 v13, v12\n"
    "s_nop 1\n"
    "v_permlane16_swap_b32 v12, v13\n"
    "v_max_f32 v12, v12, v13\n"
    "v_mov_b32 v13, v12\n"
    "s_nop 1\n"
    "v_permlane32_swap_b32 v12, v13\n"
    "v_max_f32 v12, v12, v13\n"
    "v_exp_f32 v13, -v12\n"
    "v_accvgpr_read_b32 v14, a64\n"
    "v_accvgpr_read_b32 v15, a65\n"
    "v_mul_f32 v14, v13, v14\n"
    "v_mul_f32 v15, v13, v15\n"
    "v_accvgpr_write_b32 a64, v14\n"
    "v_accvgpr_write_b32 a65, v15\n"
    "v_accvgpr_read_b32 v14, a66\n"
    "v_accvgpr_read_b32 v15, a67\n"
    "v_mul_f32 v14, v13, v14\n"
    "v_mul_f32 v15, v13, v15\n"
    "v_accvgpr_write_b32 a66, v14\n"
    "v_accvgpr_write_b32 a67, v15\n"
    "v_accvgpr_read_b32 v14, a68\n"
    "v_accvgpr_read_b32 v15, a69\n"
    "v_mul_f32 v14, v13, v14\n"
    "v_mul_f32 v15, v13, v15\n"
    "v_accvgpr_write_b32 a68, v14\n"
    "v_accvgpr_write_b32 a69, v15\n"
    "v_accvgpr_read_b32 v14, a70\n"
    "v_accvgpr_read_b32 v15, a71\n"
    "v_mul_f32 v14, v13, v14\n"
    "v_mul_f32 v15, v13, v15\n"
    "v_accvgpr_write_b32 a70, v14\n"
    "v_accvgpr_write_b32 a71, v15\n"
    "v_accvgpr_read_b32 v14, a72\n"
    "v_accvgpr_read_b32 v15, a73\n"
    "v_mul_f32 v14, v13, v14\n"
    "v_mul_f32 v15, v13, v15\n"
    "v_accvgpr_write_b32 a72, v14\n"
    "v_accvgpr_write_b32 a73, v15\n"
    "v_accvgpr_read_b32 v14, a74\n"
    "v_accvgpr_read_b32 v15, a75\n"
    "v_mul_f32 v14, v13, v14\n"
    "v_mul_f32 v15, v13, v15\n"
    "v_accvgpr_write_b32 a74, v14\n"
    "v_accvgpr_write_b32 a75, v15\n"
    "v_accvgpr_read_b32 v14, a76\n"
    "v_accvgpr_read_b32 v15, a77\n"
    "v_mul_f32 v14, v13, v14\n"
    "v_mul_f32 v15, v13, v15\n"
    "v_accvgpr_write_b32 a76, v14\n"
    "v_accvgpr_write_b32 a77, v15\n"
    "v_accvgpr_read_b32 v14, a78\n"
    "v_accvgpr_read_b32 v15, a79\n"
    "v_mul_f32 v14, v13, v14\n"
    "v_mul_f32 v15, v13, v15\n"
    "v_accvgpr_write_b32 a78, v14\n"
    "v_accvgpr_write_b32 a79, v15\n"
    "v_accvgpr_read_b32 v14, a80\n"
    "v_accvgpr_read_b32 v15, a81\n"
    "v_mul_f32 v14, v13, v14\n"
    "v_mul_f32 v15, v13, v15\n"
    "v_accvgpr_write_b32 a80, v14\n"
    "v_accvgpr_write_b32 a81, v15\n"
    "v_accvgpr_read_b32 v14, a82\n"
    "v_accvgpr_read_b32 v15, a83\n"
    "v_mul_f32 v14, v13, v14\n"
    "v_mul_f32 v15, v13, v15\n"
    "v_accvgpr_write_b32 a82, v14\n"
    "v_accvgpr_write_b32 a83, v15\n"
    "v_accvgpr_read_b32 v14, a84\n"
    "v_accvgpr_read_b32 v15, a85\n"
    "v_mul_f32 v14, v13, v14\n"
    "v_mul_f32 v15, v13, v15\n"
    "v_accvgpr_write_b32 a84, v14\n"
    "v_accvgpr_write_b32 a85, v15\n"
    "v_accvgpr_read_b32 v14, a86\n"
    "v_accvgpr_read_b32 v15, a87\n"
    "v_mul_f32 v14, v13, v14\n"
    "v_mul_f32 v15, v13, v15\n"
    "v_accvgpr_write_b32 a86, v14\n"
    "v_accvgpr_write_b32 a87, v15\n"
    "v_accvgpr_read_b32 v14, a88\n"
    "v_accvgpr_read_b32 v15, a89\n"
    "v_mul_f32 v14, v13, v14\n"
    "v_mul_f32 v15, v13, v15\n"
    "v_accvgpr_write_b32 a88, v14\n"
    "v_accvgpr_write_b32 a89, v15\n"
    "v_accvgpr_read_b32 v14, a90\n"
    "v_accvgpr_read_b32 v15, a91\n"
    "v_mul_f32 v14, v13, v14\n"
    "v_mul_f32 v15, v13, v15\n"
    "v_accvgpr_write_b32 a90, v14\n"
    "v_accvgpr_write_b32 a91, v15\n"
    "v_accvgpr_read_b32 v14, a92\n"
    "v_accvgpr_read_b32 v15, a93\n"
    "v_mul_f32 v14, v13, v14\n"
    "v_mul_f32 v15, v13, v15\n"
    "v_accvgpr_write_b32 a92, v14\n"
    "v_accvgpr_write_b32 a93, v15\n"
    "v_accvgpr_read_b32 v14, a94\n"
    "v_accvgpr_read_b32 v15, a95\n"
    "v_mul_f32 v14, v13, v14\n"
    "v_mul_f32 v15, v13, v15\n"
    "v_accvgpr_write_b32 a94, v14\n"
    "v_accvgpr_write_b32 a95, v15\n"
    "v_mul_f32 v22, v13, v22\n"
    "v_sub_f32 v88, v88, v12\n"
    "v_sub_f32 v89, v89, v12\n"
    "v_sub_f32 v90, v90, v12\n"
    "v_sub_f32 v91, v91, v12\n"
    "v_sub_f32 v14, v48, v12\n"
    "v_sub_f32 v15, v49, v12\n"
    "v_exp_f32 v14, v14\n"
    "v_exp_f32 v15, v15\n"
    "s_nop 0\n"
    "v_cvt_pk_bf16_f32 v32, v14, v15\n"
    "v_sub_f32 v14, v50, v12\n"
    "v_sub_f32 v15, v51, v12\n"
    "v_exp_f32 v14, v14\n"
    "v_exp_f32 v15, v15\n"
    "s_nop 0\n"
    "v_cvt_pk_bf16_f32 v33, v14, v15\n"
    "v_sub_f32 v14, v52, v12\n"
    "v_sub_f32 v15, v53, v12\n"
    "v_exp_f32 v14, v14\n"
    "v_exp_f32 v15, v15\n"
    "s_nop 0\n"
    "v_cvt_pk_bf16_f32 v34, v14, v15\n"
    "v_sub_f32 v14, v54, v12\n"
    "v_sub_f32 v15, v55, v12\n"
    "v_exp_f32 v14, v14\n"
    "v_exp_f32 v15, v15\n"
    "s_nop 0\n"
    "v_cvt_pk_bf16_f32 v35, v14, v15\n"
    "v_sub_f32 v14, v56, v12\n"
    "v_sub_f32 v15, v57, v12\n"
    "v_exp_f32 v14, v14\n"
    "v_exp_f32 v15, v15\n"
    "s_nop 0\n"
    "v_cvt_pk_bf16_f32 v36, v14, v15\n"
    "v_sub_f32 v14, v58, v12\n"
    "v_sub_f32 v15, v59, v12\n"
    "v_exp_f32 v14, v14\n"
    "v_exp_f32 v15, v15\n"
    "s_nop 0\n"
    "v_cvt_pk_bf16_f32 v37, v14, v15\n"
    "v_sub_f32 v14, v60, v12\n"
    "v_sub_f32 v15, v61, v12\n"
    "v_exp_f32 v14, v14\n"
    "v_exp_f32 v15, v15\n"
    "s_nop 0\n"
    "v_cvt_pk_bf16_f32 v38, v14, v15\n"
    "v_sub_f32 v14, v62, v12\n"
    "v_sub_f32 v15, v63, v12\n"
    "v_exp_f32 v14, v14\n"
    "v_exp_f32 v15, v15\n"
    "s_nop 0\n"
    "v_cvt_pk_bf16_f32 v39, v14, v15\n"
    "v_mfma_f32_16x16x32_bf16 v[12:15], v[16:19], v[32:35], 0\n"
    "s_nop 0\n"
    "v_mfma_f32_16x16x32_bf16 v[12:15], v[16:19], v[36:39], v[12:15]\n"
    "s_nop 11\n"
    "s_add_u32 s46, s46, 4\n"
    "s_addc_u32 s47, s47, 0\n"
    "s_setpc_b64 s[46:47]\n"
    "LSLOW_3_1_1:\n"
    "s_nop 15\n"
    "v_max3_f32 v12, v64, v65, 0\n"
    "v_max3_f32 v12, v12, v66, v67\n"
    "v_max3_f32 v12, v12, v68, v69\n"
    "v_max3_f32 v12, v12, v70, v71\n"
    "v_max3_f32 v12, v12, v72, v73\n"
    "v_max3_f32 v12, v12, v74, v75\n"
    "v_max3_f32 v12, v12, v76, v77\n"
    "v_max3_f32 v12, v12, v78, v79\n"
    "v_mov_b32 v13, v12\n"
    "s_nop 1\n"
    "v_permlane16_swap_b32 v12, v13\n"
    "v_max_f32 v12, v12, v13\n"
    "v_mov_b32 v13, v12\n"
    "s_nop 1\n"
    "v_permlane32_swap_b32 v12, v13\n"
    "v_max_f32 v12, v12, v13\n"
    "v_exp_f32 v13, -v12\n"
    "v_accvgpr_read_b32 v14, a96\n"
    "v_accvgpr_read_b32 v15, a97\n"
    "v_mul_f32 v14, v13, v14\n"
    "v_mul_f32 v15, v13, v15\n"
    "v_accvgpr_write_b32 a96, v14\n"
    "v_accvgpr_write_b32 a97, v15\n"
    "v_accvgpr_read_b32 v14, a98\n"
    "v_accvgpr_read_b32 v15, a99\n"
    "v_mul_f32 v14, v13, v14\n"
    "v_mul_f32 v15, v13, v15\n"
    "v_accvgpr_write_b32 a98, v14\n"
    "v_accvgpr_write_b32 a99, v15\n"
    "v_accvgpr_read_b32 v14, a100\n"
    "v_accvgpr_read_b32 v15, a101\n"
    "v_mul_f32 v14, v13, v14\n"
    "v_mul_f32 v15, v13, v15\n"
    "v_accvgpr_write_b32 a100, v14\n"
    "v_accvgpr_write_b32 a101, v15\n"
    "v_accvgpr_read_b32 v14, a102\n"
    "v_accvgpr_read_b32 v15, a103\n"
    "v_mul_f32 v14, v13, v14\n"
    "v_mul_f32 v15, v13, v15\n"
    "v_accvgpr_write_b32 a102, v14\n"
    "v_accvgpr_write_b32 a103, v15\n"
    "v_accvgpr_read_b32 v14, a104\n"
    "v_accvgpr_read_b32 v15, a105\n"
    "v_mul_f32 v14, v13, v14\n"
    "v_mul_f32 v15, v13, v15\n"
    "v_accvgpr_write_b32 a104, v14\n"
    "v_accvgpr_write_b32 a105, v15\n"
    "v_accvgpr_read_b32 v14, a106\n"
    "v_accvgpr_read_b32 v15, a107\n"
    "v_mul_f32 v14, v13, v14\n"
    "v_mul_f32 v15, v13, v15\n"
    "v_accvgpr_write_b32 a106, v14\n"
    "v_accvgpr_write_b32 a107, v15\n"
    "v_accvgpr_read_b32 v14, a108\n"
    "v_accvgpr_read_b32 v15, a109\n"
    "v_mul_f32 v14, v13, v14\n"
    "v_mul_f32 v15, v13, v15\n"
    "v_accvgpr_write_b32 a108, v14\n"
    "v_accvgpr_write_b32 a109, v15\n"
    "v_accvgpr_read_b32 v14, a110\n"
    "v_accvgpr_read_b32 v15, a111\n"
    "v_mul_f32 v14, v13, v14\n"
    "v_mul_f32 v15, v13, v15\n"
    "v_accvgpr_write_b32 a110, v14\n"
    "v_accvgpr_write_b32 a111, v15\n"
    "v_accvgpr_read_b32 v14, a112\n"
    "v_accvgpr_read_b32 v15, a113\n"
    "v_mul_f32 v14, v13, v14\n"
    "v_mul_f32 v15, v13, v15\n"
    "v_accvgpr_write_b32 a112, v14\n"
    "v_accvgpr_write_b32 a113, v15\n"
    "v_accvgpr_read_b32 v14, a114\n"
    "v_accvgpr_read_b32 v15, a115\n"
    "v_mul_f32 v14, v13, v14\n"
    "v_mul_f32 v15, v13, v15\n"
    "v_accvgpr_write_b32 a114, v14\n"
    "v_accvgpr_write_b32 a115, v15\n"
    "v_accvgpr_read_b32 v14, a116\n"
    "v_accvgpr_read_b32 v15, a117\n"
    "v_mul_f32 v14, v13, v14\n"
    "v_mul_f32 v15, v13, v15\n"
    "v_accvgpr_write_b32 a116, v14\n"
    "v_accvgpr_write_b32 a117, v15\n"
    "v_accvgpr_read_b32 v14, a118\n"
    "v_accvgpr_read_b32 v15, a119\n"
    "v_mul_f32 v14, v13, v14\n"
    "v_mul_f32 v15, v13, v15\n"
    "v_accvgpr_write_b32 a118, v14\n"
    "v_accvgpr_write_b32 a119, v15\n"
    "v_accvgpr_read_b32 v14, a120\n"
    "v_accvgpr_read_b32 v15, a121\n"
    "v_mul_f32 v14, v13, v14\n"
    "v_mul_f32 v15, v13, v15\n"
    "v_accvgpr_write_b32 a120, v14\n"
    "v_accvgpr_write_b32 a121, v15\n"
    "v_accvgpr_read_b32 v14, a122\n"
    "v_accvgpr_read_b32 v15, a123\n"
    "v_mul_f32 v14, v13, v14\n"
    "v_mul_f32 v15, v13, v15\n"
    "v_accvgpr_write_b32 a122, v14\n"
    "v_accvgpr_write_b32 a123, v15\n"
    "v_accvgpr_read_b32 v14, a124\n"
    "v_accvgpr_read_b32 v15, a125\n"
    "v_mul_f32 v14, v13, v14\n"
    "v_mul_f32 v15, v13, v15\n"
    "v_accvgpr_write_b32 a124, v14\n"
    "v_accvgpr_write_b32 a125, v15\n"
    "v_accvgpr_read_b32 v14, a126\n"
    "v_accvgpr_read_b32 v15, a127\n"
    "v_mul_f32 v14, v13, v14\n"
    "v_mul_f32 v15, v13, v15\n"
    "v_accvgpr_write_b32 a126, v14\n"
    "v_accvgpr_write_b32 a127, v15\n"
    "v_mul_f32 v23, v13, v23\n"
    "v_sub_f32 v92, v92, v12\n"
    "v_sub_f32 v93, v93, v12\n"
    "v_sub_f32 v94, v94, v12\n"
    "v_sub_f32 v95, v95, v12\n"
    "v_sub_f32 v14, v64, v12\n"
    "v_sub_f32 v15, v65, v12\n"
    "v_exp_f32 v14, v14\n"
    "v_exp_f32 v15, v15\n"
    "s_nop 0\n"
    "v_cvt_pk_bf16_f32 v40, v14, v15\n"
    "v_sub_f32 v14, v66, v12\n"
    "v_sub_f32 v15, v67, v12\n"
    "v_exp_f32 v14, v14\n"
    "v_exp_f32 v15, v15\n"
    "s_nop 0\n"
    "v_cvt_pk_bf16_f32 v41, v14, v15\n"
    "v_sub_f32 v14, v68, v12\n"
    "v_sub_f32 v15, v69, v12\n"
    "v_exp_f32 v14, v14\n"
    "v_exp_f32 v15, v15\n"
    "s_nop 0\n"
    "v_cvt_pk_bf16_f32 v42, v14, v15\n"
    "v_sub_f32 v14, v70, v12\n"
    "v_sub_f32 v15, v71, v12\n"
    "v_exp_f32 v14, v14\n"
    "v_exp_f32 v15, v15\n"
    "s_nop 0\n"
    "v_cvt_pk_bf16_f32 v43, v14, v15\n"
    "v_sub_f32 v14, v72, v12\n"
    "v_sub_f32 v15, v73, v12\n"
    "v_exp_f32 v14, v14\n"
    "v_exp_f32 v15, v15\n"
    "s_nop 0\n"
    "v_cvt_pk_bf16_f32 v44, v14, v15\n"
    "v_sub_f32 v14, v74, v12\n"
    "v_sub_f32 v15, v75, v12\n"
    "v_exp_f32 v14, v14\n"
    "v_exp_f32 v15, v15\n"
    "s_nop 0\n"
    "v_cvt_pk_bf16_f32 v45, v14, v15\n"
    "v_sub_f32 v14, v76, v12\n"
    "v_sub_f32 v15, v77, v12\n"
    "v_exp_f32 v14, v14\n"
    "v_exp_f32 v15, v15\n"
    "s_nop 0\n"
    "v_cvt_pk_bf16_f32 v46, v14, v15\n"
    "v_sub_f32 v14, v78, v12\n"
    "v_sub_f32 v15, v79, v12\n"
    "v_exp_f32 v14, v14\n"
    "v_exp_f32 v15, v15\n"
    "s_nop 0\n"
    "v_cvt_pk_bf16_f32 v47, v14, v15\n"
    "v_mfma_f32_16x16x32_bf16 v[12:15], v[16:19], v[40:43], 0\n"
    "s_nop 0\n"
    "v_mfma_f32_16x16x32_bf16 v[12:15], v[16:19], v[44:47], v[12:15]\n"
    "s_nop 11\n"
    "s_add_u32 s46, s46, 4\n"
    "s_addc_u32 s47, s47, 0\n"
    "s_setpc_b64 s[46:47]\n"
    "LSLOW_4_0_0:\n"
    "s_nop 15\n"
    "v_max3_f32 v12, v48, v49, 0\n"
    "v_max3_f32 v12, v12, v50, v51\n"
    "v_max3_f32 v12, v12, v52, v53\n"
    "v_max3_f32 v12, v12, v54, v55\n"
    "v_max3_f32 v12, v12, v56, v57\n"
    "v_max3_f32 v12, v12, v58, v59\n"
    "v_max3_f32 v12, v12, v60, v61\n"
    "v_max3_f32 v12, v12, v62, v63\n"
    "v_mov_b32 v13, v12\n"
    "s_nop 1\n"
    "v_permlane16_swap_b32 v12, v13\n"
    "v_max_f32 v12, v12, v13\n"
    "v_mov_b32 v13, v12\n"
    "s_nop 1\n"
    "v_permlane32_swap_b32 v12, v13\n"
    "v_max_f32 v12, v12, v13\n"
    "v_exp_f32 v13, -v12\n"
    "v_accvgpr_read_b32 v14, a128\n"
    "v_accvgpr_read_b32 v15, a129\n"
    "v_mul_f32 v14, v13, v14\n"
    "v_mul_f32 v15, v13, v15\n"
    "v_accvgpr_write_b32 a128, v14\n"
    "v_accvgpr_write_b32 a129, v15\n"
    "v_accvgpr_read_b32 v14, a130\n"
    "v_accvgpr_read_b32 v15, a131\n"
    "v_mul_f32 v14, v13, v14\n"
    "v_mul_f32 v15, v13, v15\n"
    "v_accvgpr_write_b32 a130, v14\n"
    "v_accvgpr_write_b32 a131, v15\n"
    "v_accvgpr_read_b32 v14, a132\n"
    "v_accvgpr_read_b32 v15, a133\n"
    "v_mul_f32 v14, v13, v14\n"
    "v_mul_f32 v15, v13, v15\n"
    "v_accvgpr_write_b32 a132, v14\n"
    "v_accvgpr_write_b32 a133, v15\n"
    "v_accvgpr_read_b32 v14, a134\n"
    "v_accvgpr_read_b32 v15, a135\n"
    "v_mul_f32 v14, v13, v14\n"
    "v_mul_f32 v15, v13, v15\n"
    "v_accvgpr_write_b32 a134, v14\n"
    "v_accvgpr_write_b32 a135, v15\n"
    "v_accvgpr_read_b32 v14, a136\n"
    "v_accvgpr_read_b32 v15, a137\n"
    "v_mul_f32 v14, v13, v14\n"
    "v_mul_f32 v15, v13, v15\n"
    "v_accvgpr_write_b32 a136, v14\n"
    "v_accvgpr_write_b32 a137, v15\n"
    "v_accvgpr_read_b32 v14, a138\n"
    "v_accvgpr_read_b32 v15, a139\n"
    "v_mul_f32 v14, v13, v14\n"
    "v_mul_f32 v15, v13, v15\n"
    "v_accvgpr_write_b32 a138, v14\n"
    "v_accvgpr_write_b32 a139, v15\n"
    "v_accvgpr_read_b32 v14, a140\n"
    "v_accvgpr_read_b32 v15, a141\n"
    "v_mul_f32 v14, v13, v14\n"
    "v_mul_f32 v15, v13, v15\n"
    "v_accvgpr_write_b32 a140, v14\n"
    "v_accvgpr_write_b32 a141, v15\n"
    "v_accvgpr_read_b32 v14, a142\n"
    "v_accvgpr_read_b32 v15, a143\n"
    "v_mul_f32 v14, v13, v14\n"
    "v_mul_f32 v15, v13, v15\n"
    "v_accvgpr_write_b32 a142, v14\n"
    "v_accvgpr_write_b32 a143, v15\n"
    "v_accvgpr_read_b32 v14, a144\n"
    "v_accvgpr_read_b32 v15, a145\n"
    "v_mul_f32 v14, v13, v14\n"
    "v_mul_f32 v15, v13, v15\n"
    "v_accvgpr_write_b32 a144, v14\n"
    "v_accvgpr_write_b32 a145, v15\n"
    "v_accvgpr_read_b32 v14, a146\n"
    "v_accvgpr_read_b32 v15, a147\n"
    "v_mul_f32 v14, v13, v14\n"
    "v_mul_f32 v15, v13, v15\n"
    "v_accvgpr_write_b32 a146, v14\n"
    "v_accvgpr_write_b32 a147, v15\n"
    "v_accvgpr_read_b32 v14, a148\n"
    "v_accvgpr_read_b32 v15, a149\n"
    "v_mul_f32 v14, v13, v14\n"
    "v_mul_f32 v15, v13, v15\n"
    "v_accvgpr_write_b32 a148, v14\n"
    "v_accvgpr_write_b32 a149, v15\n"
    "v_accvgpr_read_b32 v14, a150\n"
    "v_accvgpr_read_b32 v15, a151\n"
    "v_mul_f32 v14, v13, v14\n"
    "v_mul_f32 v15, v13, v15\n"
    "v_accvgpr_write_b32 a150, v14\n"
    "v_accvgpr_write_b32 a151, v15\n"
    "v_accvgpr_read_b32 v14, a152\n"
    "v_accvgpr_read_b32 v15, a153\n"
    "v_mul_f32 v14, v13, v14\n"
    "v_mul_f32 v15, v13, v15\n"
    "v_accvgpr_write_b32 a152, v14\n"
    "v_accvgpr_write_b32 a153, v15\n"
    "v_accvgpr_read_b32 v14, a154\n"
    "v_accvgpr_read_b32 v15, a155\n"
    "v_mul_f32 v14, v13, v14\n"
    "v_mul_f32 v15, v13, v15\n"
    "v_accvgpr_write_b32 a154, v14\n"
    "v_accvgpr_write_b32 a155, v15\n"
    "v_accvgpr_read_b32 v14, a156\n"
    "v_accvgpr_read_b32 v15, a157\n"
    "v_mul_f32 v14, v13, v14\n"
    "v_mul_f32 v15, v13, v15\n"
    "v_accvgpr_write_b32 a156, v14\n"
    "v_accvgpr_write_b32 a157, v15\n"
    "v_accvgpr_read_b32 v14, a158\n"
    "v_accvgpr_read_b32 v15, a159\n"
    "v_mul_f32 v14, v13, v14\n"
    "v_mul_f32 v15, v13, v15\n"
    "v_accvgpr_write_b32 a158, v14\n"
    "v_accvgpr_write_b32 a159, v15\n"
    "v_mul_f32 v24, v13, v24\n"
    "v_sub_f32 v96, v96, v12\n"
    "v_sub_f32 v97, v97, v12\n"
    "v_sub_f32 v98, v98, v12\n"
    "v_sub_f32 v99, v99, v12\n"
    "v_sub_f32 v14, v48, v12\n"
    "v_sub_f32 v15, v49, v12\n"
    "v_exp_f32 v14, v14\n"
    "v_exp_f32 v15, v15\n"
    "s_nop 0\n"
    "v_cvt_pk_bf16_f32 v32, v14, v15\n"
    "v_sub_f32 v14, v50, v12\n"
    "v_sub_f32 v15, v51, v12\n"
    "v_exp_f32 v14, v14\n"
    "v_exp_f32 v15, v15\n"
    "s_nop 0\n"
    "v_cvt_pk_bf16_f32 v33, v14, v15\n"
    "v_sub_f32 v14, v52, v12\n"
    "v_sub_f32 v15, v53, v12\n"
    "v_exp_f32 v14, v14\n"
    "v_exp_f32 v15, v15\n"
    "s_nop 0\n"
    "v_cvt_pk_bf16_f32 v34, v14, v15\n"
    "v_sub_f32 v14, v54, v12\n"
    "v_sub_f32 v15, v55, v12\n"
    "v_exp_f32 v14, v14\n"
    "v_exp_f32 v15, v15\n"
    "s_nop 0\n"
    "v_cvt_pk_bf16_f32 v35, v14, v15\n"
    "v_sub_f32 v14, v56, v12\n"
    "v_sub_f32 v15, v57, v12\n"
    "v_exp_f32 v14, v14\n"
    "v_exp_f32 v15, v15\n"
    "s_nop 0\n"
    "v_cvt_pk_bf16_f32 v36, v14, v15\n"
    "v_sub_f32 v14, v58, v12\n"
    "v_sub_f32 v15, v59, v12\n"
    "v_exp_f32 v14, v14\n"
    "v_exp_f32 v15, v15\n"
    "s_nop 0\n"
    "v_cvt_pk_bf16_f32 v37, v14, v15\n"
    "v_sub_f32 v14, v60, v12\n"
    "v_sub_f32 v15, v61, v12\n"
    "v_exp_f32 v14, v14\n"
    "v_exp_f32 v15, v15\n"
    "s_nop 0\n"
    "v_cvt_pk_bf16_f32 v38, v14, v15\n"
    "v_sub_f32 v14, v62, v12\n"
    "v_sub_f32 v15, v63, v12\n"
    "v_exp_f32 v14, v14\n"
    "v_exp_f32 v15, v15\n"
    "s_nop 0\n"
    "v_cvt_pk_bf16_f32 v39, v14, v15\n"
    "v_mfma_f32_16x16x32_bf16 v[12:15], v[16:19], v[32:35], 0\n"
    "s_nop 0\n"
    "v_mfma_f32_16x16x32_bf16 v[12:15], v[16:19], v[36:39], v[12:15]\n"
    "s_nop 11\n"
    "s_add_u32 s46, s46, 4\n"
    "s_addc_u32 s47, s47, 0\n"
    "s_setpc_b64 s[46:47]\n"
    "LEND:\n"
    :: "s"(Q), "s"(K), "s"(V), "s"(O), "s"(blockIdx.x), "v"(threadIdx.x)
    : "v1", "v2", "v3", "v4", "v5", "v6", "v7", "v8", "v9", "v10", "v11", "v12", "v13", "v14", "v15", "v16", "v17", "v18", "v19", "v20", "v21", "v22", "v23", "v24", "v25", "v26", "v27", "v28", "v29", "v30", "v31", "v32", "v33", "v34", "v35", "v36", "v37", "v38", "v39", "v40", "v41", "v42", "v43", "v44", "v45", "v46", "v47", "v48", "v49", "v50", "v51", "v52", "v53", "v54", "v55", "v56", "v57", "v58", "v59", "v60", "v61", "v62", "v63", "v64", "v65", "v66", "v67", "v68", "v69", "v70", "v71", "v72", "v73", "v74", "v75", "v76", "v77", "v78", "v79", "v80", "v81", "v82", "v83", "v84", "v85", "v86", "v87", "v88", "v89", "v90", "v91", "v92", "v93", "v94", "v95", "v96", "v97", "v98", "v99", "v100", "v101", "v102", "v103", "v104", "v105", "v106", "v107", "v108", "v109", "v110", "v111", "v112", "v113", "v114", "v115", "v116", "v117", "v118", "v119", "v120", "v121", "v122", "v123", "v124", "v125", "v126", "v127", "v128", "v129", "v130", "v131", "v132", "v133", "v134", "v135", "v136", "v137", "v138", "v139", "v140", "v141", "v142", "v143", "v144", "v145", "v146", "v147", "v148", "v149", "v150", "v151", "v152", "v153", "v154", "v155", "v156", "v157", "v158", "v159", "v160", "v161", "v162", "v163", "v164", "v165", "v166", "v167", "v168", "v169", "v170", "v171", "v172", "v173", "v174", "v175", "v176", "v177", "v178", "v179", "v180", "v181", "v182", "v183", "v184", "v185", "v186", "v187", "v188", "v189", "v190", "v191", "v192", "v193", "v194", "v195", "v196", "v197", "v198", "v199", "v200", "v201", "v202", "v203", "v204", "v205", "v206", "v207", "v208", "v209", "v210", "v211", "v212", "v213", "v214", "v215", "v216", "v217", "v218", "v219", "v220", "v221", "v222", "v223", "v224", "v225", "v226", "v227", "v228", "v229", "v230", "v231", "v232", "v233", "v234", "v235", "v236", "v237", "v238", "v239", "v240", "v241", "v242", "v243", "v244", "v245", "v246", "v247", "v248", "v249", "v250", "v251", "v252", "v253", "v254", "v255", "a0", "a1", "a2", "a3", "a4", "a5", "a6", "a7", "a8", "a9", "a10", "a11", "a12", "a13", "a14", "a15", "a16", "a17", "a18", "a19", "a20", "a21", "a22", "a23", "a24", "a25", "a26", "a27", "a28", "a29", "a30", "a31", "a32", "a33", "a34", "a35", "a36", "a37", "a38", "a39", "a40", "a41", "a42", "a43", "a44", "a45", "a46", "a47", "a48", "a49", "a50", "a51", "a52", "a53", "a54", "a55", "a56", "a57", "a58", "a59", "a60", "a61", "a62", "a63", "a64", "a65", "a66", "a67", "a68", "a69", "a70", "a71", "a72", "a73", "a74", "a75", "a76", "a77", "a78", "a79", "a80", "a81", "a82", "a83", "a84", "a85", "a86", "a87", "a88", "a89", "a90", "a91", "a92", "a93", "a94", "a95", "a96", "a97", "a98", "a99", "a100", "a101", "a102", "a103", "a104", "a105", "a106", "a107", "a108", "a109", "a110", "a111", "a112", "a113", "a114", "a115", "a116", "a117", "a118", "a119", "a120", "a121", "a122", "a123", "a124", "a125", "a126", "a127", "a128", "a129", "a130", "a131", "a132", "a133", "a134", "a135", "a136", "a137", "a138", "a139", "a140", "a141", "a142", "a143", "a144", "a145", "a146", "a147", "a148", "a149", "a150", "a151", "a152", "a153", "a154", "a155", "a156", "a157", "a158", "a159", "a160", "a161", "a162", "a163", "a164", "a165", "a166", "a167", "a168", "a169", "a170", "a171", "a172", "a173", "a174", "a175", "a176", "a177", "a178", "a179", "a180", "a181", "a182", "a183", "a184", "a185", "a186", "a187", "a188", "a189", "a190", "a191", "a192", "a193", "a194", "a195", "a196", "a197", "a198", "a199", "a200", "a201", "a202", "a203", "a204", "a205", "a206", "a207", "a208", "a209", "a210", "a211", "a212", "a213", "a214", "a215", "a216", "a217", "a218", "a219", "a220", "a221", "a222", "a223", "a224", "a225", "a226", "a227", "a228", "a229", "a230", "a231", "a232", "a233", "a234", "a235", "a236", "a237", "a238", "a239", "a240", "a241", "a242", "a243", "a244", "a245", "a246", "a247", "a248", "a249", "a250", "a251", "a252", "a253", "a254", "a255", "s16", "s17", "s18", "s19", "s20", "s21", "s22", "s23", "s24", "s25", "s26", "s27", "s28", "s29", "s30", "s31", "s32", "s33", "s34", "s35", "s36", "s37", "s38", "s39", "s40", "s41", "s42", "s43", "s44", "s45", "s46", "s47", "s48", "s49", "s50", "s51", "s52", "s53", "s54", "s55", "s56", "s57", "s58", "s59", "s60", "s61", "s62", "s63", "s64", "s65", "s66", "s67", "s68", "s69", "s70", "s71", "s72", "s73", "s74", "s75", "s76", "s77", "s78", "s79", "s80", "s81", "s82", "s83", "s84", "s85", "s86", "s87", "s88", "s89", "s90", "s91", "s92", "s93", "s94", "s95", "s96", "s97", "s98", "s99", "vcc", "scc", "memory");
}

extern "C" void kernel_launch(void* const* d_in, const int* in_sizes, int n_in, void* d_out, int out_size, void* d_ws, size_t ws_size, hipStream_t stream) {
  (void)hipFuncSetAttribute((const void*)attn_fwd_asm, hipFuncAttributeMaxDynamicSharedMemorySize, 162816);
  hipLaunchKernelGGL(attn_fwd_asm, dim3(256), dim3(256), 162816, stream, d_in[0], d_in[1], d_in[2], d_out);
}
```
